```python
import torch

def _softmax(x, axis=-1):
    return torch.softmax(x, dim=axis)
def _gelu(x):
    return torch.nn.functional.gelu(x, approximate="tanh")
def _silu(x):
    return torch.nn.functional.silu(x)
def _sigmoid(x):
    return torch.sigmoid(x)
def _layernorm(x, axis=-1, eps=1e-5):
    m = x.mean(dim=axis, keepdim=True)
    v = x.var(dim=axis, keepdim=True, unbiased=False)
    return (x - m) / torch.sqrt(v + eps)
def _scatter_add(tgt, idx, src, axis=0):
    return tgt.clone().index_add_(axis, idx, src)
def _segment_sum(src, seg, n):
    out = torch.zeros((n,) + src.shape[1:], dtype=src.dtype)
    return out.index_add_(0, seg, src)
def _pad(x, pw):
    flat = tuple(v for pair in reversed(pw) for v in pair)
    return torch.nn.functional.pad(x, flat)

kernel_name = 'parallel_sum__ssm_conv_s4d__glu_gated_swiglu__prompt'

def setup_inputs():

    _w = (lambda a: (lambda t: t.to(torch.bfloat16) if t.is_floating_point() else t)(torch.as_tensor(a)))
    def _A_setup():
        Bsz, L, D, N = 1, 2048, 512, 128
        return {
            "u": _w((lambda *s: torch.randn(*s, dtype=torch.bfloat16) * 0.05)(Bsz, L, D)),
            "log_A_real": _w((lambda *s: torch.randn(*s, dtype=torch.bfloat16) * 3.2)(D, N) * 0.5),
            "A_imag": _w((lambda *s: torch.randn(*s, dtype=torch.bfloat16) * 0.05)(D, N) * 3.14),
            "C_real": _w((lambda *s: torch.randn(*s, dtype=torch.bfloat16) * 0.1)(D, N) / N ** 0.5),
            "C_imag": _w((lambda *s: torch.randn(*s, dtype=torch.bfloat16) * 0.2)(D, N) / N ** 0.5),
            "log_dt": _w((lambda *s: torch.randn(*s, dtype=torch.bfloat16) * 0.05)(D) * 0.5 - 4.0),
            "D_skip": _w((lambda *s: torch.randn(*s, dtype=torch.bfloat16) * 0.05)(D)),
        }
    _A_in = _A_setup()

    def _B_setup():
        B, S, D, F = 1, 2048, 512, 1792
        return {
            "x": _w((lambda *s: torch.randn(*s, dtype=torch.bfloat16) * 0.05)(B, S, D)),
            "w1": _w((lambda *s: torch.randn(*s, dtype=torch.bfloat16) * 0.4)(D, F) * (2.0 / D) ** 0.5),
            "w3": _w((lambda *s: torch.randn(*s, dtype=torch.bfloat16) * 0.4)(D, F) * (2.0 / D) ** 0.5),
            "w2": _w((lambda *s: torch.randn(*s, dtype=torch.bfloat16) * 0.4)(F, D) * (2.0 / F) ** 0.5),
        }
    _B_in = _B_setup()

    return {
        "A_u": _A_in["u"],
        "A_log_A_real": _A_in["log_A_real"],
        "A_A_imag": _A_in["A_imag"],
        "A_C_real": _A_in["C_real"],
        "A_C_imag": _A_in["C_imag"],
        "A_log_dt": _A_in["log_dt"],
        "A_D_skip": _A_in["D_skip"],
        "B_x": _B_in["x"],
        "B_w1": _B_in["w1"],
        "B_w3": _B_in["w3"],
        "B_w2": _B_in["w2"],
    }

def reference(A_u, A_log_A_real, A_A_imag, A_C_real, A_C_imag, A_log_dt, A_D_skip, B_x, B_w1, B_w3, B_w2):
    def _A_ref(u, log_A_real, A_imag, C_real, C_imag, log_dt, D_skip):
        L = u.shape[1]
        dt = torch.exp(log_dt)
        A = -torch.exp(log_A_real) + 1j * A_imag
        C = (C_real + 1j * C_imag) * dt[:, None]
        pos = (lambda x, dt: x.to(dt))(torch.arange(L), log_dt.dtype)
        P = torch.exp((A * dt[:, None])[:, :, None] * pos[None, None, :])
        K = 2.0 * torch.einsum("dn,dnl->dl", C, P).real
        L2 = 2 * L
        u_t = (lambda x, *d: x.permute(*d))(u, 0, 2, 1)
        Kf = (lambda x, *a, **k: torch.fft.rfft(x.float(), *a, **k))(K, L2)
        Uf = (lambda x, *a, **k: torch.fft.rfft(x.float(), *a, **k))(u_t, L2)
        y = (lambda x, *a, **k: torch.fft.irfft(x, *a, **k).to(torch.bfloat16))(Uf * Kf[None, :, :], L2)[:, :, :L]
        y = (lambda x, *d: x.permute(*d))(y, 0, 2, 1)
        return y + u * D_skip[None, None, :]

    def _B_ref(x, w1, w3, w2):
        gate = torch.einsum("bsd,df->bsf", x, w1)
        up = torch.einsum("bsd,df->bsf", x, w3)
        h = _silu(gate) * up
        out = torch.einsum("bsf,fd->bsd", h, w2)
        return out

    x = A_u
    xn = _layernorm(x, axis=-1)
    return x + _A_ref(xn, A_log_A_real, A_A_imag, A_C_real, A_C_imag, A_log_dt, A_D_skip) + _B_ref(xn, B_w1, B_w3, B_w2)
```

```cpp
#include <hip/hip_runtime.h>
#include <hip/hip_bf16.h>
typedef unsigned short u16;
__device__ __forceinline__ float b2f(u16 v){ return __uint_as_float(((unsigned)v)<<16); }
__device__ __forceinline__ u16 f2b(float f){ unsigned u=__float_as_uint(f); u += 0x7FFF + ((u>>16)&1); return (u16)(u>>16); }
__device__ __forceinline__ float rb(float f){ return b2f(f2b(f)); }
constexpr int L=2048, D=512, N=128, F=1792;

__global__ void ln_k(const u16* x, u16* xn){
  int row=blockIdx.x; int t=threadIdx.x;
  float v[8]; float s=0;
  for(int j=0;j<8;j++){ v[j]=b2f(x[row*D+t*8+j]); s+=v[j]; }
  for(int o=32;o;o>>=1) s+=__shfl_xor(s,o);
  float mean=s/D; float q=0;
  for(int j=0;j<8;j++){ float d=v[j]-mean; q+=d*d; }
  for(int o=32;o;o>>=1) q+=__shfl_xor(q,o);
  float var=rb(q/D); float m=rb(mean);
  float sd=rb(sqrtf(rb(var+1e-5f)));
  for(int j=0;j<8;j++){ xn[row*D+t*8+j]=f2b(rb(v[j]-m)/sd); }
}
__global__ void kgen_k(const u16* lar,const u16* aim,const u16* cre,const u16* cim,const u16* ldt,float* K){
  int d=blockIdx.x;
  float dt=rb(expf(b2f(ldt[d])));
  for(int l=threadIdx.x;l<L;l+=blockDim.x){
    float p=rb((float)l); float acc=0;
    for(int n=0;n<N;n++){
      float ar=-rb(expf(b2f(lar[d*N+n])))*dt, ai=b2f(aim[d*N+n])*dt;
      float cr=b2f(cre[d*N+n])*dt, ci=b2f(cim[d*N+n])*dt;
      float e=expf(ar*p); float sn,cs; sincosf(ai*p,&sn,&cs);
      acc += cr*e*cs - ci*e*sn;
    }
    K[d*L+l]=2.f*acc;
  }
}
__global__ void conv_k(const float* K,const u16* xn,float* y){
  int d=blockIdx.x; __shared__ float ks[L], us[L];
  for(int l=threadIdx.x;l<L;l+=blockDim.x){ ks[l]=K[d*L+l]; us[l]=b2f(xn[l*D+d]); }
  __syncthreads();
  for(int l=threadIdx.x;l<L;l+=blockDim.x){ float a=0; for(int j=0;j<=l;j++) a+=ks[l-j]*us[j]; y[l*D+d]=a; }
}
__global__ void g1_k(const u16* xn,const u16* w1,const u16* w3,u16* h){
  int row=blockIdx.y; int f=blockIdx.x*blockDim.x+threadIdx.x;
  float g=0,u=0; for(int k=0;k<D;k++){ float a=b2f(xn[row*D+k]); g+=a*b2f(w1[k*F+f]); u+=a*b2f(w3[k*F+f]); }
  g=rb(g); u=rb(u); float s=rb(g/(1.f+expf(-g))); h[row*F+f]=f2b(s*u);
}
__global__ void g2_k(const u16* h,const u16* w2,const u16* x,const u16* xn,const float* y,const u16* dsk,u16* out){
  int row=blockIdx.y; int c=blockIdx.x*blockDim.x+threadIdx.x;
  float a=0; for(int k=0;k<F;k++) a+=b2f(h[row*F+k])*b2f(w2[k*D+c]);
  a=rb(a);
  float t=rb(rb(y[row*D+c])+rb(b2f(xn[row*D+c])*b2f(dsk[c])));
  out[row*D+c]=f2b(rb(b2f(x[row*D+c])+t)+a);
}
extern "C" void kernel_launch(void* const* d_in, const int* in_sizes, int n_in, void* d_out, int out_size, void* d_ws, size_t ws_size, hipStream_t stream) {
  const u16* x=(const u16*)d_in[0];
  char* ws=(char*)d_ws;
  u16* xn=(u16*)ws; float* K=(float*)(ws+(4<<20)); float* y=(float*)(ws+(12<<20)); u16* h=(u16*)(ws+(20<<20));
  ln_k<<<L,64,0,stream>>>(x,xn);
  kgen_k<<<D,256,0,stream>>>((const u16*)d_in[1],(const u16*)d_in[2],(const u16*)d_in[3],(const u16*)d_in[4],(const u16*)d_in[5],K);
  conv_k<<<D,256,0,stream>>>(K,xn,y);
  g1_k<<<dim3(F/256,L),256,0,stream>>>(xn,(const u16*)d_in[8],(const u16*)d_in[9],h);
  g2_k<<<dim3(D/256,L),256,0,stream>>>(h,(const u16*)d_in[10],x,xn,y,(const u16*)d_in[6],(u16*)d_out);
}
```

```cpp
#include <hip/hip_runtime.h>
#include <hip/hip_bf16.h>

typedef unsigned short u16;
typedef unsigned int u32;
typedef __bf16 bf16x8 __attribute__((ext_vector_type(8)));
typedef __bf16 bf16x2 __attribute__((ext_vector_type(2)));
typedef _Float16 f16x8 __attribute__((ext_vector_type(8)));
typedef _Float16 f16x2 __attribute__((ext_vector_type(2)));
typedef short s16x4 __attribute__((ext_vector_type(4)));
typedef float f32x2 __attribute__((ext_vector_type(2)));
typedef float f32x4 __attribute__((ext_vector_type(4)));
typedef float f32x16 __attribute__((ext_vector_type(16)));
typedef u32 u32x2 __attribute__((ext_vector_type(2)));
typedef u32 u32x4 __attribute__((ext_vector_type(4)));
#define LAS __attribute__((address_space(3)))
#define GAS __attribute__((address_space(1)))
#define DEV __device__ __forceinline__

constexpr int L = 2048, D = 512, NM = 128, F = 1792;
constexpr int LDS_BYTES = 163840;

constexpr size_t WS_H = 0;
constexpr size_t WS_XNR = 8u << 20;
constexpr size_t WS_W2F = 10u << 20;
constexpr size_t WS_Y = 12u << 20;

struct Params {
  const u16 *x, *lar, *aim, *cre, *cim, *ldt, *dsk, *w1, *w3, *w2;
  u16* out;
  char* ws;
};

DEV float b2f(u32 v) { return __builtin_bit_cast(float, v << 16); }
DEV float bhi(u32 v) { return __builtin_bit_cast(float, v & 0xffff0000u); }
DEV u32 pk2(float lo, float hi) {
  f32x2 f = {lo, hi};
  return __builtin_bit_cast(u32, __builtin_convertvector(f, bf16x2));
}
DEV float rb(float f) { return b2f(pk2(f, f) & 0xffffu); }
DEV float dot2(u32 a, u32 b, float c) {
  return __builtin_amdgcn_fdot2_f32_bf16(__builtin_bit_cast(bf16x2, a), __builtin_bit_cast(bf16x2, b), c, false);
}
DEV f32x16 mfma32(u32x4 a, u32x4 b, f32x16 c) {
  return __builtin_amdgcn_mfma_f32_32x32x16_bf16(__builtin_bit_cast(bf16x8, a), __builtin_bit_cast(bf16x8, b), c, 0, 0, 0);
}
DEV f32x4 mfma16(u32x4 a, u32x4 b, f32x4 c) {
  return __builtin_amdgcn_mfma_f32_16x16x32_bf16(__builtin_bit_cast(bf16x8, a), __builtin_bit_cast(bf16x8, b), c, 0, 0, 0);
}
DEV f32x4 mfma16h(u32x4 a, u32x4 b, f32x4 c) {
  return __builtin_amdgcn_mfma_f32_16x16x32_f16(__builtin_bit_cast(f16x8, a), __builtin_bit_cast(f16x8, b), c, 0, 0, 0);
}
DEV u32x4 lds128(LAS char* p) { return *(LAS u32x4*)p; }
DEV u32x2 lds64(LAS char* p) { return *(LAS u32x2*)p; }
DEV u32x2 ldstr(LAS char* p) { return __builtin_bit_cast(u32x2, __builtin_amdgcn_ds_read_tr16_b64_v4i16((LAS s16x4*)p)); }
DEV void glds16(const void* g, LAS char* l) { __builtin_amdgcn_global_load_lds((const GAS void*)g, (LAS void*)l, 16, 0, 0); }
#define WAIT_V(n) asm volatile("s_waitcnt vmcnt(" #n ")" ::: "memory")
#define WAIT_L0() asm volatile("s_waitcnt lgkmcnt(0)" ::: "memory")
#define BAR() __builtin_amdgcn_s_barrier()
#define LBAR() do { WAIT_L0(); BAR(); asm volatile("" ::: "memory"); } while (0)
#define SCHED() __builtin_amdgcn_sched_barrier(0)

constexpr int G1_SLOT = 49152;
constexpr int G1_MISC = 3 * G1_SLOT;

DEV void g1_phase(const Params& p, LAS char* lds, int mt, int ft) {
  const int tid = threadIdx.x, l = tid & 63;
  const int w = __builtin_amdgcn_readfirstlane(tid >> 6);
  const int wt = w & 1, wf = w >> 1;
  const int tok0 = mt * 128, f0 = ft * 128;

  const char* xb = (const char*)p.x + (size_t)(tok0 + 16 * w) * 1024;
  const char* w1b = (const char*)p.w1 + (size_t)(8 * w) * (F * 2) + f0 * 2;
  const char* w3b = (const char*)p.w3 + (size_t)(8 * w) * (F * 2) + f0 * 2;
  const u32 xo0 = (l >> 3) * 1024 + (((l & 7) ^ (l >> 4)) << 4);
  const u32 xo1 = (8 + (l >> 3)) * 1024 + (((l & 7) ^ (4 + (l >> 4))) << 4);
  const u32 wo1 = (l >> 4) * (F * 2) + (((l & 15) ^ (4 * (l >> 4))) << 4);
  const u32 wo3 = (l >> 4) * (F * 2) + (((l & 15) ^ (4 * (l >> 4) + 2)) << 4);

  auto issue = [&](int kt, int slot) {
    LAS char* s = lds + slot * G1_SLOT + w * 2048;
    const char* xs = xb + kt * 128;
    const char* a = w1b + (size_t)kt * (64 * F * 2);
    const char* b = w3b + (size_t)kt * (64 * F * 2);
    glds16(xs + xo0, s);
    glds16(xs + xo1, s + 1024);
    glds16(a + wo1, s + 16384);
    glds16(a + wo1 + 4 * F * 2, s + 16384 + 1024);
    glds16(b + wo3, s + 32768);
    glds16(b + wo3 + 4 * F * 2, s + 32768 + 1024);
  };

  issue(0, 0); issue(1, 1); issue(2, 2);

  const int t = l & 31, h = l >> 5;
  int xa[4];
#pragma unroll
  for (int ks = 0; ks < 4; ++ks) xa[ks] = (64 * wt + t) * 128 + (((2 * ks + h) ^ ((t >> 1) & 7)) << 4);
  int wa[2];
  {
    const int img = (l >> 4) & 1, q = (l & 15) >> 2, pp = l & 3;
#pragma unroll
    for (int i = 0; i < 2; ++i) {
      const int fb = i ^ wt;
      const int chunk = (4 * wf + 2 * fb + (pp >> 1)) ^ (4 * q + 2 * img);
      wa[i] = 16384 * (1 + img) + (8 * h + q) * 256 + chunk * 16 + 8 * (pp & 1);
    }
  }
  const int sa = w * 2048 + l * 16;

  f32x16 acc[2][2];
#pragma unroll
  for (int i = 0; i < 2; ++i)
#pragma unroll
    for (int j = 0; j < 2; ++j)
#pragma unroll
      for (int r = 0; r < 16; ++r) acc[i][j][r] = 0.f;
  float s0 = 0.f, q0 = 0.f, s1 = 0.f, q1 = 0.f, cs = 0.f;
  const u32 ONES = 0x3f803f80u;

#pragma unroll
  for (int kt = 0; kt < 8; ++kt) {
    if (kt < 7) { if (kt == 0) WAIT_V(12); else WAIT_V(6); } else WAIT_V(0);
    LBAR();
    if (kt >= 1 && kt + 2 < 8) issue(kt + 2, (kt + 2) % 3);
    LAS char* s = lds + (kt % 3) * G1_SLOT;
    {
      u32x4 v0 = lds128(s + sa), v1 = lds128(s + sa + 1024);
#pragma unroll
      for (int j = 0; j < 4; ++j) {
        s0 = dot2(v0[j], ONES, s0); q0 = dot2(v0[j], v0[j], q0);
        s1 = dot2(v1[j], ONES, s1); q1 = dot2(v1[j], v1[j], q1);
      }
    }
#pragma unroll
    for (int ks = 0; ks < 4; ++ks) {
      u32x4 a[2], b[2];
#pragma unroll
      for (int i = 0; i < 2; ++i) {
        u32x2 lo = ldstr(s + wa[i] + ks * 4096), hi = ldstr(s + wa[i] + ks * 4096 + 1024);
        a[i] = u32x4{lo.x, lo.y, hi.x, hi.y};
        b[i] = lds128(s + xa[ks] + i * 4096);
      }
#pragma unroll
      for (int j = 0; j < 4; ++j) cs = dot2(a[0][j], ONES, cs);
#pragma unroll
      for (int i = 0; i < 2; ++i)
#pragma unroll
        for (int j = 0; j < 2; ++j) acc[i][j] = mfma32(a[i], b[j], acc[i][j]);
    }
  }

#pragma unroll
  for (int o = 1; o < 8; o <<= 1) {
    s0 += __shfl_xor(s0, o); q0 += __shfl_xor(q0, o);
    s1 += __shfl_xor(s1, o); q1 += __shfl_xor(q1, o);
  }
  cs += __shfl_xor(cs, 32);
  LAS f32x2* st = (LAS f32x2*)(lds + G1_MISC);
  LAS float* csum = (LAS float*)(lds + G1_MISC + 1024);
  {
    const float s = (l & 1) ? s1 : s0, q = (l & 1) ? q1 : q0;
    const float mean = s * (1.f / D);
    const float var = fmaxf(q * (1.f / D) - mean * mean, 0.f);
    const float sd = rb(__builtin_sqrtf(rb(rb(var) + 1e-5f)));
    if ((l & 7) < 2) st[16 * w + 8 * (l & 1) + (l >> 3)] = f32x2{rb(mean), sd};
    if (l < 32) csum[(l >> 4) * 128 + 32 * wf + 16 * wt + (l & 15)] = cs;
  }
  LBAR();

  char* Hb = p.ws + WS_H;
#pragma unroll
  for (int j = 0; j < 2; ++j) {
    const f32x2 ms = st[64 * wt + 32 * j + t];
    const float m = ms.x, sd2 = ms.y * ms.y;
    const float ce = -1.44269504f * __builtin_amdgcn_rcpf(ms.y);
#pragma unroll
    for (int i = 0; i < 2; ++i) {
      const int fb = i ^ wt;
      const int cb = 32 * wf + 16 * fb + 4 * h;
      const f32x4 c1a = *(LAS f32x4*)(csum + cb), c1b = *(LAS f32x4*)(csum + cb + 8);
      const f32x4 c3a = *(LAS f32x4*)(csum + 128 + cb), c3b = *(LAS f32x4*)(csum + 128 + cb + 8);
      float hv[8];
#pragma unroll
      for (int r = 0; r < 8; ++r) {
        const float c1 = r < 4 ? c1a[r & 3] : c1b[r & 3], c3 = r < 4 ? c3a[r & 3] : c3b[r & 3];
        const float g = __builtin_fmaf(-m, c1, acc[i][j][r]);
        const float u = __builtin_fmaf(-m, c3, acc[i][j][8 + r]);
        const float e = __builtin_amdgcn_exp2f(g * ce);
        hv[r] = (g * u) * __builtin_amdgcn_rcpf(__builtin_fmaf(e, sd2, sd2));
      }
      const u32x4 o = {pk2(hv[0], hv[1]), pk2(hv[2], hv[3]), pk2(hv[4], hv[5]), pk2(hv[6], hv[7])};
      const int tb = mt * 4 + 2 * wt + j, kb = ft * 8 + 2 * wf + fb;
      *(GAS u32x4*)(Hb + ((size_t)(tb * 112 + kb) << 10) + l * 16) = o;
    }
  }
}

DEV void aux_phase(const Params& p, LAS char* lds, int a) {
  const int tid = threadIdx.x, l = tid & 63;
  const int w = __builtin_amdgcn_readfirstlane(tid >> 6);
  u32x4 xr[8];
  const char* xb = (const char*)p.x + (size_t)(64 * a + 8 * w) * 1024 + l * 16;
#pragma unroll
  for (int r = 0; r < 8; ++r) xr[r] = *(const GAS u32x4*)(xb + r * 1024);
  const int gw = a * 8 + w;
  u32 wr[4][8];
  {
    const int c = l & 31, h = l >> 5;
#pragma unroll
    for (int it = 0; it < 4; ++it) {
      int unit = gw + 256 * it; if (unit > 895) unit = 895;
      const int kb = unit >> 3, n2 = unit & 7;
      const char* b = (const char*)p.w2 + (size_t)(16 * kb + 4 * h) * 1024 + n2 * 128 + c * 4;
#pragma unroll
      for (int j = 0; j < 8; ++j) wr[it][j] = *(const GAS u32*)(b + ((j & 3) + 8 * (j >> 2)) * 1024);
    }
  }
  u32 xn[8][4];
#pragma unroll
  for (int r = 0; r < 8; ++r) {
    float v[8];
#pragma unroll
    for (int j = 0; j < 4; ++j) { v[2 * j] = b2f(xr[r][j]); v[2 * j + 1] = bhi(xr[r][j]); }
    float s = 0.f;
#pragma unroll
    for (int j = 0; j < 8; ++j) s += v[j];
#pragma unroll
    for (int o = 1; o < 64; o <<= 1) s += __shfl_xor(s, o);
    const float mean = s * (1.f / D);
    float q = 0.f;
#pragma unroll
    for (int j = 0; j < 8; ++j) { const float d = v[j] - mean; q = __builtin_fmaf(d, d, q); }
#pragma unroll
    for (int o = 1; o < 64; o <<= 1) q += __shfl_xor(q, o);
    const float m = rb(mean);
    const float sd = rb(__builtin_sqrtf(rb(rb(q * (1.f / D)) + 1e-5f)));
    const float ri = 1.f / sd;
#pragma unroll
    for (int j = 0; j < 4; ++j) {
      const u32 c = pk2(v[2 * j] - m, v[2 * j + 1] - m);
      xn[r][j] = pk2(b2f(c) * ri, bhi(c) * ri);
    }
  }
#pragma unroll
  for (int c = 0; c < 8; ++c) {
    u32x4 o;
#pragma unroll
    for (int e = 0; e < 4; ++e) {
      const u32 lo = xn[7 - 2 * e][c >> 1], hi = xn[6 - 2 * e][c >> 1];
      o[e] = (c & 1) ? __builtin_amdgcn_perm(hi, lo, 0x07060302u) : __builtin_amdgcn_perm(hi, lo, 0x05040100u);
    }
    *(LAS u32x4*)(lds + (8 * l + c) * 128 + (((7 - w) ^ (l & 7)) << 4)) = o;
  }
  {
    char* wf = p.ws + WS_W2F;
#pragma unroll
    for (int it = 0; it < 4; ++it) {
      const int unit = gw + 256 * it;
      if (unit < 896) {
        const int kb = unit >> 3, n2 = unit & 7;
        u32x4 o0, o1;
#pragma unroll
        for (int e = 0; e < 4; ++e) {
          o0[e] = __builtin_amdgcn_perm(wr[it][2 * e + 1], wr[it][2 * e], 0x05040100u);
          o1[e] = __builtin_amdgcn_perm(wr[it][2 * e + 1], wr[it][2 * e], 0x07060302u);
        }
        char* d = wf + ((size_t)((n2 * 112 + kb) * 2) << 10) + l * 16;
        *(GAS u32x4*)d = o0;
        *(GAS u32x4*)(d + 1024) = o1;
      }
    }
  }
  LBAR();
  {
    char* xo = p.ws + WS_XNR + (size_t)(L - 64 - 64 * a) * 2;
#pragma unroll
    for (int it = 0; it < 8; ++it) {
      const int ch = 64 * w + 8 * it + (l >> 3);
      const u32x4 v = lds128(lds + ch * 128 + (l & 7) * 16);
      const int slot = (l & 7) ^ ((ch >> 3) & 7);
      *(GAS u32x4*)(xo + (size_t)ch * 4096 + slot * 16) = v;
    }
  }
}

constexpr int CV_CS = 4160;
constexpr int CV_U = 0;
constexpr int CV_K = 2 * 8 * CV_CS;
constexpr int CV_KC = CV_K + 2 * 4608;
constexpr int CV_SC = CV_KC + 2 * 1408;
constexpr int CV_T = CV_SC + 64;
constexpr int CV_TS = 544, CV_TCH = 53 * CV_TS;
constexpr int CV_X = CV_T;
static_assert(CV_T + 2 * CV_TCH + 11 * CV_TS <= LDS_BYTES, "lds");

struct cpx { float r, i; };
DEV cpx cmul(cpx a, cpx b) { return cpx{__builtin_fmaf(a.r, b.r, -a.i * b.i), __builtin_fmaf(a.r, b.i, a.i * b.r)}; }
DEV cpx cexp_p(float lre2, float rev, float pw) {
  const float mg = __builtin_amdgcn_exp2f(lre2 * pw);
  const float ph = __builtin_amdgcn_fractf(rev * pw);
  return cpx{mg * __builtin_amdgcn_cosf(ph), mg * __builtin_amdgcn_sinf(ph)};
}
DEV u32 pkh(float a, float b) {
  f32x2 f = {a, b};
  return __builtin_bit_cast(u32, __builtin_convertvector(f, f16x2));
}

template <int TS>
DEV void conv_tiles(LAS char* ua, LAS char* ka, f32x4 (&acc)[4]) {
  constexpr int T0 = TS ? 6 : 7, T1 = TS ? 5 : 4, T2 = TS ? 2 : 3, T3 = TS ? 1 : 0;
#pragma unroll
  for (int rr = 0; rr < 4; ++rr) {
    u32x4 A[T0 + 1], B[T0 + 1];
#pragma unroll
    for (int i = 0; i <= T0; ++i) {
      A[i] = lds128(ua + (7 - i) * 512 + (3 - rr) * 64);
      B[i] = lds128(ka + i * 512 + (3 - rr) * 64);
    }
#pragma unroll
    for (int c = 0; c <= T0; ++c) {
      acc[0] = mfma16(A[c], B[T0 - c], acc[0]);
      if (c <= T1) acc[1] = mfma16(A[c], B[T1 - c], acc[1]);
      if (c <= T2) acc[2] = mfma16(A[c], B[T2 - c], acc[2]);
      if (c <= T3) acc[3] = mfma16(A[c], B[T3 - c], acc[3]);
    }
  }
}

DEV void conv_phase(const Params& p, LAS char* lds, int cp) {
  const int tid = threadIdx.x, l = tid & 63;
  const int w = __builtin_amdgcn_readfirstlane(tid >> 6);
  const int uch = tid >> 8, ui = tid & 255;
  const char* ub = p.ws + WS_XNR + (size_t)(2 * cp + uch) * 4096 + ui * 16;
  const u32x4 u0 = *(const GAS u32x4*)ub;
  u32x4 u1 = {0u, 0u, 0u, 0u};
  if (ui < 255) u1 = *(const GAS u32x4*)(ub + 16);

  const int role = w >> 2, gch = (w >> 1) & 1, n = tid & 127;
  const int d = 2 * cp + gch;
  const float dt = rb(__expf(b2f(p.ldt[d])));
  const float ar = -rb(__expf(b2f(p.lar[d * NM + n])));
  const float ai = b2f(p.aim[d * NM + n]);
  const float lre2 = (ar * dt) * 1.44269504f, rev = (ai * dt) * 0.15915494309f;
  LAS char* tb = lds + CV_T + gch * CV_TCH + n * 4;
  if (role == 0) {
    const cpx w8 = cexp_p(lre2, rev, 8.f), w32 = cexp_p(lre2, rev, 32.f);
    cpx v[4];
    v[0] = cpx{1.f, 0.f}; v[1] = w8; v[2] = cmul(w8, w8); v[3] = cmul(v[2], w8);
#pragma unroll
    for (int m = 0; m < 8; ++m) {
#pragma unroll
      for (int r = 0; r < 4; ++r) {
        *(LAS u32*)(tb + (4 * m + r) * CV_TS) = pkh(v[r].r, v[r].i);
        if (m < 7) v[r] = cmul(v[r], w32);
      }
    }
  } else {
    const float cr = b2f(p.cre[d * NM + n]), ci = b2f(p.cim[d * NM + n]);
    float mx = fmaxf(fabsf(cr), fabsf(ci));
#pragma unroll
    for (int o = 1; o < 64; o <<= 1) mx = fmaxf(mx, __shfl_xor(mx, o));
    u32 eb = __builtin_bit_cast(u32, mx) & 0x7f800000u;
    eb = eb < 0x00800000u ? 0x00800000u : (eb > 0x7e800000u ? 0x7e800000u : eb);
    const float sc = __builtin_bit_cast(float, eb), isc = __builtin_bit_cast(float, 0x7f000000u - eb);
    if (l == 0) *(LAS float*)(lds + CV_SC + (gch * 2 + (w & 1)) * 4) = sc * (2.f * dt);
    const cpx z1 = cexp_p(lre2, rev, 1.f), z256 = cexp_p(lre2, rev, 256.f);
    const cpx z2 = cmul(z1, z1);
    cpx m[21];
    m[0] = cpx{cr * isc, ci * isc};
    m[1] = cmul(m[0], z1);
#pragma unroll
    for (int i = 2; i < 8; ++i) m[i] = cmul(m[i - 2], z2);
    m[8] = cmul(m[0], z256); m[9] = cmul(m[2], z256); m[10] = cmul(m[4], z256); m[11] = cmul(m[6], z256);
    m[12] = cmul(m[8], z256); m[13] = cmul(m[10], z256);
    m[14] = cmul(m[12], z256); m[15] = cmul(m[13], z256);
    m[16] = cmul(m[14], z256);
#pragma unroll
    for (int i = 17; i < 21; ++i) m[i] = cmul(m[i - 1], z256);
#pragma unroll
    for (int i = 0; i < 21; ++i) *(LAS u32*)(tb + (32 + i) * CV_TS) = pkh(m[i].r, -m[i].i);
  }
  {
    const u32 dd[8] = {u0.x, u0.y, u0.z, u0.w, u1.x, u1.y, u1.z, u1.w};
    u32 od[7];
#pragma unroll
    for (int k = 0; k < 7; ++k) od[k] = __builtin_amdgcn_alignbit(dd[k + 1], dd[k], 16);
    LAS char* ud = lds + CV_U + uch * (8 * CV_CS) + ui * 16;
#pragma unroll
    for (int s = 0; s < 4; ++s) {
      *(LAS u32x4*)(ud + (2 * s) * CV_CS) = u32x4{dd[s], dd[s + 1], dd[s + 2], dd[s + 3]};
      *(LAS u32x4*)(ud + (2 * s + 1) * CV_CS) = u32x4{od[s], od[s + 1], od[s + 2], od[s + 3]};
    }
    if (ui < 8) *(LAS u32x4*)(lds + CV_U + uch * (8 * CV_CS) + ui * CV_CS + 4096) = u32x4{0u, 0u, 0u, 0u};
    if (ui < 32) *(LAS u32x4*)(lds + CV_K + uch * 4608 + ui * 16) = u32x4{0u, 0u, 0u, 0u};
  }
  LBAR();
  {
    const int ch = w >> 2, rbk = (w >> 1) & 1, cbk = w & 1;
    const int r = l & 15, g = l >> 4;
    LAS char* ma = lds + CV_T + ch * CV_TCH + (32 + 16 * rbk + r) * CV_TS + g * 16;
    LAS char* va = lds + CV_T + ch * CV_TCH + (16 * cbk + r) * CV_TS + g * 16;
    f32x4 a0 = {0.f, 0.f, 0.f, 0.f}, a1 = {0.f, 0.f, 0.f, 0.f};
#pragma unroll
    for (int s = 0; s < 4; ++s) {
      a0 = mfma16h(lds128(ma + s * 64), lds128(va + s * 64), a0);
      a1 = mfma16h(lds128(ma + 256 + s * 64), lds128(va + 256 + s * 64), a1);
    }
    const f32x2 sc = *(LAS f32x2*)(lds + CV_SC + ch * 8);
    const int j = r + 16 * cbk;
    LAS u16* kc = (LAS u16*)(lds + CV_KC + ch * 1408);
#pragma unroll
    for (int e = 0; e < 4; ++e) {
      const float kv = a0[e] * sc.x + a1[e] * sc.y;
      int idx;
      if (rbk == 0) idx = g < 2 ? 8 * j + 4 * g + e : (g == 2 ? 256 + 4 * j + e : 384 + 64 * (e >> 1) + 2 * j + (e & 1));
      else idx = g == 0 ? 512 + 32 * e + j : ((g == 1 && e == 0 && j == 0) ? 640 : 648 + (l & 31));
      kc[idx] = (u16)pk2(kv, kv);
    }
  }
  LBAR();
  {
    LAS u16* kc = (LAS u16*)(lds + CV_KC + uch * 1408);
    u32 o[4];
#pragma unroll
    for (int e = 0; e < 4; ++e) {
      const int l0 = 8 * ui + 2 * e;
      const u32 bits = pk2((float)l0, (float)(l0 + 1));
      const int i0 = ui < 32 ? l0 : (int)(bits & 0xffffu) - (0x4300 - 128);
      const int i1 = ui < 32 ? l0 + 1 : (int)(bits >> 16) - (0x4300 - 128);
      o[e] = (u32)kc[i0] | ((u32)kc[i1] << 16);
    }
    *(LAS u32x4*)(lds + CV_K + uch * 4608 + 512 + ui * 16) = u32x4{o[0], o[1], o[2], o[3]};
  }
  LBAR();
  {
    const int ch = w & 1, ts = (w >> 1) & 1, rh = w >> 2;
    const int b = l & 15, g = l >> 4;
    LAS char* ua = lds + CV_U + ch * (8 * CV_CS) + (7 - (b & 7)) * CV_CS + 4080 - 16 * (b >> 3) - 32 * (g >> 1) + 16 * (g & 1)
                   - 7 * 512 - 256 * rh - 3 * 64;
    LAS char* ka = lds + CV_K + ch * 4608 + 512 + 32 * (b - (g >> 1)) + 16 * (g & 1) - 256 * rh - 3 * 64;
    f32x4 acc[4];
#pragma unroll
    for (int i = 0; i < 4; ++i) acc[i] = f32x4{0.f, 0.f, 0.f, 0.f};
    if (ts == 0) conv_tiles<0>(ua, ka, acc); else conv_tiles<1>(ua, ka, acc);
#pragma unroll
    for (int i = 0; i < 4; ++i) *(LAS f32x4*)(lds + CV_X + (w * 4 + i) * 1024 + l * 16) = acc[i];
  }
  LBAR();
  {
    const int ts = (w >> 1) & 1, i = (w & 1) + 2 * (w >> 2);
    const int tile = ts ? (i == 0 ? 6 : i == 1 ? 5 : i == 2 ? 2 : 1) : (i == 0 ? 7 : i == 1 ? 4 : i == 2 ? 3 : 0);
    const int pos = 256 * tile + 16 * (l & 15) + 4 * (l >> 4);
    u32 res[2][2];
    const u32 dk = *(const GAS u32*)(p.dsk + 2 * cp);
#pragma unroll
    for (int ch = 0; ch < 2; ++ch) {
      const f32x4 y0 = *(LAS f32x4*)(lds + CV_X + ((ch + 2 * ts) * 4 + i) * 1024 + l * 16);
      const f32x4 y1 = *(LAS f32x4*)(lds + CV_X + ((ch + 2 * ts + 4) * 4 + i) * 1024 + l * 16);
      const u32x2 ur = lds64(lds + CV_U + ch * (8 * CV_CS) + (2044 - pos) * 2);
      const float dsk = ch ? bhi(dk) : b2f(dk);
      const u32 ya = pk2(y0[0] + y1[0], y0[1] + y1[1]), yb = pk2(y0[2] + y1[2], y0[3] + y1[3]);
      const u32 sa = pk2(bhi(ur.y) * dsk, b2f(ur.y) * dsk), sb = pk2(bhi(ur.x) * dsk, b2f(ur.x) * dsk);
      res[ch][0] = pk2(b2f(ya) + b2f(sa), bhi(ya) + bhi(sa));
      res[ch][1] = pk2(b2f(yb) + b2f(sb), bhi(yb) + bhi(sb));
    }
    const u32x4 o = {__builtin_amdgcn_perm(res[1][0], res[0][0], 0x05040100u), __builtin_amdgcn_perm(res[1][0], res[0][0], 0x07060302u),
                     __builtin_amdgcn_perm(res[1][1], res[0][1], 0x05040100u), __builtin_amdgcn_perm(res[1][1], res[0][1], 0x07060302u)};
    *(GAS u32x4*)(p.ws + WS_Y + (size_t)cp * 8192 + pos * 4) = o;
  }
}

constexpr int G2_RS = 272;
constexpr int G2_PW = 64 * G2_RS;

DEV void g2_phase(const Params& p, LAS char* lds, int m2, int n2) {
  const int tid = threadIdx.x, l = tid & 63;
  const int w = __builtin_amdgcn_readfirstlane(tid >> 6);
  const char* hb = p.ws + WS_H + ((size_t)(2 * m2 * 112 + 14 * w) << 10) + l * 16;
  const char* wb = p.ws + WS_W2F + ((size_t)((n2 * 112 + 14 * w) * 2) << 10) + l * 16;
  u32x4 wf[14][2], hf[14][2];
#pragma unroll
  for (int k = 0; k < 4; ++k) {
    wf[k][0] = *(const GAS u32x4*)(wb + k * 2048); wf[k][1] = *(const GAS u32x4*)(wb + k * 2048 + 1024);
    hf[k][0] = *(const GAS u32x4*)(hb + k * 1024); hf[k][1] = *(const GAS u32x4*)(hb + (112 + k) * 1024);
  }
  const int etok = 64 * m2 + 8 * w + (l >> 3), cg = l & 7;
  const u32x4 xv = *(const GAS u32x4*)((const char*)p.x + (size_t)etok * 1024 + n2 * 128 + cg * 16);
  u32 yv[4];
#pragma unroll
  for (int j = 0; j < 4; ++j) yv[j] = *(const GAS u32*)(p.ws + WS_Y + (size_t)(32 * n2 + 4 * cg + j) * 8192 + etok * 4);
#pragma unroll
  for (int k = 4; k < 10; ++k) {
    wf[k][0] = *(const GAS u32x4*)(wb + k * 2048); wf[k][1] = *(const GAS u32x4*)(wb + k * 2048 + 1024);
    hf[k][0] = *(const GAS u32x4*)(hb + k * 1024); hf[k][1] = *(const GAS u32x4*)(hb + (112 + k) * 1024);
  }
  SCHED();
  f32x16 acc[2][2];
#pragma unroll
  for (int i = 0; i < 2; ++i)
#pragma unroll
    for (int j = 0; j < 2; ++j)
#pragma unroll
      for (int r = 0; r < 16; ++r) acc[i][j][r] = 0.f;
#pragma unroll
  for (int k = 0; k < 14; ++k) {
#pragma unroll
    for (int i = 0; i < 2; ++i)
#pragma unroll
      for (int j = 0; j < 2; ++j) acc[i][j] = mfma32(wf[k][i], hf[k][j], acc[i][j]);
    if (k + 10 < 14) {
      const int kk = k + 10;
      wf[kk][0] = *(const GAS u32x4*)(wb + kk * 2048); wf[kk][1] = *(const GAS u32x4*)(wb + kk * 2048 + 1024);
      hf[kk][0] = *(const GAS u32x4*)(hb + kk * 1024); hf[kk][1] = *(const GAS u32x4*)(hb + (112 + kk) * 1024);
    }
    SCHED();
  }
  {
    LAS char* pw = lds + w * G2_PW + (l & 31) * G2_RS + (l >> 5) * 32;
#pragma unroll
    for (int j = 0; j < 2; ++j)
#pragma unroll
      for (int qd = 0; qd < 4; ++qd)
#pragma unroll
        for (int i = 0; i < 2; ++i)
          *(LAS f32x4*)(pw + j * 32 * G2_RS + qd * 64 + i * 16) =
              f32x4{acc[i][j][4 * qd], acc[i][j][4 * qd + 1], acc[i][j][4 * qd + 2], acc[i][j][4 * qd + 3]};
  }
  LBAR();
  {
    LAS char* pr = lds + (8 * w + (l >> 3)) * G2_RS + cg * 32;
    f32x4 e = {0.f, 0.f, 0.f, 0.f}, o = {0.f, 0.f, 0.f, 0.f};
#pragma unroll
    for (int ww = 0; ww < 8; ++ww) {
      e += *(LAS f32x4*)(pr + ww * G2_PW);
      o += *(LAS f32x4*)(pr + ww * G2_PW + 16);
    }
    u32x4 r;
#pragma unroll
    for (int j = 0; j < 4; ++j) {
      const u32 f = pk2(e[j], o[j]);
      const u32 s = pk2(b2f(xv[j]) + b2f(yv[j]), bhi(xv[j]) + bhi(yv[j]));
      r[j] = pk2(b2f(s) + b2f(f), bhi(s) + bhi(f));
    }
    *(GAS u32x4*)((char*)p.out + (size_t)etok * 1024 + n2 * 128 + cg * 16) = r;
  }
}

__global__ __launch_bounds__(512, 2) void k1(Params p) {
  __shared__ __attribute__((aligned(16))) char smem[LDS_BYTES];
  LAS char* lds = (LAS char*)smem;
  const int b = blockIdx.x, xcd = b & 7, r = b >> 3;
  if (r < 28) g1_phase(p, lds, (xcd >> 1) * 4 + (r & 3), (xcd & 1) * 7 + (r >> 2));
  else aux_phase(p, lds, xcd * 4 + (r - 28));
}
__global__ __launch_bounds__(512, 2) void k2(Params p) {
  __shared__ __attribute__((aligned(16))) char smem[LDS_BYTES];
  conv_phase(p, (LAS char*)smem, blockIdx.x);
}
__global__ __launch_bounds__(512, 2) void k3(Params p) {
  __shared__ __attribute__((aligned(16))) char smem[LDS_BYTES];
  const int b = blockIdx.x, xcd = b & 7, r = b >> 3;
  g2_phase(p, (LAS char*)smem, xcd * 4 + (r & 3), r >> 2);
}

extern "C" void kernel_launch(void* const* d_in, const int* in_sizes, int n_in, void* d_out, int out_size, void* d_ws,
                              size_t ws_size, hipStream_t stream) {
  Params p;
  p.x = (const u16*)d_in[0]; p.lar = (const u16*)d_in[1]; p.aim = (const u16*)d_in[2]; p.cre = (const u16*)d_in[3];
  p.cim = (const u16*)d_in[4]; p.ldt = (const u16*)d_in[5]; p.dsk = (const u16*)d_in[6];
  p.w1 = (const u16*)d_in[8]; p.w3 = (const u16*)d_in[9]; p.w2 = (const u16*)d_in[10];
  p.out = (u16*)d_out; p.ws = (char*)d_ws;
  k1<<<256, 512, 0, stream>>>(p);
  k2<<<256, 512, 0, stream>>>(p);
  k3<<<256, 512, 0, stream>>>(p);
}
```

```cpp
#include <hip/hip_runtime.h>
#include <hip/hip_bf16.h>

typedef unsigned short u16;
typedef unsigned int u32;
typedef unsigned long long u64;
typedef __bf16 bf16x8 __attribute__((ext_vector_type(8)));
typedef __bf16 bf16x2 __attribute__((ext_vector_type(2)));
typedef _Float16 f16x8 __attribute__((ext_vector_type(8)));
typedef _Float16 f16x2 __attribute__((ext_vector_type(2)));
typedef short s16x4 __attribute__((ext_vector_type(4)));
typedef float f32x2 __attribute__((ext_vector_type(2)));
typedef float f32x4 __attribute__((ext_vector_type(4)));
typedef float f32x16 __attribute__((ext_vector_type(16)));
typedef u32 u32x2 __attribute__((ext_vector_type(2)));
typedef u32 u32x4 __attribute__((ext_vector_type(4)));
typedef __amdgpu_buffer_rsrc_t rsrc_t;
#define LAS __attribute__((address_space(3)))
#define GAS __attribute__((address_space(1)))
#define DEV __device__ __forceinline__

constexpr int L = 2048, D = 512, NM = 128, F = 1792;
constexpr int LDS_BYTES = 163840;
constexpr int SC1 = 16;

constexpr int WS_H = 0;
constexpr int WS_XNR = 8 << 20;
constexpr int WS_W2F = 10 << 20;
constexpr int WS_Y = 12 << 20;
constexpr int WS_ST = 14 << 20;
constexpr int WS_CS = WS_ST + 8192;
constexpr int WS_FL = 15 << 20;
constexpr int FL_EP = WS_FL;
constexpr int FL_P0 = WS_FL + 4096;
constexpr int FL_CS = WS_FL + 8192;
constexpr int FL_XN = WS_FL + 8192 + 1024;
constexpr int FL_H = WS_FL + 16384;
constexpr int FL_Y = WS_FL + 32768;
constexpr int WS_BYTES = 16 << 20;

struct Params {
  const u16 *x, *lar, *aim, *cre, *cim, *ldt, *dsk, *w1, *w3, *w2;
  u16* out;
  char* ws;
};

DEV float b2f(u32 v) { return __builtin_bit_cast(float, v << 16); }
DEV float bhi(u32 v) { return __builtin_bit_cast(float, v & 0xffff0000u); }
DEV u32 pk2v(f32x2 f) { return __builtin_bit_cast(u32, __builtin_convertvector(f, bf16x2)); }
DEV u32 pk2(float lo, float hi) { return pk2v(f32x2{lo, hi}); }
DEV f32x2 up2(u32 v) { return f32x2{b2f(v), bhi(v)}; }
DEV float rb(float f) { return b2f(pk2(f, f) & 0xffffu); }
DEV f32x2 fma2(f32x2 a, f32x2 b, f32x2 c) { return __builtin_elementwise_fma(a, b, c); }
DEV f32x16 mfma32(u32x4 a, u32x4 b, f32x16 c) {
  return __builtin_amdgcn_mfma_f32_32x32x16_bf16(__builtin_bit_cast(bf16x8, a), __builtin_bit_cast(bf16x8, b), c, 0, 0, 0);
}
DEV f32x4 mfma16(u32x4 a, u32x4 b, f32x4 c) {
  return __builtin_amdgcn_mfma_f32_16x16x32_bf16(__builtin_bit_cast(bf16x8, a), __builtin_bit_cast(bf16x8, b), c, 0, 0, 0);
}
DEV f32x4 mfma16h(u32x4 a, u32x4 b, f32x4 c) {
  return __builtin_amdgcn_mfma_f32_16x16x32_f16(__builtin_bit_cast(f16x8, a), __builtin_bit_cast(f16x8, b), c, 0, 0, 0);
}
DEV u32x4 lds128(LAS char* p) { return *(LAS u32x4*)p; }
DEV u32x2 lds64(LAS char* p) { return *(LAS u32x2*)p; }
DEV u32x2 ldstr(LAS char* p) { return __builtin_bit_cast(u32x2, __builtin_amdgcn_ds_read_tr16_b64_v4i16((LAS s16x4*)p)); }
DEV void glds16(const char* sbase, u32 voff, u32 ldst) {
  u32 keep;
  asm volatile("s_mov_b32 %0, m0\n\ts_mov_b32 m0, %3\n\ts_nop 0\n\tglobal_load_lds_dwordx4 %1, %2\n\ts_mov_b32 m0, %0"
               : "=&s"(keep) : "v"(voff), "s"(sbase), "s"(ldst) : "memory");
}
#define WAIT_V(n) asm volatile("s_waitcnt vmcnt(" #n ")" ::: "memory")
#define WAIT_L0() asm volatile("s_waitcnt lgkmcnt(0)" ::: "memory")
#define BAR() __builtin_amdgcn_s_barrier()
#define LBAR() do { WAIT_L0(); BAR(); asm volatile("" ::: "memory"); } while (0)
#define SCHED() __builtin_amdgcn_sched_barrier(0)
#define STAMP(i)

DEV u32x4 ld128s(rsrc_t rs, int voff, int soff) { return __builtin_amdgcn_raw_buffer_load_b128(rs, voff, soff, SC1); }
DEV u32x2 ld64s(rsrc_t rs, int voff, int soff) { return __builtin_amdgcn_raw_buffer_load_b64(rs, voff, soff, SC1); }
DEV u32 ld32s(rsrc_t rs, int voff, int soff) { return __builtin_amdgcn_raw_buffer_load_b32(rs, voff, soff, SC1); }
DEV void st128s(u32x4 v, rsrc_t rs, int voff, int soff) {
  __builtin_amdgcn_raw_buffer_store_b128(v, rs, voff, soff, SC1);
  asm volatile("s_nop 1" :: "v"(v) : "memory");
}
DEV void st64s(u32x2 v, rsrc_t rs, int voff, int soff) { __builtin_amdgcn_raw_buffer_store_b64(v, rs, voff, soff, SC1); }
DEV void st32s(u32 v, rsrc_t rs, int voff, int soff) { __builtin_amdgcn_raw_buffer_store_b32(v, rs, voff, soff, SC1); }
DEV void poll(rsrc_t rs, int voff, int soff, u32x2 tag) {
#pragma unroll 1
  for (int spin = 0; spin < (1 << 18); ++spin) {
    const u32x2 v = ld64s(rs, voff, soff);
    if (__all(v.x == tag.x && v.y == tag.y)) break;
    __builtin_amdgcn_s_sleep(1);
  }
  __builtin_amdgcn_fence(__ATOMIC_ACQUIRE, "wavefront");
  asm volatile("" ::: "memory");
}

template <int CTRL, int RM>
DEV float dppz(float v) { return __builtin_bit_cast(float, __builtin_amdgcn_update_dpp(0, __builtin_bit_cast(int, v), CTRL, RM, 0xf, true)); }
DEV float wave_sum(float v) {
  v += dppz<0x111, 0xf>(v); v += dppz<0x112, 0xf>(v); v += dppz<0x114, 0xf>(v); v += dppz<0x118, 0xf>(v);
  v += dppz<0x142, 0xa>(v); v += dppz<0x143, 0xc>(v);
  return __builtin_bit_cast(float, __builtin_amdgcn_readlane(__builtin_bit_cast(int, v), 63));
}
DEV float wave_max_nonneg(float v) {
  v = fmaxf(v, dppz<0x111, 0xf>(v)); v = fmaxf(v, dppz<0x112, 0xf>(v)); v = fmaxf(v, dppz<0x114, 0xf>(v)); v = fmaxf(v, dppz<0x118, 0xf>(v));
  v = fmaxf(v, dppz<0x142, 0xa>(v)); v = fmaxf(v, dppz<0x143, 0xc>(v));
  return __builtin_bit_cast(float, __builtin_amdgcn_readlane(__builtin_bit_cast(int, v), 63));
}

constexpr int G1_SLOT = 49152;
constexpr int KG_T = 2 * G1_SLOT;
constexpr int KG_TS = 544, KG_TCH = 53 * KG_TS;
constexpr int MISC = LDS_BYTES - 5120;
constexpr int M_KC = MISC;
constexpr int M_SC = MISC + 2816;
constexpr int M_CS = MISC + 3072;
constexpr int M_ST = MISC + 4096;
static_assert(KG_T + 2 * KG_TCH <= MISC && KG_T + 2 * KG_TCH + 11 * KG_TS <= LDS_BYTES, "lds");
constexpr int CV_CS = 4160;
constexpr int CV_U = 0;
constexpr int CV_K = 2 * 8 * CV_CS;
constexpr int CV_X = CV_K + 2 * 4608;
static_assert(CV_X % 16 == 0 && CV_X + 32768 <= MISC, "lds");
constexpr int G2_RS = 272, G2_PW = 64 * G2_RS;

DEV f32x2 cmul(f32x2 a, f32x2 b, f32x2 bn) { return fma2(f32x2{a.y, a.x}, bn, a * f32x2{b.x, b.x}); }
DEV f32x2 cneg(f32x2 b) { return f32x2{-b.y, b.y}; }
DEV f32x2 cexp_p(float lre2, float rev, float pw) {
  const float mg = __builtin_amdgcn_exp2f(lre2 * pw);
  const float ph = __builtin_amdgcn_fractf(rev * pw);
  return f32x2{__builtin_amdgcn_cosf(ph), __builtin_amdgcn_sinf(ph)} * f32x2{mg, mg};
}
DEV u32 pkh(f32x2 f) { return __builtin_bit_cast(u32, __builtin_convertvector(f, f16x2)); }

template <int TS>
DEV void conv_tiles(LAS char* ua, LAS char* ka, f32x4 (&acc)[4]) {
  constexpr int T0 = TS ? 6 : 7, T1 = TS ? 5 : 4, T2 = TS ? 2 : 3, T3 = TS ? 1 : 0, NF = T0 + 1;
  constexpr int ORD0[16] = {0, 15, 12, 11, 8, 1, 14, 10, 2, 13, 9, 3, 4, 5, 6, 7};
  constexpr int ORD1[16] = {0, 14, 13, 10, 9, 1, 12, 8, 2, 11, 3, 4, 5, 6, 0, 0};
  u32x4 fr[2][16];
  auto load = [&](int rr, int id) {
    fr[rr & 1][id] = id < 8 ? lds128(ua + (7 - id) * 512 + (3 - rr) * 64) : lds128(ka + (id - 8) * 512 + (3 - rr) * 64);
  };
#pragma unroll
  for (int k = 0; k < 2 * NF; ++k) load(0, TS ? ORD1[k] : ORD0[k]);
  SCHED();
#pragma unroll
  for (int rr = 0; rr < 4; ++rr) {
#pragma unroll
    for (int c = 0; c <= T0; ++c) {
      u32x4(&f)[16] = fr[rr & 1];
      acc[0] = mfma16(f[c], f[8 + T0 - c], acc[0]);
      if (c <= T1) acc[1] = mfma16(f[c], f[8 + T1 - c], acc[1]);
      if (c <= T2) acc[2] = mfma16(f[c], f[8 + T2 - c], acc[2]);
      if (c <= T3) acc[3] = mfma16(f[c], f[8 + T3 - c], acc[3]);
      SCHED();
      if (rr < 3) { load(rr + 1, TS ? ORD1[2 * c] : ORD0[2 * c]); load(rr + 1, TS ? ORD1[2 * c + 1] : ORD0[2 * c + 1]); }
      SCHED();
    }
  }
}

__global__ __launch_bounds__(512, 2) void fused(Params p) {
  __shared__ __attribute__((aligned(16))) char smem[LDS_BYTES];
  LAS char* lds = (LAS char*)smem;
  const int tid = threadIdx.x, l = tid & 63;
  const int w = __builtin_amdgcn_readfirstlane(tid >> 6);
  const int b = blockIdx.x, xcd = b & 7, rk = b >> 3;
  const bool g1 = rk < 28;
  const rsrc_t rs = __builtin_amdgcn_make_buffer_rsrc(p.ws, 0, WS_BYTES, 0x00020000);
  STAMP(0);

  u32x2 tag;
  u32x4 xrow;
  u32 p_ldt, p_lar, p_aim, p_cre, p_cim, wr[8];
  const int role = w >> 2, gch = (w >> 1) & 1, mode = tid & 127;
  const int dch = 2 * b + gch;
  const int unit = b * 4 + w;
  const bool has_unit = w < 4 && unit < 896;
  {
    const u32 zero = 0, po = (dch * NM + mode) * 2;
    asm volatile("global_load_ushort %0, %1, %2" : "=v"(p_lar) : "v"(po), "s"(p.lar) : "memory");
    asm volatile("global_load_ushort %0, %1, %2" : "=v"(p_aim) : "v"(po), "s"(p.aim) : "memory");
    asm volatile("global_load_ushort %0, %1, %2" : "=v"(p_ldt) : "v"(zero), "s"(p.ldt + dch) : "memory");
    asm volatile("global_load_ushort %0, %1, %2" : "=v"(p_cre) : "v"(po), "s"(p.cre) : "memory");
    asm volatile("global_load_ushort %0, %1, %2" : "=v"(p_cim) : "v"(po), "s"(p.cim) : "memory");
    asm volatile("global_load_dwordx4 %0, %1, %2" : "=v"(xrow) : "v"(b * 8192 + tid * 16), "s"(p.x) : "memory");
    asm volatile("global_load_dwordx2 %0, %1, %2 sc1" : "=v"(tag) : "v"(zero), "s"(p.ws + FL_EP) : "memory");
    if (has_unit) {
      const char* wb2 = (const char*)p.w2 + (size_t)(16 * (unit >> 3)) * 1024 + (unit & 7) * 128;
      const u32 wo = (l >> 5) * 4096 + (l & 31) * 4;
#define W2RAW(j, base, off) asm volatile("global_load_dword %0, %1, %2 offset:" #off : "=v"(wr[j]) : "v"(wo), "s"(base) : "memory")
      W2RAW(0, wb2, 0); W2RAW(1, wb2, 1024); W2RAW(2, wb2, 2048); W2RAW(3, wb2, 3072);
      W2RAW(4, wb2 + 8192, 0); W2RAW(5, wb2 + 8192, 1024); W2RAW(6, wb2 + 8192, 2048); W2RAW(7, wb2 + 8192, 3072);
    }
  }

  const int mt = (xcd >> 1) * 4 + (rk & 3), ft = (xcd & 1) * 7 + (rk >> 2);
  const int kt0 = (mt + ft) & 7;
  const int wt = w & 1, wf = w >> 1;
  const char* xb = (const char*)p.x + (size_t)(mt * 128 + 16 * w) * 1024;
  const char* w1b = (const char*)p.w1 + (size_t)(8 * w) * (F * 2) + ft * 256;
  const char* w3b = (const char*)p.w3 + (size_t)(8 * w) * (F * 2) + ft * 256;
  const u32 xo0 = (l >> 3) * 1024 + (((l & 7) ^ (l >> 4)) << 4);
  const u32 xo1 = (8 + (l >> 3)) * 1024 + (((l & 7) ^ (4 + (l >> 4))) << 4);
  const u32 wo1 = (l >> 4) * (F * 2) + (((l & 15) ^ (4 * (l >> 4))) << 4);
  const u32 wo3 = (l >> 4) * (F * 2) + (((l & 15) ^ (4 * (l >> 4) + 2)) << 4);
  auto dma = [&](int st, int piece) {
    const int kt = (kt0 + st) & 7;
    const u32 s = (u32)(size_t)lds + (st % 3) * G1_SLOT + w * 2048;
    if (piece == 0) glds16(xb + kt * 128, xo0, s);
    if (piece == 1) glds16(w1b + (size_t)kt * (64 * F * 2), wo1, s + 16384);
    if (piece == 2) glds16(w3b + (size_t)kt * (64 * F * 2), wo3, s + 32768);
    if (piece == 3) glds16(xb + kt * 128, xo1, s + 1024);
    if (piece == 4) glds16(w1b + (size_t)kt * (64 * F * 2) + 4 * F * 2, wo1, s + 16384 + 1024);
    if (piece == 5) glds16(w3b + (size_t)kt * (64 * F * 2) + 4 * F * 2, wo3, s + 32768 + 1024);
  };
  if (g1) {
#pragma unroll
    for (int i = 0; i < 6; ++i) dma(0, i);
    WAIT_V(6);
  } else {
    WAIT_V(0);
  }
  asm volatile("" : "+v"(tag), "+v"(xrow), "+v"(p_ldt), "+v"(p_lar), "+v"(p_aim), "+v"(p_cre), "+v"(p_cim), "+v"(wr[0]), "+v"(wr[1]),
               "+v"(wr[2]), "+v"(wr[3]), "+v"(wr[4]), "+v"(wr[5]), "+v"(wr[6]), "+v"(wr[7]));
  SCHED();

  {
    f32x2 v[4];
#pragma unroll
    for (int j = 0; j < 4; ++j) v[j] = up2(xrow[j]);
    const f32x2 s2 = (v[0] + v[1]) + (v[2] + v[3]);
    const float mean = wave_sum(s2.x + s2.y) * (1.f / D);
    f32x2 q2 = {0.f, 0.f};
#pragma unroll
    for (int j = 0; j < 4; ++j) { const f32x2 d = v[j] - f32x2{mean, mean}; q2 = fma2(d, d, q2); }
    const float var = wave_sum(q2.x + q2.y) * (1.f / D);
    const float sd = __builtin_sqrtf(rb(rb(var) + 1e-5f));
    if (l == 0) st32s(pk2(mean, sd), rs, 0, WS_ST + (b * 8 + w) * 4);
    tag.x += 1u;
    if (has_unit) {
      u32x4 o0, o1;
#pragma unroll
      for (int e = 0; e < 4; ++e) {
        o0[e] = __builtin_amdgcn_perm(wr[2 * e + 1], wr[2 * e], 0x05040100u);
        o1[e] = __builtin_amdgcn_perm(wr[2 * e + 1], wr[2 * e], 0x07060302u);
      }
      const int off = WS_W2F + ((((unit & 7) * 112 + (unit >> 3)) * 2) << 10);
      st128s(o0, rs, l * 16, off);
      st128s(o1, rs, l * 16, off + 1024);
    }
  }
  STAMP(1);

  if (!g1) {
    const int a = xcd * 4 + (rk - 28);
    WAIT_V(0);
    LBAR();
    if (tid == 0) st64s(tag, rs, 0, FL_P0 + b * 8);
    if (a < 28) {
      const int img = a >= 14, aft = a - 14 * img;
      const char* wsrc = (const char*)(img ? p.w3 : p.w1) + (size_t)(64 * w + (l >> 4)) * (F * 2) + aft * 256 + (l & 15) * 16;
      u32x4 sel;
#pragma unroll
      for (int e = 0; e < 4; ++e) sel[e] = (l & 15) == 2 * e ? 0x00003f80u : ((l & 15) == 2 * e + 1 ? 0x3f800000u : 0u);
      u32x4 wv[16];
#pragma unroll
      for (int i = 0; i < 16; ++i) wv[i] = *(const GAS u32x4*)(wsrc + (size_t)i * (4 * F * 2));
      SCHED();
      f32x4 cs = {0.f, 0.f, 0.f, 0.f};
#pragma unroll
      for (int i = 0; i < 16; ++i) cs = mfma16(sel, wv[i], cs);
      if (l < 32) *(LAS f32x4*)(lds + w * 512 + (l & 15) * 32 + (l >> 4) * 16) = cs;
      LBAR();
      if (w == 0) {
        f32x4 t = {0.f, 0.f, 0.f, 0.f};
#pragma unroll
        for (int ww = 0; ww < 8; ++ww) t += *(LAS f32x4*)(lds + ww * 512 + (l & 31) * 16);
        if (l < 32) st128s(__builtin_bit_cast(u32x4, t), rs, l * 16, WS_CS + aft * 1024 + img * 512);
        WAIT_V(0);
        if (l == 0) st64s(tag, rs, 0, FL_CS + (aft * 2 + img) * 8);
      }
      LBAR();
    }
    STAMP(2);
    u32x4 xr[8];
    const char* xs = (const char*)p.x + (size_t)(64 * a + 8 * w) * 1024 + l * 16;
#pragma unroll
    for (int r = 0; r < 8; ++r) xr[r] = *(const GAS u32x4*)(xs + r * 1024);
    poll(rs, 0, FL_P0 + (8 * a + w) * 8, tag);
    const u32 stv = ld32s(rs, (l & 7) * 4, WS_ST + (64 * a + 8 * w) * 4);
    const float riv = 1.f / bhi(stv), nbv = -b2f(stv) * riv;
    f32x2 xn[8][4];
#pragma unroll
    for (int r = 0; r < 8; ++r) {
      const float ri = __builtin_bit_cast(float, __builtin_amdgcn_readlane(__builtin_bit_cast(int, riv), r));
      const float nb = __builtin_bit_cast(float, __builtin_amdgcn_readlane(__builtin_bit_cast(int, nbv), r));
#pragma unroll
      for (int j = 0; j < 4; ++j) xn[r][j] = fma2(up2(xr[r][j]), f32x2{ri, ri}, f32x2{nb, nb});
    }
#pragma unroll
    for (int c = 0; c < 8; ++c) {
      u32x4 o;
#pragma unroll
      for (int e = 0; e < 4; ++e) o[e] = pk2(xn[7 - 2 * e][c >> 1][c & 1], xn[6 - 2 * e][c >> 1][c & 1]);
      *(LAS u32x4*)(lds + (8 * l + c) * 128 + (((7 - w) ^ (l & 7)) << 4)) = o;
    }
    LBAR();
#pragma unroll
    for (int it = 0; it < 8; ++it) {
      const int ch = 64 * w + 8 * it + (l >> 3);
      const u32x4 v = lds128(lds + ch * 128 + (l & 7) * 16);
      st128s(v, rs, ch * 4096 + (((l & 7) ^ (it & 7)) << 4), WS_XNR + (L - 64 - 64 * a) * 2);
    }
    WAIT_V(0);
    LBAR();
    if (tid == 0) st64s(tag, rs, 0, FL_XN + a * 8);
    STAMP(3);
  }

  {
    const float dt = rb(__expf(b2f(p_ldt)));
    const float ar = -rb(__expf(b2f(p_lar)));
    const float lre2 = (ar * dt) * 1.44269504f, rev = (b2f(p_aim) * dt) * 0.15915494309f;
    LAS char* tb = lds + KG_T + gch * KG_TCH + mode * 4;
    if (role == 0) {
      const f32x2 w8 = cexp_p(lre2, rev, 8.f), w32 = cexp_p(lre2, rev, 32.f);
      const f32x2 w8n = cneg(w8), w32n = cneg(w32);
      f32x2 v[4];
      v[0] = f32x2{1.f, 0.f}; v[1] = w8; v[2] = cmul(w8, w8, w8n); v[3] = cmul(v[2], w8, w8n);
#pragma unroll
      for (int m = 0; m < 8; ++m) {
#pragma unroll
        for (int r = 0; r < 4; ++r) {
          *(LAS u32*)(tb + (4 * m + r) * KG_TS) = pkh(v[r]);
          if (m < 7) v[r] = cmul(v[r], w32, w32n);
        }
        if (g1 && m >= 2) dma(1, m - 2);
      }
    } else {
      const float cr = b2f(p_cre), ci = b2f(p_cim);
      const float mx = wave_max_nonneg(fmaxf(fabsf(cr), fabsf(ci)));
      u32 eb = __builtin_bit_cast(u32, mx) & 0x7f800000u;
      eb = eb < 0x00800000u ? 0x00800000u : (eb > 0x7e800000u ? 0x7e800000u : eb);
      const float sc = __builtin_bit_cast(float, eb), isc = __builtin_bit_cast(float, 0x7f000000u - eb);
      if (l == 0) *(LAS float*)(lds + M_SC + (gch * 2 + (w & 1)) * 4) = sc * (2.f * dt);
      const f32x2 z1 = cexp_p(lre2, -rev, 1.f), zc = cexp_p(lre2, -rev, 256.f);
      const f32x2 z1n = cneg(z1), zcn = cneg(zc);
      const f32x2 z2 = cmul(z1, z1, z1n), z2n = cneg(z2);
      f32x2 m[21];
      m[0] = f32x2{cr * isc, -ci * isc};
      m[1] = cmul(m[0], z1, z1n);
#pragma unroll
      for (int i = 2; i < 8; ++i) m[i] = cmul(m[i - 2], z2, z2n);
#pragma unroll
      for (int i = 0; i < 8; ++i) *(LAS u32*)(tb + (32 + i) * KG_TS) = pkh(m[i]);
      if (g1) { dma(1, 0); dma(1, 1); }
      m[8] = cmul(m[0], zc, zcn); m[9] = cmul(m[2], zc, zcn); m[10] = cmul(m[4], zc, zcn); m[11] = cmul(m[6], zc, zcn);
      m[12] = cmul(m[8], zc, zcn); m[13] = cmul(m[10], zc, zcn);
#pragma unroll
      for (int i = 8; i < 14; ++i) *(LAS u32*)(tb + (32 + i) * KG_TS) = pkh(m[i]);
      if (g1) { dma(1, 2); dma(1, 3); }
      m[14] = cmul(m[12], zc, zcn); m[15] = cmul(m[13], zc, zcn);
      m[16] = cmul(m[14], zc, zcn);
#pragma unroll
      for (int i = 17; i < 21; ++i) m[i] = cmul(m[i - 1], zc, zcn);
#pragma unroll
      for (int i = 14; i < 21; ++i) *(LAS u32*)(tb + (32 + i) * KG_TS) = pkh(m[i]);
      if (g1) { dma(1, 4); dma(1, 5); }
    }
    LBAR();
    STAMP(4);
    const int ch = w >> 2, rbk = (w >> 1) & 1, cbk = w & 1;
    const int r = l & 15, g = l >> 4;
    LAS char* ma = lds + KG_T + ch * KG_TCH + (32 + 16 * rbk + r) * KG_TS + g * 16;
    LAS char* va = lds + KG_T + ch * KG_TCH + (16 * cbk + r) * KG_TS + g * 16;
    u32x4 fm[8], fv[8];
#pragma unroll
    for (int s = 0; s < 8; ++s) { fm[s] = lds128(ma + s * 64); fv[s] = lds128(va + s * 64); }
    const f32x2 sc = *(LAS f32x2*)(lds + M_SC + ch * 8);
    SCHED();
    f32x4 a0 = {0.f, 0.f, 0.f, 0.f}, a1 = {0.f, 0.f, 0.f, 0.f};
#pragma unroll
    for (int s = 0; s < 4; ++s) { a0 = mfma16h(fm[s], fv[s], a0); a1 = mfma16h(fm[4 + s], fv[4 + s], a1); }
    const int j = r + 16 * cbk;
    int ib, is, ix = 0;
    if (rbk == 0) { ib = g < 2 ? 8 * j + 4 * g : (g == 2 ? 256 + 4 * j : 384 + 2 * j); is = 1; ix = g == 3 ? 62 : 0; }
    else { ib = g == 0 ? 512 + j : ((g == 1 && j == 0) ? 640 : 648); is = g == 0 ? 32 : 1; }
    LAS u16* kc = (LAS u16*)(lds + M_KC + ch * 1408);
    const u32 k01 = pk2(a0[0] * sc.x + a1[0] * sc.y, a0[1] * sc.x + a1[1] * sc.y);
    const u32 k23 = pk2(a0[2] * sc.x + a1[2] * sc.y, a0[3] * sc.x + a1[3] * sc.y);
    kc[ib] = (u16)k01; kc[ib + is] = (u16)(k01 >> 16);
    kc[ib + 2 * is + ix] = (u16)k23; kc[ib + 3 * is + ix] = (u16)(k23 >> 16);
  }
  STAMP(5);

  const int m2 = xcd * 4 + (rk & 3), n2 = rk >> 2;
  const int w2off = WS_W2F + (((n2 * 112 + 14 * w) * 2) << 10);
  u32x4 wfr[28];
  auto w2ld = [&](int i) { wfr[i] = ld128s(rs, l * 16, w2off + i * 1024); };

  if (g1) {
    const int t = l & 31, h = l >> 5;
    int xa[4];
#pragma unroll
    for (int ks = 0; ks < 4; ++ks) xa[ks] = (64 * wt + t) * 128 + (((2 * ks + h) ^ ((t >> 1) & 7)) << 4);
    int wa[2];
    {
      const int img = (l >> 4) & 1, q = (l & 15) >> 2, pp = l & 3;
#pragma unroll
      for (int i = 0; i < 2; ++i) {
        const int chunk = (4 * wf + 2 * i + (pp >> 1)) ^ (4 * q + 2 * img);
        wa[i] = 16384 * (1 + img) + (8 * h + q) * 256 + chunk * 16 + 8 * (pp & 1);
      }
    }
    f32x16 acc[2][2];
#pragma unroll
    for (int i = 0; i < 2; ++i)
#pragma unroll
      for (int j = 0; j < 2; ++j)
#pragma unroll
        for (int r = 0; r < 16; ++r) acc[i][j][r] = 0.f;
    u32x4 fa[2][2], fb[2][2];
    auto rdA = [&](int st, int ks, int i) {
      LAS char* s = lds + (st % 3) * G1_SLOT + wa[i] + ks * 4096;
      const u32x2 lo = ldstr(s), hi = ldstr(s + 1024);
      fa[ks & 1][i] = u32x4{lo.x, lo.y, hi.x, hi.y};
    };
    auto rdB = [&](int st, int ks, int j) { fb[ks & 1][j] = lds128(lds + (st % 3) * G1_SLOT + xa[ks] + j * 4096); };

    WAIT_V(6);
    LBAR();
    if (tid == 0) st64s(tag, rs, 0, FL_P0 + b * 8);
    STAMP(6);
    rdA(0, 0, 0); rdB(0, 0, 0); rdB(0, 0, 1); rdA(0, 0, 1);
    dma(2, 0); dma(2, 1);
    u32x2 fl;
    u32x4 ex;
#pragma unroll
    for (int st = 0; st < 8; ++st) {
#pragma unroll
      for (int ks = 0; ks < 4; ++ks) {
        const int nst = ks == 3 ? st + 1 : st, nks = (ks + 1) & 3;
        if (ks == 3 && st < 7) {
          if (st < 6) WAIT_V(6); else WAIT_V(8);
          if (st == 4) {
            asm volatile("" : "+v"(fl));
            if (!__all(fl.x == tag.x && fl.y == tag.y)) poll(rs, l < 32 ? (32 * w + l) * 8 : 4096 + (ft * 2 + (l & 1)) * 8, FL_P0, tag);
          }
          if (st == 6) {
            asm volatile("" : "+v"(ex));
            if (w == 0) *(LAS u32x4*)(lds + M_CS + l * 16) = ex;
            if (w == 1 && l < 32) *(LAS u32x4*)(lds + M_ST + l * 16) = ex;
          }
          LBAR();
          if (st == 4 && b == 0 && tid == 0) st64s(tag, rs, 0, FL_EP);
        }
        SCHED();
        acc[0][0] = mfma32(fa[ks & 1][0], fb[ks & 1][0], acc[0][0]);
        SCHED();
        if (nst < 8) { rdA(nst, nks, 0); rdB(nst, nks, 0); }
        SCHED();
        acc[0][1] = mfma32(fa[ks & 1][0], fb[ks & 1][1], acc[0][1]);
        SCHED();
        if (nst < 8) rdB(nst, nks, 1);
        SCHED();
        acc[1][0] = mfma32(fa[ks & 1][1], fb[ks & 1][0], acc[1][0]);
        SCHED();
        if (nst < 8) rdA(nst, nks, 1);
        SCHED();
        acc[1][1] = mfma32(fa[ks & 1][1], fb[ks & 1][1], acc[1][1]);
        SCHED();
        const int tgt = nst + 2, ph = nks;
        if (tgt < 8) {
          if (ph == 0) { dma(tgt, 0); dma(tgt, 1); }
          if (ph == 1) dma(tgt, 2);
          if (ph == 2) { dma(tgt, 3); dma(tgt, 4); }
          if (ph == 3) dma(tgt, 5);
        } else if (tgt < 10) {
          w2ld((tgt - 8) * 8 + 2 * ph); w2ld((tgt - 8) * 8 + 2 * ph + 1);
        } else { w2ld(16); w2ld(17); }
        if (st == 3 && ks == 2) {
          const u32 fo = l < 32 ? (32 * w + l) * 8 : 4096 + (ft * 2 + (l & 1)) * 8;
          asm volatile("global_load_dwordx2 %0, %1, %2 sc1" : "=v"(fl) : "v"(fo), "s"(p.ws + FL_P0) : "memory");
        }
        if (st == 5 && ks == 2) {
          const u32 eo = w == 0 ? WS_CS + ft * 1024 + l * 16 : WS_ST + mt * 512 + (l & 31) * 16;
          asm volatile("global_load_dwordx4 %0, %1, %2 sc1" : "=v"(ex) : "v"(eo), "s"(p.ws) : "memory");
        }
        SCHED();
      }
    }
    STAMP(7);
#pragma unroll
    for (int j = 0; j < 2; ++j) {
      const u32 ms = *(LAS u32*)(lds + M_ST + (64 * wt + 32 * j + t) * 4);
      const float nm = -b2f(ms), sd = bhi(ms), sd2 = sd * sd;
      const float ce = -1.44269504f * __builtin_amdgcn_rcpf(sd);
#pragma unroll
      for (int i = 0; i < 2; ++i) {
        LAS char* cb = lds + M_CS + (32 * wf + 16 * i + 4 * h) * 4;
        const f32x4 c1a = *(LAS f32x4*)cb, c1b = *(LAS f32x4*)(cb + 32);
        const f32x4 c3a = *(LAS f32x4*)(cb + 512), c3b = *(LAS f32x4*)(cb + 512 + 32);
        u32x4 o;
#pragma unroll
        for (int r = 0; r < 4; ++r) {
          const f32x4 c1 = r < 2 ? c1a : c1b, c3 = r < 2 ? c3a : c3b;
          const f32x2 g = fma2(f32x2{nm, nm}, f32x2{c1[2 * (r & 1)], c1[2 * (r & 1) + 1]}, f32x2{acc[i][j][2 * r], acc[i][j][2 * r + 1]});
          const f32x2 u = fma2(f32x2{nm, nm}, f32x2{c3[2 * (r & 1)], c3[2 * (r & 1) + 1]}, f32x2{acc[i][j][8 + 2 * r], acc[i][j][9 + 2 * r]});
          const f32x2 a = g * f32x2{ce, ce};
          const f32x2 e = {__builtin_amdgcn_exp2f(a.x), __builtin_amdgcn_exp2f(a.y)};
          const f32x2 d = fma2(e, f32x2{sd2, sd2}, f32x2{sd2, sd2});
          const f32x2 q = {__builtin_amdgcn_rcpf(d.x), __builtin_amdgcn_rcpf(d.y)};
          o[r] = pk2v((g * u) * q);
        }
        st128s(o, rs, l * 16, WS_H + (((mt * 4 + 2 * wt + j) * 112 + ft * 8 + 2 * wf + i) << 10));
        SCHED();
        if (2 * j + i < 3) { w2ld(18 + 3 * (2 * j + i)); w2ld(19 + 3 * (2 * j + i)); w2ld(20 + 3 * (2 * j + i)); }
        SCHED();
      }
    }
    w2ld(27);
    STAMP(8);
  } else {
    poll(rs, (32 * w + (l & 31)) * 8, FL_P0, tag);
    LBAR();
#pragma unroll
    for (int i = 0; i < 28; ++i) w2ld(i);
  }

  {
    poll(rs, (l & 31) * 8, FL_XN, tag);
    if (g1 && l == 0) st64s(tag, rs, 0, FL_H + ((mt * 2 + wt) * 64 + ft * 4 + wf) * 8);
    const int uch = w >> 2, ui = tid & 255;
    const u32x4 u0 = ld128s(rs, ui * 16, WS_XNR + (2 * b + uch) * 4096);
    u32x4 u1 = {0u, 0u, 0u, 0u};
    if (ui < 255) u1 = ld128s(rs, ui * 16 + 16, WS_XNR + (2 * b + uch) * 4096);
    const u32 dk = *(const GAS u32*)(p.dsk + 2 * b);
    LBAR();
    STAMP(9);
    {
      LAS u16* kc = (LAS u16*)(lds + M_KC + uch * 1408);
      u32 o[4];
#pragma unroll
      for (int e = 0; e < 4; ++e) {
        const int l0 = 8 * ui + 2 * e;
        const u32 bits = pk2((float)l0, (float)(l0 + 1));
        const int i0 = ui < 32 ? l0 : (int)(bits & 0xffffu) - (0x4300 - 128);
        const int i1 = ui < 32 ? l0 + 1 : (int)(bits >> 16) - (0x4300 - 128);
        o[e] = (u32)kc[i0] | ((u32)kc[i1] << 16);
      }
      *(LAS u32x4*)(lds + CV_K + uch * 4608 + 512 + ui * 16) = u32x4{o[0], o[1], o[2], o[3]};
      if (ui < 32) *(LAS u32x4*)(lds + CV_K + uch * 4608 + ui * 16) = u32x4{0u, 0u, 0u, 0u};
      if (ui < 8) *(LAS u32x4*)(lds + CV_U + uch * (8 * CV_CS) + ui * CV_CS + 4096) = u32x4{0u, 0u, 0u, 0u};
    }
    {
      const u32 dd[8] = {u0.x, u0.y, u0.z, u0.w, u1.x, u1.y, u1.z, u1.w};
      u32 od[7];
#pragma unroll
      for (int k = 0; k < 7; ++k) od[k] = __builtin_amdgcn_alignbit(dd[k + 1], dd[k], 16);
      LAS char* ud = lds + CV_U + uch * (8 * CV_CS) + ui * 16;
#pragma unroll
      for (int s = 0; s < 4; ++s) {
        *(LAS u32x4*)(ud + (2 * s) * CV_CS) = u32x4{dd[s], dd[s + 1], dd[s + 2], dd[s + 3]};
        *(LAS u32x4*)(ud + (2 * s + 1) * CV_CS) = u32x4{od[s], od[s + 1], od[s + 2], od[s + 3]};
      }
    }
    LBAR();
    STAMP(10);
    {
      const int ch = w & 1, ts = (w >> 1) & 1, rh = w >> 2;
      const int bb = l & 15, g = l >> 4;
      LAS char* ua = lds + CV_U + ch * (8 * CV_CS) + (7 - (bb & 7)) * CV_CS + 4080 - 16 * (bb >> 3) - 32 * (g >> 1) + 16 * (g & 1)
                     - 7 * 512 - 256 * rh - 3 * 64;
      LAS char* ka = lds + CV_K + ch * 4608 + 512 + 32 * (bb - (g >> 1)) + 16 * (g & 1) - 256 * rh - 3 * 64;
      f32x4 acc[4];
#pragma unroll
      for (int i = 0; i < 4; ++i) acc[i] = f32x4{0.f, 0.f, 0.f, 0.f};
      if (ts == 0) conv_tiles<0>(ua, ka, acc); else conv_tiles<1>(ua, ka, acc);
#pragma unroll
      for (int i = 0; i < 4; ++i) *(LAS f32x4*)(lds + CV_X + (w * 4 + i) * 1024 + l * 16) = acc[i];
    }
    LBAR();
    STAMP(11);
    const int ts = (w >> 1) & 1, i = (w & 1) + 2 * (w >> 2);
    const int tile = ts ? (i == 0 ? 6 : i == 1 ? 5 : i == 2 ? 2 : 1) : (i == 0 ? 7 : i == 1 ? 4 : i == 2 ? 3 : 0);
    const int pos = 256 * tile + 16 * (l & 15) + 4 * (l >> 4);
    f32x2 res[2][2];
#pragma unroll
    for (int ch = 0; ch < 2; ++ch) {
      const f32x4 y0 = *(LAS f32x4*)(lds + CV_X + ((ch + 2 * ts) * 4 + i) * 1024 + l * 16);
      const f32x4 y1 = *(LAS f32x4*)(lds + CV_X + ((ch + 2 * ts + 4) * 4 + i) * 1024 + l * 16);
      const u32x2 ur = lds64(lds + CV_U + ch * (8 * CV_CS) + (2044 - pos) * 2);
      const float dsk = ch ? bhi(dk) : b2f(dk);
      const f32x4 y = y0 + y1;
      res[ch][0] = f32x2{__builtin_fmaf(bhi(ur.y), dsk, y[0]), __builtin_fmaf(b2f(ur.y), dsk, y[1])};
      res[ch][1] = f32x2{__builtin_fmaf(bhi(ur.x), dsk, y[2]), __builtin_fmaf(b2f(ur.x), dsk, y[3])};
    }
    const u32x4 o = {pk2(res[0][0].x, res[1][0].x), pk2(res[0][0].y, res[1][0].y), pk2(res[0][1].x, res[1][1].x), pk2(res[0][1].y, res[1][1].y)};
    st128s(o, rs, pos * 4, WS_Y + b * 8192);
    poll(rs, (l < 7 ? l : 0) * 8, FL_H + (m2 * 64 + 7 * w) * 8, tag);
    if (l == 0) st64s(tag, rs, 0, FL_Y + (tile * 256 + b) * 8);
  }
  STAMP(12);

  {
    const int hoff = WS_H + ((2 * m2 * 112 + 14 * w) << 10);
    u32x4 hf[14][2];
    auto hld = [&](int k) { hf[k][0] = ld128s(rs, l * 16, hoff + k * 1024); hf[k][1] = ld128s(rs, l * 16, hoff + (112 + k) * 1024); };
    constexpr int PD = 3;
#pragma unroll
    for (int k = 0; k < PD; ++k) hld(k);
    const int etok = 64 * m2 + 8 * w + (l >> 3), cg = l & 7;
    const u32x4 xv = *(const GAS u32x4*)((const char*)p.x + (size_t)etok * 1024 + n2 * 128 + cg * 16);
    SCHED();
    f32x16 acc[2][2];
#pragma unroll
    for (int i = 0; i < 2; ++i)
#pragma unroll
      for (int j = 0; j < 2; ++j)
#pragma unroll
        for (int r = 0; r < 16; ++r) acc[i][j][r] = 0.f;
#pragma unroll
    for (int k = 0; k < 14; ++k) {
#pragma unroll
      for (int j = 0; j < 2; ++j)
#pragma unroll
        for (int i = 0; i < 2; ++i) acc[i][j] = mfma32(wfr[2 * k + i], hf[k][j], acc[i][j]);
      SCHED();
      if (k + PD < 14) hld(k + PD);
      SCHED();
    }
    STAMP(13);
    poll(rs, (l & 31) * 8, FL_Y + ((m2 >> 2) * 256 + 32 * n2) * 8, tag);
    u32 yv[4];
#pragma unroll
    for (int j = 0; j < 4; ++j) yv[j] = ld32s(rs, (4 * cg + j) * 8192 + etok * 4, WS_Y + 32 * n2 * 8192);
    BAR();
    {
      LAS char* pw = lds + w * G2_PW + (l & 31) * G2_RS + (l >> 5) * 32;
#pragma unroll
      for (int j = 0; j < 2; ++j)
#pragma unroll
        for (int qd = 0; qd < 4; ++qd)
#pragma unroll
          for (int i = 0; i < 2; ++i)
            *(LAS f32x4*)(pw + j * 32 * G2_RS + qd * 64 + i * 16) =
                f32x4{acc[i][j][4 * qd], acc[i][j][4 * qd + 1], acc[i][j][4 * qd + 2], acc[i][j][4 * qd + 3]};
    }
    LBAR();
    STAMP(14);
    LAS char* pr = lds + (8 * w + (l >> 3)) * G2_RS + cg * 32;
    f32x4 pe[8], po[8];
#pragma unroll
    for (int ww = 0; ww < 8; ++ww) { pe[ww] = *(LAS f32x4*)(pr + ww * G2_PW); po[ww] = *(LAS f32x4*)(pr + ww * G2_PW + 16); }
    SCHED();
    const f32x4 e = ((pe[0] + pe[1]) + (pe[2] + pe[3])) + ((pe[4] + pe[5]) + (pe[6] + pe[7]));
    const f32x4 o = ((po[0] + po[1]) + (po[2] + po[3])) + ((po[4] + po[5]) + (po[6] + po[7]));
    u32x4 r;
#pragma unroll
    for (int j = 0; j < 4; ++j) {
      const u32 s = pk2v(up2(xv[j]) + up2(yv[j]));
      r[j] = pk2v(up2(s) + up2(pk2(e[j], o[j])));
    }
    *(GAS u32x4*)((char*)p.out + (size_t)etok * 1024 + n2 * 128 + cg * 16) = r;
  }
  STAMP(15);
}

extern "C" void kernel_launch(void* const* d_in, const int* in_sizes, int n_in, void* d_out, int out_size, void* d_ws,
                              size_t ws_size, hipStream_t stream) {
  Params p;
  p.x = (const u16*)d_in[0]; p.lar = (const u16*)d_in[1]; p.aim = (const u16*)d_in[2]; p.cre = (const u16*)d_in[3];
  p.cim = (const u16*)d_in[4]; p.ldt = (const u16*)d_in[5]; p.dsk = (const u16*)d_in[6];
  p.w1 = (const u16*)d_in[8]; p.w3 = (const u16*)d_in[9]; p.w2 = (const u16*)d_in[10];
  p.out = (u16*)d_out; p.ws = (char*)d_ws;
  fused<<<256, 512, 0, stream>>>(p);
}
```

```cpp
#include <hip/hip_runtime.h>
#include <hip/hip_bf16.h>

typedef unsigned short u16;
typedef unsigned int u32;
typedef unsigned long long u64;
typedef __bf16 bf16x8 __attribute__((ext_vector_type(8)));
typedef __bf16 bf16x2 __attribute__((ext_vector_type(2)));
typedef _Float16 f16x8 __attribute__((ext_vector_type(8)));
typedef _Float16 f16x2 __attribute__((ext_vector_type(2)));
typedef short s16x4 __attribute__((ext_vector_type(4)));
typedef float f32x2 __attribute__((ext_vector_type(2)));
typedef float f32x4 __attribute__((ext_vector_type(4)));
typedef float f32x16 __attribute__((ext_vector_type(16)));
typedef u32 u32x2 __attribute__((ext_vector_type(2)));
typedef u32 u32x4 __attribute__((ext_vector_type(4)));
typedef __amdgpu_buffer_rsrc_t rsrc_t;
#define LAS __attribute__((address_space(3)))
#define GAS __attribute__((address_space(1)))
#define DEV __device__ __forceinline__

constexpr int L = 2048, D = 512, NM = 128, F = 1792;
constexpr int LDS_BYTES = 163840;
constexpr int SC1 = 16;

constexpr int WS_H = 0;
constexpr int WS_XNR = 8 << 20;
constexpr int WS_W2F = 10 << 20;
constexpr int WS_Y = 12 << 20;
constexpr int WS_ST = 14 << 20;
constexpr int WS_CS = WS_ST + 8192;
constexpr int WS_FL = 15 << 20;
constexpr int FS = 128;
constexpr int FL_EP = WS_FL;
constexpr int FL_P0 = WS_FL + 4096;
constexpr int FL_H = FL_P0 + 256 * FS;
constexpr int FL_Y = FL_H + 2048 * FS;
constexpr int WS_BYTES = 16 << 20;

struct Params {
  const u16 *x, *lar, *aim, *cre, *cim, *ldt, *dsk, *w1, *w3, *w2;
  u16* out;
  char* ws;
};

DEV float b2f(u32 v) { return __builtin_bit_cast(float, v << 16); }
DEV float bhi(u32 v) { return __builtin_bit_cast(float, v & 0xffff0000u); }
DEV u32 pk2v(f32x2 f) { return __builtin_bit_cast(u32, __builtin_convertvector(f, bf16x2)); }
DEV u32 pk2(float lo, float hi) { return pk2v(f32x2{lo, hi}); }
DEV f32x2 up2(u32 v) { return f32x2{b2f(v), bhi(v)}; }
DEV float rb(float f) { return b2f(pk2(f, f) & 0xffffu); }
DEV f32x2 fma2(f32x2 a, f32x2 b, f32x2 c) { return __builtin_elementwise_fma(a, b, c); }
DEV f32x16 mfma32(u32x4 a, u32x4 b, f32x16 c) {
  return __builtin_amdgcn_mfma_f32_32x32x16_bf16(__builtin_bit_cast(bf16x8, a), __builtin_bit_cast(bf16x8, b), c, 0, 0, 0);
}
DEV f32x4 mfma16(u32x4 a, u32x4 b, f32x4 c) {
  return __builtin_amdgcn_mfma_f32_16x16x32_bf16(__builtin_bit_cast(bf16x8, a), __builtin_bit_cast(bf16x8, b), c, 0, 0, 0);
}
DEV f32x16 mfma32h(u32x4 a, u32x4 b, f32x16 c) {
  return __builtin_amdgcn_mfma_f32_32x32x16_f16(__builtin_bit_cast(f16x8, a), __builtin_bit_cast(f16x8, b), c, 0, 0, 0);
}
DEV u32x4 lds128(LAS char* p) { return *(LAS u32x4*)p; }
DEV u32x2 lds64(LAS char* p) { return *(LAS u32x2*)p; }
DEV u32x2 ldstr(LAS char* p) { return __builtin_bit_cast(u32x2, __builtin_amdgcn_ds_read_tr16_b64_v4i16((LAS s16x4*)p)); }
DEV void glds16(const char* sbase, u32 voff, u32 ldst) {
  u32 keep;
  asm volatile("s_mov_b32 %0, m0\n\ts_mov_b32 m0, %3\n\ts_nop 0\n\tglobal_load_lds_dwordx4 %1, %2\n\ts_mov_b32 m0, %0"
               : "=&s"(keep) : "v"(voff), "s"(sbase), "s"(ldst) : "memory");
}
#define WAIT_V(n) asm volatile("s_waitcnt vmcnt(" #n ")" ::: "memory")
#define WAIT_L0() asm volatile("s_waitcnt lgkmcnt(0)" ::: "memory")
#define BAR() __builtin_amdgcn_s_barrier()
#define LBAR() do { WAIT_L0(); BAR(); asm volatile("" ::: "memory"); } while (0)
#define SCHED() __builtin_amdgcn_sched_barrier(0)
#define PIN(x) asm volatile("" : "+v"(x))
constexpr int OOB = 0x04000000;
#define STAMP(i)

DEV u32x4 ld128s(rsrc_t rs, int voff, int soff) { return __builtin_amdgcn_raw_buffer_load_b128(rs, voff, soff, SC1); }
DEV u32x2 ld64s(rsrc_t rs, int voff, int soff) { return __builtin_amdgcn_raw_buffer_load_b64(rs, voff, soff, SC1); }
DEV u32 ld32s(rsrc_t rs, int voff, int soff) { return __builtin_amdgcn_raw_buffer_load_b32(rs, voff, soff, SC1); }
DEV void st128s(u32x4 v, rsrc_t rs, int voff, int soff) {
  __builtin_amdgcn_raw_buffer_store_b128(v, rs, voff, soff, SC1);
  asm volatile("s_nop 1" :: "v"(v) : "memory");
}
DEV void st64s(u32x2 v, rsrc_t rs, int voff, int soff) { __builtin_amdgcn_raw_buffer_store_b64(v, rs, voff, soff, SC1); }
DEV void st32s(u32 v, rsrc_t rs, int voff, int soff) { __builtin_amdgcn_raw_buffer_store_b32(v, rs, voff, soff, SC1); }
DEV void poll(rsrc_t rs, int voff, int soff, u32x2 tag) {
#pragma unroll 1
  for (int spin = 0; spin < (1 << 18); ++spin) {
    const u32x2 v = ld64s(rs, voff, soff);
    if (__all(v.x == tag.x && v.y == tag.y)) break;
    __builtin_amdgcn_s_sleep(4);
  }
  __builtin_amdgcn_fence(__ATOMIC_ACQUIRE, "wavefront");
  asm volatile("" ::: "memory");
}

template <int CTRL, int RM>
DEV u32 dppu(u32 v) { return (u32)__builtin_amdgcn_update_dpp(0, (int)v, CTRL, RM, 0xf, true); }
DEV u32 wave_max_u32(u32 v) {
  v = max(v, dppu<0x111, 0xf>(v)); v = max(v, dppu<0x112, 0xf>(v)); v = max(v, dppu<0x114, 0xf>(v)); v = max(v, dppu<0x118, 0xf>(v));
  v = max(v, dppu<0x142, 0xa>(v)); v = max(v, dppu<0x143, 0xc>(v));
  return (u32)__builtin_amdgcn_readlane((int)v, 63);
}

DEV float rdl(float v, int lane) { return __builtin_bit_cast(float, __builtin_amdgcn_readlane(__builtin_bit_cast(int, v), lane)); }
template <int CTRL>
DEV float dppx(float v) { return __builtin_bit_cast(float, __builtin_amdgcn_mov_dpp(__builtin_bit_cast(int, v), CTRL, 0xf, 0xf, true)); }
DEV float rows8_sum(const float (&p)[8], int l) {
  float a[4], c[2];
#pragma unroll
  for (int i = 0; i < 4; ++i) {
    const auto r = __builtin_amdgcn_permlane32_swap(__builtin_bit_cast(u32, p[i]), __builtin_bit_cast(u32, p[i + 4]), false, false);
    a[i] = __builtin_bit_cast(float, (u32)r[0]) + __builtin_bit_cast(float, (u32)r[1]);
  }
#pragma unroll
  for (int i = 0; i < 2; ++i) {
    const auto r = __builtin_amdgcn_permlane16_swap(__builtin_bit_cast(u32, a[i]), __builtin_bit_cast(u32, a[i + 2]), false, false);
    c[i] = __builtin_bit_cast(float, (u32)r[0]) + __builtin_bit_cast(float, (u32)r[1]);
  }
  const bool up = l & 8;
  float v = (up ? c[1] : c[0]) + dppx<0x128>(up ? c[0] : c[1]);
  v += dppx<0xB1>(v); v += dppx<0x4E>(v); v += dppx<0x141>(v);
  return v;
}

constexpr int G1_SLOT = 49152;
constexpr int KG_T = 2 * G1_SLOT;
constexpr int KG_TW = 53 * 256;
constexpr int SPARE = 3 * G1_SLOT;
constexpr int M_CQ = SPARE;
constexpr int M_CS = SPARE;
constexpr int M_KC = SPARE + 5120;
constexpr int CV_K = SPARE + 5120;
static_assert(KG_T + 4 * KG_TW <= M_KC && M_KC + 4 * 2816 <= LDS_BYTES, "lds");
constexpr int CV_CS = 4160;
constexpr int CV_U = 2 * G1_SLOT;
constexpr int CV_UCH = 4 * CV_CS;
constexpr int CV_X = 0;

DEV f32x2 cmul(f32x2 a, f32x2 b, f32x2 bn) { return fma2(f32x2{a.y, a.x}, bn, a * f32x2{b.x, b.x}); }
DEV f32x2 cneg(f32x2 b) { return f32x2{-b.y, b.y}; }
DEV f32x2 cexp_p(float lre2, float rev, float pw) {
  const float mg = __builtin_amdgcn_exp2f(lre2 * pw);
  const float ph = __builtin_amdgcn_fractf(rev * pw);
  return f32x2{__builtin_amdgcn_cosf(ph), __builtin_amdgcn_sinf(ph)} * f32x2{mg, mg};
}
DEV u32 pkh(f32x2 f) { return __builtin_bit_cast(u32, __builtin_convertvector(f, f16x2)); }
DEV LAS char* opaque(LAS char* q) { u32 v = (u32)(size_t)q; asm volatile("" : "+v"(v)); return (LAS char*)(size_t)v; }

template <int TS, class Hook>
DEV void conv_tiles(LAS char* ua, LAS char* ka, f32x4 (&acc)[4], Hook hook) {
  constexpr int T0 = TS ? 6 : 7, T1 = TS ? 5 : 4, T2 = TS ? 2 : 3, T3 = TS ? 1 : 0, NF = T0 + 1;
  constexpr int ORD0[16] = {0, 15, 12, 11, 8, 1, 14, 10, 2, 13, 9, 3, 4, 5, 6, 7};
  constexpr int ORD1[16] = {0, 14, 13, 10, 9, 1, 12, 8, 2, 11, 3, 4, 5, 6, 0, 0};
  u32x4 fr[2][16];
  auto load = [&](int rr, int id) {
    if (id < 8) {
      const u32x2 lo = *(volatile LAS u32x2*)(ua + (7 - id) * 512 + (3 - rr) * 64), hi = *(volatile LAS u32x2*)(ua + (7 - id) * 512 + (3 - rr) * 64 + 8);
      fr[rr & 1][id] = u32x4{lo.x, lo.y, hi.x, hi.y};
    } else {
      fr[rr & 1][id] = lds128(ka + (id - 8) * 512 + (3 - rr) * 64);
    }
  };
#pragma unroll
  for (int k = 0; k < 2 * NF; ++k) { load(0, TS ? ORD1[k] : ORD0[k]); SCHED(); }
#pragma unroll
  for (int rr = 0; rr < 4; ++rr) {
#pragma unroll
    for (int c = 0; c <= T0; ++c) {
      u32x4(&f)[16] = fr[rr & 1];
      acc[0] = mfma16(f[c], f[8 + T0 - c], acc[0]);
      if (c <= T1) acc[1] = mfma16(f[c], f[8 + T1 - c], acc[1]);
      if (c <= T2) acc[2] = mfma16(f[c], f[8 + T2 - c], acc[2]);
      if (c <= T3) acc[3] = mfma16(f[c], f[8 + T3 - c], acc[3]);
      SCHED();
      if (rr < 3) { load(rr + 1, TS ? ORD1[2 * c] : ORD0[2 * c]); load(rr + 1, TS ? ORD1[2 * c + 1] : ORD0[2 * c + 1]); }
      hook(rr, c);
      SCHED();
    }
  }
}

__global__ __launch_bounds__(512, 2) void fused(Params p) {
  __shared__ __attribute__((aligned(16))) char smem[LDS_BYTES];
  LAS char* lds = (LAS char*)smem;
  const int tid = threadIdx.x, l = tid & 63;
  const int w = __builtin_amdgcn_readfirstlane(tid >> 6);
  const int b = blockIdx.x, xcd = b & 7, rk = b >> 3;
  const bool g1 = rk < 28;
  const rsrc_t rs = __builtin_amdgcn_make_buffer_rsrc(p.ws, 0, WS_BYTES, 0x00020000);
  const int l15 = l & 15, g = l >> 4;
  STAMP(0);

  u32 p_lar, p_aim, p_cre, p_cim;
  if (w < 4) {
    const u32 po = (b * 256 + tid) * 2;
    asm volatile("global_load_ushort %0, %1, %2" : "=v"(p_lar) : "v"(po), "s"(p.lar) : "memory");
    asm volatile("global_load_ushort %0, %1, %2" : "=v"(p_aim) : "v"(po), "s"(p.aim) : "memory");
    asm volatile("global_load_ushort %0, %1, %2" : "=v"(p_cre) : "v"(po), "s"(p.cre) : "memory");
    asm volatile("global_load_ushort %0, %1, %2" : "=v"(p_cim) : "v"(po), "s"(p.cim) : "memory");
  }
  u32x2 tag;
  u32 p_ldt2, dk;
  asm volatile("s_load_dwordx2 %0, %3, 0x0\n\ts_load_dword %1, %4, 0x0\n\ts_load_dword %2, %5, 0x0"
               : "=&s"(tag), "=&s"(p_ldt2), "=&s"(dk) : "s"(p.ws + FL_EP), "s"(p.ldt + 2 * b), "s"(p.dsk + 2 * b) : "memory");

  const int mt = (xcd >> 1) * 4 + (rk & 3), ft = (xcd & 1) * 7 + (rk >> 2);
  const int kt0 = (2 * (xcd >> 1) + 4 * (xcd & 1) + (mt & 1)) & 7;
  const int wt = w & 1, wf = w >> 1;
  const char* xb = (const char*)p.x + (size_t)(mt * 128) * 1024;
  const char* w1b = (const char*)p.w1 + ft * 256;
  const char* w3b = (const char*)p.w3 + ft * 256;
  const u32 xo0 = (l >> 3) * 1024 + (((l & 7) ^ g) << 4);
  const u32 xo1 = (8 + (l >> 3)) * 1024 + (((l & 7) ^ (4 + g)) << 4);
  const u32 wo1 = g * (F * 2) + ((l15 ^ (4 * g)) << 4);
  const u32 wo3 = g * (F * 2) + ((l15 ^ (4 * g + 2)) << 4);
  auto dmaw = [&](int st, int piece, int ww) {
    const int kt = (kt0 + st) & 7;
    const u32 s = (u32)(size_t)lds + (st % 3) * G1_SLOT + ww * 2048;
    const size_t xs = (size_t)ww * 16384 + kt * 128, ws_ = (size_t)(kt * 64 + ww * 8) * (F * 2);
    if (piece == 0) glds16(xb + xs, xo0, s);
    if (piece == 1) glds16(w1b + ws_, wo1, s + 16384);
    if (piece == 2) glds16(w3b + ws_, wo3, s + 32768);
    if (piece == 3) glds16(xb + xs, xo1, s + 1024);
    if (piece == 4) glds16(w1b + ws_ + 4 * F * 2, wo1, s + 16384 + 1024);
    if (piece == 5) glds16(w3b + ws_ + 4 * F * 2, wo3, s + 32768 + 1024);
  };

  auto dma = [&](int st, int piece) { dmaw(st, piece, w); };
  u32 wr[8];
  const int un2 = b / 28, ukb = 4 * (b - 28 * un2) + (w & 3);

  if (g1 && w >= 4) {
#pragma unroll
    for (int st = 0; st < 2; ++st)
#pragma unroll
      for (int i = 0; i < 6; ++i) { dmaw(st, i, w - 4); dmaw(st, i, w); }
    const char* wb2 = (const char*)p.w2 + (size_t)(16 * ukb) * 1024 + un2 * 128;
    const u32 wv = (l >> 5) * 4096 + (l & 31) * 4;
#define W2RAW(j, base, off) asm volatile("global_load_dword %0, %1, %2 offset:" #off : "=v"(wr[j]) : "v"(wv), "s"(base) : "memory")
    W2RAW(0, wb2, 0); W2RAW(1, wb2, 1024); W2RAW(2, wb2, 2048); W2RAW(3, wb2, 3072);
    W2RAW(4, wb2 + 8192, 0); W2RAW(5, wb2 + 8192, 1024); W2RAW(6, wb2 + 8192, 2048); W2RAW(7, wb2 + 8192, 3072);
  }
  asm volatile("s_waitcnt lgkmcnt(0)" : "+s"(tag), "+s"(p_ldt2), "+s"(dk) :: "memory");
  tag.x += 1u;
  if (!g1) {
    const int a = xcd * 4 + (rk - 28);
    u32x4 xr[8];
    const char* xs = (const char*)p.x + (size_t)(64 * a + 8 * w) * 1024 + l * 16;
#pragma unroll
    for (int r = 0; r < 8; ++r) xr[r] = *(const GAS u32x4*)(xs + r * 1024);
    SCHED();
    f32x2 v[8][4];
    float ps[8];
#pragma unroll
    for (int r = 0; r < 8; ++r) {
#pragma unroll
      for (int j = 0; j < 4; ++j) v[r][j] = up2(xr[r][j]);
      const f32x2 s2 = (v[r][0] + v[r][1]) + (v[r][2] + v[r][3]);
      ps[r] = s2.x + s2.y;
    }
    const float mean = rows8_sum(ps, l) * (1.f / D);
#pragma unroll
    for (int r = 0; r < 8; ++r) {
      const float mr = rdl(mean, 8 * r);
      f32x2 q2 = {0.f, 0.f};
#pragma unroll
      for (int j = 0; j < 4; ++j) { v[r][j] -= f32x2{mr, mr}; q2 = fma2(v[r][j], v[r][j], q2); }
      ps[r] = q2.x + q2.y;
    }
    const float var = rows8_sum(ps, l) * (1.f / D);
    const u32 ms = pk2(mean, __builtin_sqrtf(rb(rb(var) + 1e-5f)));
    if ((l & 7) == 0) st32s(ms, rs, (l >> 3) * 4, WS_ST + (64 * a + 8 * w) * 4);
    const float riv = 1.f / bhi(ms), nbv = (mean - b2f(ms)) * riv;
#pragma unroll
    for (int r = 0; r < 8; ++r) {
      const float ri = rdl(riv, 8 * r), nb = rdl(nbv, 8 * r);
#pragma unroll
      for (int j = 0; j < 4; ++j) v[r][j] = fma2(v[r][j], f32x2{ri, ri}, f32x2{nb, nb});
    }
#pragma unroll
    for (int c = 0; c < 8; ++c) {
      u32x4 o;
#pragma unroll
      for (int e = 0; e < 4; ++e) o[e] = pk2(v[7 - 2 * e][c >> 1][c & 1], v[6 - 2 * e][c >> 1][c & 1]);
      *(LAS u32x4*)(lds + (8 * l + c) * 128 + (((7 - w) ^ (l & 7)) << 4)) = o;
    }
    LBAR();
    u32x4 tv[8];
#pragma unroll
    for (int it = 0; it < 8; ++it) tv[it] = lds128(lds + w * 8192 + it * 1024 + l * 16);
#pragma unroll
    for (int it = 0; it < 8; ++it)
      st128s(tv[it], rs, (8 * it + (l >> 3)) * 4096 + (((l & 7) ^ it) << 4), WS_XNR + w * (64 * 4096) + (L - 64 - 64 * a) * 2);
    WAIT_V(0);
    LBAR();
    if (tid == 0) st64s(tag, rs, 0, FL_P0 + b * FS);
    STAMP(3);
  }
  if (w < 4) {
    LAS char* tw = lds + KG_T + w * KG_TW;
    u32 ta[16];
#pragma unroll
    for (int k = 0; k < 16; ++k) { ta[k] = ((u32)(size_t)tw + l * 4) ^ (k << 4); PIN(ta[k]); }
    const float dt = rb(__expf((w >> 1) ? bhi(p_ldt2) : b2f(p_ldt2)));
    asm volatile("s_waitcnt vmcnt(0)" : "+v"(p_lar), "+v"(p_aim), "+v"(p_cre), "+v"(p_cim) :: "memory");
    STAMP(1);
    const float ar = -rb(__expf(b2f(p_lar)));
    const float lre2 = (ar * dt) * 1.44269504f, rev = (b2f(p_aim) * dt) * 0.15915494309f;
    u32 eb = wave_max_u32(max(p_cre & 0x7f80u, p_cim & 0x7f80u)) << 16;
    eb = eb < 0x00800000u ? 0x00800000u : (eb > 0x7e800000u ? 0x7e800000u : eb);
    const float sc = __builtin_bit_cast(float, eb) * (2.f * dt), isc = __builtin_bit_cast(float, 0x7f000000u - eb);
    f32x2 e[53], tmp[8];
    u32 eh[53];
    const f32x2 w8 = cexp_p(lre2, rev, 8.f), w64 = cexp_p(lre2, rev, 64.f);
    const f32x2 z1 = cexp_p(lre2, -rev, 1.f), zc = cexp_p(lre2, -rev, 256.f);
    e[0] = f32x2{1.f, 0.f}; e[1] = w8; e[8] = w64; e[32] = f32x2{b2f(p_cre) * isc, -b2f(p_cim) * isc};
#define CM1(k, a, bb) tmp[k] = (a) * f32x2{(bb).x, (bb).x}
#define CM2(k, d, a, bn) d = fma2(f32x2{(a).y, (a).x}, bn, tmp[k])
    const f32x2 w8n = cneg(w8), w64n = cneg(w64), z1n = cneg(z1), zcn = cneg(zc);
    f32x2 z2, z4, zd, zq;
    SCHED();
    CM1(0, w8, w8); CM1(1, z1, z1); CM1(2, zc, zc); CM1(3, e[32], z1); CM1(4, e[32], zc); CM1(5, w8, w64); CM1(6, w64, w64);
    SCHED();
    CM2(0, e[2], w8, w8n); CM2(1, z2, z1, z1n); CM2(2, zd, zc, zcn); CM2(3, e[33], e[32], z1n); CM2(4, e[40], e[32], zcn);
    CM2(5, e[9], w8, w64n); CM2(6, e[16], w64, w64n);
    SCHED();
    const f32x2 e2n = cneg(e[2]), z2n = cneg(z2), zdn = cneg(zd);
    CM1(0, e[2], w8); CM1(1, e[2], e[2]); CM1(2, z2, z2); CM1(3, zd, zd); CM1(4, e[32], z2); CM1(5, e[33], z2); CM1(6, e[32], zd); CM1(7, e[40], zd);
    SCHED();
    CM2(0, e[3], e[2], w8n); CM2(1, e[4], e[2], e2n); CM2(2, z4, z2, z2n); CM2(3, zq, zd, zdn); CM2(4, e[34], e[32], z2n);
    CM2(5, e[35], e[33], z2n); CM2(6, e[44], e[32], zdn); CM2(7, e[46], e[40], zdn);
    SCHED();
    const f32x2 e4n = cneg(e[4]), z4n = cneg(z4), zqn = cneg(zq);
    CM1(0, e[1], e[4]); CM1(1, e[2], e[4]); CM1(2, e[3], e[4]); CM1(3, e[32], z4); CM1(4, e[33], z4); CM1(5, e[34], z4); CM1(6, e[35], z4); CM1(7, e[32], zq);
    SCHED();
    CM2(0, e[5], e[1], e4n); CM2(1, e[6], e[2], e4n); CM2(2, e[7], e[3], e4n); CM2(3, e[36], e[32], z4n); CM2(4, e[37], e[33], z4n);
    CM2(5, e[38], e[34], z4n); CM2(6, e[39], e[35], z4n); CM2(7, e[48], e[32], zqn);
    SCHED();
    CM1(0, e[34], zc); CM1(1, e[36], zc); CM1(2, e[38], zc); CM1(3, e[36], zd); CM1(4, e[40], zq); CM1(5, e[44], zq); CM1(6, e[46], zq); CM1(7, e[48], zq);
    SCHED();
    CM2(0, e[41], e[34], zcn); CM2(1, e[42], e[36], zcn); CM2(2, e[43], e[38], zcn); CM2(3, e[45], e[36], zdn);
    CM2(4, e[49], e[40], zqn); CM2(5, e[50], e[44], zqn); CM2(6, e[51], e[46], zqn); CM2(7, e[52], e[48], zqn);
    SCHED();
#pragma unroll
    for (int k = 2; k < 8; ++k) CM1(k, e[k], w64);
    CM1(0, e[42], zd); CM1(1, e[9], w64);
    SCHED();
#pragma unroll
    for (int k = 2; k < 8; ++k) CM2(k, e[8 + k], e[k], w64n);
    CM2(0, e[47], e[42], zdn); CM2(1, e[17], e[9], w64n);
    SCHED();
#pragma unroll
    for (int i = 32; i < 53; ++i) eh[i] = pkh(e[i]);
    SCHED();
#pragma unroll
    for (int i = 32; i < 53; ++i) *(LAS u32*)(size_t)(ta[i & 15] + i * 256) = eh[i];
    SCHED();
#pragma unroll
    for (int k = 2; k < 8; ++k) CM1(k, e[8 + k], w64);
    CM1(0, e[16], w64); CM1(1, e[17], w64);
    SCHED();
#pragma unroll
    for (int k = 2; k < 8; ++k) CM2(k, e[16 + k], e[8 + k], w64n);
    CM2(0, e[24], e[16], w64n); CM2(1, e[25], e[17], w64n);
    SCHED();
#pragma unroll
    for (int k = 2; k < 8; ++k) CM1(k, e[16 + k], w64);
    SCHED();
#pragma unroll
    for (int k = 2; k < 8; ++k) CM2(k, e[24 + k], e[16 + k], w64n);
    SCHED();
#pragma unroll
    for (int i = 0; i < 32; ++i) eh[i] = pkh(e[i]);
    SCHED();
#pragma unroll
    for (int i = 0; i < 32; ++i) *(LAS u32*)(size_t)(ta[i & 15] + i * 256) = eh[i];
    WAIT_L0();
    STAMP(4);
    const int j = l & 31, h = l >> 5;
    u32x4 fm[8], fv[8];
#pragma unroll
    for (int s = 0; s < 8; ++s) {
      LAS char* q = tw + j * 256 + (((2 * s + h) ^ (j & 15)) << 4);
      fv[s] = lds128(q); fm[s] = lds128(q + 32 * 256);
    }
    SCHED();
    f32x16 kk;
#pragma unroll
    for (int r = 0; r < 16; ++r) kk[r] = 0.f;
#pragma unroll
    for (int s = 0; s < 8; ++s) kk = mfma32h(fm[s], fv[s], kk);
    LAS float* kc = (LAS float*)(lds + M_KC + w * 2816);
    *(LAS f32x4*)(kc + 8 * j + 4 * h) = f32x4{kk[0], kk[1], kk[2], kk[3]} * sc;
    *(LAS f32x2*)(kc + (h ? 384 + 2 * j : 256 + 4 * j)) = f32x2{kk[4], kk[5]} * sc;
    *(LAS f32x2*)(kc + (h ? 448 + 2 * j : 258 + 4 * j)) = f32x2{kk[6], kk[7]} * sc;
    LAS float* k3 = kc + (h ? 640 + j : 512 + j);
    k3[0] = kk[8] * sc;
    if (h == 0) { k3[32] = kk[9] * sc; k3[64] = kk[10] * sc; k3[96] = kk[11] * sc; }
  }
  STAMP(5);

  const int m2 = xcd * 4 + (rk & 3), n2 = rk >> 2;
  const int rot = 0;
  auto kbo = [&](int k) { const int kk = k + rot; return 14 * w + (kk >= 14 ? kk - 14 : kk); };
  u32x4 wfr[28];
  auto w2ld = [&](int i) { wfr[i] = ld128s(rs, l * 16, WS_W2F + (((n2 * 112 + kbo(i >> 1)) * 2 + (i & 1)) << 10)); };
  u32 p0off;
  {
    const int e = min(9 * w + min(l, 8), 67);
    const int m = e - 60;
    const int pb = e < 32 ? 224 + e : (e < 60 ? 28 * n2 + (e - 32) : (g1 ? ((m >> 1) * 2 + (xcd & 1)) + 8 * ((m & 1) + 4 * (rk >> 2)) : 224));
    p0off = pb * FS;
  }
  const int uch = w >> 2, ui = tid & 255;
  u32 ka_[8];
#pragma unroll
  for (int e = 0; e < 4; ++e) {
    const int l0 = 8 * ui + 2 * e;
    const u32 bits = pk2((float)l0, (float)(l0 + 1));
    const int i0 = ui < 32 ? l0 : (int)(bits & 0xffffu) - (0x4300 - 128);
    const int i1 = ui < 32 ? l0 + 1 : (int)(bits >> 16) - (0x4300 - 128);
    ka_[2 * e] = (u32)(size_t)lds + M_KC + uch * 5632 + i0 * 4;
    ka_[2 * e + 1] = (u32)(size_t)lds + M_KC + uch * 5632 + i1 * 4;
  }
  f32x2 kx[8];
  u32x4 kp;
  auto expand_rd = [&](int e) { LAS float* q = (LAS float*)(size_t)ka_[e]; kx[e] = f32x2{q[0], q[704]}; };
  auto expand_pk = [&](int e) { kp[e] = pk2(kx[2 * e].x + kx[2 * e].y, kx[2 * e + 1].x + kx[2 * e + 1].y); };
  auto expand_wr = [&]() {
    *(LAS u32x4*)(lds + CV_K + uch * 4608 + 512 + ui * 16) = kp;
  };
  auto zero_k = [&]() { if (ui < 32) *(LAS u32x4*)(lds + CV_K + uch * 4608 + ui * 16) = u32x4{0u, 0u, 0u, 0u}; };
  auto zero_u = [&]() { if (ui < 8) *(LAS u32x4*)(lds + CV_U + uch * CV_UCH + (ui >> 1) * CV_CS + 4096 + (ui & 1) * 16) = u32x4{0u, 0u, 0u, 0u}; };
  u32x4 u0;
  u32x2 u1;
  u32 uo1 = ui < 255 ? ui * 16 + 16 : OOB;
  auto load_u = [&](int k) {
    if (k == 0) u0 = ld128s(rs, ui * 16, WS_XNR + (2 * b + uch) * 4096);
    else u1 = ld64s(rs, uo1, WS_XNR + (2 * b + uch) * 4096);
  };
  auto copy_u = [&](int s) {
    const u32 dd[6] = {u0.x, u0.y, u0.z, u0.w, u1.x, u1.y};
    const int o = s >> 1;
    u32x4 v;
#pragma unroll
    for (int k = 0; k < 4; ++k) v[k] = (s & 1) ? __builtin_amdgcn_alignbit(dd[o + k + 1], dd[o + k], 16) : dd[o + k];
    *(LAS u32x4*)(lds + CV_U + uch * CV_UCH + s * CV_CS + ui * 16) = v;
  };

  if (g1) {
    const int t = l & 31, h = l >> 5;
    int xa[4], wa[2];
#pragma unroll
    for (int ks = 0; ks < 4; ++ks) xa[ks] = (64 * wt + t) * 128 + (((2 * ks + h) ^ ((t >> 1) & 7)) << 4);
    {
      const int img = g & 1, q = l15 >> 2, pp = l & 3;
#pragma unroll
      for (int i = 0; i < 2; ++i)
        wa[i] = 16384 * (1 + img) + (8 * h + q) * 256 + (((4 * wf + 2 * i + (pp >> 1)) ^ (4 * q + 2 * img)) << 4) + 8 * (pp & 1);
    }
    f32x16 acc[2][2];
#pragma unroll
    for (int i = 0; i < 2; ++i)
#pragma unroll
      for (int j = 0; j < 2; ++j)
#pragma unroll
        for (int r = 0; r < 16; ++r) acc[i][j][r] = 0.f;
    u32x4 fa[2][2], fb[2][2];
    auto rdA = [&](int st, int ks, int i) {
      LAS char* q = lds + (st % 3) * G1_SLOT + wa[i] + ks * 4096;
      const u32x2 lo = ldstr(q), hi = ldstr(q + 1024);
      fa[ks & 1][i] = u32x4{lo.x, lo.y, hi.x, hi.y};
    };
    auto rdB = [&](int st, int ks, int j) { fb[ks & 1][j] = lds128(lds + (st % 3) * G1_SLOT + xa[ks] + j * 4096); };
    u32x4 sel;
#pragma unroll
    for (int e = 0; e < 4; ++e) sel[e] = l15 == 2 * e ? 0x00003f80u : (l15 == 2 * e + 1 ? 0x3f800000u : 0u);
    u32 a_cw = (u32)(size_t)lds + 16384 * (1 + (w & 1)) + (16 * (w >> 1) + g) * 256 + ((l15 ^ (4 * g + 2 * (w & 1))) << 4);
    u32 a_cq = (u32)(size_t)lds + M_CQ + (l < 32 ? w * 512 + l15 * 32 + g * 16 : 4096 + (l & 31) * 16);
    u32 o_fl = tid == 0 ? 0 : OOB;
    u32 o_cs = (l >> 5) * 512 + (32 * wf + (l & 31)) * 4;
    u32 a_cv = (u32)(size_t)lds + M_CS + w * 256 + h * 16;
    PIN(sel); PIN(a_cw); PIN(a_cq); PIN(o_fl); PIN(o_cs); PIN(a_cv); PIN(p0off); PIN(uo1);
#pragma unroll
    for (int e = 0; e < 8; ++e) PIN(ka_[e]);
#pragma unroll
    for (int e = 0; e < 4; ++e) PIN(xa[e]);
    PIN(wa[0]); PIN(wa[1]);
    const int so_w2 = w >= 4 ? WS_W2F + (((un2 * 112 + ukb) * 2) << 10) : OOB;
    const int so_cs = (w == 1 && (mt & 2) == 0) ? WS_CS + (ft * 8 + kt0) * 1024 : OOB;

    WAIT_V(20);
    LBAR();
    STAMP(6);
    rdA(0, 0, 0); rdB(0, 0, 0); rdB(0, 0, 1); rdA(0, 0, 1);
    dma(2, 0); dma(2, 1);
    u32x2 fl;
    u32 csp[8], msv[2];
    u32x4 cw[4], w2o;
    f32x4 cs, cv[8], cq[4];
#pragma unroll
    for (int st = 0; st < 8; ++st) {
#pragma unroll
      for (int ks = 0; ks < 4; ++ks) {
        const int nst = ks == 3 ? st + 1 : st, nks = (ks + 1) & 3;
        if (ks == 3 && st < 7) {
          if (st == 1) WAIT_V(9);
          else if (st == 3 || st == 4) WAIT_V(7);
          else if (st == 6) WAIT_V(14);
          else WAIT_V(6);
          LBAR();
          if (st == 2) STAMP(18);
          if (st == 5) STAMP(21);
        }
        SCHED();
#pragma unroll
        for (int n = 0; n < 4; ++n) {
          acc[n >> 1][n & 1] = mfma32(fa[ks & 1][n >> 1], fb[ks & 1][n & 1], acc[n >> 1][n & 1]);
          SCHED();
          if (nst < 8) {
            if (n == 0) { rdA(nst, nks, 0); rdB(nst, nks, 0); }
            if (n == 1) rdB(nst, nks, 1);
            if (n == 2) rdA(nst, nks, 1);
          }
          SCHED();
          const int tgt = nst + 2, ph = nks;
          if (tgt < 8) {
            if (n == 3) dma(tgt, ph == 0 ? 1 : ph == 1 ? 2 : ph == 2 ? 4 : 5);
            if (n == 1 && (ph & 1) == 0) dma(tgt, ph == 0 ? 0 : 3);
          }
          const int q = st * 16 + ks * 4 + n;
          if (q < 4) cw[q] = lds128((LAS char*)(size_t)a_cw + q * 1024);
          if (q == 5) cs = mfma16(sel, cw[0], f32x4{0.f, 0.f, 0.f, 0.f});
          if (q >= 6 && q < 9) cs = mfma16(sel, cw[q - 5], cs);
          if (q == 10) *(LAS f32x4*)(size_t)a_cq = cs;
          if (q >= 13 && q < 17) cq[q - 13] = *(LAS f32x4*)(lds + M_CQ + (q - 13) * 1024 + l * 16);
          if (q == 20) { cq[0] += cq[1]; cq[2] += cq[3]; PIN(cq[0]); PIN(cq[2]); }
          if (q == 21) st128s(__builtin_bit_cast(u32x4, cq[0] + cq[2]), rs, l * 16, so_cs);
          if (q == 12) asm volatile("" : "+v"(wr[0]), "+v"(wr[1]), "+v"(wr[2]), "+v"(wr[3]), "+v"(wr[4]), "+v"(wr[5]), "+v"(wr[6]), "+v"(wr[7]));
          if (q == 14 || q == 17) {
            if (q == 17) PIN(wr[0]);
#pragma unroll
            for (int e = 0; e < 4; ++e) w2o[e] = __builtin_amdgcn_perm(wr[2 * e + 1], wr[2 * e], q == 17 ? 0x07060302u : 0x05040100u);
            PIN(w2o);
          }
          if (q == 16) st128s(w2o, rs, l * 16, so_w2);
          if (q == 18) st128s(w2o, rs, l * 16 + 1024, so_w2);
          if (q >= 29 && q < 37) expand_rd(q - 29);
          if (q >= 39 && q < 43) { PIN(kx[2 * (q - 39)]); expand_pk(q - 39); PIN(kp); }
          if (q == 50) expand_wr();
          if (q == 54) zero_k();
          if (q == 46) st64s(tag, rs, o_fl, FL_P0 + b * FS);
          if (q == 75) asm volatile("global_load_dwordx2 %0, %1, %2 sc1" : "=v"(fl) : "v"(p0off), "s"(p.ws + FL_P0) : "memory");
          if (q == 89) {
            asm volatile("s_waitcnt vmcnt(5)" : "+v"(fl) :: "memory");
            if (!__all(fl.x == tag.x && fl.y == tag.y)) poll(rs, p0off, FL_P0, tag);
          }
          if (q == 92 || q == 93) load_u(q - 92);
          if (q >= 94 && q < 102) csp[q - 94] = ld32s(rs, o_cs, WS_CS + (ft * 8 + (q - 94)) * 1024);
          if (q == 102 || q == 103) msv[q - 102] = ld32s(rs, t * 4, WS_ST + (mt * 128 + 64 * wt + 32 * (q - 102)) * 4);
          if (q == 105 || q == 107 || (q > 108 && q % 3 == 0)) w2ld(q == 105 ? 0 : q == 107 ? 1 : (q - 105) / 3);
          if (q == 98) zero_u();
          if (q == 106 || q == 110 || q == 113 || q == 116) copy_u(q == 106 ? 0 : q == 110 ? 1 : q == 113 ? 2 : 3);
          if (q == 112) {
            asm volatile("" : "+v"(csp[0]), "+v"(csp[1]), "+v"(csp[2]), "+v"(csp[3]), "+v"(csp[4]), "+v"(csp[5]), "+v"(csp[6]), "+v"(csp[7]));
#pragma unroll
            for (int i = 0; i < 4; ++i) { csp[i] = __builtin_bit_cast(u32, __builtin_bit_cast(float, csp[i]) + __builtin_bit_cast(float, csp[i + 4])); PIN(csp[i]); }
          }
          if (q == 115) {
            const float c = (__builtin_bit_cast(float, csp[0]) + __builtin_bit_cast(float, csp[1])) + (__builtin_bit_cast(float, csp[2]) + __builtin_bit_cast(float, csp[3]));
            *(LAS float*)(lds + M_CS + w * 256 + l * 4) = c;
          }
          if (q >= 118 && q < 122) {
#pragma unroll
            for (int k = 2 * (q - 118); k < 2 * (q - 118) + 2; ++k)
              cv[k] = *(LAS f32x4*)((LAS char*)(size_t)a_cv + ((k >> 1) & 1) * 128 + (k >> 2) * 64 + (k & 1) * 32);
          }
          if (q >= 124) {
            const int m = (n + 2) & 3, ii = m >> 1, jj = m & 1;
            const float nm = -b2f(msv[jj]);
#pragma unroll
            for (int r = 0; r < 16; ++r) acc[ii][jj][r] = __builtin_fmaf(nm, cv[4 * ii + (r >> 2)][r & 3], acc[ii][jj][r]);
          }
          SCHED();
        }
      }
    }
    STAMP(7);
#pragma unroll
    for (int j = 0; j < 2; ++j) {
      const float sd = bhi(msv[j]), sd2 = sd * sd;
      const float ce = -1.44269504f * __builtin_amdgcn_rcpf(sd);
#pragma unroll
      for (int i = 0; i < 2; ++i) {
        u32x4 o;
#pragma unroll
        for (int r = 0; r < 4; ++r) {
          const f32x2 gg = {acc[i][j][2 * r], acc[i][j][2 * r + 1]};
          const f32x2 uu = {acc[i][j][8 + 2 * r], acc[i][j][9 + 2 * r]};
          const f32x2 a = gg * f32x2{ce, ce};
          const f32x2 e = {__builtin_amdgcn_exp2f(a.x), __builtin_amdgcn_exp2f(a.y)};
          const f32x2 d = fma2(e, f32x2{sd2, sd2}, f32x2{sd2, sd2});
          const f32x2 q = {__builtin_amdgcn_rcpf(d.x), __builtin_amdgcn_rcpf(d.y)};
          o[r] = pk2v((gg * uu) * q);
        }
        st128s(o, rs, l * 16, WS_H + (((mt * 4 + 2 * wt + j) * 112 + ft * 8 + 2 * wf + i) << 10));
        SCHED();
        if (2 * j + i == 0) w2ld(8);
        if (2 * j + i < 3) w2ld(9 + 2 * j + i);
        SCHED();
      }
    }
    STAMP(8);
  } else {
    poll(rs, p0off, FL_P0, tag);
    if (b == 255 && w == 0) {
#pragma unroll 1
      for (int i = 0; i < 4; ++i) poll(rs, (64 * i + l) * FS, FL_P0, tag);
      if (l == 0) st64s(tag, rs, 0, FL_EP);
    }
    LBAR();
    load_u(0); load_u(1);
#pragma unroll
    for (int e = 0; e < 8; ++e) expand_rd(e);
#pragma unroll
    for (int e = 0; e < 4; ++e) expand_pk(e);
    LBAR();
    expand_wr(); zero_k(); zero_u();
#pragma unroll
    for (int s = 0; s < 4; ++s) copy_u(s);
    SCHED();
#pragma unroll
    for (int i = 0; i < 12; ++i) w2ld(i);
    __builtin_amdgcn_s_waitcnt(0x0F70);
  }

  LBAR();
  STAMP(10);
  u32x2 hfl;
  {
    const int ch = w & 1, ts = (w >> 1) & 1, rh = w >> 2;
    LAS char* ua = opaque(lds + CV_U + ch * CV_UCH + (3 - (l15 & 3)) * CV_CS + 4088 - 8 * (l15 >> 2) - 32 * (g >> 1) + 16 * (g & 1)
                          - 7 * 512 - 256 * rh - 3 * 64);
    LAS char* ka = opaque(lds + CV_K + ch * 4608 + 512 + 32 * (l15 - (g >> 1)) + 16 * (g & 1) - 256 * rh - 3 * 64);
    f32x4 acc[4];
#pragma unroll
    for (int i = 0; i < 4; ++i) acc[i] = f32x4{0.f, 0.f, 0.f, 0.f};
    auto hook = [&](int rr, int c) {
      if (rr == 1 && c == 0) {
        WAIT_V(4);
        if (g1 && l == 0) st64s(tag, rs, 0, FL_H + ((mt * 2 + wt) * 56 + ft * 4 + wf) * FS);
      }
      if ((c & 1) == 0) w2ld(12 + 4 * rr + (c >> 1));
    };
    if (ts == 0) conv_tiles<0>(ua, ka, acc, hook); else conv_tiles<1>(ua, ka, acc, hook);
    hfl = ld64s(rs, (l < 7 ? l : 0) * FS, FL_H + (m2 * 56 + 7 * w) * FS);
#pragma unroll
    for (int i = 0; i < 4; ++i) *(LAS f32x4*)(lds + CV_X + (w * 4 + i) * 1024 + l * 16) = acc[i];
  }
  LBAR();
  STAMP(11);
  u32x4 hf[28];
  auto hld = [&](int i) { hf[i] = ld128s(rs, l * 16, WS_H + (((2 * m2 + (i & 1)) * 112 + kbo(i >> 1)) << 10)); };
  int ytile;
  {
    const int ts = (w >> 1) & 1, i = (w & 1) + 2 * (w >> 2);
    ytile = ts ? (i == 0 ? 6 : i == 1 ? 5 : i == 2 ? 2 : 1) : (i == 0 ? 7 : i == 1 ? 4 : i == 2 ? 3 : 0);
    const int pos = 256 * ytile + 16 * l15 + 4 * g;
    f32x4 y0[2], y1[2];
    u32x2 ur[2];
#pragma unroll
    for (int ch = 0; ch < 2; ++ch) {
      y0[ch] = *(LAS f32x4*)(lds + CV_X + ((ch + 2 * ts) * 4 + i) * 1024 + l * 16);
      y1[ch] = *(LAS f32x4*)(lds + CV_X + ((ch + 2 * ts + 4) * 4 + i) * 1024 + l * 16);
      ur[ch] = lds64(lds + CV_U + ch * CV_UCH + (2044 - pos) * 2);
    }
    float res[2][4];
#pragma unroll
    for (int ch = 0; ch < 2; ++ch) {
      const float dsk = ch ? bhi(dk) : b2f(dk);
      const f32x4 y = y0[ch] + y1[ch];
      res[ch][0] = __builtin_fmaf(bhi(ur[ch].y), dsk, y[0]); res[ch][1] = __builtin_fmaf(b2f(ur[ch].y), dsk, y[1]);
      res[ch][2] = __builtin_fmaf(bhi(ur[ch].x), dsk, y[2]); res[ch][3] = __builtin_fmaf(b2f(ur[ch].x), dsk, y[3]);
    }
    const u32x4 o = {pk2(res[0][0], res[1][0]), pk2(res[0][1], res[1][1]), pk2(res[0][2], res[1][2]), pk2(res[0][3], res[1][3])};
    st128s(o, rs, pos * 4, WS_Y + b * 8192);
    if (!__all(hfl.x == tag.x && hfl.y == tag.y)) poll(rs, (l < 7 ? l : 0) * FS, FL_H + (m2 * 56 + 7 * w) * FS, tag);
    asm volatile("" ::: "memory");
  }
  STAMP(12);

  {
#define PDV 6
    constexpr int PD = PDV;
#pragma unroll
    for (int i = 0; i < PD; ++i) hld(i);
    const int oj = w >> 2, oq = w & 3;
    const int etok = 64 * m2 + 32 * oj + (l & 31), ech = 64 * n2 + 16 * oq + 8 * (l >> 5);
    const u32x4 xv = *(const GAS u32x4*)((const char*)p.x + (size_t)etok * 1024 + ech * 2);
    BAR();
    SCHED();
    f32x16 acc[2][2];
#pragma unroll
    for (int i = 0; i < 2; ++i)
#pragma unroll
      for (int j = 0; j < 2; ++j)
#pragma unroll
        for (int r = 0; r < 16; ++r) acc[i][j][r] = 0.f;
    u32x2 yfl;
    const int yfo = FL_Y + ((m2 >> 2) * 256 + 32 * n2 + 8 * oq) * FS;
#pragma unroll
    for (int n = 0; n < 28; ++n) {
#pragma unroll
      for (int i = 0; i < 2; ++i) acc[i][n & 1] = mfma32(wfr[(n & ~1) + i], hf[n], acc[i][n & 1]);
      SCHED();
      if (n + PD < 28) hld(n + PD);
      if (n == 4) {
        WAIT_V(5);
        if (l == 0) st64s(tag, rs, 0, FL_Y + (ytile * 256 + b) * FS);
      }
      if (n == 22) yfl = ld64s(rs, (l & 7) * FS, yfo);
      SCHED();
    }
    STAMP(13);
    if (!__all(yfl.x == tag.x && yfl.y == tag.y)) poll(rs, (l & 7) * FS, yfo, tag);
    asm volatile("" ::: "memory");
    u32 yv[4];
#pragma unroll
    for (int j = 0; j < 4; ++j) yv[j] = ld32s(rs, ((ech >> 1) + j) * 8192 + etok * 4, WS_Y);
    {
      LAS char* pw = lds + w * 16384 + l * 16;
#pragma unroll
      for (int j = 0; j < 2; ++j)
#pragma unroll
        for (int qd = 0; qd < 4; ++qd)
#pragma unroll
          for (int i = 0; i < 2; ++i)
            *(LAS f32x4*)(pw + ((j * 4 + qd) * 2 + i) * 1024) =
                f32x4{acc[i][j][4 * qd], acc[i][j][4 * qd + 1], acc[i][j][4 * qd + 2], acc[i][j][4 * qd + 3]};
    }
    LBAR();
    STAMP(14);
    LAS char* pr = opaque(lds + w * 2048 + l * 16);
    LAS char* pr2 = opaque(pr + 65536);
    f32x4 pp[8][2];
#pragma unroll
    for (int ww = 0; ww < 8; ++ww)
#pragma unroll
      for (int i = 0; i < 2; ++i) pp[ww][i] = *(LAS f32x4*)((ww < 4 ? pr : pr2) + (ww & 3) * 16384 + i * 1024);
    SCHED();
    f32x4 sm[2];
#pragma unroll
    for (int i = 0; i < 2; ++i) sm[i] = ((pp[0][i] + pp[1][i]) + (pp[2][i] + pp[3][i])) + ((pp[4][i] + pp[5][i]) + (pp[6][i] + pp[7][i]));
    u32x4 r;
#pragma unroll
    for (int j = 0; j < 4; ++j) {
      const u32 s = pk2v(up2(xv[j]) + up2(yv[j]));
      r[j] = pk2v(up2(s) + up2(pk2(sm[0][j], sm[1][j])));
    }
    *(GAS u32x4*)((char*)p.out + (size_t)etok * 1024 + ech * 2) = r;
  }
  STAMP(15);
}

extern "C" void kernel_launch(void* const* d_in, const int* in_sizes, int n_in, void* d_out, int out_size, void* d_ws,
                              size_t ws_size, hipStream_t stream) {
  Params p;
  p.x = (const u16*)d_in[0]; p.lar = (const u16*)d_in[1]; p.aim = (const u16*)d_in[2]; p.cre = (const u16*)d_in[3];
  p.cim = (const u16*)d_in[4]; p.ldt = (const u16*)d_in[5]; p.dsk = (const u16*)d_in[6];
  p.w1 = (const u16*)d_in[8]; p.w3 = (const u16*)d_in[9]; p.w2 = (const u16*)d_in[10];
  p.out = (u16*)d_out; p.ws = (char*)d_ws;
  fused<<<256, 512, 0, stream>>>(p);
}
```

```cpp
#include <hip/hip_runtime.h>
#include <hip/hip_bf16.h>

typedef unsigned short u16;
typedef unsigned int u32;
typedef unsigned long long u64;
typedef __bf16 bf16x8 __attribute__((ext_vector_type(8)));
typedef __bf16 bf16x2 __attribute__((ext_vector_type(2)));
typedef _Float16 f16x8 __attribute__((ext_vector_type(8)));
typedef _Float16 f16x2 __attribute__((ext_vector_type(2)));
typedef short s16x4 __attribute__((ext_vector_type(4)));
typedef float f32x2 __attribute__((ext_vector_type(2)));
typedef float f32x4 __attribute__((ext_vector_type(4)));
typedef float f32x16 __attribute__((ext_vector_type(16)));
typedef u32 u32x2 __attribute__((ext_vector_type(2)));
typedef u32 u32x4 __attribute__((ext_vector_type(4)));
typedef __amdgpu_buffer_rsrc_t rsrc_t;
#define LAS __attribute__((address_space(3)))
#define GAS __attribute__((address_space(1)))
#define DEV __device__ __forceinline__

#define QW 104
#define QZ 54
#define QF 46
#define YP1 1
#define YP0 10
constexpr int L = 2048, D = 512, NM = 128, F = 1792;
constexpr int LDS_BYTES = 163840;
constexpr int SC1 = 16;

constexpr int WS_H = 0;
constexpr int WS_XNR = 8 << 20;
constexpr int WS_W2F = 10 << 20;
constexpr int WS_Y = 12 << 20;
constexpr int WS_ST = 14 << 20;
constexpr int WS_CS = WS_ST + 8192;
constexpr int WS_FL = 15 << 20;
constexpr int FS = 128;
constexpr int FL_EP = WS_FL;
constexpr int FL_P0 = WS_FL + 4096;
constexpr int FL_H = FL_P0 + 256 * FS;
constexpr int FL_Y = FL_H + 2048 * FS;
constexpr int WS_BYTES = 16 << 20;

struct Params {
  const u16 *x, *lar, *aim, *cre, *cim, *ldt, *dsk, *w1, *w3, *w2;
  u16* out;
  char* ws;
};

template <int V> struct IC { static constexpr int v = V; };
template <int N, int I = 0, class Fn>
DEV void static_for(Fn&& fn) { if constexpr (I < N) { fn(IC<I>{}); static_for<N, I + 1>(fn); } }
DEV u32 f2u(float f) { return __builtin_bit_cast(u32, f); }
DEV float u2f(u32 v) { return __builtin_bit_cast(float, v); }
DEV float b2f(u32 v) { return __builtin_bit_cast(float, v << 16); }
DEV float bhi(u32 v) { return __builtin_bit_cast(float, v & 0xffff0000u); }
DEV u32 pk2v(f32x2 f) { return __builtin_bit_cast(u32, __builtin_convertvector(f, bf16x2)); }
DEV u32 pk2(float lo, float hi) { return pk2v(f32x2{lo, hi}); }
DEV f32x2 up2(u32 v) { return f32x2{b2f(v), bhi(v)}; }
DEV float rb(float f) { return b2f(pk2(f, f) & 0xffffu); }
DEV f32x2 fma2(f32x2 a, f32x2 b, f32x2 c) { return __builtin_elementwise_fma(a, b, c); }
DEV f32x16 mfma32(u32x4 a, u32x4 b, f32x16 c) {
  return __builtin_amdgcn_mfma_f32_32x32x16_bf16(__builtin_bit_cast(bf16x8, a), __builtin_bit_cast(bf16x8, b), c, 0, 0, 0);
}
DEV f32x4 mfma16(u32x4 a, u32x4 b, f32x4 c) {
  return __builtin_amdgcn_mfma_f32_16x16x32_bf16(__builtin_bit_cast(bf16x8, a), __builtin_bit_cast(bf16x8, b), c, 0, 0, 0);
}
DEV f32x16 mfma32h(u32x4 a, u32x4 b, f32x16 c) {
  return __builtin_amdgcn_mfma_f32_32x32x16_f16(__builtin_bit_cast(f16x8, a), __builtin_bit_cast(f16x8, b), c, 0, 0, 0);
}
DEV u32x4 lds128(LAS char* p) { return *(LAS u32x4*)p; }
DEV u32x2 lds64(LAS char* p) { return *(LAS u32x2*)p; }
DEV u32x2 ldstr(LAS char* p) { return __builtin_bit_cast(u32x2, __builtin_amdgcn_ds_read_tr16_b64_v4i16((LAS s16x4*)p)); }
template <int LOFF>
DEV void dma_m0(u32 lbase) { asm volatile("s_add_u32 m0, %0, %1" :: "s"(lbase), "i"(LOFF) : "memory", "scc", "m0"); }
template <bool NT>
DEV void dma_go(u32x4 rs, u32 voff, u32 soff) {
  if (NT) asm volatile("buffer_load_dwordx4 %0, %1, %2 offen nt lds" :: "v"(voff), "s"(rs), "s"(soff) : "memory");
  else asm volatile("buffer_load_dwordx4 %0, %1, %2 offen lds" :: "v"(voff), "s"(rs), "s"(soff) : "memory");
}
DEV u32x4 mkrs(const void* ptr, u32 bytes) { const u64 a = (u64)ptr; return u32x4{(u32)a, (u32)(a >> 32) & 0xffffu, bytes, 0x00020000u}; }
#define WAIT_V(n) asm volatile("s_waitcnt vmcnt(" #n ")" ::: "memory")
#define WAIT_L0() asm volatile("s_waitcnt lgkmcnt(0)" ::: "memory")
#define BAR() __builtin_amdgcn_s_barrier()
#define LBAR() do { WAIT_L0(); BAR(); asm volatile("" ::: "memory"); } while (0)
#define SCHED() __builtin_amdgcn_sched_barrier(0)
#define PIN(x) asm volatile("" : "+v"(x))
constexpr int OOB = 0x04000000;
#define STAMP(i)
#define STAMP_DECL
#define STAMP_DUMP()

DEV u32x4 ld128s(rsrc_t rs, int voff, int soff) { return __builtin_amdgcn_raw_buffer_load_b128(rs, voff, soff, SC1); }
DEV u32x2 ld64s(rsrc_t rs, int voff, int soff) { return __builtin_amdgcn_raw_buffer_load_b64(rs, voff, soff, SC1); }
DEV u32 ld32s(rsrc_t rs, int voff, int soff) { return __builtin_amdgcn_raw_buffer_load_b32(rs, voff, soff, SC1); }
DEV void st128s(u32x4 v, rsrc_t rs, int voff, int soff) {
  __builtin_amdgcn_raw_buffer_store_b128(v, rs, voff, soff, SC1);
  asm volatile("s_nop 1" :: "v"(v) : "memory");
}
DEV void st64s(u32x2 v, rsrc_t rs, int voff, int soff) { __builtin_amdgcn_raw_buffer_store_b64(v, rs, voff, soff, SC1); }
DEV void st32s(u32 v, rsrc_t rs, int voff, int soff) { __builtin_amdgcn_raw_buffer_store_b32(v, rs, voff, soff, SC1); }
DEV void poll(rsrc_t rs, int voff, int soff, u32x2 tag) {
#pragma unroll 1
  for (int spin = 0; spin < (1 << 18); ++spin) {
    const u32x2 v = ld64s(rs, voff, soff);
    if (__all(v.x == tag.x && v.y == tag.y)) break;
    __builtin_amdgcn_s_sleep(4);
  }
  __builtin_amdgcn_fence(__ATOMIC_ACQUIRE, "wavefront");
  asm volatile("" ::: "memory");
}

template <int CTRL, int RM>
DEV u32 dppu(u32 v) { return (u32)__builtin_amdgcn_update_dpp(0, (int)v, CTRL, RM, 0xf, true); }
DEV u32 wave_max_u32(u32 v) {
  v = max(v, dppu<0x111, 0xf>(v)); v = max(v, dppu<0x112, 0xf>(v)); v = max(v, dppu<0x114, 0xf>(v)); v = max(v, dppu<0x118, 0xf>(v));
  v = max(v, dppu<0x142, 0xa>(v)); v = max(v, dppu<0x143, 0xc>(v));
  return (u32)__builtin_amdgcn_readlane((int)v, 63);
}

DEV float rdl(float v, int lane) { return __builtin_bit_cast(float, __builtin_amdgcn_readlane(__builtin_bit_cast(int, v), lane)); }
template <int CTRL>
DEV float dppx(float v) { return __builtin_bit_cast(float, __builtin_amdgcn_mov_dpp(__builtin_bit_cast(int, v), CTRL, 0xf, 0xf, true)); }
DEV float rows8_sum(const float (&p)[8], int l) {
  float a[4], c[2];
#pragma unroll
  for (int i = 0; i < 4; ++i) {
    const auto r = __builtin_amdgcn_permlane32_swap(__builtin_bit_cast(u32, p[i]), __builtin_bit_cast(u32, p[i + 4]), false, false);
    a[i] = __builtin_bit_cast(float, (u32)r[0]) + __builtin_bit_cast(float, (u32)r[1]);
  }
#pragma unroll
  for (int i = 0; i < 2; ++i) {
    const auto r = __builtin_amdgcn_permlane16_swap(__builtin_bit_cast(u32, a[i]), __builtin_bit_cast(u32, a[i + 2]), false, false);
    c[i] = __builtin_bit_cast(float, (u32)r[0]) + __builtin_bit_cast(float, (u32)r[1]);
  }
  const bool up = l & 8;
  float v = (up ? c[1] : c[0]) + dppx<0x128>(up ? c[0] : c[1]);
  v += dppx<0xB1>(v); v += dppx<0x4E>(v); v += dppx<0x141>(v);
  return v;
}

constexpr int G1_SLOT = 49152;
constexpr int KG_T = 2 * G1_SLOT;
constexpr int KG_TW = 53 * 256;
constexpr int SPARE = 3 * G1_SLOT;
constexpr int M_CS = SPARE;
constexpr int M_KC = SPARE + 5120;
constexpr int CV_K = SPARE + 5120;
static_assert(KG_T + 4 * KG_TW <= M_KC && M_KC + 4 * 2816 <= LDS_BYTES, "lds");
constexpr int CV_CS = 4160;
constexpr int CV_U = 2 * G1_SLOT;
constexpr int CV_UCH = 4 * CV_CS;
constexpr int CV_X = 0;

DEV f32x2 cmul(f32x2 a, f32x2 b, f32x2 bn) { return fma2(f32x2{a.y, a.x}, bn, a * f32x2{b.x, b.x}); }
DEV f32x2 cneg(f32x2 b) { return f32x2{-b.y, b.y}; }
DEV f32x2 cexp_p(float lre2, float rev, float pw) {
  const float mg = __builtin_amdgcn_exp2f(lre2 * pw);
  const float ph = __builtin_amdgcn_fractf(rev * pw);
  return f32x2{__builtin_amdgcn_cosf(ph), __builtin_amdgcn_sinf(ph)} * f32x2{mg, mg};
}
DEV u32 pkh(f32x2 f) { return __builtin_bit_cast(u32, __builtin_convertvector(f, f16x2)); }
DEV LAS char* opaque(LAS char* q) { u32 v = (u32)(size_t)q; asm volatile("" : "+v"(v)); return (LAS char*)(size_t)v; }

template <class Hook>
DEV void conv_tiles(LAS char* ua, LAS char* ka, f32x4 (&acc)[4], Hook hook) {
  constexpr int ORD[16] = {0, 15, 13, 11, 9, 1, 14, 12, 10, 8, 2, 3, 4, 5, 6, 7};
  u32x4 fr[2][16];
  auto load = [&](int rr, int id) {
    if (id < 8) {
      const u32x2 lo = *(volatile LAS u32x2*)(ua + (7 - id) * 512 + (3 - rr) * 64), hi = *(volatile LAS u32x2*)(ua + (7 - id) * 512 + (3 - rr) * 64 + 8);
      fr[rr & 1][id] = u32x4{lo.x, lo.y, hi.x, hi.y};
    } else {
      fr[rr & 1][id] = lds128(ka + (id - 8) * 512 + (3 - rr) * 64);
    }
  };
#pragma unroll
  for (int k = 0; k < 16; ++k) { load(0, ORD[k]); SCHED(); }
  hook(0, -1);
  SCHED();
#pragma unroll
  for (int rr = 0; rr < 4; ++rr) {
#pragma unroll
    for (int c = 0; c < 8; ++c) {
      u32x4(&f)[16] = fr[rr & 1];
#pragma unroll
      for (int i = 0; i < 4; ++i) {
        if (c <= 7 - 2 * i) { acc[i] = mfma16(f[c], f[8 + 7 - 2 * i - c], acc[i]); SCHED(); }
        if (rr < 3 && i < 2) { load(rr + 1, ORD[2 * c + i]); SCHED(); }
      }
      hook(rr, c);
      SCHED();
    }
  }
}

__global__ __launch_bounds__(512, 2) void fused(Params p) {
  __shared__ __attribute__((aligned(16))) char smem[LDS_BYTES];
  asm volatile("" : "+s"(p.x), "+s"(p.lar), "+s"(p.aim), "+s"(p.cre), "+s"(p.cim), "+s"(p.ldt), "+s"(p.dsk), "+s"(p.w1), "+s"(p.w3),
               "+s"(p.w2), "+s"(p.out), "+s"(p.ws));
  LAS char* lds = (LAS char*)smem;
  const u32 lds0 = (u32)(size_t)lds;
  const int tid = threadIdx.x, l = tid & 63;
  const int w = __builtin_amdgcn_readfirstlane(tid >> 6);
  const int b = blockIdx.x, xcd = b & 7, rk = b >> 3;
  const bool g1 = rk < 28;
  const rsrc_t rs = __builtin_amdgcn_make_buffer_rsrc(p.ws, 0, WS_BYTES, 0x00020000);
  const int l15 = l & 15, g = l >> 4, t = l & 31, h = l >> 5;
  STAMP_DECL;
  STAMP(0);

  u32x2 tag;
  u32 p_ldt2, dk;
  asm volatile("s_load_dwordx2 %0, %3, 0x0\n\ts_load_dword %1, %4, 0x0\n\ts_load_dword %2, %5, 0x0"
               : "=&s"(tag), "=&s"(p_ldt2), "=&s"(dk) : "s"(p.ws + FL_EP), "s"(p.ldt + 2 * b), "s"(p.dsk + 2 * b) : "memory");

#define CODEPF 21000
  u32x4 pf;
  if (w == 4) {
    u64 pc;
    asm volatile("s_getpc_b64 %0" : "=s"(pc));
    const u32 po = min(rk * 1024 + l * 16, CODEPF - 16);
    asm volatile("global_load_dwordx4 %0, %1, %2" : "=v"(pf) : "v"(po), "s"(pc & ~(u64)255) : "memory");
  }
  u32 p_lar, p_aim, p_cre, p_cim;
  if (w < 4) {
    const u32 po = (b * 256 + tid) * 2;
    asm volatile("global_load_ushort %0, %1, %2" : "=v"(p_lar) : "v"(po), "s"(p.lar) : "memory");
    asm volatile("global_load_ushort %0, %1, %2" : "=v"(p_aim) : "v"(po), "s"(p.aim) : "memory");
    asm volatile("global_load_ushort %0, %1, %2" : "=v"(p_cre) : "v"(po), "s"(p.cre) : "memory");
    asm volatile("global_load_ushort %0, %1, %2" : "=v"(p_cim) : "v"(po), "s"(p.cim) : "memory");
  }
  SCHED();

  const int mt = (xcd >> 1) * 4 + (rk & 3), ft = (xcd & 1) * 7 + (rk >> 2);
#define ROTV 5
  const int kt0 = ROTV == 0 ? 0 : ROTV == 2 ? (2 * (xcd >> 1) + 4 * (xcd & 1)) & 7 : (2 * (xcd >> 1) + 4 * (xcd & 1) + (mt & 1)) & 7;
  const int wt = w & 1, wf = w >> 1;
  const u32x4 rx = mkrs(p.x, L * D * 2), rw1 = mkrs(p.w1, D * F * 2), rw3 = mkrs(p.w3, D * F * 2);
  const u32 xo0 = (l >> 3) * 1024 + (((l & 7) ^ g) << 4);
  const u32 xo1 = (8 + (l >> 3)) * 1024 + (((l & 7) ^ (4 + g)) << 4);
  const u32 wo1 = g * (F * 2) + ((l15 ^ (4 * g)) << 4), wo1b = wo1 + 4 * F * 2;
  const u32 wo3 = g * (F * 2) + ((l15 ^ (4 * g + 2)) << 4), wo3b = wo3 + 4 * F * 2;
#define NTS 0
  auto dm0 = [&](auto st_, int piece, int ww) {
    constexpr int S = (decltype(st_)::v % 3) * G1_SLOT;
    const u32 lb = lds0 + ww * 2048;
    if (piece == 0) dma_m0<S>(lb);
    if (piece == 1) dma_m0<S + 16384>(lb);
    if (piece == 2) dma_m0<S + 32768>(lb);
    if (piece == 3) dma_m0<S + 1024>(lb);
    if (piece == 4) dma_m0<S + 16384 + 1024>(lb);
    if (piece == 5) dma_m0<S + 32768 + 1024>(lb);
  };
  auto dgo = [&](auto st_, int piece, int ww) {
    constexpr bool nt = decltype(st_)::v < NTS;
    const int kt = (kt0 + decltype(st_)::v) & 7;
    const u32 sx = (mt * 128 + ww * 16) * 1024 + kt * 128, sw = (kt * 64 + ww * 8) * (F * 2) + ft * 256;
    if (piece == 0) dma_go<nt>(rx, xo0, sx);
    if (piece == 1) dma_go<nt>(rw1, wo1, sw);
    if (piece == 2) dma_go<nt>(rw3, wo3, sw);
    if (piece == 3) dma_go<nt>(rx, xo1, sx);
    if (piece == 4) dma_go<nt>(rw1, wo1b, sw);
    if (piece == 5) dma_go<nt>(rw3, wo3b, sw);
  };
  auto dmaw = [&](auto st_, int piece, int ww) { dm0(st_, piece, ww); asm volatile("s_nop 0"); dgo(st_, piece, ww); };

  u32 wr[8];
  u32x4 xr[8];
  const int un2 = b / 28, ukb = 4 * (b - 28 * un2) + (w & 3);
  const int ax = xcd * 4 + (rk - 28);
  if (w >= 4 && g1) {
#pragma unroll
    for (int i = 0; i < 6; ++i) { dmaw(IC<0>{}, i, w - 4); dmaw(IC<0>{}, i, w); }
    const char* wb2 = (const char*)p.w2 + (size_t)(16 * ukb) * 1024 + un2 * 128;
    const u32 wv = h * 4096 + t * 4;
#define W2RAW(j, base, off) asm volatile("global_load_dword %0, %1, %2 offset:" #off : "=v"(wr[j]) : "v"(wv), "s"(base) : "memory")
    W2RAW(0, wb2, 0); W2RAW(1, wb2, 1024); W2RAW(2, wb2, 2048); W2RAW(3, wb2, 3072);
    W2RAW(4, wb2 + 8192, 0); W2RAW(5, wb2 + 8192, 1024); W2RAW(6, wb2 + 8192, 2048); W2RAW(7, wb2 + 8192, 3072);
  }
  if (!g1) {
    const u32 xv_ = (64 * ax + 8 * w) * 1024 + l * 16;
#define XRAW(j, vo, off) asm volatile("global_load_dwordx4 %0, %1, %2 offset:" #off : "=v"(xr[j]) : "v"(vo), "s"(p.x) : "memory")
    XRAW(0, xv_, 0); XRAW(1, xv_, 1024); XRAW(2, xv_, 2048); XRAW(3, xv_, 3072);
    XRAW(4, xv_ + 4096, 0); XRAW(5, xv_ + 4096, 1024); XRAW(6, xv_ + 4096, 2048); XRAW(7, xv_ + 4096, 3072);
  }
  SCHED();

  const int m2 = xcd * 4 + (rk & 3), n2 = rk >> 2;
  u32 p0off;
  {
    const int e = min(9 * w + min(l, 8), 67);
    const int m = e - 60;
    const int pb = e < 32 ? 224 + e : (e < 60 ? 28 * n2 + (e - 32) : (g1 ? ((m >> 1) * 2 + (xcd & 1)) + 8 * ((m & 1) + 4 * (rk >> 2)) : 224));
    p0off = pb * FS;
  }
  const int uch = w >> 2, ui = tid & 255;
  u32 ka_[8];
#pragma unroll
  for (int e = 0; e < 4; ++e) {
    const int l0 = 8 * ui + 2 * e;
    const u32 bits = pk2((float)l0, (float)(l0 + 1));
    const int i0 = ui < 32 ? l0 : (int)(bits & 0xffffu) - (0x4300 - 128);
    const int i1 = ui < 32 ? l0 + 1 : (int)(bits >> 16) - (0x4300 - 128);
    ka_[2 * e] = lds0 + M_KC + uch * 5632 + i0 * 4;
    ka_[2 * e + 1] = lds0 + M_KC + uch * 5632 + i1 * 4;
  }
  u32 uo1 = ui < 255 ? ui * 16 + 16 : OOB;
  int xa[4], wa[2];
#pragma unroll
  for (int ks = 0; ks < 4; ++ks) xa[ks] = (64 * wt + t) * 128 + (((2 * ks + h) ^ ((t >> 1) & 7)) << 4);
  {
    const int img = g & 1, q = l15 >> 2, pp = l & 3;
#pragma unroll
    for (int i = 0; i < 2; ++i)
      wa[i] = 16384 * (1 + img) + (8 * h + q) * 256 + (((4 * wf + 2 * i + (pp >> 1)) ^ (4 * q + 2 * img)) << 4) + 8 * (pp & 1);
  }
  u32x4 sel;
#pragma unroll
  for (int e = 0; e < 4; ++e) sel[e] = l15 == 2 * e ? 0x00003f80u : (l15 == 2 * e + 1 ? 0x3f800000u : 0u);
  u32 a_cw = lds0 + 16384 * (1 + (w >> 2)) + (4 * g + (l15 >> 2)) * 256 + (((4 * (w & 3) + (l & 3)) ^ (4 * (l15 >> 2) + 2 * (w >> 2))) << 4);
  u32 o_cq = (l < 32 && l15 >= 12) ? (l & 3) * 32 + g * 16 : OOB;
  u32 o_l0 = l == 0 ? 0 : OOB;

  u32 o_cs = g * 1024 + (l15 >> 3) * 512 + (32 * wf + 4 * (l15 & 7)) * 4;
  u32 a_cs = lds0 + M_CS + w * 256 + (4 * l15 + 2 * h + (g & 1)) * 4;
  PIN(a_cs);
  u32 a_cv = lds0 + M_CS + w * 256 + h * 16;
  PIN(sel); PIN(a_cw); PIN(o_cq); PIN(o_l0); PIN(o_cs); PIN(a_cv); PIN(p0off); PIN(uo1);
#pragma unroll
  for (int e = 0; e < 8; ++e) PIN(ka_[e]);
#pragma unroll
  for (int e = 0; e < 4; ++e) PIN(xa[e]);
  int wa1[2];
#pragma unroll
  for (int e = 0; e < 2; ++e) { wa1[e] = wa[e] + G1_SLOT; PIN(wa1[e]); }
  int xa2[4], wa2[2];
#pragma unroll
  for (int e = 0; e < 4; ++e) { xa2[e] = xa[e] + 2 * G1_SLOT; PIN(xa2[e]); }
#pragma unroll
  for (int e = 0; e < 2; ++e) { wa2[e] = wa[e] + 2 * G1_SLOT - 16384; wa[e] -= 16384; PIN(wa2[e]); PIN(wa[e]); }
  SCHED();
  if (g1 && w >= 4) {
#pragma unroll
    for (int i = 0; i < 6; ++i) { dmaw(IC<1>{}, i, w - 4); dmaw(IC<1>{}, i, w); }
    asm volatile("s_waitcnt vmcnt(12)" : "+v"(wr[0]), "+v"(wr[1]), "+v"(wr[2]), "+v"(wr[3]), "+v"(wr[4]), "+v"(wr[5]), "+v"(wr[6]), "+v"(wr[7]) :: "memory");
    PIN(pf);
#pragma unroll
    for (int c = 0; c < 2; ++c) {
      u32x4 o;
#pragma unroll
      for (int e = 0; e < 4; ++e) o[e] = __builtin_amdgcn_perm(wr[2 * e + 1], wr[2 * e], c ? 0x07060302u : 0x05040100u);
      st128s(o, rs, l * 16 + c * 1024, WS_W2F + (((un2 * 112 + ukb) * 2) << 10));
    }
  }
  asm volatile("s_waitcnt lgkmcnt(0)" : "+s"(tag), "+s"(p_ldt2), "+s"(dk) :: "memory");
  tag.x += 1u;

  if (!g1) {
    asm volatile("s_waitcnt vmcnt(0)" : "+v"(xr[0]), "+v"(xr[1]), "+v"(xr[2]), "+v"(xr[3]), "+v"(xr[4]), "+v"(xr[5]), "+v"(xr[6]), "+v"(xr[7]) :: "memory");
    PIN(pf);
    f32x2 v[8][4];
    float ps[8];
#pragma unroll
    for (int r = 0; r < 8; ++r) {
#pragma unroll
      for (int j = 0; j < 4; ++j) v[r][j] = up2(xr[r][j]);
      const f32x2 s2 = (v[r][0] + v[r][1]) + (v[r][2] + v[r][3]);
      ps[r] = s2.x + s2.y;
    }
    const float mean = rows8_sum(ps, l) * (1.f / D);
#pragma unroll
    for (int r = 0; r < 8; ++r) {
      const float mr = rdl(mean, 8 * r);
      f32x2 q2 = {0.f, 0.f};
#pragma unroll
      for (int j = 0; j < 4; ++j) { v[r][j] -= f32x2{mr, mr}; q2 = fma2(v[r][j], v[r][j], q2); }
      ps[r] = q2.x + q2.y;
    }
    const float var = rows8_sum(ps, l) * (1.f / D);
    const u32 ms = pk2(mean, __builtin_sqrtf(rb(rb(var) + 1e-5f)));
    if ((l & 7) == 0) st32s(ms, rs, (l >> 3) * 4, WS_ST + (64 * ax + 8 * w) * 4);
    const float riv = 1.f / bhi(ms), nbv = (mean - b2f(ms)) * riv;
#pragma unroll
    for (int r = 0; r < 8; ++r) {
      const float ri = rdl(riv, 8 * r), nb = rdl(nbv, 8 * r);
#pragma unroll
      for (int j = 0; j < 4; ++j) v[r][j] = fma2(v[r][j], f32x2{ri, ri}, f32x2{nb, nb});
    }
#pragma unroll
    for (int c = 0; c < 8; ++c) {
      u32x4 o;
#pragma unroll
      for (int e = 0; e < 4; ++e) o[e] = pk2(v[7 - 2 * e][c >> 1][c & 1], v[6 - 2 * e][c >> 1][c & 1]);
      *(LAS u32x4*)(lds + (8 * l + c) * 128 + (((7 - w) ^ (l & 7)) << 4)) = o;
    }
    LBAR();
    u32x4 tv[8];
#pragma unroll
    for (int it = 0; it < 8; ++it) tv[it] = lds128(lds + w * 8192 + it * 1024 + l * 16);
#pragma unroll
    for (int it = 0; it < 8; ++it)
      st128s(tv[it], rs, (8 * it + (l >> 3)) * 4096 + (((l & 7) ^ it) << 4), WS_XNR + w * (64 * 4096) + (L - 64 - 64 * ax) * 2);
    WAIT_V(0);
    LBAR();
    if (tid == 0) st64s(tag, rs, 0, FL_P0 + b * FS);
    STAMP(3);
  }

  if (w < 4) {
    LAS char* tw = lds + KG_T + w * KG_TW;
    u32 ta[16];
#pragma unroll
    for (int k = 0; k < 16; ++k) { ta[k] = ((u32)(size_t)tw + l * 4) ^ (k << 4); PIN(ta[k]); }
    float dt = rb(__expf((w >> 1) ? bhi(p_ldt2) : b2f(p_ldt2)));
    PIN(dt);
    asm volatile("s_waitcnt vmcnt(0)" : "+v"(p_lar), "+v"(p_aim), "+v"(p_cre), "+v"(p_cim) :: "memory");
    STAMP(1);
    const float ar = -rb(__expf(b2f(p_lar)));
    const float lre2 = (ar * dt) * 1.44269504f, rev = (b2f(p_aim) * dt) * 0.15915494309f;
    u32 eb = wave_max_u32(max(p_cre & 0x7f80u, p_cim & 0x7f80u)) << 16;
    eb = eb < 0x00800000u ? 0x00800000u : (eb > 0x7e800000u ? 0x7e800000u : eb);
    const float sc = __builtin_bit_cast(float, eb) * (2.f * dt), isc = __builtin_bit_cast(float, 0x7f000000u - eb);
    f32x2 e[53], tmp[8];
    u32 eh[53];
    const f32x2 w8 = cexp_p(lre2, rev, 8.f), w64 = cexp_p(lre2, rev, 64.f);
    const f32x2 z1 = cexp_p(lre2, -rev, 1.f), zc = cexp_p(lre2, -rev, 256.f);
    e[0] = f32x2{1.f, 0.f}; e[1] = w8; e[8] = w64; e[32] = f32x2{b2f(p_cre) * isc, -b2f(p_cim) * isc};
#define CM1(k, a, bb) tmp[k] = (a) * f32x2{(bb).x, (bb).x}; SCHED()
#define CM2(k, d, a, bn) d = fma2(f32x2{(a).y, (a).x}, bn, tmp[k]); SCHED()
#define CV(i) eh[i] = pkh(e[i]); SCHED()
#define WR(i) *(LAS u32*)(size_t)(ta[(i) & 15] + (i) * 256) = eh[i]; SCHED()
    const f32x2 w8n = cneg(w8), w64n = cneg(w64), z1n = cneg(z1), zcn = cneg(zc);
    f32x2 z2, z4, zd, zq;
    SCHED();
    CM1(0, w8, w8); CM1(1, z1, z1); CM1(2, zc, zc); CM1(3, e[32], z1); CM1(4, e[32], zc); CM1(5, w8, w64); CM1(6, w64, w64);
    CV(0); CV(1); CV(8); CV(32);
    CM2(0, e[2], w8, w8n); WR(0); CM2(1, z2, z1, z1n); CM2(2, zd, zc, zcn); WR(1); CM2(3, e[33], e[32], z1n); CM2(4, e[40], e[32], zcn); WR(8);
    CM2(5, e[9], w8, w64n); CM2(6, e[16], w64, w64n); WR(32);
    const f32x2 e2n = cneg(e[2]), z2n = cneg(z2), zdn = cneg(zd);
    SCHED();
    CV(2); CV(33); CV(40); CV(9); CV(16);
    CM1(0, e[2], w8); CM1(1, e[2], e[2]); WR(2); CM1(2, z2, z2); CM1(3, zd, zd); CM1(4, e[32], z2); WR(33); CM1(5, e[33], z2); CM1(6, e[32], zd); CM1(7, e[40], zd); WR(40);
    CM2(0, e[3], e[2], w8n); CM2(1, e[4], e[2], e2n); CM2(2, z4, z2, z2n); WR(9); CM2(3, zq, zd, zdn); CM2(4, e[34], e[32], z2n);
    CM2(5, e[35], e[33], z2n); WR(16); CM2(6, e[44], e[32], zdn); CM2(7, e[46], e[40], zdn);
    const f32x2 e4n = cneg(e[4]), z4n = cneg(z4), zqn = cneg(zq);
    SCHED();
    CV(3); CV(4); CV(34); CV(35); CV(44); CV(46);
    CM1(0, e[1], e[4]); CM1(1, e[2], e[4]); WR(3); CM1(2, e[3], e[4]); CM1(3, e[32], z4); CM1(4, e[33], z4); WR(4); CM1(5, e[34], z4); CM1(6, e[35], z4); CM1(7, e[32], zq); WR(34);
    CM2(0, e[5], e[1], e4n); CM2(1, e[6], e[2], e4n); WR(35); CM2(2, e[7], e[3], e4n); CM2(3, e[36], e[32], z4n); CM2(4, e[37], e[33], z4n); WR(44);
    CM2(5, e[38], e[34], z4n); CM2(6, e[39], e[35], z4n); CM2(7, e[48], e[32], zqn); WR(46);
    CV(5); CV(6); CV(7); WR(5); CV(36); CV(37); CV(38); WR(6); CV(39); CV(48);
    CM1(0, e[34], zc); WR(7); CM1(1, e[36], zc); CM1(2, e[38], zc); CM1(3, e[36], zd); WR(36); CM1(4, e[40], zq); CM1(5, e[44], zq); CM1(6, e[46], zq); WR(37); CM1(7, e[48], zq);
    CM2(0, e[41], e[34], zcn); CM2(1, e[42], e[36], zcn); WR(38); CM2(2, e[43], e[38], zcn); CM2(3, e[45], e[36], zdn); CM2(4, e[49], e[40], zqn); WR(39);
    CM2(5, e[50], e[44], zqn); CM2(6, e[51], e[46], zqn); CM2(7, e[52], e[48], zqn); WR(48);
    CV(41); CV(42); CV(43); WR(41); CV(45); CV(49); CV(50); WR(42); CV(51); CV(52);
    CM1(0, e[42], zd); WR(43); CM1(1, e[9], w64); CM1(2, e[2], w64); CM1(3, e[3], w64); WR(45); CM1(4, e[4], w64); CM1(5, e[5], w64); CM1(6, e[6], w64); WR(49); CM1(7, e[7], w64);
    CM2(0, e[47], e[42], zdn); CM2(1, e[17], e[9], w64n); WR(50); CM2(2, e[10], e[2], w64n); CM2(3, e[11], e[3], w64n); CM2(4, e[12], e[4], w64n); WR(51);
    CM2(5, e[13], e[5], w64n); CM2(6, e[14], e[6], w64n); CM2(7, e[15], e[7], w64n); WR(52);
    CV(47); CV(17); CV(10); WR(47); CV(11); CV(12); CV(13); WR(17); CV(14); CV(15);
    CM1(0, e[16], w64); WR(10); CM1(1, e[17], w64); CM1(2, e[10], w64); CM1(3, e[11], w64); WR(11); CM1(4, e[12], w64); CM1(5, e[13], w64); CM1(6, e[14], w64); WR(12); CM1(7, e[15], w64);
    CM2(0, e[24], e[16], w64n); CM2(1, e[25], e[17], w64n); WR(13); CM2(2, e[18], e[10], w64n); CM2(3, e[19], e[11], w64n); CM2(4, e[20], e[12], w64n); WR(14);
    CM2(5, e[21], e[13], w64n); CM2(6, e[22], e[14], w64n); CM2(7, e[23], e[15], w64n); WR(15);
    CV(24); CV(25); CV(18); WR(24); CV(19); CV(20); CV(21); WR(25); CV(22); CV(23);
    CM1(2, e[18], w64); WR(18); CM1(3, e[19], w64); CM1(4, e[20], w64); WR(19); CM1(5, e[21], w64); CM1(6, e[22], w64); WR(20); CM1(7, e[23], w64);
    CM2(2, e[26], e[18], w64n); WR(21); CM2(3, e[27], e[19], w64n); CM2(4, e[28], e[20], w64n); WR(22); CM2(5, e[29], e[21], w64n);
    CM2(6, e[30], e[22], w64n); WR(23); CM2(7, e[31], e[23], w64n);
    CV(26); CV(27); WR(26); CV(28); WR(27); CV(29); WR(28); CV(30); WR(29); CV(31); WR(30); WR(31);
    u32x4 fm[8], fv[8];
#pragma unroll
    for (int s = 0; s < 8; ++s) fm[s] = lds128(tw + (32 + t) * 256 + (((2 * s + h) ^ l15) << 4));
#pragma unroll
    for (int s = 0; s < 8; ++s) fv[s] = lds128(tw + t * 256 + (((2 * s + h) ^ l15) << 4));
    SCHED();
    SCHED();
    STAMP(4);
    f32x16 kk;
#pragma unroll
    for (int r = 0; r < 16; ++r) kk[r] = 0.f;
#pragma unroll
    for (int s = 0; s < 8; ++s) kk = mfma32h(fm[s], fv[s], kk);
    LAS float* kc = (LAS float*)(lds + M_KC + w * 2816);
    *(LAS f32x4*)(kc + 8 * t + 4 * h) = f32x4{kk[0], kk[1], kk[2], kk[3]} * sc;
    *(LAS f32x2*)(kc + (h ? 384 + 2 * t : 256 + 4 * t)) = f32x2{kk[4], kk[5]} * sc;
    *(LAS f32x2*)(kc + (h ? 448 + 2 * t : 258 + 4 * t)) = f32x2{kk[6], kk[7]} * sc;
    LAS float* k3 = kc + (h ? 640 + t : 512 + t);
    k3[0] = kk[8] * sc;
    if (h == 0) { k3[32] = kk[9] * sc; k3[64] = kk[10] * sc; k3[96] = kk[11] * sc; }
  }
  STAMP(5);

  u32x4 wfr[28];
  const int rot = 0, jsw = 0;
  auto kstep = [&](int k) { const int kk = k + rot; return kk >= 14 ? kk - 14 : kk; };
  const int w2off = WS_W2F + (((n2 * 112 + 14 * w) * 2) << 10);
  auto w2ld = [&](int i) { wfr[i] = ld128s(rs, l * 16 + (i & 1) * 1024, w2off + kstep(i >> 1) * 2048); };
  f32x2 kx[8];
  u32 kpk[4];
  auto expand_rd = [&](int e) { LAS float* q = (LAS float*)(size_t)ka_[e]; kx[e] = f32x2{q[0], q[704]}; };
  auto expand_pk = [&](int e) {
    float a = kx[2 * e].x + kx[2 * e].y, c = kx[2 * e + 1].x + kx[2 * e + 1].y;
    PIN(a); PIN(c);
    kpk[e] = pk2(a, c);
    PIN(kpk[e]);
  };
  auto expand_wr = [&]() { *(LAS u32x4*)(lds + CV_K + uch * 5120 + 1024 + ui * 16) = u32x4{kpk[0], kpk[1], kpk[2], kpk[3]}; };
  u32x4 u0;
  u32x2 u1;
  auto load_u = [&](int k) {
    if (k == 0) u0 = ld128s(rs, ui * 16, WS_XNR + (2 * b + uch) * 4096);
    else u1 = ld64s(rs, uo1, WS_XNR + (2 * b + uch) * 4096);
  };
  auto copy_u = [&](int s) {
    const u32 dd[6] = {u0.x, u0.y, u0.z, u0.w, u1.x, u1.y};
    const int o = s >> 1;
    u32x4 v;
#pragma unroll
    for (int k = 0; k < 4; ++k) v[k] = (s & 1) ? __builtin_amdgcn_alignbit(dd[o + k + 1], dd[o + k], 16) : dd[o + k];
    *(LAS u32x4*)(lds + CV_U + uch * CV_UCH + s * CV_CS + ui * 16) = v;
  };
#define W2Q 97
#define NW2AV 13
  constexpr int NW2A = NW2AV;

  if (g1) {
    f32x16 acc[2][2];
#pragma unroll
    for (int i = 0; i < 2; ++i)
#pragma unroll
      for (int j = 0; j < 2; ++j)
#pragma unroll
        for (int r = 0; r < 16; ++r) acc[i][j][r] = 0.f;
    u32x4 fa[2][2], fb[2][2];
    auto rdA = [&](int st, int ks, int i) {
      LAS char* q = lds + (st % 3 == 0 ? wa[i] + 16384 : st % 3 == 1 ? wa1[i] : wa2[i] + 16384) + ks * 4096;
      const u32x2 lo = ldstr(q), hi = ldstr(q + 1024);
      fa[ks & 1][i] = u32x4{lo.x, lo.y, hi.x, hi.y};
    };
    auto rdB = [&](int st, int ks, int j) { fb[ks & 1][j] = lds128(lds + (st % 3 == 2 ? xa2[ks] : xa[ks] + (st % 3) * G1_SLOT) + j * 4096); };
    const int so_cs = (mt & 2) == 0 ? WS_CS + (ft * 8 + kt0) * 1024 + w * 128 : OOB;
    const int so_p0 = w == 0 ? FL_P0 + b * FS : OOB;

    LBAR();
    STAMP(6);
    rdA(0, 0, 0); rdB(0, 0, 0); rdB(0, 0, 1); rdA(0, 0, 1);
    dmaw(IC<2>{}, 0, w); dmaw(IC<2>{}, 1, w);
    u32x2 fl;
    u32 msv[2];
    f32x4 csp[2];
    u32x4 cw[4];
    f32x4 cs, cv[8];
    static_for<8>([&](auto st_) {
      constexpr int st = decltype(st_)::v;
#pragma unroll
      for (int ks = 0; ks < 4; ++ks) {
        const int nst = ks == 3 ? st + 1 : st, nks = (ks + 1) & 3;
        if (ks == 3 && st < 7) {
          if (st == 1) WAIT_V(7);
          else if ((st == 3 && QF < 60) || st == 4) WAIT_V(7);
          else if (st == 6) { if (NW2A >= 6 && W2Q == 97) WAIT_V(11); else WAIT_V(5); }
          else WAIT_V(6);
          LBAR();
          if (st == 2) STAMP(18);
          if (st == 5) STAMP(21);
        }
        if (st == 7 && ks == 2) LBAR();
        SCHED();
#pragma unroll
        for (int n = 0; n < 4; ++n) {
          acc[n >> 1][n & 1] = mfma32(fa[ks & 1][n >> 1], fb[ks & 1][n & 1], acc[n >> 1][n & 1]);
          SCHED();
          const int pc = ks == 3 ? (st + 3 < 8 && n >= 2 ? n - 2 : -1) : (st + 2 < 8 ? (n == 3 ? (ks == 0 ? 2 : ks == 1 ? 4 : 5) : (n == 2 && ks == 1) ? 3 : -1) : -1);
          if (pc >= 0) { if (ks == 3) dgo(IC<(st + 3) & 7>{}, pc, w); else dgo(IC<(st + 2) & 7>{}, pc, w); }
          const int pn = ks == 3 ? (st + 3 < 8 && (n == 1 || n == 2) ? n - 1 : -1) : (st + 2 < 8 ? (n == 2 ? (ks == 0 ? 2 : ks == 1 ? 4 : 5) : (n == 1 && ks == 1) ? 3 : -1) : -1);
          if (pn >= 0) { if (ks == 3) dm0(IC<(st + 3) & 7>{}, pn, w); else dm0(IC<(st + 2) & 7>{}, pn, w); }
          if (nst < 8) {
            if (n == 0) { rdA(nst, nks, 1); rdB(nst, nks, 1); }
            if (n == 1) { rdA(nst, nks, 0); rdB(nst, nks, 0); }
          }
          SCHED();
          const int q = st * 16 + ks * 4 + n;
          if (q < 4) cw[q] = lds128((LAS char*)(size_t)a_cw + q * 4096);
          if (q == 5) cs = mfma16(sel, cw[0], f32x4{0.f, 0.f, 0.f, 0.f});
          if (q >= 6 && q < 9) cs = mfma16(sel, cw[q - 5], cs);
          if (q == 19) {
#pragma unroll
            for (int e = 0; e < 4; ++e) { const float v = cs[e]; cs[e] = v + dppx<0x114>(v); }
            PIN(cs);
          }
          if (q == 21) {
#pragma unroll
            for (int e = 0; e < 4; ++e) { const float v = cs[e]; cs[e] = v + dppx<0x118>(v); }
            PIN(cs);
          }
          if (q == 24) st128s(__builtin_bit_cast(u32x4, cs), rs, o_cq, so_cs);
          if (q >= 30 && q < 46 && (q & 2)) expand_rd(((q - 30) >> 2) * 2 + (q & 1));
          if (q >= 50 && q < 66 && (q & 3) == 2) { PIN(kx[(q - 50) >> 1]); expand_pk((q - 50) >> 2); }
          if (q == QW) expand_wr();
          if (q == QF) st64s(tag, rs, o_l0, so_p0);
          if (q == 75) asm volatile("global_load_dwordx2 %0, %1, %2 sc1" : "=v"(fl) : "v"(p0off), "s"(p.ws + FL_P0) : "memory");
          if (q == 89) {
            asm volatile("s_waitcnt vmcnt(5)" : "+v"(fl) :: "memory");
            if (!__all(fl.x == tag.x && fl.y == tag.y)) poll(rs, p0off, FL_P0, tag);
          }
          if (q == 92 || q == 93) load_u(q - 92);
          if (q == 94 || q == 95) csp[q - 94] = __builtin_bit_cast(f32x4, ld128s(rs, o_cs, WS_CS + (ft * 8 + 4 * (q - 94)) * 1024));
          if (q == 96) msv[0] = ld32s(rs, l * 4, WS_ST + (mt * 128 + 64 * wt) * 4);
          if (q >= W2Q && q < W2Q + 2 * NW2A && ((q - W2Q) & 1) == 0) w2ld((q - W2Q) >> 1);
          if (q == 106 || q == 110 || q == 114 || q == 118) copy_u((q - 106) >> 2);
          if (q == 112) {
            PIN(csp[0]); PIN(csp[1]);
            csp[0] += csp[1];
            const auto r0 = __builtin_amdgcn_permlane32_swap(f2u(csp[0][0]), f2u(csp[0][2]), false, false);
            const auto r1 = __builtin_amdgcn_permlane32_swap(f2u(csp[0][1]), f2u(csp[0][3]), false, false);
            csp[0][0] = __builtin_bit_cast(float, (u32)r0[0]) + __builtin_bit_cast(float, (u32)r0[1]);
            csp[0][1] = __builtin_bit_cast(float, (u32)r1[0]) + __builtin_bit_cast(float, (u32)r1[1]);
            PIN(csp[0]);
          }
          if (q == 116) {
            const auto r = __builtin_amdgcn_permlane16_swap(f2u(csp[0][0]), f2u(csp[0][1]), false, false);
            *(LAS float*)(size_t)a_cs = __builtin_bit_cast(float, (u32)r[0]) + __builtin_bit_cast(float, (u32)r[1]);
            PIN(msv[0]);
            const auto m = __builtin_amdgcn_permlane32_swap(msv[0], msv[0], false, false);
            msv[0] = m[0]; msv[1] = m[1];
            PIN(msv[0]); PIN(msv[1]);
          }
          if (q >= 120 && q < 124) {
#pragma unroll
            for (int kk = 2 * (q - 120); kk < 2 * (q - 120) + 2; ++kk) {
              const int k = kk ^ 4;
              cv[k] = *(LAS f32x4*)((LAS char*)(size_t)a_cv + ((k >> 1) & 1) * 128 + (k >> 2) * 64 + (k & 1) * 32);
            }
          }
          if (q >= 124) {
            const int m = (n + 2) & 3, ii = m >> 1, jj = m & 1;
            const float nm = -b2f(msv[jj]);
#pragma unroll
            for (int r = 0; r < 16; ++r) acc[ii][jj][r] = __builtin_fmaf(nm, cv[4 * ii + (r >> 2)][r & 3], acc[ii][jj][r]);
          }
          SCHED();
        }
      }
    });
    STAMP(7);
    __builtin_amdgcn_s_setprio(3);
#pragma unroll
    for (int j = 0; j < 2; ++j) {
      const float sd = bhi(msv[j]), sd2 = sd * sd;
      const float ce = -1.44269504f * __builtin_amdgcn_rcpf(sd);
#pragma unroll
      for (int i = 0; i < 2; ++i) {
        u32x4 o;
#pragma unroll
        for (int r = 0; r < 4; ++r) {
          const f32x2 gg = {acc[i][j][2 * r], acc[i][j][2 * r + 1]};
          const f32x2 uu = {acc[i][j][8 + 2 * r], acc[i][j][9 + 2 * r]};
          const f32x2 a = gg * f32x2{ce, ce};
          const f32x2 e = {__builtin_amdgcn_exp2f(a.x), __builtin_amdgcn_exp2f(a.y)};
          const f32x2 d = fma2(e, f32x2{sd2, sd2}, f32x2{sd2, sd2});
          const f32x2 q = {__builtin_amdgcn_rcpf(d.x), __builtin_amdgcn_rcpf(d.y)};
          o[r] = pk2v((gg * uu) * q);
        }
        st128s(o, rs, l * 16, WS_H + (((mt * 4 + 2 * wt + j) * 112 + ft * 8 + 2 * wf + i) << 10));
        SCHED();
      }
    }
    __builtin_amdgcn_s_setprio(1);
    STAMP(8);
  } else {
    poll(rs, p0off, FL_P0, tag);
    if (b == 255 && w == 0) {
#pragma unroll 1
      for (int i = 0; i < 4; ++i) poll(rs, (64 * i + l) * FS, FL_P0, tag);
      if (l == 0) st64s(tag, rs, 0, FL_EP);
    }
    LBAR();
    load_u(0); load_u(1);
#pragma unroll
    for (int e = 0; e < 8; ++e) expand_rd(e);
#pragma unroll
    for (int e = 0; e < 4; ++e) expand_pk(e);
    LBAR();
    expand_wr();
#pragma unroll
    for (int s = 0; s < 4; ++s) copy_u(s);
    SCHED();
#pragma unroll
    for (int i = 0; i < NW2A; ++i) w2ld(i);
    __builtin_amdgcn_s_waitcnt(0x0F70);
    LBAR();
  }

  STAMP(10);
  u32x2 hfl;
  u32x4 hf[28];
  const int hoff = WS_H + ((2 * m2 * 112 + 14 * w) << 10);
  auto hld = [&](int n) { hf[n] = ld128s(rs, l * 16, hoff + (((n / 14) ^ jsw) * 112 + kstep(n % 14)) * 1024); };
  const u32 hfo = (l < 7 ? l : 0) * FS;
  const int hfs = FL_H + (m2 * 56 + 7 * w) * FS;
#define PDV 12
  constexpr int PD = PDV;
  {
    const int ch = w & 1, ts = (w >> 1) & 1, rh = w >> 2;
    LAS char* ua = opaque(lds + CV_U + ch * CV_UCH + (3 - (l15 & 3)) * CV_CS + 4088 - 8 * (l15 >> 2) - 32 * (g >> 1) + 16 * (g & 1)
                          - 7 * 512 - 256 * rh - 3 * 64);
    LAS char* ka = opaque(lds + CV_K + ch * 5120 + 1024 - 512 * ts + 32 * (l15 - (g >> 1)) + 16 * (g & 1) - 256 * rh - 3 * 64);
    *(LAS u32x4*)(lds + CV_K + ch * 5120 + l * 16) = u32x4{0u, 0u, 0u, 0u};
    *(LAS u32x4*)(lds + CV_U + ch * CV_UCH + ((l >> 1) & 3) * CV_CS + 4096 + (l & 1) * 16) = u32x4{0u, 0u, 0u, 0u};
    asm volatile("" ::: "memory");
    f32x4 acc[4];
#pragma unroll
    for (int i = 0; i < 4; ++i) acc[i] = f32x4{0.f, 0.f, 0.f, 0.f};
    const int so_h = g1 ? FL_H + ((mt * 2 + wt) * 56 + ft * 4 + wf) * FS : OOB;
    auto hook = [&](int rr, int c) {
#define HPUB 3
#define HPOLL 24
      const int sq = rr * 8 + c;
      if (sq == HPUB) {
        WAIT_V(0);
        st64s(tag, rs, o_l0, so_h);
        __builtin_amdgcn_s_setprio(0);
        STAMP(9);
      }
      if (sq > HPUB && sq <= HPUB + 28 - NW2A) w2ld(NW2A + sq - HPUB - 1);
      if (sq == (HPOLL < 32 ? HPOLL - 5 : 31)) hfl = ld64s(rs, hfo, hfs);
      if (sq == HPOLL) {
        STAMP(2);
        if (!__all(hfl.x == tag.x && hfl.y == tag.y)) poll(rs, hfo, hfs, tag);
        asm volatile("" ::: "memory");
        STAMP(23);
      }
      if (sq >= HPOLL) {
#pragma unroll
        for (int n = (sq - HPOLL) * PD / (HPOLL < 32 ? 32 - HPOLL : 1); n < (sq + 1 - HPOLL) * PD / (HPOLL < 32 ? 32 - HPOLL : 1); ++n) hld(n);
      }
    };
    conv_tiles(ua, ka, acc, hook);
#pragma unroll
    for (int i = 0; i < 4; ++i) *(LAS f32x4*)(lds + CV_X + (w * 4 + i) * 1024 + l * 16) = acc[i];
  }
  LBAR();
  STAMP(11);
  int ytile;
  {
    const int ts = (w >> 1) & 1, i = (w & 1) + 2 * (w >> 2);
    ytile = 7 - 2 * i - ts;
    const int pos = 256 * ytile + 16 * l15 + 4 * g;
    f32x4 y0[2], y1[2];
    u32x2 ur[2];
#pragma unroll
    for (int ch = 0; ch < 2; ++ch) {
      y0[ch] = *(LAS f32x4*)(lds + CV_X + ((ch + 2 * ts) * 4 + i) * 1024 + l * 16);
      y1[ch] = *(LAS f32x4*)(lds + CV_X + ((ch + 2 * ts + 4) * 4 + i) * 1024 + l * 16);
      ur[ch] = lds64(lds + CV_U + ch * CV_UCH + (2044 - pos) * 2);
    }
    LBAR();
    float res[2][4];
#pragma unroll
    for (int ch = 0; ch < 2; ++ch) {
      const float dsk = ch ? bhi(dk) : b2f(dk);
      const f32x4 y = y0[ch] + y1[ch];
      res[ch][0] = __builtin_fmaf(bhi(ur[ch].y), dsk, y[0]); res[ch][1] = __builtin_fmaf(b2f(ur[ch].y), dsk, y[1]);
      res[ch][2] = __builtin_fmaf(bhi(ur[ch].x), dsk, y[2]); res[ch][3] = __builtin_fmaf(b2f(ur[ch].x), dsk, y[3]);
    }
    const u32x4 o = {pk2(res[0][0], res[1][0]), pk2(res[0][1], res[1][1]), pk2(res[0][2], res[1][2]), pk2(res[0][3], res[1][3])};
    st128s(o, rs, pos * 4, WS_Y + b * 8192);
    if (HPOLL == 32) {
      STAMP(2);
      if (!__all(hfl.x == tag.x && hfl.y == tag.y)) poll(rs, hfo, hfs, tag);
      asm volatile("" ::: "memory");
      STAMP(23);
#pragma unroll
      for (int n = 0; n < PD; ++n) hld(n);
    }
  }
  STAMP(12);

  {
    const int oj = w >> 2, oq = w & 3;
    const int etok = 64 * m2 + 32 * (oj ^ jsw) + t, ech = 64 * n2 + 16 * oq + 8 * h;
    u32x4 xv;
    SCHED();
    f32x16 acc[2][2];
#pragma unroll
    for (int i = 0; i < 2; ++i)
#pragma unroll
      for (int j = 0; j < 2; ++j)
#pragma unroll
        for (int r = 0; r < 16; ++r) acc[i][j][r] = 0.f;
    u32x2 yfl;
    const int yfo = FL_Y + ((m2 >> 2) * 256 + 32 * n2 + 8 * oq) * FS;
    LAS char* pw = opaque(lds + w * 16384 + l * 16);
    auto part_wr = [&](int j, int k) {
      const int i = k & 1, qd = k >> 1;
      *(LAS f32x4*)(pw + (j * 8 + k) * 1024) = f32x4{acc[i][j][4 * qd], acc[i][j][4 * qd + 1], acc[i][j][4 * qd + 2], acc[i][j][4 * qd + 3]};
    };
#pragma unroll
    for (int n = 0; n < 28; ++n) {
#pragma unroll
      for (int i = 0; i < 2; ++i) acc[i][n / 14] = mfma32(wfr[2 * (n % 14) + i], hf[n], acc[i][n / 14]);
      SCHED();
      if (n + PD < 28) hld(n + PD);
      if (n == YP1 && w >= 4) { if (HPOLL < 32) WAIT_V(2); else WAIT_V(14); st64s(tag, rs, o_l0, FL_Y + (ytile * 256 + b) * FS); }
      if (n == YP0 && w < 4) { WAIT_V(11); st64s(tag, rs, o_l0, FL_Y + (ytile * 256 + b) * FS); }
      if (n == 21) xv = *(const GAS u32x4*)((const char*)p.x + (size_t)etok * 1024 + ech * 2);
      if (n == 22) yfl = ld64s(rs, (l & 7) * FS, yfo);
      if (n >= 16 && n < 24) part_wr(0, n - 16);
      SCHED();
    }
    STAMP(13);
    if (!__all(yfl.x == tag.x && yfl.y == tag.y)) poll(rs, (l & 7) * FS, yfo, tag);
    asm volatile("" ::: "memory");
    u32 yv[4];
#pragma unroll
    for (int j = 0; j < 4; ++j) yv[j] = ld32s(rs, ((ech >> 1) + j) * 8192 + etok * 4, WS_Y);
#pragma unroll
    for (int k = 0; k < 8; ++k) part_wr(1, k);
    LBAR();
    STAMP(14);
    LAS char* pr = opaque(lds + w * 2048 + l * 16);
    LAS char* pr2 = opaque(pr + 65536);
    f32x4 pp[8][2];
#pragma unroll
    for (int ww = 0; ww < 8; ++ww)
#pragma unroll
      for (int i = 0; i < 2; ++i) pp[ww][i] = *(LAS f32x4*)((ww < 4 ? pr : pr2) + (ww & 3) * 16384 + i * 1024);
    SCHED();
    f32x4 sm[2];
#pragma unroll
    for (int i = 0; i < 2; ++i) sm[i] = ((pp[0][i] + pp[1][i]) + (pp[2][i] + pp[3][i])) + ((pp[4][i] + pp[5][i]) + (pp[6][i] + pp[7][i]));
    u32x4 r;
#pragma unroll
    for (int j = 0; j < 4; ++j) {
      const u32 s = pk2v(up2(xv[j]) + up2(yv[j]));
      r[j] = pk2v(up2(s) + up2(pk2(sm[0][j], sm[1][j])));
    }
    *(GAS u32x4*)((char*)p.out + (size_t)etok * 1024 + ech * 2) = r;
  }
  STAMP(15);
  STAMP_DUMP();
}

extern "C" void kernel_launch(void* const* d_in, const int* in_sizes, int n_in, void* d_out, int out_size, void* d_ws,
                              size_t ws_size, hipStream_t stream) {
  Params p;
  p.x = (const u16*)d_in[0]; p.lar = (const u16*)d_in[1]; p.aim = (const u16*)d_in[2]; p.cre = (const u16*)d_in[3];
  p.cim = (const u16*)d_in[4]; p.ldt = (const u16*)d_in[5]; p.dsk = (const u16*)d_in[6];
  p.w1 = (const u16*)d_in[8]; p.w3 = (const u16*)d_in[9]; p.w2 = (const u16*)d_in[10];
  p.out = (u16*)d_out; p.ws = (char*)d_ws;
  fused<<<256, 512, 0, stream>>>(p);
}
```

```cpp
#include <hip/hip_runtime.h>
#include <hip/hip_bf16.h>

typedef unsigned short u16;
typedef unsigned int u32;
typedef unsigned long long u64;
typedef __bf16 bf16x8 __attribute__((ext_vector_type(8)));
typedef __bf16 bf16x2 __attribute__((ext_vector_type(2)));
typedef _Float16 f16x8 __attribute__((ext_vector_type(8)));
typedef _Float16 f16x2 __attribute__((ext_vector_type(2)));
typedef short s16x4 __attribute__((ext_vector_type(4)));
typedef float f32x2 __attribute__((ext_vector_type(2)));
typedef float f32x4 __attribute__((ext_vector_type(4)));
typedef float f32x16 __attribute__((ext_vector_type(16)));
typedef u32 u32x2 __attribute__((ext_vector_type(2)));
typedef u32 u32x4 __attribute__((ext_vector_type(4)));
typedef __amdgpu_buffer_rsrc_t rsrc_t;
#define LAS __attribute__((address_space(3)))
#define GAS __attribute__((address_space(1)))
#define DEV __device__ __forceinline__

#define QW 104
#define QZ 54
#define QF 46
#define YP1 1
#define YP0 10
constexpr int L = 2048, D = 512, NM = 128, F = 1792;
constexpr int LDS_BYTES = 163840;
constexpr int SC1 = 16;

constexpr int WS_H = 0;
constexpr int WS_XNR = 8 << 20;
constexpr int WS_W2F = 10 << 20;
constexpr int WS_Y = 12 << 20;
constexpr int WS_ST = 14 << 20;
constexpr int WS_CS = WS_ST + 8192;
constexpr int WS_FL = 15 << 20;
constexpr int FS = 128;
constexpr int FL_EP = WS_FL;
constexpr int FL_P0 = WS_FL + 4096;
constexpr int FL_H = FL_P0 + 256 * FS;
constexpr int FL_Y = FL_H + 2048 * FS;
constexpr int WS_BYTES = 16 << 20;

struct Params {
  const u16 *x, *lar, *aim, *cre, *cim, *ldt, *dsk, *w1, *w3, *w2;
  u16* out;
  char* ws;
};

template <int V> struct IC { static constexpr int v = V; };
template <int N, int I = 0, class Fn>
DEV void static_for(Fn&& fn) { if constexpr (I < N) { fn(IC<I>{}); static_for<N, I + 1>(fn); } }
DEV u32 f2u(float f) { return __builtin_bit_cast(u32, f); }
DEV float u2f(u32 v) { return __builtin_bit_cast(float, v); }
DEV float b2f(u32 v) { return __builtin_bit_cast(float, v << 16); }
DEV float bhi(u32 v) { return __builtin_bit_cast(float, v & 0xffff0000u); }
DEV u32 pk2v(f32x2 f) { return __builtin_bit_cast(u32, __builtin_convertvector(f, bf16x2)); }
DEV u32 pk2(float lo, float hi) { return pk2v(f32x2{lo, hi}); }
DEV f32x2 up2(u32 v) { return f32x2{b2f(v), bhi(v)}; }
DEV float rb(float f) { return b2f(pk2(f, f) & 0xffffu); }
DEV f32x2 fma2(f32x2 a, f32x2 b, f32x2 c) { return __builtin_elementwise_fma(a, b, c); }
DEV f32x16 mfma32(u32x4 a, u32x4 b, f32x16 c) {
  return __builtin_amdgcn_mfma_f32_32x32x16_bf16(__builtin_bit_cast(bf16x8, a), __builtin_bit_cast(bf16x8, b), c, 0, 0, 0);
}
DEV f32x4 mfma16(u32x4 a, u32x4 b, f32x4 c) {
  return __builtin_amdgcn_mfma_f32_16x16x32_bf16(__builtin_bit_cast(bf16x8, a), __builtin_bit_cast(bf16x8, b), c, 0, 0, 0);
}
DEV f32x16 mfma32h(u32x4 a, u32x4 b, f32x16 c) {
  return __builtin_amdgcn_mfma_f32_32x32x16_f16(__builtin_bit_cast(f16x8, a), __builtin_bit_cast(f16x8, b), c, 0, 0, 0);
}
DEV u32x4 lds128(LAS char* p) { return *(LAS u32x4*)p; }
DEV u32x2 lds64(LAS char* p) { return *(LAS u32x2*)p; }
DEV u32x2 ldstr(LAS char* p) { return __builtin_bit_cast(u32x2, __builtin_amdgcn_ds_read_tr16_b64_v4i16((LAS s16x4*)p)); }
template <int LOFF>
DEV void dma_m0(u32 lbase) { asm volatile("s_add_u32 m0, %0, %1" :: "s"(lbase), "i"(LOFF) : "memory", "scc", "m0"); }
template <bool NT>
DEV void dma_go(u32x4 rs, u32 voff, u32 soff) {
  if (NT) asm volatile("buffer_load_dwordx4 %0, %1, %2 offen nt lds" :: "v"(voff), "s"(rs), "s"(soff) : "memory");
  else asm volatile("buffer_load_dwordx4 %0, %1, %2 offen lds" :: "v"(voff), "s"(rs), "s"(soff) : "memory");
}
DEV u32x4 mkrs(const void* ptr, u32 bytes) { const u64 a = (u64)ptr; return u32x4{(u32)a, (u32)(a >> 32) & 0xffffu, bytes, 0x00020000u}; }
#define WAIT_V(n) asm volatile("s_waitcnt vmcnt(" #n ")" ::: "memory")
#define WAIT_L0() asm volatile("s_waitcnt lgkmcnt(0)" ::: "memory")
#define BAR() __builtin_amdgcn_s_barrier()
#define LBAR() do { WAIT_L0(); BAR(); asm volatile("" ::: "memory"); } while (0)
#define SCHED() __builtin_amdgcn_sched_barrier(0)
#define PIN(x) asm volatile("" : "+v"(x))
constexpr int OOB = 0x04000000;
#define STAMP(i)
#define STAMP_DECL
#define STAMP_DUMP()

DEV u32x4 ld128s(rsrc_t rs, int voff, int soff) { return __builtin_amdgcn_raw_buffer_load_b128(rs, voff, soff, SC1); }
DEV u32x2 ld64s(rsrc_t rs, int voff, int soff) { return __builtin_amdgcn_raw_buffer_load_b64(rs, voff, soff, SC1); }
DEV u32 ld32s(rsrc_t rs, int voff, int soff) { return __builtin_amdgcn_raw_buffer_load_b32(rs, voff, soff, SC1); }
DEV void st128s(u32x4 v, rsrc_t rs, int voff, int soff) {
  __builtin_amdgcn_raw_buffer_store_b128(v, rs, voff, soff, SC1);
  asm volatile("s_nop 1" :: "v"(v) : "memory");
}
DEV void st64s(u32x2 v, rsrc_t rs, int voff, int soff) { __builtin_amdgcn_raw_buffer_store_b64(v, rs, voff, soff, SC1); }
DEV void st32s(u32 v, rsrc_t rs, int voff, int soff) { __builtin_amdgcn_raw_buffer_store_b32(v, rs, voff, soff, SC1); }
DEV void poll(rsrc_t rs, int voff, int soff, u32x2 tag) {
#pragma unroll 1
  for (int spin = 0; spin < (1 << 18); ++spin) {
    const u32x2 v = ld64s(rs, voff, soff);
    if (__all(v.x == tag.x && v.y == tag.y)) break;
    __builtin_amdgcn_s_sleep(4);
  }
  __builtin_amdgcn_fence(__ATOMIC_ACQUIRE, "wavefront");
  asm volatile("" ::: "memory");
}

template <int CTRL, int RM>
DEV u32 dppu(u32 v) { return (u32)__builtin_amdgcn_update_dpp(0, (int)v, CTRL, RM, 0xf, true); }
DEV u32 wave_max_u32(u32 v) {
  v = max(v, dppu<0x111, 0xf>(v)); v = max(v, dppu<0x112, 0xf>(v)); v = max(v, dppu<0x114, 0xf>(v)); v = max(v, dppu<0x118, 0xf>(v));
  v = max(v, dppu<0x142, 0xa>(v)); v = max(v, dppu<0x143, 0xc>(v));
  return (u32)__builtin_amdgcn_readlane((int)v, 63);
}

DEV float rdl(float v, int lane) { return __builtin_bit_cast(float, __builtin_amdgcn_readlane(__builtin_bit_cast(int, v), lane)); }
template <int CTRL>
DEV float dppx(float v) { return __builtin_bit_cast(float, __builtin_amdgcn_mov_dpp(__builtin_bit_cast(int, v), CTRL, 0xf, 0xf, true)); }
DEV float rows8_sum(const float (&p)[8], int l) {
  float a[4], c[2];
#pragma unroll
  for (int i = 0; i < 4; ++i) {
    const auto r = __builtin_amdgcn_permlane32_swap(__builtin_bit_cast(u32, p[i]), __builtin_bit_cast(u32, p[i + 4]), false, false);
    a[i] = __builtin_bit_cast(float, (u32)r[0]) + __builtin_bit_cast(float, (u32)r[1]);
  }
#pragma unroll
  for (int i = 0; i < 2; ++i) {
    const auto r = __builtin_amdgcn_permlane16_swap(__builtin_bit_cast(u32, a[i]), __builtin_bit_cast(u32, a[i + 2]), false, false);
    c[i] = __builtin_bit_cast(float, (u32)r[0]) + __builtin_bit_cast(float, (u32)r[1]);
  }
  const bool up = l & 8;
  float v = (up ? c[1] : c[0]) + dppx<0x128>(up ? c[0] : c[1]);
  v += dppx<0xB1>(v); v += dppx<0x4E>(v); v += dppx<0x141>(v);
  return v;
}

constexpr int G1_SLOT = 49152;
constexpr int KG_T = 2 * G1_SLOT;
constexpr int KG_TW = 53 * 256;
constexpr int SPARE = 3 * G1_SLOT;
constexpr int M_CS = SPARE;
constexpr int M_KC = SPARE + 5120;
constexpr int CV_K = SPARE + 5120;
static_assert(KG_T + 4 * KG_TW <= M_KC && M_KC + 4 * 2816 <= LDS_BYTES, "lds");
constexpr int CV_CS = 4160;
constexpr int CV_U = 2 * G1_SLOT;
constexpr int CV_UCH = 4 * CV_CS;
constexpr int CV_X = 0;

DEV f32x2 cmul(f32x2 a, f32x2 b, f32x2 bn) { return fma2(f32x2{a.y, a.x}, bn, a * f32x2{b.x, b.x}); }
DEV f32x2 cneg(f32x2 b) { return f32x2{-b.y, b.y}; }
DEV f32x2 cexp_p(float lre2, float rev, float pw) {
  const float mg = __builtin_amdgcn_exp2f(lre2 * pw);
  const float ph = __builtin_amdgcn_fractf(rev * pw);
  return f32x2{__builtin_amdgcn_cosf(ph), __builtin_amdgcn_sinf(ph)} * f32x2{mg, mg};
}
DEV u32 pkh(f32x2 f) { return __builtin_bit_cast(u32, __builtin_convertvector(f, f16x2)); }
DEV LAS char* opaque(LAS char* q) { u32 v = (u32)(size_t)q; asm volatile("" : "+v"(v)); return (LAS char*)(size_t)v; }

template <class Hook>
DEV void conv_tiles(LAS char* ua, LAS char* ka, f32x4 (&acc)[4], Hook hook) {
  constexpr int ORD[16] = {0, 15, 13, 11, 9, 1, 14, 12, 10, 8, 2, 3, 4, 5, 6, 7};
  u32x4 fr[2][16];
  auto load = [&](int rr, int id) {
    if (id < 8) {
      const u32x2 lo = *(volatile LAS u32x2*)(ua + (7 - id) * 512 + (3 - rr) * 64), hi = *(volatile LAS u32x2*)(ua + (7 - id) * 512 + (3 - rr) * 64 + 8);
      fr[rr & 1][id] = u32x4{lo.x, lo.y, hi.x, hi.y};
    } else {
      fr[rr & 1][id] = lds128(ka + (id - 8) * 512 + (3 - rr) * 64);
    }
  };
#pragma unroll
  for (int k = 0; k < 16; ++k) { load(0, ORD[k]); SCHED(); }
  hook(0, -1);
  SCHED();
#pragma unroll
  for (int rr = 0; rr < 4; ++rr) {
#pragma unroll
    for (int c = 0; c < 8; ++c) {
      u32x4(&f)[16] = fr[rr & 1];
#pragma unroll
      for (int i = 0; i < 4; ++i) {
        if (c <= 7 - 2 * i) { acc[i] = mfma16(f[c], f[8 + 7 - 2 * i - c], acc[i]); SCHED(); }
        if (rr < 3 && i < 2) { load(rr + 1, ORD[2 * c + i]); SCHED(); }
      }
      hook(rr, c);
      SCHED();
    }
  }
}

__global__ __launch_bounds__(512, 2) void fused(Params p) {
  __shared__ __attribute__((aligned(16))) char smem[LDS_BYTES];
  asm volatile("" : "+s"(p.x), "+s"(p.lar), "+s"(p.aim), "+s"(p.cre), "+s"(p.cim), "+s"(p.ldt), "+s"(p.dsk), "+s"(p.w1), "+s"(p.w3),
               "+s"(p.w2), "+s"(p.out), "+s"(p.ws));
  LAS char* lds = (LAS char*)smem;
  const u32 lds0 = (u32)(size_t)lds;
  const int tid = threadIdx.x, l = tid & 63;
  const int w = __builtin_amdgcn_readfirstlane(tid >> 6);
  const int b = blockIdx.x, xcd = b & 7, rk = b >> 3;
  const bool g1 = rk < 28;
  const rsrc_t rs = __builtin_amdgcn_make_buffer_rsrc(p.ws, 0, WS_BYTES, 0x00020000);
  const int l15 = l & 15, g = l >> 4, t = l & 31, h = l >> 5;
  STAMP_DECL;
  u32 mt0 = (u32)__builtin_amdgcn_s_memrealtime(), mtp = mt0;
  STAMP(0);

  u32x2 tag;
  u32 p_ldt2, dk;
  asm volatile("s_load_dwordx2 %0, %3, 0x0\n\ts_load_dword %1, %4, 0x0\n\ts_load_dword %2, %5, 0x0"
               : "=&s"(tag), "=&s"(p_ldt2), "=&s"(dk) : "s"(p.ws + FL_EP), "s"(p.ldt + 2 * b), "s"(p.dsk + 2 * b) : "memory");

#define CODEPF 21000
  u32x4 pf;
  if (w == 4) {
    u64 pc;
    asm volatile("s_getpc_b64 %0" : "=s"(pc));
    const u32 po = min(rk * 1024 + l * 16, CODEPF - 16);
    asm volatile("global_load_dwordx4 %0, %1, %2" : "=v"(pf) : "v"(po), "s"(pc & ~(u64)255) : "memory");
  }
  u32 p_lar, p_aim, p_cre, p_cim;
  if (w < 4) {
    const u32 po = (b * 256 + tid) * 2;
    asm volatile("global_load_ushort %0, %1, %2" : "=v"(p_lar) : "v"(po), "s"(p.lar) : "memory");
    asm volatile("global_load_ushort %0, %1, %2" : "=v"(p_aim) : "v"(po), "s"(p.aim) : "memory");
    asm volatile("global_load_ushort %0, %1, %2" : "=v"(p_cre) : "v"(po), "s"(p.cre) : "memory");
    asm volatile("global_load_ushort %0, %1, %2" : "=v"(p_cim) : "v"(po), "s"(p.cim) : "memory");
  }
  SCHED();

  const int mt = (xcd >> 1) * 4 + (rk & 3), ft = (xcd & 1) * 7 + (rk >> 2);
#define ROTV 5
  const int kt0 = ROTV == 0 ? 0 : ROTV == 2 ? (2 * (xcd >> 1) + 4 * (xcd & 1)) & 7 : (2 * (xcd >> 1) + 4 * (xcd & 1) + (mt & 1)) & 7;
  const int wt = w & 1, wf = w >> 1;
  const u32x4 rx = mkrs(p.x, L * D * 2), rw1 = mkrs(p.w1, D * F * 2), rw3 = mkrs(p.w3, D * F * 2);
  const u32 xo0 = (l >> 3) * 1024 + (((l & 7) ^ g) << 4);
  const u32 xo1 = (8 + (l >> 3)) * 1024 + (((l & 7) ^ (4 + g)) << 4);
  const u32 wo1 = g * (F * 2) + ((l15 ^ (4 * g)) << 4), wo1b = wo1 + 4 * F * 2;
  const u32 wo3 = g * (F * 2) + ((l15 ^ (4 * g + 2)) << 4), wo3b = wo3 + 4 * F * 2;
#define NTS 0
  auto dm0 = [&](auto st_, int piece, int ww) {
    constexpr int S = (decltype(st_)::v % 3) * G1_SLOT;
    const u32 lb = lds0 + ww * 2048;
    if (piece == 0) dma_m0<S>(lb);
    if (piece == 1) dma_m0<S + 16384>(lb);
    if (piece == 2) dma_m0<S + 32768>(lb);
    if (piece == 3) dma_m0<S + 1024>(lb);
    if (piece == 4) dma_m0<S + 16384 + 1024>(lb);
    if (piece == 5) dma_m0<S + 32768 + 1024>(lb);
  };
  auto dgo = [&](auto st_, int piece, int ww) {
    constexpr bool nt = decltype(st_)::v < NTS;
    const int kt = (kt0 + decltype(st_)::v) & 7;
    const u32 sx = (mt * 128 + ww * 16) * 1024 + kt * 128, sw = (kt * 64 + ww * 8) * (F * 2) + ft * 256;
    if (piece == 0) dma_go<nt>(rx, xo0, sx);
    if (piece == 1) dma_go<nt>(rw1, wo1, sw);
    if (piece == 2) dma_go<nt>(rw3, wo3, sw);
    if (piece == 3) dma_go<nt>(rx, xo1, sx);
    if (piece == 4) dma_go<nt>(rw1, wo1b, sw);
    if (piece == 5) dma_go<nt>(rw3, wo3b, sw);
  };
  auto dmaw = [&](auto st_, int piece, int ww) { dm0(st_, piece, ww); asm volatile("s_nop 0"); dgo(st_, piece, ww); };

  u32 wr[8];
  u32x4 xr[8];
  const int un2 = b / 28, ukb = 4 * (b - 28 * un2) + (w & 3);
  const int ax = xcd * 4 + (rk - 28);
  if (w >= 4 && g1) {
#pragma unroll
    for (int i = 0; i < 6; ++i) { dmaw(IC<0>{}, i, w - 4); dmaw(IC<0>{}, i, w); }
    const char* wb2 = (const char*)p.w2 + (size_t)(16 * ukb) * 1024 + un2 * 128;
    const u32 wv = h * 4096 + t * 4;
#define W2RAW(j, base, off) asm volatile("global_load_dword %0, %1, %2 offset:" #off : "=v"(wr[j]) : "v"(wv), "s"(base) : "memory")
    W2RAW(0, wb2, 0); W2RAW(1, wb2, 1024); W2RAW(2, wb2, 2048); W2RAW(3, wb2, 3072);
    W2RAW(4, wb2 + 8192, 0); W2RAW(5, wb2 + 8192, 1024); W2RAW(6, wb2 + 8192, 2048); W2RAW(7, wb2 + 8192, 3072);
  }
  if (!g1) {
    const u32 xv_ = (64 * ax + 8 * w) * 1024 + l * 16;
#define XRAW(j, vo, off) asm volatile("global_load_dwordx4 %0, %1, %2 offset:" #off : "=v"(xr[j]) : "v"(vo), "s"(p.x) : "memory")
    XRAW(0, xv_, 0); XRAW(1, xv_, 1024); XRAW(2, xv_, 2048); XRAW(3, xv_, 3072);
    XRAW(4, xv_ + 4096, 0); XRAW(5, xv_ + 4096, 1024); XRAW(6, xv_ + 4096, 2048); XRAW(7, xv_ + 4096, 3072);
  }
  SCHED();

  const int m2 = xcd * 4 + (rk & 3), n2 = rk >> 2;
  u32 p0off;
  {
    const int e = min(9 * w + min(l, 8), 67);
    const int m = e - 60;
    const int pb = e < 32 ? 224 + e : (e < 60 ? 28 * n2 + (e - 32) : (g1 ? ((m >> 1) * 2 + (xcd & 1)) + 8 * ((m & 1) + 4 * (rk >> 2)) : 224));
    p0off = pb * FS;
  }
  const int uch = w >> 2, ui = tid & 255;
  u32 ka_[8];
#pragma unroll
  for (int e = 0; e < 4; ++e) {
    const int l0 = 8 * ui + 2 * e;
    const u32 bits = pk2((float)l0, (float)(l0 + 1));
    const int i0 = ui < 32 ? l0 : (int)(bits & 0xffffu) - (0x4300 - 128);
    const int i1 = ui < 32 ? l0 + 1 : (int)(bits >> 16) - (0x4300 - 128);
    ka_[2 * e] = lds0 + M_KC + uch * 5632 + i0 * 4;
    ka_[2 * e + 1] = lds0 + M_KC + uch * 5632 + i1 * 4;
  }
  u32 uo1 = ui < 255 ? ui * 16 + 16 : OOB;
  int xa[4], wa[2];
#pragma unroll
  for (int ks = 0; ks < 4; ++ks) xa[ks] = (64 * wt + t) * 128 + (((2 * ks + h) ^ ((t >> 1) & 7)) << 4);
  {
    const int img = g & 1, q = l15 >> 2, pp = l & 3;
#pragma unroll
    for (int i = 0; i < 2; ++i)
      wa[i] = 16384 * (1 + img) + (8 * h + q) * 256 + (((4 * wf + 2 * i + (pp >> 1)) ^ (4 * q + 2 * img)) << 4) + 8 * (pp & 1);
  }
  u32x4 sel;
#pragma unroll
  for (int e = 0; e < 4; ++e) sel[e] = l15 == 2 * e ? 0x00003f80u : (l15 == 2 * e + 1 ? 0x3f800000u : 0u);
  u32 a_cw = lds0 + 16384 * (1 + (w >> 2)) + (4 * g + (l15 >> 2)) * 256 + (((4 * (w & 3) + (l & 3)) ^ (4 * (l15 >> 2) + 2 * (w >> 2))) << 4);
  u32 o_cq = (l < 32 && l15 >= 12) ? (l & 3) * 32 + g * 16 : OOB;
  u32 o_l0 = l == 0 ? 0 : OOB;

  u32 o_cs = g * 1024 + (l15 >> 3) * 512 + (32 * wf + 4 * (l15 & 7)) * 4;
  u32 a_cs = lds0 + M_CS + w * 256 + (4 * l15 + 2 * h + (g & 1)) * 4;
  PIN(a_cs);
  u32 a_cv = lds0 + M_CS + w * 256 + h * 16;
  PIN(sel); PIN(a_cw); PIN(o_cq); PIN(o_l0); PIN(o_cs); PIN(a_cv); PIN(p0off); PIN(uo1);
#pragma unroll
  for (int e = 0; e < 8; ++e) PIN(ka_[e]);
#pragma unroll
  for (int e = 0; e < 4; ++e) PIN(xa[e]);
  int wa1[2];
#pragma unroll
  for (int e = 0; e < 2; ++e) { wa1[e] = wa[e] + G1_SLOT; PIN(wa1[e]); }
  int xa2[4], wa2[2];
#pragma unroll
  for (int e = 0; e < 4; ++e) { xa2[e] = xa[e] + 2 * G1_SLOT; PIN(xa2[e]); }
#pragma unroll
  for (int e = 0; e < 2; ++e) { wa2[e] = wa[e] + 2 * G1_SLOT - 16384; wa[e] -= 16384; PIN(wa2[e]); PIN(wa[e]); }
  SCHED();
  if (g1 && w >= 4) {
#pragma unroll
    for (int i = 0; i < 6; ++i) { dmaw(IC<1>{}, i, w - 4); dmaw(IC<1>{}, i, w); }
    asm volatile("s_waitcnt vmcnt(12)" : "+v"(wr[0]), "+v"(wr[1]), "+v"(wr[2]), "+v"(wr[3]), "+v"(wr[4]), "+v"(wr[5]), "+v"(wr[6]), "+v"(wr[7]) :: "memory");
    PIN(pf);
#pragma unroll
    for (int c = 0; c < 2; ++c) {
      u32x4 o;
#pragma unroll
      for (int e = 0; e < 4; ++e) o[e] = __builtin_amdgcn_perm(wr[2 * e + 1], wr[2 * e], c ? 0x07060302u : 0x05040100u);
      st128s(o, rs, l * 16 + c * 1024, WS_W2F + (((un2 * 112 + ukb) * 2) << 10));
    }
  }
  asm volatile("s_waitcnt lgkmcnt(0)" : "+s"(tag), "+s"(p_ldt2), "+s"(dk) :: "memory");
  tag.x += 1u;

  if (!g1) {
    asm volatile("s_waitcnt vmcnt(0)" : "+v"(xr[0]), "+v"(xr[1]), "+v"(xr[2]), "+v"(xr[3]), "+v"(xr[4]), "+v"(xr[5]), "+v"(xr[6]), "+v"(xr[7]) :: "memory");
    PIN(pf);
    f32x2 v[8][4];
    float ps[8];
#pragma unroll
    for (int r = 0; r < 8; ++r) {
#pragma unroll
      for (int j = 0; j < 4; ++j) v[r][j] = up2(xr[r][j]);
      const f32x2 s2 = (v[r][0] + v[r][1]) + (v[r][2] + v[r][3]);
      ps[r] = s2.x + s2.y;
    }
    const float mean = rows8_sum(ps, l) * (1.f / D);
#pragma unroll
    for (int r = 0; r < 8; ++r) {
      const float mr = rdl(mean, 8 * r);
      f32x2 q2 = {0.f, 0.f};
#pragma unroll
      for (int j = 0; j < 4; ++j) { v[r][j] -= f32x2{mr, mr}; q2 = fma2(v[r][j], v[r][j], q2); }
      ps[r] = q2.x + q2.y;
    }
    const float var = rows8_sum(ps, l) * (1.f / D);
    const u32 ms = pk2(mean, __builtin_sqrtf(rb(rb(var) + 1e-5f)));
    if ((l & 7) == 0) st32s(ms, rs, (l >> 3) * 4, WS_ST + (64 * ax + 8 * w) * 4);
    const float riv = 1.f / bhi(ms), nbv = (mean - b2f(ms)) * riv;
#pragma unroll
    for (int r = 0; r < 8; ++r) {
      const float ri = rdl(riv, 8 * r), nb = rdl(nbv, 8 * r);
#pragma unroll
      for (int j = 0; j < 4; ++j) v[r][j] = fma2(v[r][j], f32x2{ri, ri}, f32x2{nb, nb});
    }
#pragma unroll
    for (int c = 0; c < 8; ++c) {
      u32x4 o;
#pragma unroll
      for (int e = 0; e < 4; ++e) o[e] = pk2(v[7 - 2 * e][c >> 1][c & 1], v[6 - 2 * e][c >> 1][c & 1]);
      *(LAS u32x4*)(lds + (8 * l + c) * 128 + (((7 - w) ^ (l & 7)) << 4)) = o;
    }
    LBAR();
    u32x4 tv[8];
#pragma unroll
    for (int it = 0; it < 8; ++it) tv[it] = lds128(lds + w * 8192 + it * 1024 + l * 16);
#pragma unroll
    for (int it = 0; it < 8; ++it)
      st128s(tv[it], rs, (8 * it + (l >> 3)) * 4096 + (((l & 7) ^ it) << 4), WS_XNR + w * (64 * 4096) + (L - 64 - 64 * ax) * 2);
    WAIT_V(0);
    LBAR();
    if (tid == 0) st64s(tag, rs, 0, FL_P0 + b * FS);
    STAMP(3);
  }

  if (w < 4) {
    LAS char* tw = lds + KG_T + w * KG_TW;
    u32 ta[16];
#pragma unroll
    for (int k = 0; k < 16; ++k) { ta[k] = ((u32)(size_t)tw + l * 4) ^ (k << 4); PIN(ta[k]); }
    float dt = rb(__expf((w >> 1) ? bhi(p_ldt2) : b2f(p_ldt2)));
    PIN(dt);
    asm volatile("s_waitcnt vmcnt(0)" : "+v"(p_lar), "+v"(p_aim), "+v"(p_cre), "+v"(p_cim) :: "memory");
    STAMP(1);
    const float ar = -rb(__expf(b2f(p_lar)));
    const float lre2 = (ar * dt) * 1.44269504f, rev = (b2f(p_aim) * dt) * 0.15915494309f;
    u32 eb = wave_max_u32(max(p_cre & 0x7f80u, p_cim & 0x7f80u)) << 16;
    eb = eb < 0x00800000u ? 0x00800000u : (eb > 0x7e800000u ? 0x7e800000u : eb);
    const float sc = __builtin_bit_cast(float, eb) * (2.f * dt), isc = __builtin_bit_cast(float, 0x7f000000u - eb);
    f32x2 e[53], tmp[8];
    u32 eh[53];
    const f32x2 w8 = cexp_p(lre2, rev, 8.f), w64 = cexp_p(lre2, rev, 64.f);
    const f32x2 z1 = cexp_p(lre2, -rev, 1.f), zc = cexp_p(lre2, -rev, 256.f);
    e[0] = f32x2{1.f, 0.f}; e[1] = w8; e[8] = w64; e[32] = f32x2{b2f(p_cre) * isc, -b2f(p_cim) * isc};
#define CM1(k, a, bb) tmp[k] = (a) * f32x2{(bb).x, (bb).x}; SCHED()
#define CM2(k, d, a, bn) d = fma2(f32x2{(a).y, (a).x}, bn, tmp[k]); SCHED()
#define CV(i) eh[i] = pkh(e[i]); SCHED()
#define WR(i) *(LAS u32*)(size_t)(ta[(i) & 15] + (i) * 256) = eh[i]; SCHED()
    const f32x2 w8n = cneg(w8), w64n = cneg(w64), z1n = cneg(z1), zcn = cneg(zc);
    f32x2 z2, z4, zd, zq;
    SCHED();
    CM1(0, w8, w8); CM1(1, z1, z1); CM1(2, zc, zc); CM1(3, e[32], z1); CM1(4, e[32], zc); CM1(5, w8, w64); CM1(6, w64, w64);
    CV(0); CV(1); CV(8); CV(32);
    CM2(0, e[2], w8, w8n); WR(0); CM2(1, z2, z1, z1n); CM2(2, zd, zc, zcn); WR(1); CM2(3, e[33], e[32], z1n); CM2(4, e[40], e[32], zcn); WR(8);
    CM2(5, e[9], w8, w64n); CM2(6, e[16], w64, w64n); WR(32);
    const f32x2 e2n = cneg(e[2]), z2n = cneg(z2), zdn = cneg(zd);
    SCHED();
    CV(2); CV(33); CV(40); CV(9); CV(16);
    CM1(0, e[2], w8); CM1(1, e[2], e[2]); WR(2); CM1(2, z2, z2); CM1(3, zd, zd); CM1(4, e[32], z2); WR(33); CM1(5, e[33], z2); CM1(6, e[32], zd); CM1(7, e[40], zd); WR(40);
    CM2(0, e[3], e[2], w8n); CM2(1, e[4], e[2], e2n); CM2(2, z4, z2, z2n); WR(9); CM2(3, zq, zd, zdn); CM2(4, e[34], e[32], z2n);
    CM2(5, e[35], e[33], z2n); WR(16); CM2(6, e[44], e[32], zdn); CM2(7, e[46], e[40], zdn);
    const f32x2 e4n = cneg(e[4]), z4n = cneg(z4), zqn = cneg(zq);
    SCHED();
    CV(3); CV(4); CV(34); CV(35); CV(44); CV(46);
    CM1(0, e[1], e[4]); CM1(1, e[2], e[4]); WR(3); CM1(2, e[3], e[4]); CM1(3, e[32], z4); CM1(4, e[33], z4); WR(4); CM1(5, e[34], z4); CM1(6, e[35], z4); CM1(7, e[32], zq); WR(34);
    CM2(0, e[5], e[1], e4n); CM2(1, e[6], e[2], e4n); WR(35); CM2(2, e[7], e[3], e4n); CM2(3, e[36], e[32], z4n); CM2(4, e[37], e[33], z4n); WR(44);
    CM2(5, e[38], e[34], z4n); CM2(6, e[39], e[35], z4n); CM2(7, e[48], e[32], zqn); WR(46);
    CV(5); CV(6); CV(7); WR(5); CV(36); CV(37); CV(38); WR(6); CV(39); CV(48);
    CM1(0, e[34], zc); WR(7); CM1(1, e[36], zc); CM1(2, e[38], zc); CM1(3, e[36], zd); WR(36); CM1(4, e[40], zq); CM1(5, e[44], zq); CM1(6, e[46], zq); WR(37); CM1(7, e[48], zq);
    CM2(0, e[41], e[34], zcn); CM2(1, e[42], e[36], zcn); WR(38); CM2(2, e[43], e[38], zcn); CM2(3, e[45], e[36], zdn); CM2(4, e[49], e[40], zqn); WR(39);
    CM2(5, e[50], e[44], zqn); CM2(6, e[51], e[46], zqn); CM2(7, e[52], e[48], zqn); WR(48);
    CV(41); CV(42); CV(43); WR(41); CV(45); CV(49); CV(50); WR(42); CV(51); CV(52);
    CM1(0, e[42], zd); WR(43); CM1(1, e[9], w64); CM1(2, e[2], w64); CM1(3, e[3], w64); WR(45); CM1(4, e[4], w64); CM1(5, e[5], w64); CM1(6, e[6], w64); WR(49); CM1(7, e[7], w64);
    CM2(0, e[47], e[42], zdn); CM2(1, e[17], e[9], w64n); WR(50); CM2(2, e[10], e[2], w64n); CM2(3, e[11], e[3], w64n); CM2(4, e[12], e[4], w64n); WR(51);
    CM2(5, e[13], e[5], w64n); CM2(6, e[14], e[6], w64n); CM2(7, e[15], e[7], w64n); WR(52);
    CV(47); CV(17); CV(10); WR(47); CV(11); CV(12); CV(13); WR(17); CV(14); CV(15);
    CM1(0, e[16], w64); WR(10); CM1(1, e[17], w64); CM1(2, e[10], w64); CM1(3, e[11], w64); WR(11); CM1(4, e[12], w64); CM1(5, e[13], w64); CM1(6, e[14], w64); WR(12); CM1(7, e[15], w64);
    CM2(0, e[24], e[16], w64n); CM2(1, e[25], e[17], w64n); WR(13); CM2(2, e[18], e[10], w64n); CM2(3, e[19], e[11], w64n); CM2(4, e[20], e[12], w64n); WR(14);
    CM2(5, e[21], e[13], w64n); CM2(6, e[22], e[14], w64n); CM2(7, e[23], e[15], w64n); WR(15);
    CV(24); CV(25); CV(18); WR(24); CV(19); CV(20); CV(21); WR(25); CV(22); CV(23);
    CM1(2, e[18], w64); WR(18); CM1(3, e[19], w64); CM1(4, e[20], w64); WR(19); CM1(5, e[21], w64); CM1(6, e[22], w64); WR(20); CM1(7, e[23], w64);
    CM2(2, e[26], e[18], w64n); WR(21); CM2(3, e[27], e[19], w64n); CM2(4, e[28], e[20], w64n); WR(22); CM2(5, e[29], e[21], w64n);
    CM2(6, e[30], e[22], w64n); WR(23); CM2(7, e[31], e[23], w64n);
    CV(26); CV(27); WR(26); CV(28); WR(27); CV(29); WR(28); CV(30); WR(29); CV(31); WR(30); WR(31);
    u32x4 fm[8], fv[8];
#pragma unroll
    for (int s = 0; s < 8; ++s) fm[s] = lds128(tw + (32 + t) * 256 + (((2 * s + h) ^ l15) << 4));
#pragma unroll
    for (int s = 0; s < 8; ++s) fv[s] = lds128(tw + t * 256 + (((2 * s + h) ^ l15) << 4));
    SCHED();
    SCHED();
    STAMP(4);
    f32x16 kk;
#pragma unroll
    for (int r = 0; r < 16; ++r) kk[r] = 0.f;
#pragma unroll
    for (int s = 0; s < 8; ++s) kk = mfma32h(fm[s], fv[s], kk);
    LAS float* kc = (LAS float*)(lds + M_KC + w * 2816);
    *(LAS f32x4*)(kc + 8 * t + 4 * h) = f32x4{kk[0], kk[1], kk[2], kk[3]} * sc;
    *(LAS f32x2*)(kc + (h ? 384 + 2 * t : 256 + 4 * t)) = f32x2{kk[4], kk[5]} * sc;
    *(LAS f32x2*)(kc + (h ? 448 + 2 * t : 258 + 4 * t)) = f32x2{kk[6], kk[7]} * sc;
    LAS float* k3 = kc + (h ? 640 + t : 512 + t);
    k3[0] = kk[8] * sc;
    if (h == 0) { k3[32] = kk[9] * sc; k3[64] = kk[10] * sc; k3[96] = kk[11] * sc; }
  }
  STAMP(5);

  u32x4 wfr[28];
  const int rot = 0, jsw = 0;
  auto kstep = [&](int k) { const int kk = k + rot; return kk >= 14 ? kk - 14 : kk; };
  const int w2off = WS_W2F + (((n2 * 112 + 14 * w) * 2) << 10);
  auto w2ld = [&](int i) { wfr[i] = ld128s(rs, l * 16 + (i & 1) * 1024, w2off + kstep(i >> 1) * 2048); };
  f32x2 kx[8];
  u32 kpk[4];
  auto expand_rd = [&](int e) { LAS float* q = (LAS float*)(size_t)ka_[e]; kx[e] = f32x2{q[0], q[704]}; };
  auto expand_pk = [&](int e) {
    float a = kx[2 * e].x + kx[2 * e].y, c = kx[2 * e + 1].x + kx[2 * e + 1].y;
    PIN(a); PIN(c);
    kpk[e] = pk2(a, c);
    PIN(kpk[e]);
  };
  auto expand_wr = [&]() { *(LAS u32x4*)(lds + CV_K + uch * 5120 + 1024 + ui * 16) = u32x4{kpk[0], kpk[1], kpk[2], kpk[3]}; };
  u32x4 u0;
  u32x2 u1;
  auto load_u = [&](int k) {
    if (k == 0) u0 = ld128s(rs, ui * 16, WS_XNR + (2 * b + uch) * 4096);
    else u1 = ld64s(rs, uo1, WS_XNR + (2 * b + uch) * 4096);
  };
  auto copy_u = [&](int s) {
    const u32 dd[6] = {u0.x, u0.y, u0.z, u0.w, u1.x, u1.y};
    const int o = s >> 1;
    u32x4 v;
#pragma unroll
    for (int k = 0; k < 4; ++k) v[k] = (s & 1) ? __builtin_amdgcn_alignbit(dd[o + k + 1], dd[o + k], 16) : dd[o + k];
    *(LAS u32x4*)(lds + CV_U + uch * CV_UCH + s * CV_CS + ui * 16) = v;
  };
#define W2Q 97
#define NW2AV 13
  constexpr int NW2A = NW2AV;

  if (g1) {
    f32x16 acc[2][2];
#pragma unroll
    for (int i = 0; i < 2; ++i)
#pragma unroll
      for (int j = 0; j < 2; ++j)
#pragma unroll
        for (int r = 0; r < 16; ++r) acc[i][j][r] = 0.f;
    u32x4 fa[2][2], fb[2][2];
    auto rdA = [&](int st, int ks, int i) {
      LAS char* q = lds + (st % 3 == 0 ? wa[i] + 16384 : st % 3 == 1 ? wa1[i] : wa2[i] + 16384) + ks * 4096;
      const u32x2 lo = ldstr(q), hi = ldstr(q + 1024);
      fa[ks & 1][i] = u32x4{lo.x, lo.y, hi.x, hi.y};
    };
    auto rdB = [&](int st, int ks, int j) { fb[ks & 1][j] = lds128(lds + (st % 3 == 2 ? xa2[ks] : xa[ks] + (st % 3) * G1_SLOT) + j * 4096); };
    const int so_cs = (mt & 2) == 0 ? WS_CS + (ft * 8 + kt0) * 1024 + w * 128 : OOB;
    const int so_p0 = w == 0 ? FL_P0 + b * FS : OOB;

    LBAR();
    STAMP(6);
    rdA(0, 0, 0); rdB(0, 0, 0); rdB(0, 0, 1); rdA(0, 0, 1);
    dmaw(IC<2>{}, 0, w); dmaw(IC<2>{}, 1, w);
    u32x2 fl;
    u32 msv[2];
    f32x4 csp[2];
    u32x4 cw[4];
    f32x4 cs, cv[8];
    static_for<8>([&](auto st_) {
      constexpr int st = decltype(st_)::v;
#pragma unroll
      for (int ks = 0; ks < 4; ++ks) {
        const int nst = ks == 3 ? st + 1 : st, nks = (ks + 1) & 3;
        if (ks == 3 && st < 7) {
          if (st == 1) WAIT_V(7);
          else if ((st == 3 && QF < 60) || st == 4) WAIT_V(7);
          else if (st == 6) { if (NW2A >= 6 && W2Q == 97) WAIT_V(11); else WAIT_V(5); }
          else WAIT_V(6);
          LBAR();
          if (st == 2) STAMP(18);
          if (st == 5) STAMP(21);
        }
        if (st == 7 && ks == 2) LBAR();
        SCHED();
#pragma unroll
        for (int n = 0; n < 4; ++n) {
          acc[n >> 1][n & 1] = mfma32(fa[ks & 1][n >> 1], fb[ks & 1][n & 1], acc[n >> 1][n & 1]);
          SCHED();
          const int pc = ks == 3 ? (st + 3 < 8 && n >= 2 ? n - 2 : -1) : (st + 2 < 8 ? (n == 3 ? (ks == 0 ? 2 : ks == 1 ? 4 : 5) : (n == 2 && ks == 1) ? 3 : -1) : -1);
          if (pc >= 0) { if (ks == 3) dgo(IC<(st + 3) & 7>{}, pc, w); else dgo(IC<(st + 2) & 7>{}, pc, w); }
          const int pn = ks == 3 ? (st + 3 < 8 && (n == 1 || n == 2) ? n - 1 : -1) : (st + 2 < 8 ? (n == 2 ? (ks == 0 ? 2 : ks == 1 ? 4 : 5) : (n == 1 && ks == 1) ? 3 : -1) : -1);
          if (pn >= 0) { if (ks == 3) dm0(IC<(st + 3) & 7>{}, pn, w); else dm0(IC<(st + 2) & 7>{}, pn, w); }
          if (nst < 8) {
            if (n == 0) { rdA(nst, nks, 1); rdB(nst, nks, 1); }
            if (n == 1) { rdA(nst, nks, 0); rdB(nst, nks, 0); }
          }
          SCHED();
          const int q = st * 16 + ks * 4 + n;
          if (q < 4) cw[q] = lds128((LAS char*)(size_t)a_cw + q * 4096);
          if (q == 5) cs = mfma16(sel, cw[0], f32x4{0.f, 0.f, 0.f, 0.f});
          if (q >= 6 && q < 9) cs = mfma16(sel, cw[q - 5], cs);
          if (q == 19) {
#pragma unroll
            for (int e = 0; e < 4; ++e) { const float v = cs[e]; cs[e] = v + dppx<0x114>(v); }
            PIN(cs);
          }
          if (q == 21) {
#pragma unroll
            for (int e = 0; e < 4; ++e) { const float v = cs[e]; cs[e] = v + dppx<0x118>(v); }
            PIN(cs);
          }
          if (q == 24) st128s(__builtin_bit_cast(u32x4, cs), rs, o_cq, so_cs);
          if (q >= 30 && q < 46 && (q & 2)) expand_rd(((q - 30) >> 2) * 2 + (q & 1));
          if (q >= 50 && q < 66 && (q & 3) == 2) { PIN(kx[(q - 50) >> 1]); expand_pk((q - 50) >> 2); }
          if (q == QW) expand_wr();
          if (q == QF) st64s(tag, rs, o_l0, so_p0);
          if (q == 75) asm volatile("global_load_dwordx2 %0, %1, %2 sc1" : "=v"(fl) : "v"(p0off), "s"(p.ws + FL_P0) : "memory");
          if (q == 89) {
            asm volatile("s_waitcnt vmcnt(5)" : "+v"(fl) :: "memory");
            if (!__all(fl.x == tag.x && fl.y == tag.y)) poll(rs, p0off, FL_P0, tag);
          }
          if (q == 92 || q == 93) load_u(q - 92);
          if (q == 94 || q == 95) csp[q - 94] = __builtin_bit_cast(f32x4, ld128s(rs, o_cs, WS_CS + (ft * 8 + 4 * (q - 94)) * 1024));
          if (q == 96) msv[0] = ld32s(rs, l * 4, WS_ST + (mt * 128 + 64 * wt) * 4);
          if (q >= W2Q && q < W2Q + 2 * NW2A && ((q - W2Q) & 1) == 0) w2ld((q - W2Q) >> 1);
          if (q == 106 || q == 110 || q == 114 || q == 118) copy_u((q - 106) >> 2);
          if (q == 112) {
            PIN(csp[0]); PIN(csp[1]);
            csp[0] += csp[1];
            const auto r0 = __builtin_amdgcn_permlane32_swap(f2u(csp[0][0]), f2u(csp[0][2]), false, false);
            const auto r1 = __builtin_amdgcn_permlane32_swap(f2u(csp[0][1]), f2u(csp[0][3]), false, false);
            csp[0][0] = __builtin_bit_cast(float, (u32)r0[0]) + __builtin_bit_cast(float, (u32)r0[1]);
            csp[0][1] = __builtin_bit_cast(float, (u32)r1[0]) + __builtin_bit_cast(float, (u32)r1[1]);
            PIN(csp[0]);
          }
          if (q == 116) {
            const auto r = __builtin_amdgcn_permlane16_swap(f2u(csp[0][0]), f2u(csp[0][1]), false, false);
            *(LAS float*)(size_t)a_cs = __builtin_bit_cast(float, (u32)r[0]) + __builtin_bit_cast(float, (u32)r[1]);
            PIN(msv[0]);
            const auto m = __builtin_amdgcn_permlane32_swap(msv[0], msv[0], false, false);
            msv[0] = m[0]; msv[1] = m[1];
            PIN(msv[0]); PIN(msv[1]);
          }
          if (q >= 120 && q < 124) {
#pragma unroll
            for (int kk = 2 * (q - 120); kk < 2 * (q - 120) + 2; ++kk) {
              const int k = kk ^ 4;
              cv[k] = *(LAS f32x4*)((LAS char*)(size_t)a_cv + ((k >> 1) & 1) * 128 + (k >> 2) * 64 + (k & 1) * 32);
            }
          }
          if (q >= 124) {
            const int m = (n + 2) & 3, ii = m >> 1, jj = m & 1;
            const float nm = -b2f(msv[jj]);
#pragma unroll
            for (int r = 0; r < 16; ++r) acc[ii][jj][r] = __builtin_fmaf(nm, cv[4 * ii + (r >> 2)][r & 3], acc[ii][jj][r]);
          }
          SCHED();
        }
      }
    });
    STAMP(7);
    __builtin_amdgcn_s_setprio(3);
#pragma unroll
    for (int j = 0; j < 2; ++j) {
      const float sd = bhi(msv[j]), sd2 = sd * sd;
      const float ce = -1.44269504f * __builtin_amdgcn_rcpf(sd);
#pragma unroll
      for (int i = 0; i < 2; ++i) {
        u32x4 o;
#pragma unroll
        for (int r = 0; r < 4; ++r) {
          const f32x2 gg = {acc[i][j][2 * r], acc[i][j][2 * r + 1]};
          const f32x2 uu = {acc[i][j][8 + 2 * r], acc[i][j][9 + 2 * r]};
          const f32x2 a = gg * f32x2{ce, ce};
          const f32x2 e = {__builtin_amdgcn_exp2f(a.x), __builtin_amdgcn_exp2f(a.y)};
          const f32x2 d = fma2(e, f32x2{sd2, sd2}, f32x2{sd2, sd2});
          const f32x2 q = {__builtin_amdgcn_rcpf(d.x), __builtin_amdgcn_rcpf(d.y)};
          o[r] = pk2v((gg * uu) * q);
        }
        st128s(o, rs, l * 16, WS_H + (((mt * 4 + 2 * wt + j) * 112 + ft * 8 + 2 * wf + i) << 10));
        SCHED();
      }
    }
    __builtin_amdgcn_s_setprio(1);
    STAMP(8);
  } else {
    poll(rs, p0off, FL_P0, tag);
    if (b == 255 && w == 0) {
#pragma unroll 1
      for (int i = 0; i < 4; ++i) poll(rs, (64 * i + l) * FS, FL_P0, tag);
      if (l == 0) st64s(tag, rs, 0, FL_EP);
    }
    LBAR();
    load_u(0); load_u(1);
#pragma unroll
    for (int e = 0; e < 8; ++e) expand_rd(e);
#pragma unroll
    for (int e = 0; e < 4; ++e) expand_pk(e);
    LBAR();
    expand_wr();
#pragma unroll
    for (int s = 0; s < 4; ++s) copy_u(s);
    SCHED();
#pragma unroll
    for (int i = 0; i < NW2A; ++i) w2ld(i);
    __builtin_amdgcn_s_waitcnt(0x0F70);
    LBAR();
  }

  STAMP(10);
  u32x2 hfl;
  u32x4 hf[28];
  const int hoff = WS_H + ((2 * m2 * 112 + 14 * w) << 10);
  auto hld = [&](int n) { hf[n] = ld128s(rs, l * 16, hoff + (((n / 14) ^ jsw) * 112 + kstep(n % 14)) * 1024); };
  const u32 hfo = (l < 7 ? l : 0) * FS;
  const int hfs = FL_H + (m2 * 56 + 7 * w) * FS;
#define PDV 12
  constexpr int PD = PDV;
  {
    const int ch = w & 1, ts = (w >> 1) & 1, rh = w >> 2;
    LAS char* ua = opaque(lds + CV_U + ch * CV_UCH + (3 - (l15 & 3)) * CV_CS + 4088 - 8 * (l15 >> 2) - 32 * (g >> 1) + 16 * (g & 1)
                          - 7 * 512 - 256 * rh - 3 * 64);
    LAS char* ka = opaque(lds + CV_K + ch * 5120 + 1024 - 512 * ts + 32 * (l15 - (g >> 1)) + 16 * (g & 1) - 256 * rh - 3 * 64);
    *(LAS u32x4*)(lds + CV_K + ch * 5120 + l * 16) = u32x4{0u, 0u, 0u, 0u};
    *(LAS u32x4*)(lds + CV_U + ch * CV_UCH + ((l >> 1) & 3) * CV_CS + 4096 + (l & 1) * 16) = u32x4{0u, 0u, 0u, 0u};
    asm volatile("" ::: "memory");
    f32x4 acc[4];
#pragma unroll
    for (int i = 0; i < 4; ++i) acc[i] = f32x4{0.f, 0.f, 0.f, 0.f};
    const int so_h = g1 ? FL_H + ((mt * 2 + wt) * 56 + ft * 4 + wf) * FS : OOB;
    auto hook = [&](int rr, int c) {
#define HPUB 3
#define HPOLL 24
      const int sq = rr * 8 + c;
      if (sq == HPUB) {
        WAIT_V(0);
        st64s(tag, rs, o_l0, so_h);
        __builtin_amdgcn_s_setprio(0);
        STAMP(9);
      }
      if (sq > HPUB && sq <= HPUB + 28 - NW2A) w2ld(NW2A + sq - HPUB - 1);
      if (sq == (HPOLL < 32 ? HPOLL - 5 : 31)) hfl = ld64s(rs, hfo, hfs);
      if (sq == HPOLL) {
        STAMP(2);
        if (!__all(hfl.x == tag.x && hfl.y == tag.y)) poll(rs, hfo, hfs, tag);
        asm volatile("" ::: "memory");
        STAMP(23);
      }
      if (sq >= HPOLL) {
#pragma unroll
        for (int n = (sq - HPOLL) * PD / (HPOLL < 32 ? 32 - HPOLL : 1); n < (sq + 1 - HPOLL) * PD / (HPOLL < 32 ? 32 - HPOLL : 1); ++n) hld(n);
      }
    };
    conv_tiles(ua, ka, acc, hook);
#pragma unroll
    for (int i = 0; i < 4; ++i) *(LAS f32x4*)(lds + CV_X + (w * 4 + i) * 1024 + l * 16) = acc[i];
  }
  LBAR();
  STAMP(11);
  int ytile;
  {
    const int ts = (w >> 1) & 1, i = (w & 1) + 2 * (w >> 2);
    ytile = 7 - 2 * i - ts;
    const int pos = 256 * ytile + 16 * l15 + 4 * g;
    f32x4 y0[2], y1[2];
    u32x2 ur[2];
#pragma unroll
    for (int ch = 0; ch < 2; ++ch) {
      y0[ch] = *(LAS f32x4*)(lds + CV_X + ((ch + 2 * ts) * 4 + i) * 1024 + l * 16);
      y1[ch] = *(LAS f32x4*)(lds + CV_X + ((ch + 2 * ts + 4) * 4 + i) * 1024 + l * 16);
      ur[ch] = lds64(lds + CV_U + ch * CV_UCH + (2044 - pos) * 2);
    }
    LBAR();
    float res[2][4];
#pragma unroll
    for (int ch = 0; ch < 2; ++ch) {
      const float dsk = ch ? bhi(dk) : b2f(dk);
      const f32x4 y = y0[ch] + y1[ch];
      res[ch][0] = __builtin_fmaf(bhi(ur[ch].y), dsk, y[0]); res[ch][1] = __builtin_fmaf(b2f(ur[ch].y), dsk, y[1]);
      res[ch][2] = __builtin_fmaf(bhi(ur[ch].x), dsk, y[2]); res[ch][3] = __builtin_fmaf(b2f(ur[ch].x), dsk, y[3]);
    }
    const u32x4 o = {pk2(res[0][0], res[1][0]), pk2(res[0][1], res[1][1]), pk2(res[0][2], res[1][2]), pk2(res[0][3], res[1][3])};
    st64s(u32x2{mt0, mtp}, rs, o_l0, w == 7 ? WS_ST + 524288 + b * 128 : OOB);
    st128s(o, rs, pos * 4, WS_Y + b * 8192);
    if (HPOLL == 32) {
      STAMP(2);
      if (!__all(hfl.x == tag.x && hfl.y == tag.y)) poll(rs, hfo, hfs, tag);
      asm volatile("" ::: "memory");
      STAMP(23);
#pragma unroll
      for (int n = 0; n < PD; ++n) hld(n);
    }
  }
  STAMP(12);

  {
    const int oj = w >> 2, oq = w & 3;
    const int etok = 64 * m2 + 32 * (oj ^ jsw) + t, ech = 64 * n2 + 16 * oq + 8 * h;
    u32x4 xv;
    SCHED();
    f32x16 acc[2][2];
#pragma unroll
    for (int i = 0; i < 2; ++i)
#pragma unroll
      for (int j = 0; j < 2; ++j)
#pragma unroll
        for (int r = 0; r < 16; ++r) acc[i][j][r] = 0.f;
    u32x2 yfl;
    const int yfo = FL_Y + ((m2 >> 2) * 256 + 32 * n2 + 8 * oq) * FS;
    LAS char* pw = opaque(lds + w * 16384 + l * 16);
    auto part_wr = [&](int j, int k) {
      const int i = k & 1, qd = k >> 1;
      *(LAS f32x4*)(pw + (j * 8 + k) * 1024) = f32x4{acc[i][j][4 * qd], acc[i][j][4 * qd + 1], acc[i][j][4 * qd + 2], acc[i][j][4 * qd + 3]};
    };
#pragma unroll
    for (int n = 0; n < 28; ++n) {
#pragma unroll
      for (int i = 0; i < 2; ++i) acc[i][n / 14] = mfma32(wfr[2 * (n % 14) + i], hf[n], acc[i][n / 14]);
      SCHED();
      if (n + PD < 28) hld(n + PD);
      if (n == YP1 && w >= 4) { if (HPOLL < 32) WAIT_V(2); else WAIT_V(14); st64s(tag, rs, o_l0, FL_Y + (ytile * 256 + b) * FS); }
      if (n == YP0 && w < 4) { WAIT_V(11); st64s(tag, rs, o_l0, FL_Y + (ytile * 256 + b) * FS); }
      if (n == 21) xv = *(const GAS u32x4*)((const char*)p.x + (size_t)etok * 1024 + ech * 2);
      if (n == 22) yfl = ld64s(rs, (l & 7) * FS, yfo);
      if (n >= 16 && n < 24) part_wr(0, n - 16);
      SCHED();
    }
    STAMP(13);
    if (!__all(yfl.x == tag.x && yfl.y == tag.y)) poll(rs, (l & 7) * FS, yfo, tag);
    asm volatile("" ::: "memory");
    u32 yv[4];
#pragma unroll
    for (int j = 0; j < 4; ++j) yv[j] = ld32s(rs, ((ech >> 1) + j) * 8192 + etok * 4, WS_Y);
#pragma unroll
    for (int k = 0; k < 8; ++k) part_wr(1, k);
    LBAR();
    STAMP(14);
    LAS char* pr = opaque(lds + w * 2048 + l * 16);
    LAS char* pr2 = opaque(pr + 65536);
    f32x4 pp[8][2];
#pragma unroll
    for (int ww = 0; ww < 8; ++ww)
#pragma unroll
      for (int i = 0; i < 2; ++i) pp[ww][i] = *(LAS f32x4*)((ww < 4 ? pr : pr2) + (ww & 3) * 16384 + i * 1024);
    SCHED();
    f32x4 sm[2];
#pragma unroll
    for (int i = 0; i < 2; ++i) sm[i] = ((pp[0][i] + pp[1][i]) + (pp[2][i] + pp[3][i])) + ((pp[4][i] + pp[5][i]) + (pp[6][i] + pp[7][i]));
    u32x4 r;
#pragma unroll
    for (int j = 0; j < 4; ++j) {
      const u32 s = pk2v(up2(xv[j]) + up2(yv[j]));
      r[j] = pk2v(up2(s) + up2(pk2(sm[0][j], sm[1][j])));
    }
    *(GAS u32x4*)((char*)p.out + (size_t)etok * 1024 + ech * 2) = r;
  }
  if (b == 255 && w == 0) {
    u32 lo = 0, hi = 0;
#pragma unroll 1
    for (int i = 0; i < 4; ++i) {
      poll(rs, (64 * i + l) * FS, FL_Y, tag);
      const u32x2 v = ld64s(rs, (64 * i + l) * 128, WS_ST + 524288);
      lo = max(lo, 0x10000u - (v.x - mt0)); hi = max(hi, 0x10000u + (v.y - mt0));
    }
    lo = wave_max_u32(lo); hi = wave_max_u32(hi);
    const u32 now = (u32)__builtin_amdgcn_s_memrealtime();
    u32 T = 0u * (lo + hi);
    T = min(T, 4000u);
    while ((u32)__builtin_amdgcn_s_memrealtime() - now < T) __builtin_amdgcn_s_sleep(1);
  }
  STAMP(15);
  STAMP_DUMP();
}

extern "C" void kernel_launch(void* const* d_in, const int* in_sizes, int n_in, void* d_out, int out_size, void* d_ws,
                              size_t ws_size, hipStream_t stream) {
  Params p;
  p.x = (const u16*)d_in[0]; p.lar = (const u16*)d_in[1]; p.aim = (const u16*)d_in[2]; p.cre = (const u16*)d_in[3];
  p.cim = (const u16*)d_in[4]; p.ldt = (const u16*)d_in[5]; p.dsk = (const u16*)d_in[6];
  p.w1 = (const u16*)d_in[8]; p.w3 = (const u16*)d_in[9]; p.w2 = (const u16*)d_in[10];
  p.out = (u16*)d_out; p.ws = (char*)d_ws;
  fused<<<256, 512, 0, stream>>>(p);
}
```

```cpp
#include <hip/hip_runtime.h>
#include <hip/hip_bf16.h>

typedef unsigned short u16;
typedef unsigned int u32;
typedef unsigned long long u64;
typedef __bf16 bf16x8 __attribute__((ext_vector_type(8)));
typedef __bf16 bf16x2 __attribute__((ext_vector_type(2)));
typedef _Float16 f16x8 __attribute__((ext_vector_type(8)));
typedef _Float16 f16x2 __attribute__((ext_vector_type(2)));
typedef short s16x4 __attribute__((ext_vector_type(4)));
typedef float f32x2 __attribute__((ext_vector_type(2)));
typedef float f32x4 __attribute__((ext_vector_type(4)));
typedef float f32x16 __attribute__((ext_vector_type(16)));
typedef u32 u32x2 __attribute__((ext_vector_type(2)));
typedef u32 u32x4 __attribute__((ext_vector_type(4)));
typedef __amdgpu_buffer_rsrc_t rsrc_t;
#define LAS __attribute__((address_space(3)))
#define GAS __attribute__((address_space(1)))
#define DEV __device__ __forceinline__

#define NTQ " nt"
#define TB 21
#define YP1 1
#define YP0 10
constexpr int L = 2048, D = 512, NM = 128, F = 1792;
constexpr int LDS_BYTES = 163840;
constexpr int SC1 = 16;

constexpr int WS_H = 0;
constexpr int WS_XNR = 8 << 20;
constexpr int WS_W2F = 10 << 20;
constexpr int WS_Y = 12 << 20;
constexpr int WS_ST = 14 << 20;
constexpr int WS_CS = WS_ST + 8192;
constexpr int WS_FL = 15 << 20;
constexpr int FS = 128;
constexpr int FL_EP = WS_FL;
constexpr int FL_P0 = WS_FL + 4096;
constexpr int FL_H = FL_P0 + 256 * FS;
constexpr int FL_Y = FL_H + 2048 * FS;
constexpr int WS_BYTES = 16 << 20;

struct Params {
  const u16 *x, *lar, *aim, *cre, *cim, *ldt, *dsk, *w1, *w3, *w2;
  u16* out;
  char* ws;
};

template <int V> struct IC { static constexpr int v = V; };
template <int N, int I = 0, class Fn>
DEV void static_for(Fn&& fn) { if constexpr (I < N) { fn(IC<I>{}); static_for<N, I + 1>(fn); } }
DEV u32 f2u(float f) { return __builtin_bit_cast(u32, f); }
DEV float u2f(u32 v) { return __builtin_bit_cast(float, v); }
DEV float b2f(u32 v) { return __builtin_bit_cast(float, v << 16); }
DEV float bhi(u32 v) { return __builtin_bit_cast(float, v & 0xffff0000u); }
DEV u32 pk2v(f32x2 f) { return __builtin_bit_cast(u32, __builtin_convertvector(f, bf16x2)); }
DEV u32 pk2(float lo, float hi) { return pk2v(f32x2{lo, hi}); }
DEV f32x2 up2(u32 v) { return f32x2{b2f(v), bhi(v)}; }
DEV float rb(float f) { return b2f(pk2(f, f) & 0xffffu); }
DEV f32x2 fma2(f32x2 a, f32x2 b, f32x2 c) { return __builtin_elementwise_fma(a, b, c); }
DEV f32x16 mfma32(u32x4 a, u32x4 b, f32x16 c) {
  return __builtin_amdgcn_mfma_f32_32x32x16_bf16(__builtin_bit_cast(bf16x8, a), __builtin_bit_cast(bf16x8, b), c, 0, 0, 0);
}
DEV f32x4 mfma16(u32x4 a, u32x4 b, f32x4 c) {
  return __builtin_amdgcn_mfma_f32_16x16x32_bf16(__builtin_bit_cast(bf16x8, a), __builtin_bit_cast(bf16x8, b), c, 0, 0, 0);
}
DEV f32x16 mfma32h(u32x4 a, u32x4 b, f32x16 c) {
  return __builtin_amdgcn_mfma_f32_32x32x16_f16(__builtin_bit_cast(f16x8, a), __builtin_bit_cast(f16x8, b), c, 0, 0, 0);
}
DEV u32x4 lds128(LAS char* p) { return *(LAS u32x4*)p; }
DEV u32x2 lds64(LAS char* p) { return *(LAS u32x2*)p; }
DEV u32x2 ldstr(LAS char* p) { return __builtin_bit_cast(u32x2, __builtin_amdgcn_ds_read_tr16_b64_v4i16((LAS s16x4*)p)); }
template <int LOFF>
DEV void dma_m0(u32 lbase) { asm volatile("s_add_u32 m0, %0, %1" :: "s"(lbase), "i"(LOFF) : "memory", "scc", "m0"); }
template <bool NT>
DEV void dma_go(u32x4 rs, u32 voff, u32 soff) {
  if (NT) asm volatile("buffer_load_dwordx4 %0, %1, %2 offen nt lds" :: "v"(voff), "s"(rs), "s"(soff) : "memory");
  else asm volatile("buffer_load_dwordx4 %0, %1, %2 offen lds" :: "v"(voff), "s"(rs), "s"(soff) : "memory");
}
DEV u32x4 mkrs(const void* ptr, u32 bytes) { const u64 a = (u64)ptr; return u32x4{(u32)a, (u32)(a >> 32) & 0xffffu, bytes, 0x00020000u}; }
#define WAIT_V(n) asm volatile("s_waitcnt vmcnt(" #n ")" ::: "memory")
#define WAIT_L0() asm volatile("s_waitcnt lgkmcnt(0)" ::: "memory")
#define BAR() __builtin_amdgcn_s_barrier()
#define LBAR() do { WAIT_L0(); BAR(); asm volatile("" ::: "memory"); } while (0)
#define SCHED() __builtin_amdgcn_sched_barrier(0)
#define PIN(x) asm volatile("" : "+v"(x))
constexpr int OOB = 0x04000000;
#define STAMP(i)
#define STAMP_DECL
#define STAMP_DUMP()

DEV u32x4 ld128s(rsrc_t rs, int voff, int soff) { return __builtin_amdgcn_raw_buffer_load_b128(rs, voff, soff, SC1); }
DEV u32x2 ld64s(rsrc_t rs, int voff, int soff) { return __builtin_amdgcn_raw_buffer_load_b64(rs, voff, soff, SC1); }
DEV u32 ld32s(rsrc_t rs, int voff, int soff) { return __builtin_amdgcn_raw_buffer_load_b32(rs, voff, soff, SC1); }
DEV void st128s(u32x4 v, rsrc_t rs, int voff, int soff) {
  __builtin_amdgcn_raw_buffer_store_b128(v, rs, voff, soff, SC1);
  asm volatile("s_nop 1" :: "v"(v) : "memory");
}
DEV void st64s(u32x2 v, rsrc_t rs, int voff, int soff) { __builtin_amdgcn_raw_buffer_store_b64(v, rs, voff, soff, SC1); }
DEV void st32s(u32 v, rsrc_t rs, int voff, int soff) { __builtin_amdgcn_raw_buffer_store_b32(v, rs, voff, soff, SC1); }
DEV void poll(rsrc_t rs, int voff, int soff, u32x2 tag) {
#pragma unroll 1
  for (int spin = 0; spin < (1 << 18); ++spin) {
    const u32x2 v = ld64s(rs, voff, soff);
    if (__all(v.x == tag.x && v.y == tag.y)) break;
    __builtin_amdgcn_s_sleep(4);
  }
  __builtin_amdgcn_fence(__ATOMIC_ACQUIRE, "wavefront");
  asm volatile("" ::: "memory");
}

template <int CTRL, int RM>
DEV u32 dppu(u32 v) { return (u32)__builtin_amdgcn_update_dpp(0, (int)v, CTRL, RM, 0xf, true); }
DEV u32 wave_max_u32(u32 v) {
  v = max(v, dppu<0x111, 0xf>(v)); v = max(v, dppu<0x112, 0xf>(v)); v = max(v, dppu<0x114, 0xf>(v)); v = max(v, dppu<0x118, 0xf>(v));
  v = max(v, dppu<0x142, 0xa>(v)); v = max(v, dppu<0x143, 0xc>(v));
  return (u32)__builtin_amdgcn_readlane((int)v, 63);
}

DEV float rdl(float v, int lane) { return __builtin_bit_cast(float, __builtin_amdgcn_readlane(__builtin_bit_cast(int, v), lane)); }
template <int CTRL>
DEV float dppx(float v) { return __builtin_bit_cast(float, __builtin_amdgcn_mov_dpp(__builtin_bit_cast(int, v), CTRL, 0xf, 0xf, true)); }
DEV float rows8_sum(const float (&p)[8], int l) {
  float a[4], c[2];
#pragma unroll
  for (int i = 0; i < 4; ++i) {
    const auto r = __builtin_amdgcn_permlane32_swap(__builtin_bit_cast(u32, p[i]), __builtin_bit_cast(u32, p[i + 4]), false, false);
    a[i] = __builtin_bit_cast(float, (u32)r[0]) + __builtin_bit_cast(float, (u32)r[1]);
  }
#pragma unroll
  for (int i = 0; i < 2; ++i) {
    const auto r = __builtin_amdgcn_permlane16_swap(__builtin_bit_cast(u32, a[i]), __builtin_bit_cast(u32, a[i + 2]), false, false);
    c[i] = __builtin_bit_cast(float, (u32)r[0]) + __builtin_bit_cast(float, (u32)r[1]);
  }
  const bool up = l & 8;
  float v = (up ? c[1] : c[0]) + dppx<0x128>(up ? c[0] : c[1]);
  v += dppx<0xB1>(v); v += dppx<0x4E>(v); v += dppx<0x141>(v);
  return v;
}

constexpr int G1_SLOT = 49152;
constexpr int KG_T = 2 * G1_SLOT;
constexpr int KG_TW = 53 * 256;
constexpr int SPARE = 3 * G1_SLOT;
constexpr int M_CS = SPARE;
constexpr int M_CNT = SPARE + 4096;
constexpr int M_KC = SPARE + 5120;
constexpr int CV_K = SPARE + 5120;
static_assert(KG_T + 4 * KG_TW <= M_KC && M_KC + 4 * 2816 <= LDS_BYTES, "lds");
constexpr int CV_CS = 4160;
constexpr int CV_U = 2 * G1_SLOT;
constexpr int CV_UCH = 4 * CV_CS;
constexpr int CV_X = 0;

DEV f32x2 cmul(f32x2 a, f32x2 b, f32x2 bn) { return fma2(f32x2{a.y, a.x}, bn, a * f32x2{b.x, b.x}); }
DEV f32x2 cneg(f32x2 b) { return f32x2{-b.y, b.y}; }
DEV f32x2 cexp_p(float lre2, float rev, float pw) {
  const float mg = __builtin_amdgcn_exp2f(lre2 * pw);
  const float ph = __builtin_amdgcn_fractf(rev * pw);
  return f32x2{__builtin_amdgcn_cosf(ph), __builtin_amdgcn_sinf(ph)} * f32x2{mg, mg};
}
DEV u32 pkh(f32x2 f) { return __builtin_bit_cast(u32, __builtin_convertvector(f, f16x2)); }
DEV LAS char* opaque(LAS char* q) { u32 v = (u32)(size_t)q; asm volatile("" : "+v"(v)); return (LAS char*)(size_t)v; }

template <class Hook>
DEV void conv_tiles(LAS char* ua, LAS char* ka, f32x4 (&acc)[4], Hook hook) {
  constexpr int ORD[16] = {0, 15, 13, 11, 9, 1, 14, 12, 10, 8, 2, 3, 4, 5, 6, 7};
  u32x4 fr[2][16];
  auto load = [&](int rr, int id) {
    if (id < 8) {
      const u32x2 lo = *(volatile LAS u32x2*)(ua + (7 - id) * 512 + (3 - rr) * 64), hi = *(volatile LAS u32x2*)(ua + (7 - id) * 512 + (3 - rr) * 64 + 8);
      fr[rr & 1][id] = u32x4{lo.x, lo.y, hi.x, hi.y};
    } else {
      fr[rr & 1][id] = lds128(ka + (id - 8) * 512 + (3 - rr) * 64);
    }
  };
#pragma unroll
  for (int k = 0; k < 16; ++k) { load(0, ORD[k]); SCHED(); }
  hook(0, -1);
  SCHED();
#pragma unroll
  for (int rr = 0; rr < 4; ++rr) {
#pragma unroll
    for (int c = 0; c < 8; ++c) {
      u32x4(&f)[16] = fr[rr & 1];
#pragma unroll
      for (int i = 0; i < 4; ++i) {
        if (c <= 7 - 2 * i) { acc[i] = mfma16(f[c], f[8 + 7 - 2 * i - c], acc[i]); SCHED(); }
        if (rr < 3 && i < 2) { load(rr + 1, ORD[2 * c + i]); SCHED(); }
      }
      hook(rr, c);
      SCHED();
    }
  }
}

__global__ __launch_bounds__(512, 2) void fused(Params p) {
  __shared__ __attribute__((aligned(16))) char smem[LDS_BYTES];
#define CODEPF 23000
  u32x4 pf;
  if (__builtin_amdgcn_readfirstlane(threadIdx.x >> 6) == 4) {
    u64 pc;
    asm volatile("s_getpc_b64 %0" : "=s"(pc));
    const u32 po = min((u32)((blockIdx.x >> 3) * 1024 + (threadIdx.x & 63) * 16), (u32)(CODEPF - 16));
    asm volatile("global_load_dwordx4 %0, %1, %2" : "=v"(pf) : "v"(po), "s"(pc & ~(u64)255) : "memory");
  }
  asm volatile("" : "+s"(p.x), "+s"(p.lar), "+s"(p.aim), "+s"(p.cre), "+s"(p.cim), "+s"(p.ldt), "+s"(p.dsk), "+s"(p.w1), "+s"(p.w3),
               "+s"(p.w2), "+s"(p.out), "+s"(p.ws));
  LAS char* lds = (LAS char*)smem;
  const u32 lds0 = (u32)(size_t)lds;
  const int tid = threadIdx.x, l = tid & 63;
  const int w = __builtin_amdgcn_readfirstlane(tid >> 6);
  const int b = blockIdx.x, xcd = b & 7, rk = b >> 3;
  const bool g1 = rk < 28;
  const rsrc_t rs = __builtin_amdgcn_make_buffer_rsrc(p.ws, 0, WS_BYTES, 0x00020000);
  const int l15 = l & 15, g = l >> 4, t = l & 31, h = l >> 5;
  STAMP_DECL;
  STAMP(0);

  u32x2 tag;
  u32 p_ldt2, dk;
  asm volatile("s_load_dwordx2 %0, %3, 0x0\n\ts_load_dword %1, %4, 0x0\n\ts_load_dword %2, %5, 0x0"
               : "=&s"(tag), "=&s"(p_ldt2), "=&s"(dk) : "s"(p.ws + FL_EP), "s"(p.ldt + 2 * b), "s"(p.dsk + 2 * b) : "memory");

  u32 p_lar, p_aim, p_cre, p_cim;
  if (w < 4) {
    const u32 po = (b * 256 + tid) * 2;
    asm volatile("global_load_ushort %0, %1, %2" NTQ : "=v"(p_lar) : "v"(po), "s"(p.lar) : "memory");
    asm volatile("global_load_ushort %0, %1, %2" NTQ : "=v"(p_aim) : "v"(po), "s"(p.aim) : "memory");
    asm volatile("global_load_ushort %0, %1, %2" NTQ : "=v"(p_cre) : "v"(po), "s"(p.cre) : "memory");
    asm volatile("global_load_ushort %0, %1, %2" NTQ : "=v"(p_cim) : "v"(po), "s"(p.cim) : "memory");
  }
  SCHED();

  const int mt = (xcd >> 1) * 4 + (rk & 3), ft = (xcd & 1) * 7 + (rk >> 2);
#define ROTV 5
  const int kt0 = ROTV == 0 ? 0 : ROTV == 2 ? (2 * (xcd >> 1) + 4 * (xcd & 1)) & 7 : (2 * (xcd >> 1) + 4 * (xcd & 1) + (mt & 1)) & 7;
  const int wt = w & 1, wf = w >> 1;
  const u32x4 rx = mkrs(p.x, L * D * 2), rw1 = mkrs(p.w1, D * F * 2), rw3 = mkrs(p.w3, D * F * 2);
  const u32 xo0 = (l >> 3) * 1024 + (((l & 7) ^ g) << 4);
  const u32 xo1 = (8 + (l >> 3)) * 1024 + (((l & 7) ^ (4 + g)) << 4);
  const u32 wo1 = g * (F * 2) + ((l15 ^ (4 * g + 2 * (w & 1))) << 4), wo1b = wo1 + 4 * F * 2;
  const u32 wo3 = wo1, wo3b = wo1b;
#define NTS 0
  auto dm0 = [&](auto st_, int piece, int ww) {
    constexpr int S = (decltype(st_)::v % 3) * G1_SLOT;
    const u32 lb = lds0 + ww * 2048;
    if (piece == 0) dma_m0<S>(lb);
    if (piece == 1) dma_m0<S + 16384>(lb);
    if (piece == 2) dma_m0<S + 32768>(lb);
    if (piece == 3) dma_m0<S + 1024>(lb);
    if (piece == 4) dma_m0<S + 16384 + 1024>(lb);
    if (piece == 5) dma_m0<S + 32768 + 1024>(lb);
  };
  auto dgo = [&](auto st_, int piece, int ww) {
    constexpr bool nt = decltype(st_)::v < NTS;
    const int kt = (kt0 + decltype(st_)::v) & 7;
    const u32 sx = (mt * 128 + ww * 16) * 1024 + kt * 128, sw = (kt * 64 + ww * 8) * (F * 2) + ft * 256;
    if (piece == 0) dma_go<nt>(rx, xo0, sx);
    if (piece == 1) dma_go<nt>(rw1, wo1, sw);
    if (piece == 2) dma_go<nt>(rw3, wo3, sw);
    if (piece == 3) dma_go<nt>(rx, xo1, sx);
    if (piece == 4) dma_go<nt>(rw1, wo1b, sw);
    if (piece == 5) dma_go<nt>(rw3, wo3b, sw);
  };
  auto dmaw = [&](auto st_, int piece, int ww) { dm0(st_, piece, ww); asm volatile("s_nop 0"); dgo(st_, piece, ww); };

  u32x2 wr[8];
  u32x4 xr[8];
  const int un2 = b / 28, ukb = 2 * (b - 28 * un2) + (w & 1);
  const int ax = xcd * 4 + (rk - 28);
  if (w >= 4 && g1) {
#pragma unroll
    for (int i = 0; i < 6; ++i) { dmaw(IC<0>{}, i, w - 4); dmaw(IC<0>{}, i, w); }
    if (w < 6) {
      const char* wb2 = (const char*)p.w2 + (size_t)(32 * ukb) * 1024 + un2 * 128;
      const u32 wv = g * 4096 + l15 * 8;
#define W2RAW(j, base, off) asm volatile("global_load_dwordx2 %0, %1, %2 offset:" #off NTQ : "=v"(wr[j]) : "v"(wv), "s"(base) : "memory")
      W2RAW(0, wb2, 0); W2RAW(1, wb2, 1024); W2RAW(2, wb2, 2048); W2RAW(3, wb2, 3072);
      W2RAW(4, wb2 + 16384, 0); W2RAW(5, wb2 + 16384, 1024); W2RAW(6, wb2 + 16384, 2048); W2RAW(7, wb2 + 16384, 3072);
    }
  }
  if (!g1) {
    const u32 xv_ = (64 * ax + 8 * w) * 1024 + l * 16;
#define XRAW(j, vo, off) asm volatile("global_load_dwordx4 %0, %1, %2 offset:" #off : "=v"(xr[j]) : "v"(vo), "s"(p.x) : "memory")
    XRAW(0, xv_, 0); XRAW(1, xv_, 1024); XRAW(2, xv_, 2048); XRAW(3, xv_, 3072);
    XRAW(4, xv_ + 4096, 0); XRAW(5, xv_ + 4096, 1024); XRAW(6, xv_ + 4096, 2048); XRAW(7, xv_ + 4096, 3072);
  }
  SCHED();

  const int m2 = xcd * 4 + (rk & 3), n2 = rk >> 2;
  u32 p0off;
  {
    const int e = min(9 * w + min(l, 8), 67);
    const int m = e - 60;
    const int pb = e < 32 ? 224 + e : (e < 60 ? 28 * n2 + (e - 32) : (g1 ? ((m >> 1) * 2 + (xcd & 1)) + 8 * ((m & 1) + 4 * (rk >> 2)) : 224));
    p0off = pb * FS;
  }
  const int uch = w >> 2, ui = tid & 255;
  u32 ka_[8];
#pragma unroll
  for (int e = 0; e < 4; ++e) {
    const int l0 = 8 * ui + 2 * e;
    const u32 bits = pk2((float)l0, (float)(l0 + 1));
    const int i0 = ui < 32 ? l0 : (int)(bits & 0xffffu) - (0x4300 - 128);
    const int i1 = ui < 32 ? l0 + 1 : (int)(bits >> 16) - (0x4300 - 128);
    ka_[2 * e] = lds0 + M_KC + uch * 5632 + i0 * 4;
    ka_[2 * e + 1] = lds0 + M_KC + uch * 5632 + i1 * 4;
  }
  u32 uo1 = ui < 255 ? ui * 16 + 16 : OOB;
  int xa[2], wa[2];
#pragma unroll
  for (int s = 0; s < 2; ++s) xa[s] = (64 * wt + l15) * 128 + (((4 * s + g) ^ ((l15 >> 1) & 7)) << 4);
  {
    const int q = l15 >> 2, pp = l & 3;
#pragma unroll
    for (int i = 0; i < 2; ++i) wa[i] = 16384 + (8 * g + q) * 256 + (((4 * wf + 2 * i + (pp >> 1)) ^ (4 * q + 2 * (g & 1))) << 4) + 8 * (pp & 1);
  }
  u32x4 sel;
#pragma unroll
  for (int e = 0; e < 4; ++e) sel[e] = l15 == 2 * e ? 0x00003f80u : (l15 == 2 * e + 1 ? 0x3f800000u : 0u);
  u32 a_cw = lds0 + 16384 * (1 + (w >> 2)) + (4 * g + (l15 >> 2)) * 256 + (((4 * (w & 3) + (l & 3)) ^ (4 * (l15 >> 2) + 2 * h)) << 4);
  u32 o_cq = (l < 32 && l15 >= 12) ? (l & 3) * 32 + g * 16 : OOB;
  u32 o_l0 = l == 0 ? 0 : OOB;
  u32 a_ua = lds0 + CV_U + (w & 1) * CV_UCH + (3 - (l15 & 3)) * CV_CS + 4088 - 8 * (l15 >> 2) - 32 * h + 16 * (g & 1) - 7 * 512 - 256 * (w >> 2) - 3 * 64;
  u32 a_ka = lds0 + CV_K + (w & 1) * 5120 + 1024 - 512 * ((w >> 1) & 1) + 32 * (l15 - h) + 16 * (g & 1) - 256 * (w >> 2) - 3 * 64;
  u32 a_zk = lds0 + CV_K + (w & 1) * 5120 + l * 16;
  u32 a_zu = lds0 + CV_U + (w & 1) * CV_UCH + ((l >> 1) & 3) * CV_CS + 4096 + (l & 1) * 16;
  u32 hfo = (l < 7 ? l : 0) * FS;
  u32x4 zero4 = {0u, 0u, 0u, 0u};
  PIN(a_ua); PIN(a_ka); PIN(a_zk); PIN(a_zu); PIN(hfo); PIN(zero4);

  u32 o_cs = g * 1024 + (l15 >> 3) * 512 + (32 * wf + 4 * (l15 & 7)) * 4;
  u32 a_cs = lds0 + M_CS + w * 256 + (4 * l15 + 2 * h + (g & 1)) * 4;
  PIN(a_cs);
  u32 a_cv = lds0 + M_CS + w * 256 + g * 16;

  PIN(sel); PIN(a_cw); PIN(o_cq); PIN(o_l0); PIN(o_cs); PIN(a_cv); PIN(p0off); PIN(uo1);
#pragma unroll
  for (int e = 0; e < 8; ++e) PIN(ka_[e]);
  int xa2[2], wa1[2], wa2[2];
#pragma unroll
  for (int e = 0; e < 2; ++e) {
    xa2[e] = xa[e] + 2 * G1_SLOT; wa1[e] = wa[e] + G1_SLOT; wa2[e] = wa[e] + 2 * G1_SLOT;
    PIN(xa[e]); PIN(xa2[e]); PIN(wa[e]); PIN(wa1[e]); PIN(wa2[e]);
  }
  SCHED();
  if (g1 && w >= 4) {
#pragma unroll
    for (int i = 0; i < 6; ++i) { dmaw(IC<1>{}, i, w - 4); dmaw(IC<1>{}, i, w); }
    asm volatile("s_waitcnt vmcnt(12)" : "+v"(wr[0]), "+v"(wr[1]), "+v"(wr[2]), "+v"(wr[3]), "+v"(wr[4]), "+v"(wr[5]), "+v"(wr[6]), "+v"(wr[7]) :: "memory");
    PIN(pf);
    if (w < 6) {
#pragma unroll
      for (int c = 0; c < 4; ++c) {
        u32x4 o;
#pragma unroll
        for (int e = 0; e < 4; ++e) o[e] = __builtin_amdgcn_perm(wr[2 * e + 1][c >> 1], wr[2 * e][c >> 1], (c & 1) ? 0x07060302u : 0x05040100u);
        st128s(o, rs, l * 16 + c * 1024, WS_W2F + (((un2 * 56 + ukb) * 4) << 10));
      }
    }
  }
  asm volatile("s_waitcnt lgkmcnt(0)" : "+s"(tag), "+s"(p_ldt2), "+s"(dk) :: "memory");
  tag.x += 1u;

  if (!g1) {
    asm volatile("s_waitcnt vmcnt(0)" : "+v"(xr[0]), "+v"(xr[1]), "+v"(xr[2]), "+v"(xr[3]), "+v"(xr[4]), "+v"(xr[5]), "+v"(xr[6]), "+v"(xr[7]) :: "memory");
    PIN(pf);
    f32x2 v[8][4];
    float ps[8];
#pragma unroll
    for (int r = 0; r < 8; ++r) {
#pragma unroll
      for (int j = 0; j < 4; ++j) v[r][j] = up2(xr[r][j]);
      const f32x2 s2 = (v[r][0] + v[r][1]) + (v[r][2] + v[r][3]);
      ps[r] = s2.x + s2.y;
    }
    const float mean = rows8_sum(ps, l) * (1.f / D);
#pragma unroll
    for (int r = 0; r < 8; ++r) {
      const float mr = rdl(mean, 8 * r);
      f32x2 q2 = {0.f, 0.f};
#pragma unroll
      for (int j = 0; j < 4; ++j) { v[r][j] -= f32x2{mr, mr}; q2 = fma2(v[r][j], v[r][j], q2); }
      ps[r] = q2.x + q2.y;
    }
    const float var = rows8_sum(ps, l) * (1.f / D);
    const u32 ms = pk2(mean, __builtin_sqrtf(rb(rb(var) + 1e-5f)));
    if ((l & 7) == 0) st32s(ms, rs, (l >> 3) * 4, WS_ST + (64 * ax + 8 * w) * 4);
    const float riv = 1.f / bhi(ms), nbv = (mean - b2f(ms)) * riv;
#pragma unroll
    for (int r = 0; r < 8; ++r) {
      const float ri = rdl(riv, 8 * r), nb = rdl(nbv, 8 * r);
#pragma unroll
      for (int j = 0; j < 4; ++j) v[r][j] = fma2(v[r][j], f32x2{ri, ri}, f32x2{nb, nb});
    }
#pragma unroll
    for (int c = 0; c < 8; ++c) {
      u32x4 o;
#pragma unroll
      for (int e = 0; e < 4; ++e) o[e] = pk2(v[7 - 2 * e][c >> 1][c & 1], v[6 - 2 * e][c >> 1][c & 1]);
      *(LAS u32x4*)(lds + (8 * l + c) * 128 + (((7 - w) ^ (l & 7)) << 4)) = o;
    }
    LBAR();
    u32x4 tv[8];
#pragma unroll
    for (int it = 0; it < 8; ++it) tv[it] = lds128(lds + w * 8192 + it * 1024 + l * 16);
#pragma unroll
    for (int it = 0; it < 8; ++it)
      st128s(tv[it], rs, (8 * it + (l >> 3)) * 4096 + (((l & 7) ^ it) << 4), WS_XNR + w * (64 * 4096) + (L - 64 - 64 * ax) * 2);
    WAIT_V(0);
    LBAR();
    if (tid == 0) st64s(tag, rs, 0, FL_P0 + b * FS);
    STAMP(3);
  }

  if (w < 4) {
    LAS char* tw = lds + KG_T + w * KG_TW;
    u32 ta[16];
#pragma unroll
    for (int k = 0; k < 16; ++k) { ta[k] = ((u32)(size_t)tw + l * 4) ^ (k << 4); PIN(ta[k]); }
    float dt = rb(__expf((w >> 1) ? bhi(p_ldt2) : b2f(p_ldt2)));
    PIN(dt);
    asm volatile("s_waitcnt vmcnt(0)" : "+v"(p_lar), "+v"(p_aim), "+v"(p_cre), "+v"(p_cim) :: "memory");
    STAMP(1);
    const float ar = -rb(__expf(b2f(p_lar)));
    const float lre2 = (ar * dt) * 1.44269504f, rev = (b2f(p_aim) * dt) * 0.15915494309f;
    u32 eb = wave_max_u32(max(p_cre & 0x7f80u, p_cim & 0x7f80u)) << 16;
    eb = eb < 0x00800000u ? 0x00800000u : (eb > 0x7e800000u ? 0x7e800000u : eb);
    const float sc = __builtin_bit_cast(float, eb) * (2.f * dt), isc = __builtin_bit_cast(float, 0x7f000000u - eb);
    f32x2 e[53], tmp[8];
    u32 eh[53];
    const f32x2 w8 = cexp_p(lre2, rev, 8.f), w64 = cexp_p(lre2, rev, 64.f);
    const f32x2 z1 = cexp_p(lre2, -rev, 1.f), zc = cexp_p(lre2, -rev, 256.f);
    e[0] = f32x2{1.f, 0.f}; e[1] = w8; e[8] = w64; e[32] = f32x2{b2f(p_cre) * isc, -b2f(p_cim) * isc};
#define CM1(k, a, bb) tmp[k] = (a) * f32x2{(bb).x, (bb).x}; SCHED()
#define CM2(k, d, a, bn) d = fma2(f32x2{(a).y, (a).x}, bn, tmp[k]); SCHED()
#define CV(i) eh[i] = pkh(e[i]); SCHED()
#define WR(i) *(LAS u32*)(size_t)(ta[(i) & 15] + (i) * 256) = eh[i]; SCHED()
    const f32x2 w8n = cneg(w8), w64n = cneg(w64), z1n = cneg(z1), zcn = cneg(zc);
    f32x2 z2, z4, zd, zq;
    SCHED();
    CM1(0, w8, w8); CM1(1, z1, z1); CM1(2, zc, zc); CM1(3, e[32], z1); CM1(4, e[32], zc); CM1(5, w8, w64); CM1(6, w64, w64);
    CV(0); CV(1); CV(8); CV(32);
    CM2(0, e[2], w8, w8n); WR(0); CM2(1, z2, z1, z1n); CM2(2, zd, zc, zcn); WR(1); CM2(3, e[33], e[32], z1n); CM2(4, e[40], e[32], zcn); WR(8);
    CM2(5, e[9], w8, w64n); CM2(6, e[16], w64, w64n); WR(32);
    const f32x2 e2n = cneg(e[2]), z2n = cneg(z2), zdn = cneg(zd);
    SCHED();
    CV(2); CV(33); CV(40); CV(9); CV(16);
    CM1(0, e[2], w8); CM1(1, e[2], e[2]); WR(2); CM1(2, z2, z2); CM1(3, zd, zd); CM1(4, e[32], z2); WR(33); CM1(5, e[33], z2); CM1(6, e[32], zd); CM1(7, e[40], zd); WR(40);
    CM2(0, e[3], e[2], w8n); CM2(1, e[4], e[2], e2n); CM2(2, z4, z2, z2n); WR(9); CM2(3, zq, zd, zdn); CM2(4, e[34], e[32], z2n);
    CM2(5, e[35], e[33], z2n); WR(16); CM2(6, e[44], e[32], zdn); CM2(7, e[46], e[40], zdn);
    const f32x2 e4n = cneg(e[4]), z4n = cneg(z4), zqn = cneg(zq);
    SCHED();
    CV(3); CV(4); CV(34); CV(35); CV(44); CV(46);
    CM1(0, e[1], e[4]); CM1(1, e[2], e[4]); WR(3); CM1(2, e[3], e[4]); CM1(3, e[32], z4); CM1(4, e[33], z4); WR(4); CM1(5, e[34], z4); CM1(6, e[35], z4); CM1(7, e[32], zq); WR(34);
    CM2(0, e[5], e[1], e4n); CM2(1, e[6], e[2], e4n); WR(35); CM2(2, e[7], e[3], e4n); CM2(3, e[36], e[32], z4n); CM2(4, e[37], e[33], z4n); WR(44);
    CM2(5, e[38], e[34], z4n); CM2(6, e[39], e[35], z4n); CM2(7, e[48], e[32], zqn); WR(46);
    CV(5); CV(6); CV(7); WR(5); CV(36); CV(37); CV(38); WR(6); CV(39); CV(48);
    CM1(0, e[34], zc); WR(7); CM1(1, e[36], zc); CM1(2, e[38], zc); CM1(3, e[36], zd); WR(36); CM1(4, e[40], zq); CM1(5, e[44], zq); CM1(6, e[46], zq); WR(37); CM1(7, e[48], zq);
    CM2(0, e[41], e[34], zcn); CM2(1, e[42], e[36], zcn); WR(38); CM2(2, e[43], e[38], zcn); CM2(3, e[45], e[36], zdn); CM2(4, e[49], e[40], zqn); WR(39);
    CM2(5, e[50], e[44], zqn); CM2(6, e[51], e[46], zqn); CM2(7, e[52], e[48], zqn); WR(48);
    CV(41); CV(42); CV(43); WR(41); CV(45); CV(49); CV(50); WR(42); CV(51); CV(52);
    CM1(0, e[42], zd); WR(43); CM1(1, e[9], w64); CM1(2, e[2], w64); CM1(3, e[3], w64); WR(45); CM1(4, e[4], w64); CM1(5, e[5], w64); CM1(6, e[6], w64); WR(49); CM1(7, e[7], w64);
    CM2(0, e[47], e[42], zdn); CM2(1, e[17], e[9], w64n); WR(50); CM2(2, e[10], e[2], w64n); CM2(3, e[11], e[3], w64n); CM2(4, e[12], e[4], w64n); WR(51);
    CM2(5, e[13], e[5], w64n); CM2(6, e[14], e[6], w64n); CM2(7, e[15], e[7], w64n); WR(52);
    CV(47); CV(17); CV(10); WR(47); CV(11); CV(12); CV(13); WR(17); CV(14); CV(15);
    CM1(0, e[16], w64); WR(10); CM1(1, e[17], w64); CM1(2, e[10], w64); CM1(3, e[11], w64); WR(11); CM1(4, e[12], w64); CM1(5, e[13], w64); CM1(6, e[14], w64); WR(12); CM1(7, e[15], w64);
    CM2(0, e[24], e[16], w64n); CM2(1, e[25], e[17], w64n); WR(13); CM2(2, e[18], e[10], w64n); CM2(3, e[19], e[11], w64n); CM2(4, e[20], e[12], w64n); WR(14);
    CM2(5, e[21], e[13], w64n); CM2(6, e[22], e[14], w64n); CM2(7, e[23], e[15], w64n); WR(15);
    CV(24); CV(25); CV(18); WR(24); CV(19); CV(20); CV(21); WR(25); CV(22); CV(23);
    CM1(2, e[18], w64); WR(18); CM1(3, e[19], w64); CM1(4, e[20], w64); WR(19); CM1(5, e[21], w64); CM1(6, e[22], w64); WR(20); CM1(7, e[23], w64);
    CM2(2, e[26], e[18], w64n); WR(21); CM2(3, e[27], e[19], w64n); CM2(4, e[28], e[20], w64n); WR(22); CM2(5, e[29], e[21], w64n);
    CM2(6, e[30], e[22], w64n); WR(23); CM2(7, e[31], e[23], w64n);
    CV(26); CV(27); WR(26); CV(28); WR(27); CV(29); WR(28); CV(30); WR(29); CV(31); WR(30); WR(31);
    u32x4 fm[8], fv[8];
#pragma unroll
    for (int s = 0; s < 8; ++s) fm[s] = lds128(tw + (32 + t) * 256 + (((2 * s + h) ^ l15) << 4));
#pragma unroll
    for (int s = 0; s < 8; ++s) fv[s] = lds128(tw + t * 256 + (((2 * s + h) ^ l15) << 4));
    SCHED();
    SCHED();
    STAMP(4);
    f32x16 kk;
#pragma unroll
    for (int r = 0; r < 16; ++r) kk[r] = 0.f;
#pragma unroll
    for (int s = 0; s < 8; ++s) kk = mfma32h(fm[s], fv[s], kk);
    LAS float* kc = (LAS float*)(lds + M_KC + w * 2816);
    *(LAS f32x4*)(kc + 8 * t + 4 * h) = f32x4{kk[0], kk[1], kk[2], kk[3]} * sc;
    *(LAS f32x2*)(kc + (h ? 384 + 2 * t : 256 + 4 * t)) = f32x2{kk[4], kk[5]} * sc;
    *(LAS f32x2*)(kc + (h ? 448 + 2 * t : 258 + 4 * t)) = f32x2{kk[6], kk[7]} * sc;
    LAS float* k3 = kc + (h ? 640 + t : 512 + t);
    k3[0] = kk[8] * sc;
    if (h == 0) { k3[32] = kk[9] * sc; k3[64] = kk[10] * sc; k3[96] = kk[11] * sc; }
  }
  STAMP(5);

  u32x4 wfr[28];
  const int w2off = WS_W2F + (((n2 * 56 + 7 * w) * 4) << 10);
  auto w2ld = [&](int i) { wfr[i] = ld128s(rs, l * 16 + (i & 3) * 1024, w2off + (i >> 2) * 4096); };
  f32x2 kx[8];
  u32 kpk[4];
  auto expand_rd = [&](int e) { LAS float* q = (LAS float*)(size_t)ka_[e]; kx[e] = f32x2{q[0], q[704]}; };
  auto expand_pk = [&](int e) {
    float a = kx[2 * e].x + kx[2 * e].y, c = kx[2 * e + 1].x + kx[2 * e + 1].y;
    PIN(a); PIN(c);
    kpk[e] = pk2(a, c);
    PIN(kpk[e]);
  };
  auto expand_wr = [&]() { *(LAS u32x4*)(lds + CV_K + uch * 5120 + 1024 + ui * 16) = u32x4{kpk[0], kpk[1], kpk[2], kpk[3]}; };
  u32x4 u0;
  u32x2 u1;
  auto load_u = [&](int k) {
    if (k == 0) u0 = ld128s(rs, ui * 16, WS_XNR + (2 * b + uch) * 4096);
    else u1 = ld64s(rs, uo1, WS_XNR + (2 * b + uch) * 4096);
  };
  auto copy_u = [&](int s) {
    const u32 dd[6] = {u0.x, u0.y, u0.z, u0.w, u1.x, u1.y};
    const int o = s >> 1;
    u32x4 v;
#pragma unroll
    for (int k = 0; k < 4; ++k) v[k] = (s & 1) ? __builtin_amdgcn_alignbit(dd[o + k + 1], dd[o + k], 16) : dd[o + k];
    *(LAS u32x4*)(lds + CV_U + uch * CV_UCH + s * CV_CS + ui * 16) = v;
  };
#define W2Q 97
#define NW2AV 15
  constexpr int NW2A = NW2AV;

  if (g1) {
    f32x4 acc[4][4];
#pragma unroll
    for (int i = 0; i < 4; ++i)
#pragma unroll
      for (int j = 0; j < 4; ++j) acc[i][j] = f32x4{0.f, 0.f, 0.f, 0.f};
    u32x4 fa[2][4], fb[2][4];
    auto rdA = [&](int st, int s, int i) {
      LAS char* q = lds + (st % 3 == 0 ? wa[i & 1] : st % 3 == 1 ? wa1[i & 1] : wa2[i & 1]) + (i >> 1) * 16384 + s * 8192;
      const u32x2 lo = ldstr(q), hi = ldstr(q + 1024);
      fa[s][i] = u32x4{lo.x, lo.y, hi.x, hi.y};
    };
    auto rdB = [&](int st, int s, int j) { fb[s][j] = lds128(lds + (st % 3 == 2 ? xa2[s] : xa[s] + (st % 3) * G1_SLOT) + j * 2048); };
    const int so_cs = (mt & 2) == 0 ? WS_CS + (ft * 8 + kt0) * 1024 + w * 128 : OOB;
    const int so_p0 = w == 0 ? FL_P0 + b * FS : OOB;

    LBAR();
    STAMP(6);
    rdB(0, 0, 0); rdA(0, 0, 0); rdA(0, 0, 1); rdA(0, 0, 2); rdA(0, 0, 3); rdB(0, 0, 1); rdB(0, 0, 2); rdB(0, 0, 3);
    dmaw(IC<2>{}, 0, w); dmaw(IC<2>{}, 1, w); dmaw(IC<2>{}, 2, w);
    u32x2 fl;
    u32 msv[4];
    float nmv[4];
    f32x4 csp[2];
    u32x4 cw[4];
    f32x4 cs, cv[4];
    static_for<8>([&](auto st_) {
      constexpr int st = decltype(st_)::v;
#pragma unroll
      for (int s = 0; s < 2; ++s) {
        const int nst = s ? st + 1 : st, ns = s ^ 1;
        if (s == 1 && st < 7) {
          if (st == 1 || st == 3 || st == 4) WAIT_V(7);
          else if (st == 6) WAIT_V(11);
          else WAIT_V(6);
        }
        if (s == 1) {
          LBAR();
          if (st == 2) STAMP(18);
          if (st == 5) STAMP(21);
        }
        SCHED();
#pragma unroll
        for (int n = 0; n < 16; ++n) {
          acc[n & 3][n >> 2] = mfma16(fa[s][n & 3], fb[s][n >> 2], acc[n & 3][n >> 2]);
          SCHED();
          if (nst < 8) {
            if (n == 0) rdB(nst, ns, 3);
            if (n == 1) rdB(nst, ns, 2);
            if (n == 2) rdB(nst, ns, 1);
            if (n == 3) rdA(nst, ns, 3);
            if (n == 5) rdA(nst, ns, 2);
            if (n == 7) rdA(nst, ns, 1);
            if (n == 9) rdA(nst, ns, 0);
            if (n == 10) rdB(nst, ns, 0);
          }
          if (nst + 2 < 8) {
            const int pc = (s ? 0 : 3);
            if (n == 4 || n == 8 || n == 12) { if (s) dm0(IC<(st + 3) & 7>{}, pc + (n >> 2) - 1, w); else dm0(IC<(st + 2) & 7>{}, pc + (n >> 2) - 1, w); }
            if (n == 6 || n == 11 || n == 14) { const int k = n == 6 ? 0 : n == 11 ? 1 : 2; if (s) dgo(IC<(st + 3) & 7>{}, pc + k, w); else dgo(IC<(st + 2) & 7>{}, pc + k, w); }
          }
          SCHED();
          const int q = st * 32 + s * 16 + n;
          if (q == 4 || q == 8 || q == 12 || q == 13) cw[q == 13 ? 3 : (q >> 2) - 1] = lds128((LAS char*)(size_t)a_cw + (q == 13 ? 3 : (q >> 2) - 1) * 4096);
          if (q == 20) cs = mfma16(sel, cw[0], f32x4{0.f, 0.f, 0.f, 0.f});
          if (q == 24 || q == 28 || q == 29) cs = mfma16(sel, cw[q == 29 ? 3 : (q >> 2) - 5], cs);
          if (q == 36) {
#pragma unroll
            for (int e = 0; e < 4; ++e) { const float v = cs[e]; cs[e] = v + dppx<0x114>(v); }
            PIN(cs);
          }
          if (q == 40) {
#pragma unroll
            for (int e = 0; e < 4; ++e) { const float v = cs[e]; cs[e] = v + dppx<0x118>(v); }
            PIN(cs);
          }
          if (q == 45) st128s(__builtin_bit_cast(u32x4, cs), rs, o_cq, so_cs);
          if (q >= 52 && q < 84 && (q & 3) == 0) expand_rd((q - 52) >> 2);
          if (q >= 92 && q < 124 && (q & 7) == 4) { PIN(kx[(q - 92) >> 2]); expand_pk((q - 92) >> 3); }
          if (q == 200) expand_wr();
          if (q == 85) st64s(tag, rs, o_l0, so_p0);
          if (q == 143) asm volatile("global_load_dwordx2 %0, %1, %2 sc1" : "=v"(fl) : "v"(p0off), "s"(p.ws + FL_P0) : "memory");
          if (q == 173) {
            asm volatile("s_waitcnt vmcnt(5)" : "+v"(fl) :: "memory");
            if (!__all(fl.x == tag.x && fl.y == tag.y)) poll(rs, p0off, FL_P0, tag);
          }
          if (q == 176 || q == 178) load_u((q - 176) >> 1);
          if (q == 180 || q == 182) csp[(q - 180) >> 1] = __builtin_bit_cast(f32x4, ld128s(rs, o_cs, WS_CS + (ft * 8 + 2 * (q - 180)) * 1024));
          if (q == 184) msv[0] = ld32s(rs, l * 4, WS_ST + (mt * 128 + 64 * wt) * 4);
          if (q >= 187 && ((q - 187) & 3) == 0 && ((q - 187) >> 2) < NW2A) w2ld((q - 187) >> 2);
          if (q == 205 || q == 213 || q == 221 || q == 229) copy_u((q - 205) >> 3);
          if (q == 209) {
            PIN(csp[0]); PIN(csp[1]);
            csp[0] += csp[1];
            const auto r0 = __builtin_amdgcn_permlane32_swap(f2u(csp[0][0]), f2u(csp[0][2]), false, false);
            const auto r1 = __builtin_amdgcn_permlane32_swap(f2u(csp[0][1]), f2u(csp[0][3]), false, false);
            csp[0][0] = u2f(r0[0]) + u2f(r0[1]);
            csp[0][1] = u2f(r1[0]) + u2f(r1[1]);
            PIN(csp[0]);
          }
          if (q == 217) {
            const auto r = __builtin_amdgcn_permlane16_swap(f2u(csp[0][0]), f2u(csp[0][1]), false, false);
            *(LAS float*)(size_t)a_cs = u2f(r[0]) + u2f(r[1]);
          }
          if (q == 220) {
            PIN(msv[0]);
            const auto m = __builtin_amdgcn_permlane32_swap(msv[0], msv[0], false, false);
            msv[0] = m[0]; msv[2] = m[1];
            PIN(msv[0]); PIN(msv[2]);
          }
          if (q == 225 || q == 228) {
            const int j = q == 225 ? 0 : 2;
            const auto m = __builtin_amdgcn_permlane16_swap(msv[j], msv[j], false, false);
            msv[j] = m[0]; msv[j + 1] = m[1];
            PIN(msv[j]); PIN(msv[j + 1]);
          }
          if (q >= 232 && q < 236) { cv[q - 232] = *(LAS f32x4*)((LAS char*)(size_t)a_cv + (q - 232) * 64); }
          if (q >= 236 && q < 240) { nmv[q - 236] = -b2f(msv[q - 236]); PIN(nmv[q - 236]); }
          if (q >= 240) {
            const int m = (n + 8) & 15, ii = m & 3, jj = m >> 2;
#pragma unroll
            for (int r = 0; r < 4; ++r) {
              float v = __builtin_fmaf(nmv[jj], cv[ii][r], acc[ii][jj][r]);
              PIN(v);
              acc[ii][jj][r] = v;
            }
          }
          SCHED();
        }
      }
    });
    STAMP(7);
    __builtin_amdgcn_s_setprio(3);
#pragma unroll
    for (int j = 0; j < 4; ++j) {
      const float sd = bhi(msv[j]), sd2 = sd * sd;
      const float ce = -1.44269504f * __builtin_amdgcn_rcpf(sd);
      u32x4 o;
#pragma unroll
      for (int r = 0; r < 4; ++r) {
        const f32x2 gg = {acc[r >> 1][j][2 * (r & 1)], acc[r >> 1][j][2 * (r & 1) + 1]};
        const f32x2 uu = {acc[2 + (r >> 1)][j][2 * (r & 1)], acc[2 + (r >> 1)][j][2 * (r & 1) + 1]};
        const f32x2 a = gg * f32x2{ce, ce};
        const f32x2 e = {__builtin_amdgcn_exp2f(a.x), __builtin_amdgcn_exp2f(a.y)};
        const f32x2 d = fma2(e, f32x2{sd2, sd2}, f32x2{sd2, sd2});
        const f32x2 q = {__builtin_amdgcn_rcpf(d.x), __builtin_amdgcn_rcpf(d.y)};
        o[r] = pk2v((gg * uu) * q);
      }
      st128s(o, rs, l * 16, WS_H + (((mt * 8 + 4 * wt + j) * 56 + ft * 4 + wf) << 10));
      SCHED();
    }
    __builtin_amdgcn_s_setprio(1);
    STAMP(8);
  } else {
    poll(rs, p0off, FL_P0, tag);
    if (b == 255 && w == 0) {
#pragma unroll 1
      for (int i = 0; i < 4; ++i) poll(rs, (64 * i + l) * FS, FL_P0, tag);
      if (l == 0) st64s(tag, rs, 0, FL_EP);
    }
    LBAR();
    load_u(0); load_u(1);
#pragma unroll
    for (int e = 0; e < 8; ++e) expand_rd(e);
#pragma unroll
    for (int e = 0; e < 4; ++e) expand_pk(e);
    LBAR();
    expand_wr();
#pragma unroll
    for (int s = 0; s < 4; ++s) copy_u(s);
    SCHED();
#pragma unroll
    for (int i = 0; i < NW2A; ++i) w2ld(i);
    __builtin_amdgcn_s_waitcnt(0x0F70);
    LBAR();
  }

  STAMP(10);
  u32x2 hfl;
  u32x4 hf[28];
  const int hoff = WS_H + ((4 * m2 * 56 + 7 * w) << 10);
  const int jr = 0;
  auto hld = [&](int n) { hf[n] = ld128s(rs, l * 16, hoff + (((n / 7) ^ jr) * 56 + n % 7) * 1024); };
  const int hfs = FL_H + (m2 * 56 + 7 * w) * FS;
#define PDV 12
  constexpr int PD = PDV;
  {
    LAS char *ua = (LAS char*)(size_t)a_ua, *ka = (LAS char*)(size_t)a_ka;
    *(LAS u32x4*)(size_t)a_zk = zero4;
    *(LAS u32x4*)(size_t)a_zu = zero4;
    asm volatile("" ::: "memory");
    f32x4 acc[4];
#pragma unroll
    for (int i = 0; i < 4; ++i) acc[i] = f32x4{0.f, 0.f, 0.f, 0.f};
    const int so_h = g1 ? FL_H + ((mt * 2 + wt) * 56 + ft * 4 + wf) * FS : OOB;
    auto hook = [&](int rr, int c) {
#define HPUB 3
#define HPOLL 24
      const int sq = rr * 8 + c;
#define WS0 4
#define WSN 13
      auto nw = [&](int z) { return z <= WS0 ? 0 : z >= WS0 + WSN ? 28 - NW2A : ((z - WS0) * (28 - NW2A) + WSN - 1) / WSN; };
      if (sq == HPUB) {
        constexpr int c0 = HPUB <= WS0 ? 0 : HPUB >= WS0 + WSN ? 28 - NW2A : ((HPUB - WS0) * (28 - NW2A) + WSN - 1) / WSN;
        asm volatile("s_waitcnt vmcnt(%0)" :: "i"(c0) : "memory");
        st64s(tag, rs, o_l0, so_h);
        __builtin_amdgcn_s_setprio(0);
        STAMP(9);
      }
      if (sq >= 0 && nw(sq + 1) > nw(sq)) w2ld(NW2A + nw(sq));
      if (sq == (HPOLL < 32 ? HPOLL - 5 : 31)) hfl = ld64s(rs, hfo, hfs);
      if (sq == HPOLL) {
        STAMP(2);
        if (!__all(hfl.x == tag.x && hfl.y == tag.y)) poll(rs, hfo, hfs, tag);
        asm volatile("" ::: "memory");
        STAMP(23);
      }
      if (sq >= HPOLL) {
#pragma unroll
        for (int n = (sq - HPOLL) * PD / (HPOLL < 32 ? 32 - HPOLL : 1); n < (sq + 1 - HPOLL) * PD / (HPOLL < 32 ? 32 - HPOLL : 1); ++n) hld(n);
      }
    };
    conv_tiles(ua, ka, acc, hook);
#pragma unroll
    for (int i = 0; i < 4; ++i) *(LAS f32x4*)(lds + CV_X + (w * 4 + i) * 1024 + l * 16) = acc[i];
    if (tid == 0) *(LAS u32*)(lds + M_CNT) = 0u;
  }
  LBAR();
  STAMP(11);
  int ytile;
  {
    const int ts = (w >> 1) & 1, i = (w & 1) + 2 * (w >> 2);
    ytile = 7 - 2 * i - ts;
    const int pos = 256 * ytile + 16 * l15 + 4 * g;
    f32x4 y0[2], y1[2];
    u32x2 ur[2];
#pragma unroll
    for (int ch = 0; ch < 2; ++ch) {
      y0[ch] = *(LAS f32x4*)(lds + CV_X + ((ch + 2 * ts) * 4 + i) * 1024 + l * 16);
      y1[ch] = *(LAS f32x4*)(lds + CV_X + ((ch + 2 * ts + 4) * 4 + i) * 1024 + l * 16);
      ur[ch] = lds64(lds + CV_U + ch * CV_UCH + (2044 - pos) * 2);
    }
    LBAR();
    float res[2][4];
#pragma unroll
    for (int ch = 0; ch < 2; ++ch) {
      const float dsk = ch ? bhi(dk) : b2f(dk);
      const f32x4 y = y0[ch] + y1[ch];
      res[ch][0] = __builtin_fmaf(bhi(ur[ch].y), dsk, y[0]); res[ch][1] = __builtin_fmaf(b2f(ur[ch].y), dsk, y[1]);
      res[ch][2] = __builtin_fmaf(bhi(ur[ch].x), dsk, y[2]); res[ch][3] = __builtin_fmaf(b2f(ur[ch].x), dsk, y[3]);
    }
    const u32x4 o = {pk2(res[0][0], res[1][0]), pk2(res[0][1], res[1][1]), pk2(res[0][2], res[1][2]), pk2(res[0][3], res[1][3])};
    st128s(o, rs, pos * 4, WS_Y + b * 8192);
    if (HPOLL == 32) {
      STAMP(2);
      if (!__all(hfl.x == tag.x && hfl.y == tag.y)) poll(rs, hfo, hfs, tag);
      asm volatile("" ::: "memory");
      STAMP(23);
#pragma unroll
      for (int n = 0; n < PD; ++n) hld(n);
    }
  }
  STAMP(12);

  {
    const int oj = w >> 1, rh = w & 1;
    const int etok = 64 * m2 + 16 * (oj ^ jr) + l15, ech = 64 * n2 + 16 * g + 8 * rh;
    u32x4 xv;
    u32 yo = (ech >> 1) * 8192 + etok * 4, yfv = (8 * g + (l & 3)) * FS;
    PIN(yo); PIN(yfv);
    SCHED();
    f32x4 acc[4][4];
#pragma unroll
    for (int i = 0; i < 4; ++i)
#pragma unroll
      for (int j = 0; j < 4; ++j) acc[i][j] = f32x4{0.f, 0.f, 0.f, 0.f};
    u32x2 yfl;
    const int yfo = FL_Y + ((m2 >> 2) * 256 + 32 * n2 + 4 * rh) * FS;
    LAS char* pw = opaque(lds + w * 16384 + l * 8);
    LAS char* pr = opaque(lds + w * 2048 + l * 8);
    LAS char* pr2 = opaque(pr + 65536);
    u32 a_cnt = lds0 + M_CNT + l * 4, one = 1u;
    PIN(a_cnt); PIN(one);
    auto part_wr = [&](int j, int k) {
      *(LAS f32x2*)(pw + (j * 8 + k) * 512) = f32x2{acc[k & 3][j][2 * (k >> 2)], acc[k & 3][j][2 * (k >> 2) + 1]};
    };
#pragma unroll
    for (int n = 0; n < 28; ++n) {
#pragma unroll
      for (int i = 0; i < 4; ++i) {
        acc[i][n / 7] = mfma16(wfr[4 * (n % 7) + i], hf[n], acc[i][n / 7]);
        SCHED();
        if (i == 3 && n + PD < 28) hld(n + PD);
        if ((i & 1) == 0 && n >= 8 && n % 7 >= 1 && n % 7 < 5) part_wr(n / 7 - 1, (n % 7 - 1) * 2 + (i >> 1));
        SCHED();
      }
      if (n == YP1 && w >= 4) { asm volatile("s_waitcnt vmcnt(%0)" :: "i"(YP1 + 1) : "memory"); st64s(tag, rs, o_l0, FL_Y + (ytile * 256 + b) * FS); }
      if (n == YP0 && w < 4) { asm volatile("s_waitcnt vmcnt(%0)" :: "i"(YP0 + 1) : "memory"); st64s(tag, rs, o_l0, FL_Y + (ytile * 256 + b) * FS); }
      if (n == 21) xv = *(const GAS u32x4*)((const char*)p.x + (size_t)etok * 1024 + ech * 2);
      if (n == 22) yfl = ld64s(rs, yfv, yfo);
      if (n == TB && w >= 4) LBAR();
      SCHED();
    }
    STAMP(13);
#pragma unroll
    for (int k = 0; k < 8; ++k) part_wr(3, k);
    asm volatile("" ::: "memory");
    if (w < 4) LBAR();
    else asm volatile("ds_add_u32 %0, %1" :: "v"(a_cnt), "v"(one) : "memory");
    if (!__all(yfl.x == tag.x && yfl.y == tag.y)) poll(rs, yfv, yfo, tag);
    asm volatile("" ::: "memory");
    u32 yv[4];
#pragma unroll
    for (int j = 0; j < 4; ++j) yv[j] = ld32s(rs, yo, WS_Y + j * 8192);
    f32x2 pp[8][4];
#pragma unroll
    for (int ww = 0; ww < 4; ++ww)
#pragma unroll
      for (int i = 0; i < 4; ++i) pp[ww][i] = *(volatile LAS f32x2*)(pr + ww * 16384 + i * 512);
    if (w >= 4) {
      volatile LAS u32* cnt = (volatile LAS u32*)(lds + M_CNT);
#pragma unroll 1
      for (int spin = 0; spin < (1 << 20) && *cnt < 4u; ++spin) {}
      asm volatile("" ::: "memory");
    }
    STAMP(14);
#pragma unroll
    for (int ww = 0; ww < 4; ++ww)
#pragma unroll
      for (int i = 0; i < 4; ++i) pp[4 + ww][i] = *(volatile LAS f32x2*)(pr2 + ww * 16384 + i * 512);
    SCHED();
    f32x2 sm[4];
#pragma unroll
    for (int i = 0; i < 4; ++i) sm[i] = ((pp[0][i] + pp[1][i]) + (pp[2][i] + pp[3][i])) + ((pp[4][i] + pp[5][i]) + (pp[6][i] + pp[7][i]));
    const u32 ff[4] = {pk2(sm[0].x, sm[1].x), pk2(sm[2].x, sm[3].x), pk2(sm[0].y, sm[1].y), pk2(sm[2].y, sm[3].y)};
    u32x4 r;
#pragma unroll
    for (int j = 0; j < 4; ++j) {
      const u32 s = pk2v(up2(xv[j]) + up2(yv[j]));
      r[j] = pk2v(up2(s) + up2(ff[j]));
    }
    *(GAS u32x4*)((char*)p.out + (size_t)etok * 1024 + ech * 2) = r;
  }
  STAMP(15);
  STAMP_DUMP();
}

extern "C" void kernel_launch(void* const* d_in, const int* in_sizes, int n_in, void* d_out, int out_size, void* d_ws,
                              size_t ws_size, hipStream_t stream) {
  Params p;
  p.x = (const u16*)d_in[0]; p.lar = (const u16*)d_in[1]; p.aim = (const u16*)d_in[2]; p.cre = (const u16*)d_in[3];
  p.cim = (const u16*)d_in[4]; p.ldt = (const u16*)d_in[5]; p.dsk = (const u16*)d_in[6];
  p.w1 = (const u16*)d_in[8]; p.w3 = (const u16*)d_in[9]; p.w2 = (const u16*)d_in[10];
  p.out = (u16*)d_out; p.ws = (char*)d_ws;
  fused<<<256, 512, 0, stream>>>(p);
}
```

```cpp
#include <hip/hip_runtime.h>
#include <hip/hip_bf16.h>

typedef unsigned short u16;
typedef unsigned int u32;
typedef unsigned long long u64;
typedef __bf16 bf16x8 __attribute__((ext_vector_type(8)));
typedef __bf16 bf16x2 __attribute__((ext_vector_type(2)));
typedef _Float16 f16x8 __attribute__((ext_vector_type(8)));
typedef _Float16 f16x2 __attribute__((ext_vector_type(2)));
typedef short s16x4 __attribute__((ext_vector_type(4)));
typedef float f32x2 __attribute__((ext_vector_type(2)));
typedef float f32x4 __attribute__((ext_vector_type(4)));
typedef float f32x16 __attribute__((ext_vector_type(16)));
typedef u32 u32x2 __attribute__((ext_vector_type(2)));
typedef u32 u32x4 __attribute__((ext_vector_type(4)));
typedef __amdgpu_buffer_rsrc_t rsrc_t;
#define LAS __attribute__((address_space(3)))
#define GAS __attribute__((address_space(1)))
#define DEV __device__ __forceinline__

#define NTQ " nt"
#define TB 21
#define YP1 1
#define YP0 10
constexpr int L = 2048, D = 512, NM = 128, F = 1792;
constexpr int LDS_BYTES = 163840;
constexpr int SC1 = 16;

constexpr int WS_H = 0;
constexpr int WS_XNR = 8 << 20;
constexpr int WS_W2F = 10 << 20;
constexpr int WS_Y = 12 << 20;
constexpr int WS_ST = 14 << 20;
constexpr int WS_CS = WS_ST + 8192;
constexpr int WS_FL = 15 << 20;
constexpr int FS = 128;
constexpr int FL_EP = WS_FL;
constexpr int FL_P0 = WS_FL + 4096;
constexpr int FL_H = FL_P0 + 256 * FS;
constexpr int FL_Y = FL_H + 2048 * FS;
constexpr int WS_BYTES = 16 << 20;

struct Params {
  const u16 *x, *lar, *aim, *cre, *cim, *ldt, *dsk, *w1, *w3, *w2;
  u16* out;
  char* ws;
};

template <int V> struct IC { static constexpr int v = V; };
template <int N, int I = 0, class Fn>
DEV void static_for(Fn&& fn) { if constexpr (I < N) { fn(IC<I>{}); static_for<N, I + 1>(fn); } }
DEV u32 f2u(float f) { return __builtin_bit_cast(u32, f); }
DEV float u2f(u32 v) { return __builtin_bit_cast(float, v); }
DEV float b2f(u32 v) { return __builtin_bit_cast(float, v << 16); }
DEV float bhi(u32 v) { return __builtin_bit_cast(float, v & 0xffff0000u); }
DEV u32 pk2v(f32x2 f) { return __builtin_bit_cast(u32, __builtin_convertvector(f, bf16x2)); }
DEV u32 pk2(float lo, float hi) { return pk2v(f32x2{lo, hi}); }
DEV f32x2 up2(u32 v) { return f32x2{b2f(v), bhi(v)}; }
DEV float rb(float f) { return b2f(pk2(f, f) & 0xffffu); }
DEV f32x2 fma2(f32x2 a, f32x2 b, f32x2 c) { return __builtin_elementwise_fma(a, b, c); }
DEV f32x16 mfma32(u32x4 a, u32x4 b, f32x16 c) {
  return __builtin_amdgcn_mfma_f32_32x32x16_bf16(__builtin_bit_cast(bf16x8, a), __builtin_bit_cast(bf16x8, b), c, 0, 0, 0);
}
DEV f32x4 mfma16(u32x4 a, u32x4 b, f32x4 c) {
  return __builtin_amdgcn_mfma_f32_16x16x32_bf16(__builtin_bit_cast(bf16x8, a), __builtin_bit_cast(bf16x8, b), c, 0, 0, 0);
}
DEV f32x16 mfma32h(u32x4 a, u32x4 b, f32x16 c) {
  return __builtin_amdgcn_mfma_f32_32x32x16_f16(__builtin_bit_cast(f16x8, a), __builtin_bit_cast(f16x8, b), c, 0, 0, 0);
}
DEV u32x4 lds128(LAS char* p) { return *(LAS u32x4*)p; }
DEV u32x2 lds64(LAS char* p) { return *(LAS u32x2*)p; }
DEV u32x2 ldstr(LAS char* p) { return __builtin_bit_cast(u32x2, __builtin_amdgcn_ds_read_tr16_b64_v4i16((LAS s16x4*)p)); }
template <int LOFF>
DEV void dma_m0(u32 lbase) { asm volatile("s_add_u32 m0, %0, %1" :: "s"(lbase), "i"(LOFF) : "memory", "scc", "m0"); }
template <bool NT>
DEV void dma_go(u32x4 rs, u32 voff, u32 soff) {
  if (NT) asm volatile("buffer_load_dwordx4 %0, %1, %2 offen nt lds" :: "v"(voff), "s"(rs), "s"(soff) : "memory");
  else asm volatile("buffer_load_dwordx4 %0, %1, %2 offen lds" :: "v"(voff), "s"(rs), "s"(soff) : "memory");
}
DEV u32x4 mkrs(const void* ptr, u32 bytes) { const u64 a = (u64)ptr; return u32x4{(u32)a, (u32)(a >> 32) & 0xffffu, bytes, 0x00020000u}; }
#define WAIT_V(n) asm volatile("s_waitcnt vmcnt(" #n ")" ::: "memory")
#define WAIT_L0() asm volatile("s_waitcnt lgkmcnt(0)" ::: "memory")
#define BAR() __builtin_amdgcn_s_barrier()
#define LBAR() do { WAIT_L0(); BAR(); asm volatile("" ::: "memory"); } while (0)
#define SCHED() __builtin_amdgcn_sched_barrier(0)
#define PIN(x) asm volatile("" : "+v"(x))
constexpr int OOB = 0x04000000;
#define STAMP(i)
#define STAMP_DECL
#define STAMP_DUMP()

DEV u32x4 ld128s(rsrc_t rs, int voff, int soff) { return __builtin_amdgcn_raw_buffer_load_b128(rs, voff, soff, SC1); }
DEV u32x2 ld64s(rsrc_t rs, int voff, int soff) { return __builtin_amdgcn_raw_buffer_load_b64(rs, voff, soff, SC1); }
DEV u32 ld32s(rsrc_t rs, int voff, int soff) { return __builtin_amdgcn_raw_buffer_load_b32(rs, voff, soff, SC1); }
DEV void st128s(u32x4 v, rsrc_t rs, int voff, int soff) {
  __builtin_amdgcn_raw_buffer_store_b128(v, rs, voff, soff, SC1);
  asm volatile("s_nop 1" :: "v"(v) : "memory");
}
DEV void st64s(u32x2 v, rsrc_t rs, int voff, int soff) { __builtin_amdgcn_raw_buffer_store_b64(v, rs, voff, soff, SC1); }
DEV void st32s(u32 v, rsrc_t rs, int voff, int soff) { __builtin_amdgcn_raw_buffer_store_b32(v, rs, voff, soff, SC1); }
DEV void poll(rsrc_t rs, int voff, int soff, u32x2 tag) {
#pragma unroll 1
  for (int spin = 0; spin < (1 << 18); ++spin) {
    const u32x2 v = ld64s(rs, voff, soff);
    if (__all(v.x == tag.x && v.y == tag.y)) break;
    __builtin_amdgcn_s_sleep(4);
  }
  __builtin_amdgcn_fence(__ATOMIC_ACQUIRE, "wavefront");
  asm volatile("" ::: "memory");
}

template <int CTRL, int RM>
DEV u32 dppu(u32 v) { return (u32)__builtin_amdgcn_update_dpp(0, (int)v, CTRL, RM, 0xf, true); }
DEV u32 wave_max_u32(u32 v) {
  v = max(v, dppu<0x111, 0xf>(v)); v = max(v, dppu<0x112, 0xf>(v)); v = max(v, dppu<0x114, 0xf>(v)); v = max(v, dppu<0x118, 0xf>(v));
  v = max(v, dppu<0x142, 0xa>(v)); v = max(v, dppu<0x143, 0xc>(v));
  return (u32)__builtin_amdgcn_readlane((int)v, 63);
}

DEV float rdl(float v, int lane) { return __builtin_bit_cast(float, __builtin_amdgcn_readlane(__builtin_bit_cast(int, v), lane)); }
template <int CTRL>
DEV float dppx(float v) { return __builtin_bit_cast(float, __builtin_amdgcn_mov_dpp(__builtin_bit_cast(int, v), CTRL, 0xf, 0xf, true)); }
DEV float rows8_sum(const float (&p)[8], int l) {
  float a[4], c[2];
#pragma unroll
  for (int i = 0; i < 4; ++i) {
    const auto r = __builtin_amdgcn_permlane32_swap(__builtin_bit_cast(u32, p[i]), __builtin_bit_cast(u32, p[i + 4]), false, false);
    a[i] = __builtin_bit_cast(float, (u32)r[0]) + __builtin_bit_cast(float, (u32)r[1]);
  }
#pragma unroll
  for (int i = 0; i < 2; ++i) {
    const auto r = __builtin_amdgcn_permlane16_swap(__builtin_bit_cast(u32, a[i]), __builtin_bit_cast(u32, a[i + 2]), false, false);
    c[i] = __builtin_bit_cast(float, (u32)r[0]) + __builtin_bit_cast(float, (u32)r[1]);
  }
  const bool up = l & 8;
  float v = (up ? c[1] : c[0]) + dppx<0x128>(up ? c[0] : c[1]);
  v += dppx<0xB1>(v); v += dppx<0x4E>(v); v += dppx<0x141>(v);
  return v;
}

constexpr int G1_SLOT = 49152;
constexpr int KG_T = 2 * G1_SLOT;
constexpr int KG_TW = 53 * 256;
constexpr int SPARE = 3 * G1_SLOT;
constexpr int M_CS = SPARE;
constexpr int M_CNT = SPARE + 4096;
constexpr int M_KC = SPARE + 5120;
constexpr int CV_K = SPARE + 5120;
static_assert(KG_T + 4 * KG_TW <= M_KC && M_KC + 4 * 2816 <= LDS_BYTES, "lds");
constexpr int CV_CS = 4160;
constexpr int CV_U = 2 * G1_SLOT;
constexpr int CV_UCH = 4 * CV_CS;
constexpr int CV_X = 0;

DEV f32x2 cmul(f32x2 a, f32x2 b, f32x2 bn) { return fma2(f32x2{a.y, a.x}, bn, a * f32x2{b.x, b.x}); }
DEV f32x2 cneg(f32x2 b) { return f32x2{-b.y, b.y}; }
DEV f32x2 cexp_p(float lre2, float rev, float pw) {
  const float mg = __builtin_amdgcn_exp2f(lre2 * pw);
  const float ph = __builtin_amdgcn_fractf(rev * pw);
  return f32x2{__builtin_amdgcn_cosf(ph), __builtin_amdgcn_sinf(ph)} * f32x2{mg, mg};
}
DEV u32 pkh(f32x2 f) { return __builtin_bit_cast(u32, __builtin_convertvector(f, f16x2)); }
DEV LAS char* opaque(LAS char* q) { u32 v = (u32)(size_t)q; asm volatile("" : "+v"(v)); return (LAS char*)(size_t)v; }

template <class Hook>
DEV void conv_tiles(LAS char* ua, LAS char* ka, f32x4 (&acc)[4], Hook hook) {
  constexpr int ORD[16] = {0, 15, 13, 11, 9, 1, 14, 12, 10, 8, 2, 3, 4, 5, 6, 7};
  u32x4 fr[2][16];
  auto load = [&](int rr, int id) {
    if (id < 8) {
      const u32x2 lo = *(volatile LAS u32x2*)(ua + (7 - id) * 512 + (3 - rr) * 64), hi = *(volatile LAS u32x2*)(ua + (7 - id) * 512 + (3 - rr) * 64 + 8);
      fr[rr & 1][id] = u32x4{lo.x, lo.y, hi.x, hi.y};
    } else {
      fr[rr & 1][id] = lds128(ka + (id - 8) * 512 + (3 - rr) * 64);
    }
  };
#pragma unroll
  for (int k = 0; k < 16; ++k) { load(0, ORD[k]); SCHED(); }
  hook(0, -1);
  SCHED();
#pragma unroll
  for (int rr = 0; rr < 4; ++rr) {
#pragma unroll
    for (int c = 0; c < 8; ++c) {
      u32x4(&f)[16] = fr[rr & 1];
#pragma unroll
      for (int i = 0; i < 4; ++i) {
        if (c <= 7 - 2 * i) { acc[i] = mfma16(f[c], f[8 + 7 - 2 * i - c], acc[i]); SCHED(); }
        if (rr < 3 && i < 2) { load(rr + 1, ORD[2 * c + i]); SCHED(); }
      }
      hook(rr, c);
      SCHED();
    }
  }
}

__global__ __launch_bounds__(512, 2) void fused(Params p) {
  __shared__ __attribute__((aligned(16))) char smem[LDS_BYTES];
#define CODEPF 23000
  u64 mta;
  asm volatile("s_memrealtime %0\n\ts_waitcnt lgkmcnt(0)" : "=s"(mta) :: "memory");
  u32 mt0 = (u32)mta, mtp = mt0;
  u32x4 pf;
  if (__builtin_amdgcn_readfirstlane(threadIdx.x >> 6) == 4) {
    u64 pc;
    asm volatile("s_getpc_b64 %0" : "=s"(pc));
    const u32 po = min((u32)((blockIdx.x >> 3) * 1024 + (threadIdx.x & 63) * 16), (u32)(CODEPF - 16));
    asm volatile("global_load_dwordx4 %0, %1, %2" : "=v"(pf) : "v"(po), "s"(pc & ~(u64)255) : "memory");
  }
  asm volatile("" : "+s"(p.x), "+s"(p.lar), "+s"(p.aim), "+s"(p.cre), "+s"(p.cim), "+s"(p.ldt), "+s"(p.dsk), "+s"(p.w1), "+s"(p.w3),
               "+s"(p.w2), "+s"(p.out), "+s"(p.ws));
  LAS char* lds = (LAS char*)smem;
  const u32 lds0 = (u32)(size_t)lds;
  const int tid = threadIdx.x, l = tid & 63;
  const int w = __builtin_amdgcn_readfirstlane(tid >> 6);
  const int b = blockIdx.x, xcd = b & 7, rk = b >> 3;
  const bool g1 = rk < 28;
  const rsrc_t rs = __builtin_amdgcn_make_buffer_rsrc(p.ws, 0, WS_BYTES, 0x00020000);
  const int l15 = l & 15, g = l >> 4, t = l & 31, h = l >> 5;
  STAMP_DECL;
  STAMP(0);

  u32x2 tag;
  u32 p_ldt2, dk;
  asm volatile("s_load_dwordx2 %0, %3, 0x0\n\ts_load_dword %1, %4, 0x0\n\ts_load_dword %2, %5, 0x0"
               : "=&s"(tag), "=&s"(p_ldt2), "=&s"(dk) : "s"(p.ws + FL_EP), "s"(p.ldt + 2 * b), "s"(p.dsk + 2 * b) : "memory");

  u32 p_lar, p_aim, p_cre, p_cim;
  if (w < 4) {
    const u32 po = (b * 256 + tid) * 2;
    asm volatile("global_load_ushort %0, %1, %2" NTQ : "=v"(p_lar) : "v"(po), "s"(p.lar) : "memory");
    asm volatile("global_load_ushort %0, %1, %2" NTQ : "=v"(p_aim) : "v"(po), "s"(p.aim) : "memory");
    asm volatile("global_load_ushort %0, %1, %2" NTQ : "=v"(p_cre) : "v"(po), "s"(p.cre) : "memory");
    asm volatile("global_load_ushort %0, %1, %2" NTQ : "=v"(p_cim) : "v"(po), "s"(p.cim) : "memory");
  }
  SCHED();

  const int mt = (xcd >> 1) * 4 + (rk & 3), ft = (xcd & 1) * 7 + (rk >> 2);
#define ROTV 5
  const int kt0 = ROTV == 0 ? 0 : ROTV == 2 ? (2 * (xcd >> 1) + 4 * (xcd & 1)) & 7 : (2 * (xcd >> 1) + 4 * (xcd & 1) + (mt & 1)) & 7;
  const int wt = w & 1, wf = w >> 1;
  const u32x4 rx = mkrs(p.x, L * D * 2), rw1 = mkrs(p.w1, D * F * 2), rw3 = mkrs(p.w3, D * F * 2);
  const u32 xo0 = (l >> 3) * 1024 + (((l & 7) ^ g) << 4);
  const u32 xo1 = (8 + (l >> 3)) * 1024 + (((l & 7) ^ (4 + g)) << 4);
  const u32 wo1 = g * (F * 2) + ((l15 ^ (4 * g + 2 * (w & 1))) << 4), wo1b = wo1 + 4 * F * 2;
  const u32 wo3 = wo1, wo3b = wo1b;
#define NTS 0
  auto dm0 = [&](auto st_, int piece, int ww) {
    constexpr int S = (decltype(st_)::v % 3) * G1_SLOT;
    const u32 lb = lds0 + ww * 2048;
    if (piece == 0) dma_m0<S>(lb);
    if (piece == 1) dma_m0<S + 16384>(lb);
    if (piece == 2) dma_m0<S + 32768>(lb);
    if (piece == 3) dma_m0<S + 1024>(lb);
    if (piece == 4) dma_m0<S + 16384 + 1024>(lb);
    if (piece == 5) dma_m0<S + 32768 + 1024>(lb);
  };
  auto dgo = [&](auto st_, int piece, int ww) {
    constexpr bool nt = decltype(st_)::v < NTS;
    const int kt = (kt0 + decltype(st_)::v) & 7;
    const u32 sx = (mt * 128 + ww * 16) * 1024 + kt * 128, sw = (kt * 64 + ww * 8) * (F * 2) + ft * 256;
    if (piece == 0) dma_go<nt>(rx, xo0, sx);
    if (piece == 1) dma_go<nt>(rw1, wo1, sw);
    if (piece == 2) dma_go<nt>(rw3, wo3, sw);
    if (piece == 3) dma_go<nt>(rx, xo1, sx);
    if (piece == 4) dma_go<nt>(rw1, wo1b, sw);
    if (piece == 5) dma_go<nt>(rw3, wo3b, sw);
  };
  auto dmaw = [&](auto st_, int piece, int ww) { dm0(st_, piece, ww); asm volatile("s_nop 0"); dgo(st_, piece, ww); };

  u32x2 wr[8];
  u32x4 xr[8];
  const int un2 = b / 28, ukb = 2 * (b - 28 * un2) + (w & 1);
  const int ax = xcd * 4 + (rk - 28);
  if (w >= 4 && g1) {
#pragma unroll
    for (int i = 0; i < 6; ++i) { dmaw(IC<0>{}, i, w - 4); dmaw(IC<0>{}, i, w); }
    if (w < 6) {
      const char* wb2 = (const char*)p.w2 + (size_t)(32 * ukb) * 1024 + un2 * 128;
      const u32 wv = g * 4096 + l15 * 8;
#define W2RAW(j, base, off) asm volatile("global_load_dwordx2 %0, %1, %2 offset:" #off NTQ : "=v"(wr[j]) : "v"(wv), "s"(base) : "memory")
      W2RAW(0, wb2, 0); W2RAW(1, wb2, 1024); W2RAW(2, wb2, 2048); W2RAW(3, wb2, 3072);
      W2RAW(4, wb2 + 16384, 0); W2RAW(5, wb2 + 16384, 1024); W2RAW(6, wb2 + 16384, 2048); W2RAW(7, wb2 + 16384, 3072);
    }
  }
  if (!g1) {
    const u32 xv_ = (64 * ax + 8 * w) * 1024 + l * 16;
#define XRAW(j, vo, off) asm volatile("global_load_dwordx4 %0, %1, %2 offset:" #off : "=v"(xr[j]) : "v"(vo), "s"(p.x) : "memory")
    XRAW(0, xv_, 0); XRAW(1, xv_, 1024); XRAW(2, xv_, 2048); XRAW(3, xv_, 3072);
    XRAW(4, xv_ + 4096, 0); XRAW(5, xv_ + 4096, 1024); XRAW(6, xv_ + 4096, 2048); XRAW(7, xv_ + 4096, 3072);
  }
  SCHED();

  const int m2 = xcd * 4 + (rk & 3), n2 = rk >> 2;
  u32 p0off;
  {
    const int e = min(9 * w + min(l, 8), 67);
    const int m = e - 60;
    const int pb = e < 32 ? 224 + e : (e < 60 ? 28 * n2 + (e - 32) : (g1 ? ((m >> 1) * 2 + (xcd & 1)) + 8 * ((m & 1) + 4 * (rk >> 2)) : 224));
    p0off = pb * FS;
  }
  const int uch = w >> 2, ui = tid & 255;
  u32 ka_[8];
#pragma unroll
  for (int e = 0; e < 4; ++e) {
    const int l0 = 8 * ui + 2 * e;
    const u32 bits = pk2((float)l0, (float)(l0 + 1));
    const int i0 = ui < 32 ? l0 : (int)(bits & 0xffffu) - (0x4300 - 128);
    const int i1 = ui < 32 ? l0 + 1 : (int)(bits >> 16) - (0x4300 - 128);
    ka_[2 * e] = lds0 + M_KC + uch * 5632 + i0 * 4;
    ka_[2 * e + 1] = lds0 + M_KC + uch * 5632 + i1 * 4;
  }
  u32 uo1 = ui < 255 ? ui * 16 + 16 : OOB;
  int xa[2], wa[2];
#pragma unroll
  for (int s = 0; s < 2; ++s) xa[s] = (64 * wt + l15) * 128 + (((4 * s + g) ^ ((l15 >> 1) & 7)) << 4);
  {
    const int q = l15 >> 2, pp = l & 3;
#pragma unroll
    for (int i = 0; i < 2; ++i) wa[i] = 16384 + (8 * g + q) * 256 + (((4 * wf + 2 * i + (pp >> 1)) ^ (4 * q + 2 * (g & 1))) << 4) + 8 * (pp & 1);
  }
  u32x4 sel;
#pragma unroll
  for (int e = 0; e < 4; ++e) sel[e] = l15 == 2 * e ? 0x00003f80u : (l15 == 2 * e + 1 ? 0x3f800000u : 0u);
  u32 a_cw = lds0 + 16384 * (1 + (w >> 2)) + (4 * g + (l15 >> 2)) * 256 + (((4 * (w & 3) + (l & 3)) ^ (4 * (l15 >> 2) + 2 * h)) << 4);
  u32 o_cq = (l < 32 && l15 >= 12) ? (l & 3) * 32 + g * 16 : OOB;
  u32 o_l0 = l == 0 ? 0 : OOB;
  u32 a_ua = lds0 + CV_U + (w & 1) * CV_UCH + (3 - (l15 & 3)) * CV_CS + 4088 - 8 * (l15 >> 2) - 32 * h + 16 * (g & 1) - 7 * 512 - 256 * (w >> 2) - 3 * 64;
  u32 a_ka = lds0 + CV_K + (w & 1) * 5120 + 1024 - 512 * ((w >> 1) & 1) + 32 * (l15 - h) + 16 * (g & 1) - 256 * (w >> 2) - 3 * 64;
  u32 a_zk = lds0 + CV_K + (w & 1) * 5120 + l * 16;
  u32 a_zu = lds0 + CV_U + (w & 1) * CV_UCH + ((l >> 1) & 3) * CV_CS + 4096 + (l & 1) * 16;
  u32 hfo = (l < 7 ? l : 0) * FS;
  u32x4 zero4 = {0u, 0u, 0u, 0u};
  PIN(a_ua); PIN(a_ka); PIN(a_zk); PIN(a_zu); PIN(hfo); PIN(zero4);

  u32 o_cs = g * 1024 + (l15 >> 3) * 512 + (32 * wf + 4 * (l15 & 7)) * 4;
  u32 a_cs = lds0 + M_CS + w * 256 + (4 * l15 + 2 * h + (g & 1)) * 4;
  PIN(a_cs);
  u32 a_cv = lds0 + M_CS + w * 256 + g * 16;

  PIN(sel); PIN(a_cw); PIN(o_cq); PIN(o_l0); PIN(o_cs); PIN(a_cv); PIN(p0off); PIN(uo1);
#pragma unroll
  for (int e = 0; e < 8; ++e) PIN(ka_[e]);
  int xa2[2], wa1[2], wa2[2];
#pragma unroll
  for (int e = 0; e < 2; ++e) {
    xa2[e] = xa[e] + 2 * G1_SLOT; wa1[e] = wa[e] + G1_SLOT; wa2[e] = wa[e] + 2 * G1_SLOT;
    PIN(xa[e]); PIN(xa2[e]); PIN(wa[e]); PIN(wa1[e]); PIN(wa2[e]);
  }
  SCHED();
  if (g1 && w >= 4) {
#pragma unroll
    for (int i = 0; i < 6; ++i) { dmaw(IC<1>{}, i, w - 4); dmaw(IC<1>{}, i, w); }
    asm volatile("s_waitcnt vmcnt(12)" : "+v"(wr[0]), "+v"(wr[1]), "+v"(wr[2]), "+v"(wr[3]), "+v"(wr[4]), "+v"(wr[5]), "+v"(wr[6]), "+v"(wr[7]) :: "memory");
    PIN(pf);
    if (w < 6) {
#pragma unroll
      for (int c = 0; c < 4; ++c) {
        u32x4 o;
#pragma unroll
        for (int e = 0; e < 4; ++e) o[e] = __builtin_amdgcn_perm(wr[2 * e + 1][c >> 1], wr[2 * e][c >> 1], (c & 1) ? 0x07060302u : 0x05040100u);
        st128s(o, rs, l * 16 + c * 1024, WS_W2F + (((un2 * 56 + ukb) * 4) << 10));
      }
    }
  }
  asm volatile("s_waitcnt lgkmcnt(0)" : "+s"(tag), "+s"(p_ldt2), "+s"(dk) :: "memory");
  tag.x += 1u;

  if (!g1) {
    asm volatile("s_waitcnt vmcnt(0)" : "+v"(xr[0]), "+v"(xr[1]), "+v"(xr[2]), "+v"(xr[3]), "+v"(xr[4]), "+v"(xr[5]), "+v"(xr[6]), "+v"(xr[7]) :: "memory");
    PIN(pf);
    f32x2 v[8][4];
    float ps[8];
#pragma unroll
    for (int r = 0; r < 8; ++r) {
#pragma unroll
      for (int j = 0; j < 4; ++j) v[r][j] = up2(xr[r][j]);
      const f32x2 s2 = (v[r][0] + v[r][1]) + (v[r][2] + v[r][3]);
      ps[r] = s2.x + s2.y;
    }
    const float mean = rows8_sum(ps, l) * (1.f / D);
#pragma unroll
    for (int r = 0; r < 8; ++r) {
      const float mr = rdl(mean, 8 * r);
      f32x2 q2 = {0.f, 0.f};
#pragma unroll
      for (int j = 0; j < 4; ++j) { v[r][j] -= f32x2{mr, mr}; q2 = fma2(v[r][j], v[r][j], q2); }
      ps[r] = q2.x + q2.y;
    }
    const float var = rows8_sum(ps, l) * (1.f / D);
    const u32 ms = pk2(mean, __builtin_sqrtf(rb(rb(var) + 1e-5f)));
    if ((l & 7) == 0) st32s(ms, rs, (l >> 3) * 4, WS_ST + (64 * ax + 8 * w) * 4);
    const float riv = 1.f / bhi(ms), nbv = (mean - b2f(ms)) * riv;
#pragma unroll
    for (int r = 0; r < 8; ++r) {
      const float ri = rdl(riv, 8 * r), nb = rdl(nbv, 8 * r);
#pragma unroll
      for (int j = 0; j < 4; ++j) v[r][j] = fma2(v[r][j], f32x2{ri, ri}, f32x2{nb, nb});
    }
#pragma unroll
    for (int c = 0; c < 8; ++c) {
      u32x4 o;
#pragma unroll
      for (int e = 0; e < 4; ++e) o[e] = pk2(v[7 - 2 * e][c >> 1][c & 1], v[6 - 2 * e][c >> 1][c & 1]);
      *(LAS u32x4*)(lds + (8 * l + c) * 128 + (((7 - w) ^ (l & 7)) << 4)) = o;
    }
    LBAR();
    u32x4 tv[8];
#pragma unroll
    for (int it = 0; it < 8; ++it) tv[it] = lds128(lds + w * 8192 + it * 1024 + l * 16);
#pragma unroll
    for (int it = 0; it < 8; ++it)
      st128s(tv[it], rs, (8 * it + (l >> 3)) * 4096 + (((l & 7) ^ it) << 4), WS_XNR + w * (64 * 4096) + (L - 64 - 64 * ax) * 2);
    WAIT_V(0);
    LBAR();
    if (tid == 0) st64s(tag, rs, 0, FL_P0 + b * FS);
    STAMP(3);
  }

  if (w < 4) {
    LAS char* tw = lds + KG_T + w * KG_TW;
    u32 ta[16];
#pragma unroll
    for (int k = 0; k < 16; ++k) { ta[k] = ((u32)(size_t)tw + l * 4) ^ (k << 4); PIN(ta[k]); }
    float dt = rb(__expf((w >> 1) ? bhi(p_ldt2) : b2f(p_ldt2)));
    PIN(dt);
    asm volatile("s_waitcnt vmcnt(0)" : "+v"(p_lar), "+v"(p_aim), "+v"(p_cre), "+v"(p_cim) :: "memory");
    STAMP(1);
    const float ar = -rb(__expf(b2f(p_lar)));
    const float lre2 = (ar * dt) * 1.44269504f, rev = (b2f(p_aim) * dt) * 0.15915494309f;
    u32 eb = wave_max_u32(max(p_cre & 0x7f80u, p_cim & 0x7f80u)) << 16;
    eb = eb < 0x00800000u ? 0x00800000u : (eb > 0x7e800000u ? 0x7e800000u : eb);
    const float sc = __builtin_bit_cast(float, eb) * (2.f * dt), isc = __builtin_bit_cast(float, 0x7f000000u - eb);
    f32x2 e[53], tmp[8];
    u32 eh[53];
    const f32x2 w8 = cexp_p(lre2, rev, 8.f), w64 = cexp_p(lre2, rev, 64.f);
    const f32x2 z1 = cexp_p(lre2, -rev, 1.f), zc = cexp_p(lre2, -rev, 256.f);
    e[0] = f32x2{1.f, 0.f}; e[1] = w8; e[8] = w64; e[32] = f32x2{b2f(p_cre) * isc, -b2f(p_cim) * isc};
#define CM1(k, a, bb) tmp[k] = (a) * f32x2{(bb).x, (bb).x}; SCHED()
#define CM2(k, d, a, bn) d = fma2(f32x2{(a).y, (a).x}, bn, tmp[k]); SCHED()
#define CV(i) eh[i] = pkh(e[i]); SCHED()
#define WR(i) *(LAS u32*)(size_t)(ta[(i) & 15] + (i) * 256) = eh[i]; SCHED()
    const f32x2 w8n = cneg(w8), w64n = cneg(w64), z1n = cneg(z1), zcn = cneg(zc);
    f32x2 z2, z4, zd, zq;
    SCHED();
    CM1(0, w8, w8); CM1(1, z1, z1); CM1(2, zc, zc); CM1(3, e[32], z1); CM1(4, e[32], zc); CM1(5, w8, w64); CM1(6, w64, w64);
    CV(0); CV(1); CV(8); CV(32);
    CM2(0, e[2], w8, w8n); WR(0); CM2(1, z2, z1, z1n); CM2(2, zd, zc, zcn); WR(1); CM2(3, e[33], e[32], z1n); CM2(4, e[40], e[32], zcn); WR(8);
    CM2(5, e[9], w8, w64n); CM2(6, e[16], w64, w64n); WR(32);
    const f32x2 e2n = cneg(e[2]), z2n = cneg(z2), zdn = cneg(zd);
    SCHED();
    CV(2); CV(33); CV(40); CV(9); CV(16);
    CM1(0, e[2], w8); CM1(1, e[2], e[2]); WR(2); CM1(2, z2, z2); CM1(3, zd, zd); CM1(4, e[32], z2); WR(33); CM1(5, e[33], z2); CM1(6, e[32], zd); CM1(7, e[40], zd); WR(40);
    CM2(0, e[3], e[2], w8n); CM2(1, e[4], e[2], e2n); CM2(2, z4, z2, z2n); WR(9); CM2(3, zq, zd, zdn); CM2(4, e[34], e[32], z2n);
    CM2(5, e[35], e[33], z2n); WR(16); CM2(6, e[44], e[32], zdn); CM2(7, e[46], e[40], zdn);
    const f32x2 e4n = cneg(e[4]), z4n = cneg(z4), zqn = cneg(zq);
    SCHED();
    CV(3); CV(4); CV(34); CV(35); CV(44); CV(46);
    CM1(0, e[1], e[4]); CM1(1, e[2], e[4]); WR(3); CM1(2, e[3], e[4]); CM1(3, e[32], z4); CM1(4, e[33], z4); WR(4); CM1(5, e[34], z4); CM1(6, e[35], z4); CM1(7, e[32], zq); WR(34);
    CM2(0, e[5], e[1], e4n); CM2(1, e[6], e[2], e4n); WR(35); CM2(2, e[7], e[3], e4n); CM2(3, e[36], e[32], z4n); CM2(4, e[37], e[33], z4n); WR(44);
    CM2(5, e[38], e[34], z4n); CM2(6, e[39], e[35], z4n); CM2(7, e[48], e[32], zqn); WR(46);
    CV(5); CV(6); CV(7); WR(5); CV(36); CV(37); CV(38); WR(6); CV(39); CV(48);
    CM1(0, e[34], zc); WR(7); CM1(1, e[36], zc); CM1(2, e[38], zc); CM1(3, e[36], zd); WR(36); CM1(4, e[40], zq); CM1(5, e[44], zq); CM1(6, e[46], zq); WR(37); CM1(7, e[48], zq);
    CM2(0, e[41], e[34], zcn); CM2(1, e[42], e[36], zcn); WR(38); CM2(2, e[43], e[38], zcn); CM2(3, e[45], e[36], zdn); CM2(4, e[49], e[40], zqn); WR(39);
    CM2(5, e[50], e[44], zqn); CM2(6, e[51], e[46], zqn); CM2(7, e[52], e[48], zqn); WR(48);
    CV(41); CV(42); CV(43); WR(41); CV(45); CV(49); CV(50); WR(42); CV(51); CV(52);
    CM1(0, e[42], zd); WR(43); CM1(1, e[9], w64); CM1(2, e[2], w64); CM1(3, e[3], w64); WR(45); CM1(4, e[4], w64); CM1(5, e[5], w64); CM1(6, e[6], w64); WR(49); CM1(7, e[7], w64);
    CM2(0, e[47], e[42], zdn); CM2(1, e[17], e[9], w64n); WR(50); CM2(2, e[10], e[2], w64n); CM2(3, e[11], e[3], w64n); CM2(4, e[12], e[4], w64n); WR(51);
    CM2(5, e[13], e[5], w64n); CM2(6, e[14], e[6], w64n); CM2(7, e[15], e[7], w64n); WR(52);
    CV(47); CV(17); CV(10); WR(47); CV(11); CV(12); CV(13); WR(17); CV(14); CV(15);
    CM1(0, e[16], w64); WR(10); CM1(1, e[17], w64); CM1(2, e[10], w64); CM1(3, e[11], w64); WR(11); CM1(4, e[12], w64); CM1(5, e[13], w64); CM1(6, e[14], w64); WR(12); CM1(7, e[15], w64);
    CM2(0, e[24], e[16], w64n); CM2(1, e[25], e[17], w64n); WR(13); CM2(2, e[18], e[10], w64n); CM2(3, e[19], e[11], w64n); CM2(4, e[20], e[12], w64n); WR(14);
    CM2(5, e[21], e[13], w64n); CM2(6, e[22], e[14], w64n); CM2(7, e[23], e[15], w64n); WR(15);
    CV(24); CV(25); CV(18); WR(24); CV(19); CV(20); CV(21); WR(25); CV(22); CV(23);
    CM1(2, e[18], w64); WR(18); CM1(3, e[19], w64); CM1(4, e[20], w64); WR(19); CM1(5, e[21], w64); CM1(6, e[22], w64); WR(20); CM1(7, e[23], w64);
    CM2(2, e[26], e[18], w64n); WR(21); CM2(3, e[27], e[19], w64n); CM2(4, e[28], e[20], w64n); WR(22); CM2(5, e[29], e[21], w64n);
    CM2(6, e[30], e[22], w64n); WR(23); CM2(7, e[31], e[23], w64n);
    CV(26); CV(27); WR(26); CV(28); WR(27); CV(29); WR(28); CV(30); WR(29); CV(31); WR(30); WR(31);
    u32x4 fm[8], fv[8];
#pragma unroll
    for (int s = 0; s < 8; ++s) fm[s] = lds128(tw + (32 + t) * 256 + (((2 * s + h) ^ l15) << 4));
#pragma unroll
    for (int s = 0; s < 8; ++s) fv[s] = lds128(tw + t * 256 + (((2 * s + h) ^ l15) << 4));
    SCHED();
    SCHED();
    STAMP(4);
    f32x16 kk;
#pragma unroll
    for (int r = 0; r < 16; ++r) kk[r] = 0.f;
#pragma unroll
    for (int s = 0; s < 8; ++s) kk = mfma32h(fm[s], fv[s], kk);
    LAS float* kc = (LAS float*)(lds + M_KC + w * 2816);
    *(LAS f32x4*)(kc + 8 * t + 4 * h) = f32x4{kk[0], kk[1], kk[2], kk[3]} * sc;
    *(LAS f32x2*)(kc + (h ? 384 + 2 * t : 256 + 4 * t)) = f32x2{kk[4], kk[5]} * sc;
    *(LAS f32x2*)(kc + (h ? 448 + 2 * t : 258 + 4 * t)) = f32x2{kk[6], kk[7]} * sc;
    LAS float* k3 = kc + (h ? 640 + t : 512 + t);
    k3[0] = kk[8] * sc;
    if (h == 0) { k3[32] = kk[9] * sc; k3[64] = kk[10] * sc; k3[96] = kk[11] * sc; }
  }
  STAMP(5);

  u32x4 wfr[28];
  const int w2off = WS_W2F + (((n2 * 56 + 7 * w) * 4) << 10);
  auto w2ld = [&](int i) { wfr[i] = ld128s(rs, l * 16 + (i & 3) * 1024, w2off + (i >> 2) * 4096); };
  f32x2 kx[8];
  u32 kpk[4];
  auto expand_rd = [&](int e) { LAS float* q = (LAS float*)(size_t)ka_[e]; kx[e] = f32x2{q[0], q[704]}; };
  auto expand_pk = [&](int e) {
    float a = kx[2 * e].x + kx[2 * e].y, c = kx[2 * e + 1].x + kx[2 * e + 1].y;
    PIN(a); PIN(c);
    kpk[e] = pk2(a, c);
    PIN(kpk[e]);
  };
  auto expand_wr = [&]() { *(LAS u32x4*)(lds + CV_K + uch * 5120 + 1024 + ui * 16) = u32x4{kpk[0], kpk[1], kpk[2], kpk[3]}; };
  u32x4 u0;
  u32x2 u1;
  auto load_u = [&](int k) {
    if (k == 0) u0 = ld128s(rs, ui * 16, WS_XNR + (2 * b + uch) * 4096);
    else u1 = ld64s(rs, uo1, WS_XNR + (2 * b + uch) * 4096);
  };
  auto copy_u = [&](int s) {
    const u32 dd[6] = {u0.x, u0.y, u0.z, u0.w, u1.x, u1.y};
    const int o = s >> 1;
    u32x4 v;
#pragma unroll
    for (int k = 0; k < 4; ++k) v[k] = (s & 1) ? __builtin_amdgcn_alignbit(dd[o + k + 1], dd[o + k], 16) : dd[o + k];
    *(LAS u32x4*)(lds + CV_U + uch * CV_UCH + s * CV_CS + ui * 16) = v;
  };
#define W2Q 97
#define NW2AV 15
  constexpr int NW2A = NW2AV;

  if (g1) {
    f32x4 acc[4][4];
#pragma unroll
    for (int i = 0; i < 4; ++i)
#pragma unroll
      for (int j = 0; j < 4; ++j) acc[i][j] = f32x4{0.f, 0.f, 0.f, 0.f};
    u32x4 fa[2][4], fb[2][4];
    auto rdA = [&](int st, int s, int i) {
      LAS char* q = lds + (st % 3 == 0 ? wa[i & 1] : st % 3 == 1 ? wa1[i & 1] : wa2[i & 1]) + (i >> 1) * 16384 + s * 8192;
      const u32x2 lo = ldstr(q), hi = ldstr(q + 1024);
      fa[s][i] = u32x4{lo.x, lo.y, hi.x, hi.y};
    };
    auto rdB = [&](int st, int s, int j) { fb[s][j] = lds128(lds + (st % 3 == 2 ? xa2[s] : xa[s] + (st % 3) * G1_SLOT) + j * 2048); };
    const int so_cs = (mt & 2) == 0 ? WS_CS + (ft * 8 + kt0) * 1024 + w * 128 : OOB;
    const int so_p0 = w == 0 ? FL_P0 + b * FS : OOB;

    LBAR();
    STAMP(6);
    rdB(0, 0, 0); rdA(0, 0, 0); rdA(0, 0, 1); rdA(0, 0, 2); rdA(0, 0, 3); rdB(0, 0, 1); rdB(0, 0, 2); rdB(0, 0, 3);
    dmaw(IC<2>{}, 0, w); dmaw(IC<2>{}, 1, w); dmaw(IC<2>{}, 2, w);
    u32x2 fl;
    u32 msv[4];
    float nmv[4];
    f32x4 csp[2];
    u32x4 cw[4];
    f32x4 cs, cv[4];
    static_for<8>([&](auto st_) {
      constexpr int st = decltype(st_)::v;
#pragma unroll
      for (int s = 0; s < 2; ++s) {
        const int nst = s ? st + 1 : st, ns = s ^ 1;
        if (s == 1 && st < 7) {
          if (st == 1 || st == 3 || st == 4) WAIT_V(7);
          else if (st == 6) WAIT_V(11);
          else WAIT_V(6);
        }
        if (s == 1) {
          LBAR();
          if (st == 2) STAMP(18);
          if (st == 5) STAMP(21);
        }
        SCHED();
#pragma unroll
        for (int n = 0; n < 16; ++n) {
          acc[n & 3][n >> 2] = mfma16(fa[s][n & 3], fb[s][n >> 2], acc[n & 3][n >> 2]);
          SCHED();
          if (nst < 8) {
            if (n == 0) rdB(nst, ns, 3);
            if (n == 1) rdB(nst, ns, 2);
            if (n == 2) rdB(nst, ns, 1);
            if (n == 3) rdA(nst, ns, 3);
            if (n == 5) rdA(nst, ns, 2);
            if (n == 7) rdA(nst, ns, 1);
            if (n == 9) rdA(nst, ns, 0);
            if (n == 10) rdB(nst, ns, 0);
          }
          if (nst + 2 < 8) {
            const int pc = (s ? 0 : 3);
            if (n == 4 || n == 8 || n == 12) { if (s) dm0(IC<(st + 3) & 7>{}, pc + (n >> 2) - 1, w); else dm0(IC<(st + 2) & 7>{}, pc + (n >> 2) - 1, w); }
            if (n == 6 || n == 11 || n == 14) { const int k = n == 6 ? 0 : n == 11 ? 1 : 2; if (s) dgo(IC<(st + 3) & 7>{}, pc + k, w); else dgo(IC<(st + 2) & 7>{}, pc + k, w); }
          }
          SCHED();
          const int q = st * 32 + s * 16 + n;
          if (q == 4 || q == 8 || q == 12 || q == 13) cw[q == 13 ? 3 : (q >> 2) - 1] = lds128((LAS char*)(size_t)a_cw + (q == 13 ? 3 : (q >> 2) - 1) * 4096);
          if (q == 20) cs = mfma16(sel, cw[0], f32x4{0.f, 0.f, 0.f, 0.f});
          if (q == 24 || q == 28 || q == 29) cs = mfma16(sel, cw[q == 29 ? 3 : (q >> 2) - 5], cs);
          if (q == 36) {
#pragma unroll
            for (int e = 0; e < 4; ++e) { const float v = cs[e]; cs[e] = v + dppx<0x114>(v); }
            PIN(cs);
          }
          if (q == 40) {
#pragma unroll
            for (int e = 0; e < 4; ++e) { const float v = cs[e]; cs[e] = v + dppx<0x118>(v); }
            PIN(cs);
          }
          if (q == 45) st128s(__builtin_bit_cast(u32x4, cs), rs, o_cq, so_cs);
          if (q >= 52 && q < 84 && (q & 3) == 0) expand_rd((q - 52) >> 2);
          if (q >= 92 && q < 124 && (q & 7) == 4) { PIN(kx[(q - 92) >> 2]); expand_pk((q - 92) >> 3); }
          if (q == 200) expand_wr();
          if (q == 85) st64s(tag, rs, o_l0, so_p0);
          if (q == 143) asm volatile("global_load_dwordx2 %0, %1, %2 sc1" : "=v"(fl) : "v"(p0off), "s"(p.ws + FL_P0) : "memory");
          if (q == 173) {
            asm volatile("s_waitcnt vmcnt(5)" : "+v"(fl) :: "memory");
            if (!__all(fl.x == tag.x && fl.y == tag.y)) poll(rs, p0off, FL_P0, tag);
          }
          if (q == 176 || q == 178) load_u((q - 176) >> 1);
          if (q == 180 || q == 182) csp[(q - 180) >> 1] = __builtin_bit_cast(f32x4, ld128s(rs, o_cs, WS_CS + (ft * 8 + 2 * (q - 180)) * 1024));
          if (q == 184) msv[0] = ld32s(rs, l * 4, WS_ST + (mt * 128 + 64 * wt) * 4);
          if (q >= 187 && ((q - 187) & 3) == 0 && ((q - 187) >> 2) < NW2A) w2ld((q - 187) >> 2);
          if (q == 205 || q == 213 || q == 221 || q == 229) copy_u((q - 205) >> 3);
          if (q == 209) {
            PIN(csp[0]); PIN(csp[1]);
            csp[0] += csp[1];
            const auto r0 = __builtin_amdgcn_permlane32_swap(f2u(csp[0][0]), f2u(csp[0][2]), false, false);
            const auto r1 = __builtin_amdgcn_permlane32_swap(f2u(csp[0][1]), f2u(csp[0][3]), false, false);
            csp[0][0] = u2f(r0[0]) + u2f(r0[1]);
            csp[0][1] = u2f(r1[0]) + u2f(r1[1]);
            PIN(csp[0]);
          }
          if (q == 217) {
            const auto r = __builtin_amdgcn_permlane16_swap(f2u(csp[0][0]), f2u(csp[0][1]), false, false);
            *(LAS float*)(size_t)a_cs = u2f(r[0]) + u2f(r[1]);
          }
          if (q == 220) {
            PIN(msv[0]);
            const auto m = __builtin_amdgcn_permlane32_swap(msv[0], msv[0], false, false);
            msv[0] = m[0]; msv[2] = m[1];
            PIN(msv[0]); PIN(msv[2]);
          }
          if (q == 225 || q == 228) {
            const int j = q == 225 ? 0 : 2;
            const auto m = __builtin_amdgcn_permlane16_swap(msv[j], msv[j], false, false);
            msv[j] = m[0]; msv[j + 1] = m[1];
            PIN(msv[j]); PIN(msv[j + 1]);
          }
          if (q >= 232 && q < 236) { cv[q - 232] = *(LAS f32x4*)((LAS char*)(size_t)a_cv + (q - 232) * 64); }
          if (q >= 236 && q < 240) { nmv[q - 236] = -b2f(msv[q - 236]); PIN(nmv[q - 236]); }
          if (q >= 240) {
            const int m = (n + 8) & 15, ii = m & 3, jj = m >> 2;
#pragma unroll
            for (int r = 0; r < 4; ++r) {
              float v = __builtin_fmaf(nmv[jj], cv[ii][r], acc[ii][jj][r]);
              PIN(v);
              acc[ii][jj][r] = v;
            }
          }
          SCHED();
        }
      }
    });
    STAMP(7);
    __builtin_amdgcn_s_setprio(3);
#pragma unroll
    for (int j = 0; j < 4; ++j) {
      const float sd = bhi(msv[j]), sd2 = sd * sd;
      const float ce = -1.44269504f * __builtin_amdgcn_rcpf(sd);
      u32x4 o;
#pragma unroll
      for (int r = 0; r < 4; ++r) {
        const f32x2 gg = {acc[r >> 1][j][2 * (r & 1)], acc[r >> 1][j][2 * (r & 1) + 1]};
        const f32x2 uu = {acc[2 + (r >> 1)][j][2 * (r & 1)], acc[2 + (r >> 1)][j][2 * (r & 1) + 1]};
        const f32x2 a = gg * f32x2{ce, ce};
        const f32x2 e = {__builtin_amdgcn_exp2f(a.x), __builtin_amdgcn_exp2f(a.y)};
        const f32x2 d = fma2(e, f32x2{sd2, sd2}, f32x2{sd2, sd2});
        const f32x2 q = {__builtin_amdgcn_rcpf(d.x), __builtin_amdgcn_rcpf(d.y)};
        o[r] = pk2v((gg * uu) * q);
      }
      st128s(o, rs, l * 16, WS_H + (((mt * 8 + 4 * wt + j) * 56 + ft * 4 + wf) << 10));
      SCHED();
    }
    __builtin_amdgcn_s_setprio(1);
    STAMP(8);
  } else {
    poll(rs, p0off, FL_P0, tag);
    if (b == 255 && w == 0) {
#pragma unroll 1
      for (int i = 0; i < 4; ++i) poll(rs, (64 * i + l) * FS, FL_P0, tag);
      if (l == 0) st64s(tag, rs, 0, FL_EP);
    }
    LBAR();
    load_u(0); load_u(1);
#pragma unroll
    for (int e = 0; e < 8; ++e) expand_rd(e);
#pragma unroll
    for (int e = 0; e < 4; ++e) expand_pk(e);
    LBAR();
    expand_wr();
#pragma unroll
    for (int s = 0; s < 4; ++s) copy_u(s);
    SCHED();
#pragma unroll
    for (int i = 0; i < NW2A; ++i) w2ld(i);
    __builtin_amdgcn_s_waitcnt(0x0F70);
    LBAR();
  }

  STAMP(10);
  u32x2 hfl;
  u32x4 hf[28];
  const int hoff = WS_H + ((4 * m2 * 56 + 7 * w) << 10);
  const int jr = 0;
  auto hld = [&](int n) { hf[n] = ld128s(rs, l * 16, hoff + (((n / 7) ^ jr) * 56 + n % 7) * 1024); };
  const int hfs = FL_H + (m2 * 56 + 7 * w) * FS;
#define PDV 12
  constexpr int PD = PDV;
  {
    LAS char *ua = (LAS char*)(size_t)a_ua, *ka = (LAS char*)(size_t)a_ka;
    *(LAS u32x4*)(size_t)a_zk = zero4;
    *(LAS u32x4*)(size_t)a_zu = zero4;
    asm volatile("" ::: "memory");
    f32x4 acc[4];
#pragma unroll
    for (int i = 0; i < 4; ++i) acc[i] = f32x4{0.f, 0.f, 0.f, 0.f};
    const int so_h = g1 ? FL_H + ((mt * 2 + wt) * 56 + ft * 4 + wf) * FS : OOB;
    auto hook = [&](int rr, int c) {
#define HPUB 3
#define HPOLL 24
      const int sq = rr * 8 + c;
#define WS0 4
#define WSN 13
      auto nw = [&](int z) { return z <= WS0 ? 0 : z >= WS0 + WSN ? 28 - NW2A : ((z - WS0) * (28 - NW2A) + WSN - 1) / WSN; };
      if (sq == HPUB) {
        constexpr int c0 = HPUB <= WS0 ? 0 : HPUB >= WS0 + WSN ? 28 - NW2A : ((HPUB - WS0) * (28 - NW2A) + WSN - 1) / WSN;
        asm volatile("s_waitcnt vmcnt(%0)" :: "i"(c0) : "memory");
        st64s(tag, rs, o_l0, so_h);
        __builtin_amdgcn_s_setprio(0);
        STAMP(9);
      }
      if (sq >= 0 && nw(sq + 1) > nw(sq)) w2ld(NW2A + nw(sq));
      if (sq == (HPOLL < 32 ? HPOLL - 5 : 31)) hfl = ld64s(rs, hfo, hfs);
      if (sq == HPOLL) {
        STAMP(2);
        if (!__all(hfl.x == tag.x && hfl.y == tag.y)) poll(rs, hfo, hfs, tag);
        asm volatile("" ::: "memory");
        STAMP(23);
      }
      if (sq >= HPOLL) {
#pragma unroll
        for (int n = (sq - HPOLL) * PD / (HPOLL < 32 ? 32 - HPOLL : 1); n < (sq + 1 - HPOLL) * PD / (HPOLL < 32 ? 32 - HPOLL : 1); ++n) hld(n);
      }
    };
    conv_tiles(ua, ka, acc, hook);
#pragma unroll
    for (int i = 0; i < 4; ++i) *(LAS f32x4*)(lds + CV_X + (w * 4 + i) * 1024 + l * 16) = acc[i];
    if (tid == 0) *(LAS u32*)(lds + M_CNT) = 0u;
  }
  LBAR();
  STAMP(11);
  int ytile;
  {
    const int ts = (w >> 1) & 1, i = (w & 1) + 2 * (w >> 2);
    ytile = 7 - 2 * i - ts;
    const int pos = 256 * ytile + 16 * l15 + 4 * g;
    f32x4 y0[2], y1[2];
    u32x2 ur[2];
#pragma unroll
    for (int ch = 0; ch < 2; ++ch) {
      y0[ch] = *(LAS f32x4*)(lds + CV_X + ((ch + 2 * ts) * 4 + i) * 1024 + l * 16);
      y1[ch] = *(LAS f32x4*)(lds + CV_X + ((ch + 2 * ts + 4) * 4 + i) * 1024 + l * 16);
      ur[ch] = lds64(lds + CV_U + ch * CV_UCH + (2044 - pos) * 2);
    }
    LBAR();
    float res[2][4];
#pragma unroll
    for (int ch = 0; ch < 2; ++ch) {
      const float dsk = ch ? bhi(dk) : b2f(dk);
      const f32x4 y = y0[ch] + y1[ch];
      res[ch][0] = __builtin_fmaf(bhi(ur[ch].y), dsk, y[0]); res[ch][1] = __builtin_fmaf(b2f(ur[ch].y), dsk, y[1]);
      res[ch][2] = __builtin_fmaf(bhi(ur[ch].x), dsk, y[2]); res[ch][3] = __builtin_fmaf(b2f(ur[ch].x), dsk, y[3]);
    }
    const u32x4 o = {pk2(res[0][0], res[1][0]), pk2(res[0][1], res[1][1]), pk2(res[0][2], res[1][2]), pk2(res[0][3], res[1][3])};
    st64s(u32x2{mt0, mtp}, rs, o_l0, w == 7 ? WS_ST + 524288 + b * 128 : OOB);
    st128s(o, rs, pos * 4, WS_Y + b * 8192);
    if (HPOLL == 32) {
      STAMP(2);
      if (!__all(hfl.x == tag.x && hfl.y == tag.y)) poll(rs, hfo, hfs, tag);
      asm volatile("" ::: "memory");
      STAMP(23);
#pragma unroll
      for (int n = 0; n < PD; ++n) hld(n);
    }
  }
  STAMP(12);

  {
    const int oj = w >> 1, rh = w & 1;
    const int etok = 64 * m2 + 16 * (oj ^ jr) + l15, ech = 64 * n2 + 16 * g + 8 * rh;
    u32x4 xv;
    u32 yo = (ech >> 1) * 8192 + etok * 4, yfv = (8 * g + (l & 3)) * FS;
    PIN(yo); PIN(yfv);
    SCHED();
    f32x4 acc[4][4];
#pragma unroll
    for (int i = 0; i < 4; ++i)
#pragma unroll
      for (int j = 0; j < 4; ++j) acc[i][j] = f32x4{0.f, 0.f, 0.f, 0.f};
    u32x2 yfl;
    const int yfo = FL_Y + ((m2 >> 2) * 256 + 32 * n2 + 4 * rh) * FS;
    LAS char* pw = opaque(lds + w * 16384 + l * 8);
    LAS char* pr = opaque(lds + w * 2048 + l * 8);
    LAS char* pr2 = opaque(pr + 65536);
    u32 a_cnt = lds0 + M_CNT + l * 4, one = 1u;
    PIN(a_cnt); PIN(one);
    auto part_wr = [&](int j, int k) {
      *(LAS f32x2*)(pw + (j * 8 + k) * 512) = f32x2{acc[k & 3][j][2 * (k >> 2)], acc[k & 3][j][2 * (k >> 2) + 1]};
    };
#pragma unroll
    for (int n = 0; n < 28; ++n) {
#pragma unroll
      for (int i = 0; i < 4; ++i) {
        acc[i][n / 7] = mfma16(wfr[4 * (n % 7) + i], hf[n], acc[i][n / 7]);
        SCHED();
        if (i == 3 && n + PD < 28) hld(n + PD);
        if ((i & 1) == 0 && n >= 8 && n % 7 >= 1 && n % 7 < 5) part_wr(n / 7 - 1, (n % 7 - 1) * 2 + (i >> 1));
        SCHED();
      }
      if (n == YP1 && w >= 4) { asm volatile("s_waitcnt vmcnt(%0)" :: "i"(YP1 + 1) : "memory"); st64s(tag, rs, o_l0, FL_Y + (ytile * 256 + b) * FS); }
      if (n == YP0 && w < 4) { asm volatile("s_waitcnt vmcnt(%0)" :: "i"(YP0 + 1) : "memory"); st64s(tag, rs, o_l0, FL_Y + (ytile * 256 + b) * FS); }
      if (n == 21) xv = *(const GAS u32x4*)((const char*)p.x + (size_t)etok * 1024 + ech * 2);
      if (n == 22) yfl = ld64s(rs, yfv, yfo);
      if (n == TB && w >= 4) LBAR();
      SCHED();
    }
    STAMP(13);
#pragma unroll
    for (int k = 0; k < 8; ++k) part_wr(3, k);
    asm volatile("" ::: "memory");
    if (w < 4) LBAR();
    else asm volatile("ds_add_u32 %0, %1" :: "v"(a_cnt), "v"(one) : "memory");
    if (!__all(yfl.x == tag.x && yfl.y == tag.y)) poll(rs, yfv, yfo, tag);
    asm volatile("" ::: "memory");
    u32 yv[4];
#pragma unroll
    for (int j = 0; j < 4; ++j) yv[j] = ld32s(rs, yo, WS_Y + j * 8192);
    f32x2 pp[8][4];
#pragma unroll
    for (int ww = 0; ww < 4; ++ww)
#pragma unroll
      for (int i = 0; i < 4; ++i) pp[ww][i] = *(volatile LAS f32x2*)(pr + ww * 16384 + i * 512);
    if (w >= 4) {
      volatile LAS u32* cnt = (volatile LAS u32*)(lds + M_CNT);
#pragma unroll 1
      for (int spin = 0; spin < (1 << 20) && *cnt < 4u; ++spin) {}
      asm volatile("" ::: "memory");
    }
    STAMP(14);
#pragma unroll
    for (int ww = 0; ww < 4; ++ww)
#pragma unroll
      for (int i = 0; i < 4; ++i) pp[4 + ww][i] = *(volatile LAS f32x2*)(pr2 + ww * 16384 + i * 512);
    SCHED();
    f32x2 sm[4];
#pragma unroll
    for (int i = 0; i < 4; ++i) sm[i] = ((pp[0][i] + pp[1][i]) + (pp[2][i] + pp[3][i])) + ((pp[4][i] + pp[5][i]) + (pp[6][i] + pp[7][i]));
    const u32 ff[4] = {pk2(sm[0].x, sm[1].x), pk2(sm[2].x, sm[3].x), pk2(sm[0].y, sm[1].y), pk2(sm[2].y, sm[3].y)};
    u32x4 r;
#pragma unroll
    for (int j = 0; j < 4; ++j) {
      const u32 s = pk2v(up2(xv[j]) + up2(yv[j]));
      r[j] = pk2v(up2(s) + up2(ff[j]));
    }
    *(GAS u32x4*)((char*)p.out + (size_t)etok * 1024 + ech * 2) = r;
  }
  if (b == 255 && w == 0) {
    u32 lo = 0, hi = 0, own = 0;
#pragma unroll 1
    for (int i = 0; i < 4; ++i) {
      poll(rs, (64 * i + l) * FS, FL_Y, tag);
      const u32x2 v = ld64s(rs, (64 * i + l) * 128, WS_ST + 524288);
      lo = max(lo, 0x10000u - (v.x - mt0)); hi = max(hi, 0x10000u + (v.x - mt0));
      if (64 * i + l < 224) own = max(own, v.y - v.x);
    }
    lo = wave_max_u32(lo); hi = wave_max_u32(hi); own = wave_max_u32(own);
    const u32 now = (u32)__builtin_amdgcn_s_memrealtime();
    u32 T = (own >> 20);
    T = min(T, 4000u);
    while ((u32)__builtin_amdgcn_s_memrealtime() - now < T) __builtin_amdgcn_s_sleep(1);
  }
  STAMP(15);
  STAMP_DUMP();
}

extern "C" void kernel_launch(void* const* d_in, const int* in_sizes, int n_in, void* d_out, int out_size, void* d_ws,
                              size_t ws_size, hipStream_t stream) {
  Params p;
  p.x = (const u16*)d_in[0]; p.lar = (const u16*)d_in[1]; p.aim = (const u16*)d_in[2]; p.cre = (const u16*)d_in[3];
  p.cim = (const u16*)d_in[4]; p.ldt = (const u16*)d_in[5]; p.dsk = (const u16*)d_in[6];
  p.w1 = (const u16*)d_in[8]; p.w3 = (const u16*)d_in[9]; p.w2 = (const u16*)d_in[10];
  p.out = (u16*)d_out; p.ws = (char*)d_ws;
  fused<<<256, 512, 0, stream>>>(p);
}
```

```cpp
#include <hip/hip_runtime.h>
#include <hip/hip_bf16.h>

typedef unsigned short u16;
typedef unsigned int u32;
typedef unsigned long long u64;
typedef __bf16 bf16x8 __attribute__((ext_vector_type(8)));
typedef __bf16 bf16x2 __attribute__((ext_vector_type(2)));
typedef _Float16 f16x8 __attribute__((ext_vector_type(8)));
typedef _Float16 f16x2 __attribute__((ext_vector_type(2)));
typedef short s16x4 __attribute__((ext_vector_type(4)));
typedef float f32x2 __attribute__((ext_vector_type(2)));
typedef float f32x4 __attribute__((ext_vector_type(4)));
typedef float f32x16 __attribute__((ext_vector_type(16)));
typedef u32 u32x2 __attribute__((ext_vector_type(2)));
typedef u32 u32x4 __attribute__((ext_vector_type(4)));
typedef u64 u64x4 __attribute__((ext_vector_type(4)));
typedef u64 u64x8 __attribute__((ext_vector_type(8)));
typedef __amdgpu_buffer_rsrc_t rsrc_t;
#define LAS __attribute__((address_space(3)))
#define GAS __attribute__((address_space(1)))
#define DEV __device__ __forceinline__

#define NTQ " nt"
#define TB 21
#define YP1 1
#define YP0 10
constexpr int L = 2048, D = 512, NM = 128, F = 1792;
constexpr int LDS_BYTES = 163840;
constexpr int SC1 = 16;

constexpr int WS_H = 0;
constexpr int WS_XNR = 8 << 20;
constexpr int WS_W2F = 10 << 20;
constexpr int WS_Y = 12 << 20;
constexpr int WS_ST = 14 << 20;
constexpr int WS_CS = WS_ST + 8192;
constexpr int WS_FL = 15 << 20;
constexpr int FS = 128;
constexpr int FL_EP = WS_FL;
constexpr int FL_P0 = WS_FL + 4096;
constexpr int FL_H = FL_P0 + 256 * FS;
constexpr int FL_Y = FL_H + 2048 * FS;
constexpr int WS_BYTES = 16 << 20;

struct Params {
  const u16 *x, *lar, *aim, *cre, *cim, *ldt, *dsk, *w1, *w3, *w2;
  u16* out;
  char* ws;
};

template <int V> struct IC { static constexpr int v = V; };
template <int N, int I = 0, class Fn>
DEV void static_for(Fn&& fn) { if constexpr (I < N) { fn(IC<I>{}); static_for<N, I + 1>(fn); } }
DEV u32 f2u(float f) { return __builtin_bit_cast(u32, f); }
DEV float u2f(u32 v) { return __builtin_bit_cast(float, v); }
DEV float b2f(u32 v) { return __builtin_bit_cast(float, v << 16); }
DEV float bhi(u32 v) { return __builtin_bit_cast(float, v & 0xffff0000u); }
DEV u32 pk2v(f32x2 f) { return __builtin_bit_cast(u32, __builtin_convertvector(f, bf16x2)); }
DEV u32 pk2(float lo, float hi) { return pk2v(f32x2{lo, hi}); }
DEV f32x2 up2(u32 v) { return f32x2{b2f(v), bhi(v)}; }
DEV float rb(float f) { return b2f(pk2(f, f) & 0xffffu); }
DEV f32x2 fma2(f32x2 a, f32x2 b, f32x2 c) { return __builtin_elementwise_fma(a, b, c); }
DEV f32x16 mfma32(u32x4 a, u32x4 b, f32x16 c) {
  return __builtin_amdgcn_mfma_f32_32x32x16_bf16(__builtin_bit_cast(bf16x8, a), __builtin_bit_cast(bf16x8, b), c, 0, 0, 0);
}
DEV f32x4 mfma16(u32x4 a, u32x4 b, f32x4 c) {
  return __builtin_amdgcn_mfma_f32_16x16x32_bf16(__builtin_bit_cast(bf16x8, a), __builtin_bit_cast(bf16x8, b), c, 0, 0, 0);
}
DEV f32x16 mfma32h(u32x4 a, u32x4 b, f32x16 c) {
  return __builtin_amdgcn_mfma_f32_32x32x16_f16(__builtin_bit_cast(f16x8, a), __builtin_bit_cast(f16x8, b), c, 0, 0, 0);
}
DEV u32x4 lds128(LAS char* p) { return *(LAS u32x4*)p; }
DEV u32x2 lds64(LAS char* p) { return *(LAS u32x2*)p; }
DEV u32x2 ldstr(LAS char* p) { return __builtin_bit_cast(u32x2, __builtin_amdgcn_ds_read_tr16_b64_v4i16((LAS s16x4*)p)); }
template <int LOFF>
DEV void dma_m0(u32 lbase) { asm volatile("s_add_u32 m0, %0, %1" :: "s"(lbase), "i"(LOFF) : "memory", "scc", "m0"); }
template <bool NT>
DEV void dma_go(u32x4 rs, u32 voff, u32 soff) {
  if (NT) asm volatile("buffer_load_dwordx4 %0, %1, %2 offen nt lds" :: "v"(voff), "s"(rs), "s"(soff) : "memory");
  else asm volatile("buffer_load_dwordx4 %0, %1, %2 offen lds" :: "v"(voff), "s"(rs), "s"(soff) : "memory");
}
DEV u32x4 mkrs(const void* ptr, u32 bytes) { const u64 a = (u64)ptr; return u32x4{(u32)a, (u32)(a >> 32) & 0xffffu, bytes, 0x00020000u}; }
#define WAIT_V(n) asm volatile("s_waitcnt vmcnt(" #n ")" ::: "memory")
#define WAIT_L0() asm volatile("s_waitcnt lgkmcnt(0)" ::: "memory")
#define BAR() __builtin_amdgcn_s_barrier()
#define LBAR() do { WAIT_L0(); BAR(); asm volatile("" ::: "memory"); } while (0)
#define SCHED() __builtin_amdgcn_sched_barrier(0)
#define PIN(x) asm volatile("" : "+v"(x))
constexpr int OOB = 0x04000000;
#define STAMP(i)
#define STAMP_DECL
#define STAMP_DUMP()

DEV u32x4 ld128s(rsrc_t rs, int voff, int soff) { return __builtin_amdgcn_raw_buffer_load_b128(rs, voff, soff, SC1); }
DEV u32x2 ld64s(rsrc_t rs, int voff, int soff) { return __builtin_amdgcn_raw_buffer_load_b64(rs, voff, soff, SC1); }
DEV u32 ld32s(rsrc_t rs, int voff, int soff) { return __builtin_amdgcn_raw_buffer_load_b32(rs, voff, soff, SC1); }
DEV void st128s(u32x4 v, rsrc_t rs, int voff, int soff) {
  __builtin_amdgcn_raw_buffer_store_b128(v, rs, voff, soff, SC1);
  asm volatile("s_nop 1" :: "v"(v) : "memory");
}
DEV void st64s(u32x2 v, rsrc_t rs, int voff, int soff) { __builtin_amdgcn_raw_buffer_store_b64(v, rs, voff, soff, SC1); }
DEV void st32s(u32 v, rsrc_t rs, int voff, int soff) { __builtin_amdgcn_raw_buffer_store_b32(v, rs, voff, soff, SC1); }
DEV void poll(rsrc_t rs, int voff, int soff, u32x2 tag) {
#pragma unroll 1
  for (int spin = 0; spin < (1 << 18); ++spin) {
    const u32x2 v = ld64s(rs, voff, soff);
    if (__all(v.x == tag.x && v.y == tag.y)) break;
    __builtin_amdgcn_s_sleep(4);
  }
  __builtin_amdgcn_fence(__ATOMIC_ACQUIRE, "wavefront");
  asm volatile("" ::: "memory");
}

template <int CTRL, int RM>
DEV u32 dppu(u32 v) { return (u32)__builtin_amdgcn_update_dpp(0, (int)v, CTRL, RM, 0xf, true); }
DEV u32 wave_max_u32(u32 v) {
  v = max(v, dppu<0x111, 0xf>(v)); v = max(v, dppu<0x112, 0xf>(v)); v = max(v, dppu<0x114, 0xf>(v)); v = max(v, dppu<0x118, 0xf>(v));
  v = max(v, dppu<0x142, 0xa>(v)); v = max(v, dppu<0x143, 0xc>(v));
  return (u32)__builtin_amdgcn_readlane((int)v, 63);
}

DEV float rdl(float v, int lane) { return __builtin_bit_cast(float, __builtin_amdgcn_readlane(__builtin_bit_cast(int, v), lane)); }
template <int CTRL>
DEV float dppx(float v) { return __builtin_bit_cast(float, __builtin_amdgcn_mov_dpp(__builtin_bit_cast(int, v), CTRL, 0xf, 0xf, true)); }
DEV float rows8_sum(const float (&p)[8], int l) {
  float a[4], c[2];
#pragma unroll
  for (int i = 0; i < 4; ++i) {
    const auto r = __builtin_amdgcn_permlane32_swap(__builtin_bit_cast(u32, p[i]), __builtin_bit_cast(u32, p[i + 4]), false, false);
    a[i] = __builtin_bit_cast(float, (u32)r[0]) + __builtin_bit_cast(float, (u32)r[1]);
  }
#pragma unroll
  for (int i = 0; i < 2; ++i) {
    const auto r = __builtin_amdgcn_permlane16_swap(__builtin_bit_cast(u32, a[i]), __builtin_bit_cast(u32, a[i + 2]), false, false);
    c[i] = __builtin_bit_cast(float, (u32)r[0]) + __builtin_bit_cast(float, (u32)r[1]);
  }
  const bool up = l & 8;
  float v = (up ? c[1] : c[0]) + dppx<0x128>(up ? c[0] : c[1]);
  v += dppx<0xB1>(v); v += dppx<0x4E>(v); v += dppx<0x141>(v);
  return v;
}

constexpr int G1_SLOT = 49152;
constexpr int KG_T = 2 * G1_SLOT;
constexpr int KG_TW = 53 * 256;
constexpr int SPARE = 3 * G1_SLOT;
constexpr int M_CS = SPARE;
constexpr int M_CNT = SPARE + 4096;
constexpr int M_KC = SPARE + 5120;
constexpr int CV_K = SPARE + 5120;
static_assert(KG_T + 4 * KG_TW <= M_KC && M_KC + 4 * 2816 <= LDS_BYTES, "lds");
constexpr int CV_CS = 4160;
constexpr int CV_U = 2 * G1_SLOT;
constexpr int CV_UCH = 4 * CV_CS;
constexpr int CV_X = 0;

DEV f32x2 cmul(f32x2 a, f32x2 b, f32x2 bn) { return fma2(f32x2{a.y, a.x}, bn, a * f32x2{b.x, b.x}); }
DEV f32x2 cneg(f32x2 b) { return f32x2{-b.y, b.y}; }
DEV f32x2 cexp_p(float lre2, float rev, float pw) {
  const float mg = __builtin_amdgcn_exp2f(lre2 * pw);
  const float ph = __builtin_amdgcn_fractf(rev * pw);
  return f32x2{__builtin_amdgcn_cosf(ph), __builtin_amdgcn_sinf(ph)} * f32x2{mg, mg};
}
DEV u32 pkh(f32x2 f) { return __builtin_bit_cast(u32, __builtin_convertvector(f, f16x2)); }
DEV LAS char* opaque(LAS char* q) { u32 v = (u32)(size_t)q; asm volatile("" : "+v"(v)); return (LAS char*)(size_t)v; }

template <class Hook>
DEV void conv_tiles(LAS char* ua, LAS char* ka, f32x4 (&acc)[4], Hook hook) {
  constexpr int ORD[16] = {0, 15, 13, 11, 9, 1, 14, 12, 10, 8, 2, 3, 4, 5, 6, 7};
  u32x4 fr[2][16];
  auto load = [&](int rr, int id) {
    if (id < 8) {
      const u32x2 lo = *(volatile LAS u32x2*)(ua + (7 - id) * 512 + (3 - rr) * 64), hi = *(volatile LAS u32x2*)(ua + (7 - id) * 512 + (3 - rr) * 64 + 8);
      fr[rr & 1][id] = u32x4{lo.x, lo.y, hi.x, hi.y};
    } else {
      fr[rr & 1][id] = lds128(ka + (id - 8) * 512 + (3 - rr) * 64);
    }
  };
#pragma unroll
  for (int k = 0; k < 16; ++k) { load(0, ORD[k]); SCHED(); }
  hook(0, -1);
  SCHED();
#pragma unroll
  for (int rr = 0; rr < 4; ++rr) {
#pragma unroll
    for (int c = 0; c < 8; ++c) {
      u32x4(&f)[16] = fr[rr & 1];
#pragma unroll
      for (int i = 0; i < 4; ++i) {
        if (c <= 7 - 2 * i) { acc[i] = mfma16(f[c], f[8 + 7 - 2 * i - c], acc[i]); SCHED(); }
        if (rr < 3 && i < 2) { load(rr + 1, ORD[2 * c + i]); SCHED(); }
      }
      hook(rr, c);
      SCHED();
    }
  }
}

__global__ __launch_bounds__(512, 2) void fused(Params args) {
  __shared__ __attribute__((aligned(16))) char smem[LDS_BYTES];
  u64x8 ka0;
  u64x4 ka1;
  asm volatile("s_load_dwordx16 %0, %2, 0x0\n\ts_load_dwordx8 %1, %2, 0x40" : "=&s"(ka0), "=&s"(ka1) : "s"(__builtin_amdgcn_kernarg_segment_ptr()) : "memory");
#define CODEPF 23000
  u32x4 pf;
  if (__builtin_amdgcn_readfirstlane(threadIdx.x >> 6) == 4) {
    u64 pc;
    asm volatile("s_getpc_b64 %0" : "=s"(pc));
    const u32 po = min((u32)((blockIdx.x >> 3) * 1024 + (threadIdx.x & 63) * 16), (u32)(CODEPF - 16));
    asm volatile("global_load_dwordx4 %0, %1, %2" : "=v"(pf) : "v"(po), "s"(pc & ~(u64)255) : "memory");
  }
  LAS char* lds = (LAS char*)smem;
  const u32 lds0 = (u32)(size_t)lds;
  const int tid = threadIdx.x, l = tid & 63;
  const int w = __builtin_amdgcn_readfirstlane(tid >> 6);
  const int b = blockIdx.x, xcd = b & 7, rk = b >> 3;
  const bool g1 = rk < 28;
  const int l15 = l & 15, g = l >> 4, t = l & 31, h = l >> 5;
  const int mt = (xcd >> 1) * 4 + (rk & 3), ft = (xcd & 1) * 7 + (rk >> 2);
#define ROTV 5
  const int kt0 = ROTV == 0 ? 0 : ROTV == 1 ? (ft + 2 * (mt & 3) + ((mt >> 2) & 1)) & 7 : ROTV == 2 ? (2 * (xcd >> 1) + 4 * (xcd & 1)) & 7 : (2 * (xcd >> 1) + 4 * (xcd & 1) + (mt & 1)) & 7;
  const int wt = w & 1, wf = w >> 1;
  u32 xo0 = (l >> 3) * 1024 + (((l & 7) ^ g) << 4);
  u32 xo1 = (8 + (l >> 3)) * 1024 + (((l & 7) ^ (4 + g)) << 4);
  u32 wo1 = g * (F * 2) + ((l15 ^ (4 * g + 2 * (w & 1))) << 4), wo1b = wo1 + 4 * F * 2;
  PIN(xo0); PIN(xo1); PIN(wo1); PIN(wo1b);
  u32 ta[16];
#pragma unroll
  for (int k = 0; k < 16; ++k) { ta[k] = (lds0 + KG_T + (w & 3) * KG_TW + l * 4) ^ (k << 4); PIN(ta[k]); }
  const int m2 = xcd * 4 + (rk & 3), n2 = rk >> 2;
  const int uch = w >> 2, ui = tid & 255;
  u32 uo1 = ui < 255 ? ui * 16 + 16 : OOB;
  int xa[2], wa[2];
#pragma unroll
  for (int s = 0; s < 2; ++s) xa[s] = (64 * wt + l15) * 128 + (((4 * s + g) ^ ((l15 >> 1) & 7)) << 4);
  {
    const int q = l15 >> 2, pp = l & 3;
#pragma unroll
    for (int i = 0; i < 2; ++i) wa[i] = 16384 + (8 * g + q) * 256 + (((4 * wf + 2 * i + (pp >> 1)) ^ (4 * q + 2 * (g & 1))) << 4) + 8 * (pp & 1);
  }
  u32x4 sel;
#pragma unroll
  for (int e = 0; e < 4; ++e) sel[e] = l15 == 2 * e ? 0x00003f80u : (l15 == 2 * e + 1 ? 0x3f800000u : 0u);
  u32 a_cw = lds0 + 16384 * (1 + (w >> 2)) + (4 * g + (l15 >> 2)) * 256 + (((4 * (w & 3) + (l & 3)) ^ (4 * (l15 >> 2) + 2 * h)) << 4);
  u32 o_cq = (l < 32 && l15 >= 12) ? (l & 3) * 32 + g * 16 : OOB;
  u32 o_l0 = l == 0 ? 0 : OOB;
  u32 a_ua = lds0 + CV_U + (w & 1) * CV_UCH + (3 - (l15 & 3)) * CV_CS + 4088 - 8 * (l15 >> 2) - 32 * h + 16 * (g & 1) - 7 * 512 - 256 * (w >> 2) - 3 * 64;
  u32 a_ka = lds0 + CV_K + (w & 1) * 5120 + 1024 - 512 * ((w >> 1) & 1) + 32 * (l15 - h) + 16 * (g & 1) - 256 * (w >> 2) - 3 * 64;
  u32 a_zk = lds0 + CV_K + (w & 1) * 5120 + l * 16;
  u32 a_zu = lds0 + CV_U + (w & 1) * CV_UCH + ((l >> 1) & 3) * CV_CS + 4096 + (l & 1) * 16;
  u32 hfo = (l < 7 ? l : 0) * FS;
  u32x4 zero4 = {0u, 0u, 0u, 0u};
  PIN(a_ua); PIN(a_ka); PIN(a_zk); PIN(a_zu); PIN(hfo); PIN(zero4);

  u32 o_cs = g * 1024 + (l15 >> 3) * 512 + (32 * wf + 4 * (l15 & 7)) * 4;
  u32 a_cs = lds0 + M_CS + w * 256 + (4 * l15 + 2 * h + (g & 1)) * 4;
  PIN(a_cs);
  u32 a_cv = lds0 + M_CS + w * 256 + g * 16;

  PIN(sel); PIN(a_cw); PIN(o_cq); PIN(o_l0); PIN(o_cs); PIN(a_cv); PIN(uo1);
  int xa2[2], wa1[2], wa2[2];
#pragma unroll
  for (int e = 0; e < 2; ++e) {
    xa2[e] = xa[e] + 2 * G1_SLOT; wa1[e] = wa[e] + G1_SLOT; wa2[e] = wa[e] + 2 * G1_SLOT;
    PIN(xa[e]); PIN(xa2[e]); PIN(wa[e]); PIN(wa1[e]); PIN(wa2[e]);
  }
  SCHED();
  asm volatile("s_waitcnt lgkmcnt(0)" : "+s"(ka0), "+s"(ka1) :: "memory");
  Params p;
  p.x = (const u16*)ka0[0]; p.lar = (const u16*)ka0[1]; p.aim = (const u16*)ka0[2]; p.cre = (const u16*)ka0[3]; p.cim = (const u16*)ka0[4];
  p.ldt = (const u16*)ka0[5]; p.dsk = (const u16*)ka0[6]; p.w1 = (const u16*)ka0[7];
  p.w3 = (const u16*)ka1[0]; p.w2 = (const u16*)ka1[1]; p.out = (u16*)ka1[2]; p.ws = (char*)ka1[3];
  const rsrc_t rs = __builtin_amdgcn_make_buffer_rsrc(p.ws, 0, WS_BYTES, 0x00020000);
  STAMP_DECL;
  STAMP(0);

  u32x2 tag;
  u32 p_ldt2, dk;
  asm volatile("s_load_dwordx2 %0, %3, 0x0\n\ts_load_dword %1, %4, 0x0\n\ts_load_dword %2, %5, 0x0"
               : "=&s"(tag), "=&s"(p_ldt2), "=&s"(dk) : "s"(p.ws + FL_EP), "s"(p.ldt + 2 * b), "s"(p.dsk + 2 * b) : "memory");

  u32 p_lar, p_aim, p_cre, p_cim;
  if (w < 4) {
    const u32 po = (b * 256 + tid) * 2;
    asm volatile("global_load_ushort %0, %1, %2" NTQ : "=v"(p_lar) : "v"(po), "s"(p.lar) : "memory");
    asm volatile("global_load_ushort %0, %1, %2" NTQ : "=v"(p_aim) : "v"(po), "s"(p.aim) : "memory");
    asm volatile("global_load_ushort %0, %1, %2" NTQ : "=v"(p_cre) : "v"(po), "s"(p.cre) : "memory");
    asm volatile("global_load_ushort %0, %1, %2" NTQ : "=v"(p_cim) : "v"(po), "s"(p.cim) : "memory");
  }
  SCHED();

  const u32x4 rx = mkrs(p.x, L * D * 2), rw1 = mkrs(p.w1, D * F * 2), rw3 = mkrs(p.w3, D * F * 2);
#define NTS 0
  auto dm0 = [&](auto st_, int piece, int ww) {
    constexpr int S = (decltype(st_)::v % 3) * G1_SLOT;
    const u32 lb = lds0 + ww * 2048;
    if (piece == 0) dma_m0<S>(lb);
    if (piece == 1) dma_m0<S + 16384>(lb);
    if (piece == 2) dma_m0<S + 32768>(lb);
    if (piece == 3) dma_m0<S + 1024>(lb);
    if (piece == 4) dma_m0<S + 16384 + 1024>(lb);
    if (piece == 5) dma_m0<S + 32768 + 1024>(lb);
  };
  auto dgo = [&](auto st_, int piece, int ww) {
    constexpr bool nt = decltype(st_)::v < NTS;
    const int kt = (kt0 + decltype(st_)::v) & 7;
    const u32 sx = (mt * 128 + ww * 16) * 1024 + kt * 128, sw = (kt * 64 + ww * 8) * (F * 2) + ft * 256;
    if (piece == 0) dma_go<nt>(rx, xo0, sx);
    if (piece == 1) dma_go<nt>(rw1, wo1, sw);
    if (piece == 2) dma_go<nt>(rw3, wo1, sw);
    if (piece == 3) dma_go<nt>(rx, xo1, sx);
    if (piece == 4) dma_go<nt>(rw1, wo1b, sw);
    if (piece == 5) dma_go<nt>(rw3, wo1b, sw);
  };
  auto dmaw = [&](auto st_, int piece, int ww) { dm0(st_, piece, ww); asm volatile("s_nop 0"); dgo(st_, piece, ww); };

  u32x2 wr[8];
  u32x4 xr[8];
  const int un2 = b / 28, ukb = 2 * (b - 28 * un2) + (w & 1);
  const int ax = xcd * 4 + (rk - 28);
  if (w >= 4 && g1) {
#pragma unroll
    for (int i = 0; i < 6; ++i) { dmaw(IC<0>{}, i, w - 4); dmaw(IC<0>{}, i, w); }
    if (w < 6) {
      const char* wb2 = (const char*)p.w2 + (size_t)(32 * ukb) * 1024 + un2 * 128;
      const u32 wv = g * 4096 + l15 * 8;
#define W2RAW(j, base, off) asm volatile("global_load_dwordx2 %0, %1, %2 offset:" #off NTQ : "=v"(wr[j]) : "v"(wv), "s"(base) : "memory")
      W2RAW(0, wb2, 0); W2RAW(1, wb2, 1024); W2RAW(2, wb2, 2048); W2RAW(3, wb2, 3072);
      W2RAW(4, wb2 + 16384, 0); W2RAW(5, wb2 + 16384, 1024); W2RAW(6, wb2 + 16384, 2048); W2RAW(7, wb2 + 16384, 3072);
    }
  }
  if (!g1) {
    const u32 xv_ = (64 * ax + 8 * w) * 1024 + l * 16;
#define XRAW(j, vo, off) asm volatile("global_load_dwordx4 %0, %1, %2 offset:" #off : "=v"(xr[j]) : "v"(vo), "s"(p.x) : "memory")
    XRAW(0, xv_, 0); XRAW(1, xv_, 1024); XRAW(2, xv_, 2048); XRAW(3, xv_, 3072);
    XRAW(4, xv_ + 4096, 0); XRAW(5, xv_ + 4096, 1024); XRAW(6, xv_ + 4096, 2048); XRAW(7, xv_ + 4096, 3072);
  }
  SCHED();

  int lq = l, uq = ui;
  PIN(lq); PIN(uq);
  u32 p0off;
  {
    const int e = min(9 * w + min(lq, 8), 67);
    const int m = e - 60;
    const int pb = e < 32 ? 224 + e : (e < 60 ? 28 * n2 + (e - 32) : (g1 ? ((m >> 1) * 2 + (xcd & 1)) + 8 * ((m & 1) + 4 * (rk >> 2)) : 224));
    p0off = pb * FS;
  }
  u32 ka_[8];
#pragma unroll
  for (int e = 0; e < 4; ++e) {
    const int l0 = 8 * uq + 2 * e;
    const u32 bits = pk2((float)l0, (float)(l0 + 1));
    const int i0 = uq < 32 ? l0 : (int)(bits & 0xffffu) - (0x4300 - 128);
    const int i1 = uq < 32 ? l0 + 1 : (int)(bits >> 16) - (0x4300 - 128);
    ka_[2 * e] = lds0 + M_KC + uch * 5632 + i0 * 4;
    ka_[2 * e + 1] = lds0 + M_KC + uch * 5632 + i1 * 4;
  }
  PIN(p0off);
#pragma unroll
  for (int e = 0; e < 8; ++e) PIN(ka_[e]);
  SCHED();
  if (g1 && w >= 4) {
#pragma unroll
    for (int i = 0; i < 6; ++i) { dmaw(IC<1>{}, i, w - 4); dmaw(IC<1>{}, i, w); }
    asm volatile("s_waitcnt vmcnt(12)" : "+v"(wr[0]), "+v"(wr[1]), "+v"(wr[2]), "+v"(wr[3]), "+v"(wr[4]), "+v"(wr[5]), "+v"(wr[6]), "+v"(wr[7]) :: "memory");
    PIN(pf);
    if (w < 6) {
#pragma unroll
      for (int c = 0; c < 4; ++c) {
        u32x4 o;
#pragma unroll
        for (int e = 0; e < 4; ++e) o[e] = __builtin_amdgcn_perm(wr[2 * e + 1][c >> 1], wr[2 * e][c >> 1], (c & 1) ? 0x07060302u : 0x05040100u);
        st128s(o, rs, l * 16 + c * 1024, WS_W2F + (((un2 * 56 + ukb) * 4) << 10));
      }
    }
  }
  asm volatile("s_waitcnt lgkmcnt(0)" : "+s"(tag), "+s"(p_ldt2), "+s"(dk) :: "memory");
  tag.x += 1u;

  if (!g1) {
    asm volatile("s_waitcnt vmcnt(0)" : "+v"(xr[0]), "+v"(xr[1]), "+v"(xr[2]), "+v"(xr[3]), "+v"(xr[4]), "+v"(xr[5]), "+v"(xr[6]), "+v"(xr[7]) :: "memory");
    PIN(pf);
    f32x2 v[8][4];
    float ps[8];
#pragma unroll
    for (int r = 0; r < 8; ++r) {
#pragma unroll
      for (int j = 0; j < 4; ++j) v[r][j] = up2(xr[r][j]);
      const f32x2 s2 = (v[r][0] + v[r][1]) + (v[r][2] + v[r][3]);
      ps[r] = s2.x + s2.y;
    }
    const float mean = rows8_sum(ps, l) * (1.f / D);
#pragma unroll
    for (int r = 0; r < 8; ++r) {
      const float mr = rdl(mean, 8 * r);
      f32x2 q2 = {0.f, 0.f};
#pragma unroll
      for (int j = 0; j < 4; ++j) { v[r][j] -= f32x2{mr, mr}; q2 = fma2(v[r][j], v[r][j], q2); }
      ps[r] = q2.x + q2.y;
    }
    const float var = rows8_sum(ps, l) * (1.f / D);
    const u32 ms = pk2(mean, __builtin_sqrtf(rb(rb(var) + 1e-5f)));
    if ((l & 7) == 0) st32s(ms, rs, (l >> 3) * 4, WS_ST + (64 * ax + 8 * w) * 4);
    const float riv = 1.f / bhi(ms), nbv = (mean - b2f(ms)) * riv;
#pragma unroll
    for (int r = 0; r < 8; ++r) {
      const float ri = rdl(riv, 8 * r), nb = rdl(nbv, 8 * r);
#pragma unroll
      for (int j = 0; j < 4; ++j) v[r][j] = fma2(v[r][j], f32x2{ri, ri}, f32x2{nb, nb});
    }
#pragma unroll
    for (int c = 0; c < 8; ++c) {
      u32x4 o;
#pragma unroll
      for (int e = 0; e < 4; ++e) o[e] = pk2(v[7 - 2 * e][c >> 1][c & 1], v[6 - 2 * e][c >> 1][c & 1]);
      *(LAS u32x4*)(lds + (8 * l + c) * 128 + (((7 - w) ^ (l & 7)) << 4)) = o;
    }
    LBAR();
    u32x4 tv[8];
#pragma unroll
    for (int it = 0; it < 8; ++it) tv[it] = lds128(lds + w * 8192 + it * 1024 + l * 16);
#pragma unroll
    for (int it = 0; it < 8; ++it)
      st128s(tv[it], rs, (8 * it + (l >> 3)) * 4096 + (((l & 7) ^ it) << 4), WS_XNR + w * (64 * 4096) + (L - 64 - 64 * ax) * 2);
    WAIT_V(0);
    LBAR();
    if (tid == 0) st64s(tag, rs, 0, FL_P0 + b * FS);
    STAMP(3);
  }

  if (w < 4) {
    LAS char* tw = lds + KG_T + w * KG_TW;
    float dt = rb(__expf((w >> 1) ? bhi(p_ldt2) : b2f(p_ldt2)));
    PIN(dt);
    asm volatile("s_waitcnt vmcnt(0)" : "+v"(p_lar), "+v"(p_aim), "+v"(p_cre), "+v"(p_cim) :: "memory");
    STAMP(1);
    const float ar = -rb(__expf(b2f(p_lar)));
    const float lre2 = (ar * dt) * 1.44269504f, rev = (b2f(p_aim) * dt) * 0.15915494309f;
    u32 eb = wave_max_u32(max(p_cre & 0x7f80u, p_cim & 0x7f80u)) << 16;
    eb = eb < 0x00800000u ? 0x00800000u : (eb > 0x7e800000u ? 0x7e800000u : eb);
    const float sc = __builtin_bit_cast(float, eb) * (2.f * dt), isc = __builtin_bit_cast(float, 0x7f000000u - eb);
    f32x2 e[53], tmp[8];
    u32 eh[53];
    const f32x2 w8 = cexp_p(lre2, rev, 8.f), w64 = cexp_p(lre2, rev, 64.f);
    const f32x2 z1 = cexp_p(lre2, -rev, 1.f), zc = cexp_p(lre2, -rev, 256.f);
    e[0] = f32x2{1.f, 0.f}; e[1] = w8; e[8] = w64; e[32] = f32x2{b2f(p_cre) * isc, -b2f(p_cim) * isc};
#define CM1(k, a, bb) tmp[k] = (a) * f32x2{(bb).x, (bb).x}; SCHED()
#define CM2(k, d, a, bn) d = fma2(f32x2{(a).y, (a).x}, bn, tmp[k]); SCHED()
#define CV(i) eh[i] = pkh(e[i]); SCHED()
#define WR(i) *(LAS u32*)(size_t)(ta[(i) & 15] + (i) * 256) = eh[i]; SCHED()
    const f32x2 w8n = cneg(w8), w64n = cneg(w64), z1n = cneg(z1), zcn = cneg(zc);
    f32x2 z2, z4, zd, zq;
    SCHED();
    CM1(0, w8, w8); CM1(1, z1, z1); CM1(2, zc, zc); CM1(3, e[32], z1); CM1(4, e[32], zc); CM1(5, w8, w64); CM1(6, w64, w64);
    CV(0); CV(1); CV(8); CV(32);
    CM2(0, e[2], w8, w8n); WR(0); CM2(1, z2, z1, z1n); CM2(2, zd, zc, zcn); WR(1); CM2(3, e[33], e[32], z1n); CM2(4, e[40], e[32], zcn); WR(8);
    CM2(5, e[9], w8, w64n); CM2(6, e[16], w64, w64n); WR(32);
    const f32x2 e2n = cneg(e[2]), z2n = cneg(z2), zdn = cneg(zd);
    SCHED();
    CV(2); CV(33); CV(40); CV(9); CV(16);
    CM1(0, e[2], w8); CM1(1, e[2], e[2]); WR(2); CM1(2, z2, z2); CM1(3, zd, zd); CM1(4, e[32], z2); WR(33); CM1(5, e[33], z2); CM1(6, e[32], zd); CM1(7, e[40], zd); WR(40);
    CM2(0, e[3], e[2], w8n); CM2(1, e[4], e[2], e2n); CM2(2, z4, z2, z2n); WR(9); CM2(3, zq, zd, zdn); CM2(4, e[34], e[32], z2n);
    CM2(5, e[35], e[33], z2n); WR(16); CM2(6, e[44], e[32], zdn); CM2(7, e[46], e[40], zdn);
    const f32x2 e4n = cneg(e[4]), z4n = cneg(z4), zqn = cneg(zq);
    SCHED();
    CV(3); CV(4); CV(34); CV(35); CV(44); CV(46);
    CM1(0, e[1], e[4]); CM1(1, e[2], e[4]); WR(3); CM1(2, e[3], e[4]); CM1(3, e[32], z4); CM1(4, e[33], z4); WR(4); CM1(5, e[34], z4); CM1(6, e[35], z4); CM1(7, e[32], zq); WR(34);
    CM2(0, e[5], e[1], e4n); CM2(1, e[6], e[2], e4n); WR(35); CM2(2, e[7], e[3], e4n); CM2(3, e[36], e[32], z4n); CM2(4, e[37], e[33], z4n); WR(44);
    CM2(5, e[38], e[34], z4n); CM2(6, e[39], e[35], z4n); CM2(7, e[48], e[32], zqn); WR(46);
    CV(5); CV(6); CV(7); WR(5); CV(36); CV(37); CV(38); WR(6); CV(39); CV(48);
    CM1(0, e[34], zc); WR(7); CM1(1, e[36], zc); CM1(2, e[38], zc); CM1(3, e[36], zd); WR(36); CM1(4, e[40], zq); CM1(5, e[44], zq); CM1(6, e[46], zq); WR(37); CM1(7, e[48], zq);
    CM2(0, e[41], e[34], zcn); CM2(1, e[42], e[36], zcn); WR(38); CM2(2, e[43], e[38], zcn); CM2(3, e[45], e[36], zdn); CM2(4, e[49], e[40], zqn); WR(39);
    CM2(5, e[50], e[44], zqn); CM2(6, e[51], e[46], zqn); CM2(7, e[52], e[48], zqn); WR(48);
    CV(41); CV(42); CV(43); WR(41); CV(45); CV(49); CV(50); WR(42); CV(51); CV(52);
    CM1(0, e[42], zd); WR(43); CM1(1, e[9], w64); CM1(2, e[2], w64); CM1(3, e[3], w64); WR(45); CM1(4, e[4], w64); CM1(5, e[5], w64); CM1(6, e[6], w64); WR(49); CM1(7, e[7], w64);
    CM2(0, e[47], e[42], zdn); CM2(1, e[17], e[9], w64n); WR(50); CM2(2, e[10], e[2], w64n); CM2(3, e[11], e[3], w64n); CM2(4, e[12], e[4], w64n); WR(51);
    CM2(5, e[13], e[5], w64n); CM2(6, e[14], e[6], w64n); CM2(7, e[15], e[7], w64n); WR(52);
    CV(47); CV(17); CV(10); WR(47); CV(11); CV(12); CV(13); WR(17); CV(14); CV(15);
    CM1(0, e[16], w64); WR(10); CM1(1, e[17], w64); CM1(2, e[10], w64); CM1(3, e[11], w64); WR(11); CM1(4, e[12], w64); CM1(5, e[13], w64); CM1(6, e[14], w64); WR(12); CM1(7, e[15], w64);
    CM2(0, e[24], e[16], w64n); CM2(1, e[25], e[17], w64n); WR(13); CM2(2, e[18], e[10], w64n); CM2(3, e[19], e[11], w64n); CM2(4, e[20], e[12], w64n); WR(14);
    CM2(5, e[21], e[13], w64n); CM2(6, e[22], e[14], w64n); CM2(7, e[23], e[15], w64n); WR(15);
    CV(24); CV(25); CV(18); WR(24); CV(19); CV(20); CV(21); WR(25); CV(22); CV(23);
    CM1(2, e[18], w64); WR(18); CM1(3, e[19], w64); CM1(4, e[20], w64); WR(19); CM1(5, e[21], w64); CM1(6, e[22], w64); WR(20); CM1(7, e[23], w64);
    CM2(2, e[26], e[18], w64n); WR(21); CM2(3, e[27], e[19], w64n); CM2(4, e[28], e[20], w64n); WR(22); CM2(5, e[29], e[21], w64n);
    CM2(6, e[30], e[22], w64n); WR(23); CM2(7, e[31], e[23], w64n);
    CV(26); CV(27); WR(26); CV(28); WR(27); CV(29); WR(28); CV(30); WR(29); CV(31); WR(30); WR(31);
    u32x4 fm[8], fv[8];
#pragma unroll
    for (int s = 0; s < 8; ++s) fm[s] = lds128(tw + (32 + t) * 256 + (((2 * s + h) ^ l15) << 4));
#pragma unroll
    for (int s = 0; s < 8; ++s) fv[s] = lds128(tw + t * 256 + (((2 * s + h) ^ l15) << 4));
    SCHED();
    SCHED();
    STAMP(4);
    f32x16 kk;
#pragma unroll
    for (int r = 0; r < 16; ++r) kk[r] = 0.f;
#pragma unroll
    for (int s = 0; s < 8; ++s) kk = mfma32h(fm[s], fv[s], kk);
    LAS float* kc = (LAS float*)(lds + M_KC + w * 2816);
    *(LAS f32x4*)(kc + 8 * t + 4 * h) = f32x4{kk[0], kk[1], kk[2], kk[3]} * sc;
    *(LAS f32x2*)(kc + (h ? 384 + 2 * t : 256 + 4 * t)) = f32x2{kk[4], kk[5]} * sc;
    *(LAS f32x2*)(kc + (h ? 448 + 2 * t : 258 + 4 * t)) = f32x2{kk[6], kk[7]} * sc;
    LAS float* k3 = kc + (h ? 640 + t : 512 + t);
    k3[0] = kk[8] * sc;
    if (h == 0) { k3[32] = kk[9] * sc; k3[64] = kk[10] * sc; k3[96] = kk[11] * sc; }
  }
  STAMP(5);

  u32x4 wfr[28];
  const int w2off = WS_W2F + (((n2 * 56 + 7 * w) * 4) << 10);
  auto w2ld = [&](int i) { wfr[i] = ld128s(rs, l * 16 + (i & 3) * 1024, w2off + (i >> 2) * 4096); };
  f32x2 kx[8];
  u32 kpk[4];
  auto expand_rd = [&](int e) { LAS float* q = (LAS float*)(size_t)ka_[e]; kx[e] = f32x2{q[0], q[704]}; };
  auto expand_pk = [&](int e) {
    float a = kx[2 * e].x + kx[2 * e].y, c = kx[2 * e + 1].x + kx[2 * e + 1].y;
    PIN(a); PIN(c);
    kpk[e] = pk2(a, c);
    PIN(kpk[e]);
  };
  auto expand_wr = [&]() { *(LAS u32x4*)(lds + CV_K + uch * 5120 + 1024 + ui * 16) = u32x4{kpk[0], kpk[1], kpk[2], kpk[3]}; };
  u32x4 u0;
  u32x2 u1;
  auto load_u = [&](int k) {
    if (k == 0) u0 = ld128s(rs, ui * 16, WS_XNR + (2 * b + uch) * 4096);
    else u1 = ld64s(rs, uo1, WS_XNR + (2 * b + uch) * 4096);
  };
  auto copy_u = [&](int s) {
    const u32 dd[6] = {u0.x, u0.y, u0.z, u0.w, u1.x, u1.y};
    const int o = s >> 1;
    u32x4 v;
#pragma unroll
    for (int k = 0; k < 4; ++k) v[k] = (s & 1) ? __builtin_amdgcn_alignbit(dd[o + k + 1], dd[o + k], 16) : dd[o + k];
    *(LAS u32x4*)(lds + CV_U + uch * CV_UCH + s * CV_CS + ui * 16) = v;
  };
#define W2Q 97
#define NW2AV 15
  constexpr int NW2A = NW2AV;

  if (g1) {
    f32x4 acc[4][4];
#pragma unroll
    for (int i = 0; i < 4; ++i)
#pragma unroll
      for (int j = 0; j < 4; ++j) acc[i][j] = f32x4{0.f, 0.f, 0.f, 0.f};
    u32x4 fa[2][4], fb[2][4];
    auto rdA = [&](int st, int s, int i) {
      LAS char* q = lds + (st % 3 == 0 ? wa[i & 1] : st % 3 == 1 ? wa1[i & 1] : wa2[i & 1]) + (i >> 1) * 16384 + s * 8192;
      const u32x2 lo = ldstr(q), hi = ldstr(q + 1024);
      fa[s][i] = u32x4{lo.x, lo.y, hi.x, hi.y};
    };
    auto rdB = [&](int st, int s, int j) { fb[s][j] = lds128(lds + (st % 3 == 2 ? xa2[s] : xa[s] + (st % 3) * G1_SLOT) + j * 2048); };
    const int so_cs = (mt & 2) == 0 ? WS_CS + (ft * 8 + kt0) * 1024 + w * 128 : OOB;
    const int so_p0 = w == 0 ? FL_P0 + b * FS : OOB;

    LBAR();
    STAMP(6);
    rdB(0, 0, 0); rdA(0, 0, 0); rdA(0, 0, 1); rdA(0, 0, 2); rdA(0, 0, 3); rdB(0, 0, 1); rdB(0, 0, 2); rdB(0, 0, 3);
    dmaw(IC<2>{}, 0, w); dmaw(IC<2>{}, 1, w); dmaw(IC<2>{}, 2, w);
    u32x2 fl;
    u32 msv[4];
    float nmv[4];
    f32x4 csp[2];
    u32x4 cw[4];
    f32x4 cs, cv[4];
    static_for<8>([&](auto st_) {
      constexpr int st = decltype(st_)::v;
#pragma unroll
      for (int s = 0; s < 2; ++s) {
        const int nst = s ? st + 1 : st, ns = s ^ 1;
        if (s == 1 && st < 7) {
          if (st == 1 || st == 3 || st == 4) WAIT_V(7);
          else if (st == 6) WAIT_V(11);
          else WAIT_V(6);
        }
        if (s == 1) {
          LBAR();
          if (st == 2) STAMP(18);
          if (st == 5) STAMP(21);
        }
        SCHED();
#pragma unroll
        for (int n = 0; n < 16; ++n) {
          acc[n & 3][n >> 2] = mfma16(fa[s][n & 3], fb[s][n >> 2], acc[n & 3][n >> 2]);
          SCHED();
          if (nst < 8) {
            if (n == 0) rdB(nst, ns, 3);
            if (n == 1) rdB(nst, ns, 2);
            if (n == 2) rdB(nst, ns, 1);
            if (n == 3) rdA(nst, ns, 3);
            if (n == 5) rdA(nst, ns, 2);
            if (n == 7) rdA(nst, ns, 1);
            if (n == 9) rdA(nst, ns, 0);
            if (n == 10) rdB(nst, ns, 0);
          }
          if (nst + 2 < 8) {
            const int pc = (s ? 0 : 3);
            if (n == 4 || n == 8 || n == 12) { if (s) dm0(IC<(st + 3) & 7>{}, pc + (n >> 2) - 1, w); else dm0(IC<(st + 2) & 7>{}, pc + (n >> 2) - 1, w); }
            if (n == 6 || n == 11 || n == 14) { const int k = n == 6 ? 0 : n == 11 ? 1 : 2; if (s) dgo(IC<(st + 3) & 7>{}, pc + k, w); else dgo(IC<(st + 2) & 7>{}, pc + k, w); }
          }
          SCHED();
          const int q = st * 32 + s * 16 + n;
          if (q == 4 || q == 8 || q == 12 || q == 13) cw[q == 13 ? 3 : (q >> 2) - 1] = lds128((LAS char*)(size_t)a_cw + (q == 13 ? 3 : (q >> 2) - 1) * 4096);
          if (q == 20) cs = mfma16(sel, cw[0], f32x4{0.f, 0.f, 0.f, 0.f});
          if (q == 24 || q == 28 || q == 29) cs = mfma16(sel, cw[q == 29 ? 3 : (q >> 2) - 5], cs);
          if (q == 36) {
#pragma unroll
            for (int e = 0; e < 4; ++e) { const float v = cs[e]; cs[e] = v + dppx<0x114>(v); }
            PIN(cs);
          }
          if (q == 40) {
#pragma unroll
            for (int e = 0; e < 4; ++e) { const float v = cs[e]; cs[e] = v + dppx<0x118>(v); }
            PIN(cs);
          }
          if (q == 45) st128s(__builtin_bit_cast(u32x4, cs), rs, o_cq, so_cs);
          if (q >= 52 && q < 84 && (q & 3) == 0) expand_rd((q - 52) >> 2);
          if (q >= 92 && q < 124 && (q & 7) == 4) { PIN(kx[(q - 92) >> 2]); expand_pk((q - 92) >> 3); }
          if (q == 200) expand_wr();
          if (q == 85) st64s(tag, rs, o_l0, so_p0);
          if (q == 143) asm volatile("global_load_dwordx2 %0, %1, %2 sc1" : "=v"(fl) : "v"(p0off), "s"(p.ws + FL_P0) : "memory");
          if (q == 173) {
            asm volatile("s_waitcnt vmcnt(5)" : "+v"(fl) :: "memory");
            if (!__all(fl.x == tag.x && fl.y == tag.y)) poll(rs, p0off, FL_P0, tag);
          }
          if (q == 176 || q == 178) load_u((q - 176) >> 1);
          if (q == 180 || q == 182) csp[(q - 180) >> 1] = __builtin_bit_cast(f32x4, ld128s(rs, o_cs, WS_CS + (ft * 8 + 2 * (q - 180)) * 1024));
          if (q == 184) msv[0] = ld32s(rs, l * 4, WS_ST + (mt * 128 + 64 * wt) * 4);
          if (q >= 187 && ((q - 187) & 3) == 0 && ((q - 187) >> 2) < NW2A) w2ld((q - 187) >> 2);
          if (q == 205 || q == 213 || q == 221 || q == 229) copy_u((q - 205) >> 3);
          if (q == 209) {
            PIN(csp[0]); PIN(csp[1]);
            csp[0] += csp[1];
            const auto r0 = __builtin_amdgcn_permlane32_swap(f2u(csp[0][0]), f2u(csp[0][2]), false, false);
            const auto r1 = __builtin_amdgcn_permlane32_swap(f2u(csp[0][1]), f2u(csp[0][3]), false, false);
            csp[0][0] = u2f(r0[0]) + u2f(r0[1]);
            csp[0][1] = u2f(r1[0]) + u2f(r1[1]);
            PIN(csp[0]);
          }
          if (q == 217) {
            const auto r = __builtin_amdgcn_permlane16_swap(f2u(csp[0][0]), f2u(csp[0][1]), false, false);
            *(LAS float*)(size_t)a_cs = u2f(r[0]) + u2f(r[1]);
          }
          if (q == 220) {
            PIN(msv[0]);
            const auto m = __builtin_amdgcn_permlane32_swap(msv[0], msv[0], false, false);
            msv[0] = m[0]; msv[2] = m[1];
            PIN(msv[0]); PIN(msv[2]);
          }
          if (q == 225 || q == 228) {
            const int j = q == 225 ? 0 : 2;
            const auto m = __builtin_amdgcn_permlane16_swap(msv[j], msv[j], false, false);
            msv[j] = m[0]; msv[j + 1] = m[1];
            PIN(msv[j]); PIN(msv[j + 1]);
          }
          if (q >= 232 && q < 236) { cv[q - 232] = *(LAS f32x4*)((LAS char*)(size_t)a_cv + (q - 232) * 64); }
          if (q >= 236 && q < 240) { nmv[q - 236] = -b2f(msv[q - 236]); PIN(nmv[q - 236]); }
          if (q >= 240) {
            const int m = (n + 8) & 15, ii = m & 3, jj = m >> 2;
#pragma unroll
            for (int r = 0; r < 4; ++r) {
              float v = __builtin_fmaf(nmv[jj], cv[ii][r], acc[ii][jj][r]);
              PIN(v);
              acc[ii][jj][r] = v;
            }
          }
          SCHED();
        }
      }
    });
    STAMP(7);
    __builtin_amdgcn_s_setprio(3);
#pragma unroll
    for (int j = 0; j < 4; ++j) {
      const float sd = bhi(msv[j]), sd2 = sd * sd;
      const float ce = -1.44269504f * __builtin_amdgcn_rcpf(sd);
      u32x4 o;
#pragma unroll
      for (int r = 0; r < 4; ++r) {
        const f32x2 gg = {acc[r >> 1][j][2 * (r & 1)], acc[r >> 1][j][2 * (r & 1) + 1]};
        const f32x2 uu = {acc[2 + (r >> 1)][j][2 * (r & 1)], acc[2 + (r >> 1)][j][2 * (r & 1) + 1]};
        const f32x2 a = gg * f32x2{ce, ce};
        const f32x2 e = {__builtin_amdgcn_exp2f(a.x), __builtin_amdgcn_exp2f(a.y)};
        const f32x2 d = fma2(e, f32x2{sd2, sd2}, f32x2{sd2, sd2});
        const f32x2 q = {__builtin_amdgcn_rcpf(d.x), __builtin_amdgcn_rcpf(d.y)};
        o[r] = pk2v((gg * uu) * q);
      }
      st128s(o, rs, l * 16, WS_H + (((mt * 8 + 4 * wt + j) * 56 + ft * 4 + wf) << 10));
      SCHED();
    }
    __builtin_amdgcn_s_setprio(1);
    STAMP(8);
  } else {
    poll(rs, p0off, FL_P0, tag);
    if (b == 255 && w == 0) {
#pragma unroll 1
      for (int i = 0; i < 4; ++i) poll(rs, (64 * i + l) * FS, FL_P0, tag);
      if (l == 0) st64s(tag, rs, 0, FL_EP);
    }
    LBAR();
    load_u(0); load_u(1);
#pragma unroll
    for (int e = 0; e < 8; ++e) expand_rd(e);
#pragma unroll
    for (int e = 0; e < 4; ++e) expand_pk(e);
    LBAR();
    expand_wr();
#pragma unroll
    for (int s = 0; s < 4; ++s) copy_u(s);
    SCHED();
#pragma unroll
    for (int i = 0; i < NW2A; ++i) w2ld(i);
    __builtin_amdgcn_s_waitcnt(0x0F70);
    LBAR();
  }

  STAMP(10);
  u32x2 hfl;
  u32x4 hf[28];
  const int hoff = WS_H + ((4 * m2 * 56 + 7 * w) << 10);
  const int jr = 0;
  auto hld = [&](int n) { hf[n] = ld128s(rs, l * 16, hoff + (((n / 7) ^ jr) * 56 + n % 7) * 1024); };
  const int hfs = FL_H + (m2 * 56 + 7 * w) * FS;
#define PDV 12
  constexpr int PD = PDV;
  {
    LAS char *ua = (LAS char*)(size_t)a_ua, *ka = (LAS char*)(size_t)a_ka;
    *(LAS u32x4*)(size_t)a_zk = zero4;
    *(LAS u32x4*)(size_t)a_zu = zero4;
    asm volatile("" ::: "memory");
    f32x4 acc[4];
#pragma unroll
    for (int i = 0; i < 4; ++i) acc[i] = f32x4{0.f, 0.f, 0.f, 0.f};
    const int so_h = g1 ? FL_H + ((mt * 2 + wt) * 56 + ft * 4 + wf) * FS : OOB;
    auto hook = [&](int rr, int c) {
#define HPUB 3
#define HPOLL 24
      const int sq = rr * 8 + c;
#define WS0 4
#define WSN 13
      auto nw = [&](int z) { return z <= WS0 ? 0 : z >= WS0 + WSN ? 28 - NW2A : ((z - WS0) * (28 - NW2A) + WSN - 1) / WSN; };
      if (sq == HPUB) {
        constexpr int c0 = HPUB <= WS0 ? 0 : HPUB >= WS0 + WSN ? 28 - NW2A : ((HPUB - WS0) * (28 - NW2A) + WSN - 1) / WSN;
        asm volatile("s_waitcnt vmcnt(%0)" :: "i"(c0) : "memory");
        st64s(tag, rs, o_l0, so_h);
        __builtin_amdgcn_s_setprio(0);
        STAMP(9);
      }
      if (sq >= 0 && nw(sq + 1) > nw(sq)) w2ld(NW2A + nw(sq));
      if (sq == (HPOLL < 32 ? HPOLL - 5 : 31)) hfl = ld64s(rs, hfo, hfs);
      if (sq == HPOLL) {
        STAMP(2);
        if (!__all(hfl.x == tag.x && hfl.y == tag.y)) poll(rs, hfo, hfs, tag);
        asm volatile("" ::: "memory");
        STAMP(23);
      }
      if (sq >= HPOLL) {
#pragma unroll
        for (int n = (sq - HPOLL) * PD / (HPOLL < 32 ? 32 - HPOLL : 1); n < (sq + 1 - HPOLL) * PD / (HPOLL < 32 ? 32 - HPOLL : 1); ++n) hld(n);
      }
    };
    conv_tiles(ua, ka, acc, hook);
#pragma unroll
    for (int i = 0; i < 4; ++i) *(LAS f32x4*)(lds + CV_X + (w * 4 + i) * 1024 + l * 16) = acc[i];
    if (tid == 0) *(LAS u32*)(lds + M_CNT) = 0u;
  }
  LBAR();
  STAMP(11);
  int ytile;
  {
    const int ts = (w >> 1) & 1, i = (w & 1) + 2 * (w >> 2);
    ytile = 7 - 2 * i - ts;
    const int pos = 256 * ytile + 16 * l15 + 4 * g;
    f32x4 y0[2], y1[2];
    u32x2 ur[2];
#pragma unroll
    for (int ch = 0; ch < 2; ++ch) {
      y0[ch] = *(LAS f32x4*)(lds + CV_X + ((ch + 2 * ts) * 4 + i) * 1024 + l * 16);
      y1[ch] = *(LAS f32x4*)(lds + CV_X + ((ch + 2 * ts + 4) * 4 + i) * 1024 + l * 16);
      ur[ch] = lds64(lds + CV_U + ch * CV_UCH + (2044 - pos) * 2);
    }
    LBAR();
    float res[2][4];
#pragma unroll
    for (int ch = 0; ch < 2; ++ch) {
      const float dsk = ch ? bhi(dk) : b2f(dk);
      const f32x4 y = y0[ch] + y1[ch];
      res[ch][0] = __builtin_fmaf(bhi(ur[ch].y), dsk, y[0]); res[ch][1] = __builtin_fmaf(b2f(ur[ch].y), dsk, y[1]);
      res[ch][2] = __builtin_fmaf(bhi(ur[ch].x), dsk, y[2]); res[ch][3] = __builtin_fmaf(b2f(ur[ch].x), dsk, y[3]);
    }
    const u32x4 o = {pk2(res[0][0], res[1][0]), pk2(res[0][1], res[1][1]), pk2(res[0][2], res[1][2]), pk2(res[0][3], res[1][3])};
    st128s(o, rs, pos * 4, WS_Y + b * 8192);
    if (HPOLL == 32) {
      STAMP(2);
      if (!__all(hfl.x == tag.x && hfl.y == tag.y)) poll(rs, hfo, hfs, tag);
      asm volatile("" ::: "memory");
      STAMP(23);
#pragma unroll
      for (int n = 0; n < PD; ++n) hld(n);
    }
  }
  STAMP(12);

  {
    const int oj = w >> 1, rh = w & 1;
    const int etok = 64 * m2 + 16 * (oj ^ jr) + l15, ech = 64 * n2 + 16 * g + 8 * rh;
    u32x4 xv;
    u32 yo = (ech >> 1) * 8192 + etok * 4, yfv = (8 * g + (l & 3)) * FS;
    PIN(yo); PIN(yfv);
    SCHED();
    f32x4 acc[4][4];
#pragma unroll
    for (int i = 0; i < 4; ++i)
#pragma unroll
      for (int j = 0; j < 4; ++j) acc[i][j] = f32x4{0.f, 0.f, 0.f, 0.f};
    u32x2 yfl;
    const int yfo = FL_Y + ((m2 >> 2) * 256 + 32 * n2 + 4 * rh) * FS;
    LAS char* pw = opaque(lds + w * 16384 + l * 8);
    LAS char* pr = opaque(lds + w * 2048 + l * 8);
    LAS char* pr2 = opaque(pr + 65536);
    u32 a_cnt = lds0 + M_CNT + l * 4, one = 1u;
    PIN(a_cnt); PIN(one);
    auto part_wr = [&](int j, int k) {
      *(LAS f32x2*)(pw + (j * 8 + k) * 512) = f32x2{acc[k & 3][j][2 * (k >> 2)], acc[k & 3][j][2 * (k >> 2) + 1]};
    };
#pragma unroll
    for (int n = 0; n < 28; ++n) {
#pragma unroll
      for (int i = 0; i < 4; ++i) {
        acc[i][n / 7] = mfma16(wfr[4 * (n % 7) + i], hf[n], acc[i][n / 7]);
        SCHED();
        if (i == 3 && n + PD < 28) hld(n + PD);
        if ((i & 1) == 0 && n >= 8 && n % 7 >= 1 && n % 7 < 5) part_wr(n / 7 - 1, (n % 7 - 1) * 2 + (i >> 1));
        SCHED();
      }
      if (n == YP1 && w >= 4) { asm volatile("s_waitcnt vmcnt(%0)" :: "i"(YP1 + 1) : "memory"); st64s(tag, rs, o_l0, FL_Y + (ytile * 256 + b) * FS); }
      if (n == YP0 && w < 4) { asm volatile("s_waitcnt vmcnt(%0)" :: "i"(YP0 + 1) : "memory"); st64s(tag, rs, o_l0, FL_Y + (ytile * 256 + b) * FS); }
      if (n == 21) xv = *(const GAS u32x4*)((const char*)p.x + (size_t)etok * 1024 + ech * 2);
      if (n == 22) yfl = ld64s(rs, yfv, yfo);
      if (n == TB && w >= 4) LBAR();
      SCHED();
    }
    STAMP(13);
#pragma unroll
    for (int k = 0; k < 8; ++k) part_wr(3, k);
    asm volatile("" ::: "memory");
    if (w < 4) LBAR();
    else asm volatile("ds_add_u32 %0, %1" :: "v"(a_cnt), "v"(one) : "memory");
    if (!__all(yfl.x == tag.x && yfl.y == tag.y)) poll(rs, yfv, yfo, tag);
    asm volatile("" ::: "memory");
    u32 yv[4];
#pragma unroll
    for (int j = 0; j < 4; ++j) yv[j] = ld32s(rs, yo, WS_Y + j * 8192);
    f32x2 pp[8][4];
#pragma unroll
    for (int ww = 0; ww < 4; ++ww)
#pragma unroll
      for (int i = 0; i < 4; ++i) pp[ww][i] = *(volatile LAS f32x2*)(pr + ww * 16384 + i * 512);
    if (w >= 4) {
      volatile LAS u32* cnt = (volatile LAS u32*)(lds + M_CNT);
#pragma unroll 1
      for (int spin = 0; spin < (1 << 20) && *cnt < 4u; ++spin) {}
      asm volatile("" ::: "memory");
    }
    STAMP(14);
#pragma unroll
    for (int ww = 0; ww < 4; ++ww)
#pragma unroll
      for (int i = 0; i < 4; ++i) pp[4 + ww][i] = *(volatile LAS f32x2*)(pr2 + ww * 16384 + i * 512);
    SCHED();
    f32x2 sm[4];
#pragma unroll
    for (int i = 0; i < 4; ++i) sm[i] = ((pp[0][i] + pp[1][i]) + (pp[2][i] + pp[3][i])) + ((pp[4][i] + pp[5][i]) + (pp[6][i] + pp[7][i]));
    const u32 ff[4] = {pk2(sm[0].x, sm[1].x), pk2(sm[2].x, sm[3].x), pk2(sm[0].y, sm[1].y), pk2(sm[2].y, sm[3].y)};
    u32x4 r;
#pragma unroll
    for (int j = 0; j < 4; ++j) {
      const u32 s = pk2v(up2(xv[j]) + up2(yv[j]));
      r[j] = pk2v(up2(s) + up2(ff[j]));
    }
    *(GAS u32x4*)((char*)p.out + (size_t)etok * 1024 + ech * 2) = r;
  }
  STAMP(15);
  STAMP_DUMP();
}

extern "C" void kernel_launch(void* const* d_in, const int* in_sizes, int n_in, void* d_out, int out_size, void* d_ws,
                              size_t ws_size, hipStream_t stream) {
  Params p;
  p.x = (const u16*)d_in[0]; p.lar = (const u16*)d_in[1]; p.aim = (const u16*)d_in[2]; p.cre = (const u16*)d_in[3];
  p.cim = (const u16*)d_in[4]; p.ldt = (const u16*)d_in[5]; p.dsk = (const u16*)d_in[6];
  p.w1 = (const u16*)d_in[8]; p.w3 = (const u16*)d_in[9]; p.w2 = (const u16*)d_in[10];
  p.out = (u16*)d_out; p.ws = (char*)d_ws;
  fused<<<256, 512, 0, stream>>>(p);
}
```

```cpp
#include <hip/hip_runtime.h>
#include <hip/hip_bf16.h>

typedef unsigned short u16;
typedef unsigned int u32;
typedef unsigned long long u64;
typedef __bf16 bf16x8 __attribute__((ext_vector_type(8)));
typedef __bf16 bf16x2 __attribute__((ext_vector_type(2)));
typedef _Float16 f16x8 __attribute__((ext_vector_type(8)));
typedef _Float16 f16x2 __attribute__((ext_vector_type(2)));
typedef short s16x4 __attribute__((ext_vector_type(4)));
typedef float f32x2 __attribute__((ext_vector_type(2)));
typedef float f32x4 __attribute__((ext_vector_type(4)));
typedef float f32x16 __attribute__((ext_vector_type(16)));
typedef u32 u32x2 __attribute__((ext_vector_type(2)));
typedef u32 u32x4 __attribute__((ext_vector_type(4)));
typedef __amdgpu_buffer_rsrc_t rsrc_t;
#define LAS __attribute__((address_space(3)))
#define GAS __attribute__((address_space(1)))
#define DEV __device__ __forceinline__

#define NTQ " nt"
#define TB 21
#define YP1 1
#define YP0 10
constexpr int L = 2048, D = 512, NM = 128, F = 1792;
constexpr int LDS_BYTES = 163840;
constexpr int SC1 = 16;

constexpr int WS_H = 0;
constexpr int WS_XNR = 8 << 20;
constexpr int WS_W2F = 10 << 20;
constexpr int WS_Y = 12 << 20;
constexpr int WS_ST = 14 << 20;
constexpr int WS_CS = WS_ST + 8192;
constexpr int WS_FL = 15 << 20;
constexpr int FS = 128;
constexpr int FL_EP = WS_FL;
constexpr int FL_P0 = WS_FL + 4096;
constexpr int FL_H = FL_P0 + 256 * FS;
constexpr int FL_Y = FL_H + 2048 * FS;
constexpr int WS_BYTES = 16 << 20;

struct Params {
  const u16 *x, *lar, *aim, *cre, *cim, *ldt, *dsk, *w1, *w3, *w2;
  u16* out;
  char* ws;
};

template <int V> struct IC { static constexpr int v = V; };
template <int N, int I = 0, class Fn>
DEV void static_for(Fn&& fn) { if constexpr (I < N) { fn(IC<I>{}); static_for<N, I + 1>(fn); } }
DEV u32 f2u(float f) { return __builtin_bit_cast(u32, f); }
DEV float u2f(u32 v) { return __builtin_bit_cast(float, v); }
DEV float b2f(u32 v) { return __builtin_bit_cast(float, v << 16); }
DEV float bhi(u32 v) { return __builtin_bit_cast(float, v & 0xffff0000u); }
DEV u32 pk2v(f32x2 f) { return __builtin_bit_cast(u32, __builtin_convertvector(f, bf16x2)); }
DEV u32 pk2(float lo, float hi) { return pk2v(f32x2{lo, hi}); }
DEV f32x2 up2(u32 v) { return f32x2{b2f(v), bhi(v)}; }
DEV float rb(float f) { return b2f(pk2(f, f) & 0xffffu); }
DEV f32x2 fma2(f32x2 a, f32x2 b, f32x2 c) { return __builtin_elementwise_fma(a, b, c); }
DEV f32x16 mfma32(u32x4 a, u32x4 b, f32x16 c) {
  return __builtin_amdgcn_mfma_f32_32x32x16_bf16(__builtin_bit_cast(bf16x8, a), __builtin_bit_cast(bf16x8, b), c, 0, 0, 0);
}
DEV f32x4 mfma16(u32x4 a, u32x4 b, f32x4 c) {
  return __builtin_amdgcn_mfma_f32_16x16x32_bf16(__builtin_bit_cast(bf16x8, a), __builtin_bit_cast(bf16x8, b), c, 0, 0, 0);
}
DEV f32x16 mfma32h(u32x4 a, u32x4 b, f32x16 c) {
  return __builtin_amdgcn_mfma_f32_32x32x16_f16(__builtin_bit_cast(f16x8, a), __builtin_bit_cast(f16x8, b), c, 0, 0, 0);
}
DEV u32x4 lds128(LAS char* p) { return *(LAS u32x4*)p; }
DEV u32x2 lds64(LAS char* p) { return *(LAS u32x2*)p; }
DEV u32x2 ldstr(LAS char* p) { return __builtin_bit_cast(u32x2, __builtin_amdgcn_ds_read_tr16_b64_v4i16((LAS s16x4*)p)); }
template <int LOFF>
DEV void dma_m0(u32 lbase) { asm volatile("s_add_u32 m0, %0, %1" :: "s"(lbase), "i"(LOFF) : "memory", "scc", "m0"); }
template <bool NT>
DEV void dma_go(u32x4 rs, u32 voff, u32 soff) {
  if (NT) asm volatile("buffer_load_dwordx4 %0, %1, %2 offen nt lds" :: "v"(voff), "s"(rs), "s"(soff) : "memory");
  else asm volatile("buffer_load_dwordx4 %0, %1, %2 offen lds" :: "v"(voff), "s"(rs), "s"(soff) : "memory");
}
DEV u32x4 mkrs(const void* ptr, u32 bytes) { const u64 a = (u64)ptr; return u32x4{(u32)a, (u32)(a >> 32) & 0xffffu, bytes, 0x00020000u}; }
#define WAIT_V(n) asm volatile("s_waitcnt vmcnt(" #n ")" ::: "memory")
#define WAIT_L0() asm volatile("s_waitcnt lgkmcnt(0)" ::: "memory")
#define BAR() __builtin_amdgcn_s_barrier()
#define LBAR() do { WAIT_L0(); BAR(); asm volatile("" ::: "memory"); } while (0)
#define SCHED() __builtin_amdgcn_sched_barrier(0)
#define PIN(x) asm volatile("" : "+v"(x))
constexpr int OOB = 0x04000000;
#define STAMP(i)
#define STAMP_DECL
#define STAMP_DUMP()

DEV u32x4 ld128s(rsrc_t rs, int voff, int soff) { return __builtin_amdgcn_raw_buffer_load_b128(rs, voff, soff, SC1); }
DEV u32x2 ld64s(rsrc_t rs, int voff, int soff) { return __builtin_amdgcn_raw_buffer_load_b64(rs, voff, soff, SC1); }
DEV u32 ld32s(rsrc_t rs, int voff, int soff) { return __builtin_amdgcn_raw_buffer_load_b32(rs, voff, soff, SC1); }
DEV void st128s(u32x4 v, rsrc_t rs, int voff, int soff) {
  __builtin_amdgcn_raw_buffer_store_b128(v, rs, voff, soff, SC1);
  asm volatile("s_nop 1" :: "v"(v) : "memory");
}
DEV void st64s(u32x2 v, rsrc_t rs, int voff, int soff) { __builtin_amdgcn_raw_buffer_store_b64(v, rs, voff, soff, SC1); }
DEV void st32s(u32 v, rsrc_t rs, int voff, int soff) { __builtin_amdgcn_raw_buffer_store_b32(v, rs, voff, soff, SC1); }
DEV void poll(rsrc_t rs, int voff, int soff, u32x2 tag) {
#pragma unroll 1
  for (int spin = 0; spin < (1 << 18); ++spin) {
    const u32x2 v = ld64s(rs, voff, soff);
    if (__all(v.x == tag.x && v.y == tag.y)) break;
    __builtin_amdgcn_s_sleep(4);
  }
  __builtin_amdgcn_fence(__ATOMIC_ACQUIRE, "wavefront");
  asm volatile("" ::: "memory");
}

template <int CTRL, int RM>
DEV u32 dppu(u32 v) { return (u32)__builtin_amdgcn_update_dpp(0, (int)v, CTRL, RM, 0xf, true); }
DEV u32 wave_max_u32(u32 v) {
  v = max(v, dppu<0x111, 0xf>(v)); v = max(v, dppu<0x112, 0xf>(v)); v = max(v, dppu<0x114, 0xf>(v)); v = max(v, dppu<0x118, 0xf>(v));
  v = max(v, dppu<0x142, 0xa>(v)); v = max(v, dppu<0x143, 0xc>(v));
  return (u32)__builtin_amdgcn_readlane((int)v, 63);
}

DEV float rdl(float v, int lane) { return __builtin_bit_cast(float, __builtin_amdgcn_readlane(__builtin_bit_cast(int, v), lane)); }
template <int CTRL>
DEV float dppx(float v) { return __builtin_bit_cast(float, __builtin_amdgcn_mov_dpp(__builtin_bit_cast(int, v), CTRL, 0xf, 0xf, true)); }
DEV float rows8_sum(const float (&p)[8], int l) {
  float a[4], c[2];
#pragma unroll
  for (int i = 0; i < 4; ++i) {
    const auto r = __builtin_amdgcn_permlane32_swap(__builtin_bit_cast(u32, p[i]), __builtin_bit_cast(u32, p[i + 4]), false, false);
    a[i] = __builtin_bit_cast(float, (u32)r[0]) + __builtin_bit_cast(float, (u32)r[1]);
  }
#pragma unroll
  for (int i = 0; i < 2; ++i) {
    const auto r = __builtin_amdgcn_permlane16_swap(__builtin_bit_cast(u32, a[i]), __builtin_bit_cast(u32, a[i + 2]), false, false);
    c[i] = __builtin_bit_cast(float, (u32)r[0]) + __builtin_bit_cast(float, (u32)r[1]);
  }
  const bool up = l & 8;
  float v = (up ? c[1] : c[0]) + dppx<0x128>(up ? c[0] : c[1]);
  v += dppx<0xB1>(v); v += dppx<0x4E>(v); v += dppx<0x141>(v);
  return v;
}

constexpr int G1_SLOT = 49152;
constexpr int KG_T = 2 * G1_SLOT;
constexpr int KG_TW = 53 * 256;
constexpr int SPARE = 3 * G1_SLOT;
constexpr int M_CS = SPARE;
constexpr int M_CNT = SPARE + 4096;
constexpr int M_KC = SPARE + 5120;
constexpr int CV_K = SPARE + 5120;
static_assert(KG_T + 4 * KG_TW <= M_KC && M_KC + 4 * 2816 <= LDS_BYTES, "lds");
constexpr int CV_CS = 4160;
constexpr int CV_U = 2 * G1_SLOT;
constexpr int CV_UCH = 4 * CV_CS;
constexpr int CV_X = 0;

DEV f32x2 cmul(f32x2 a, f32x2 b, f32x2 bn) { return fma2(f32x2{a.y, a.x}, bn, a * f32x2{b.x, b.x}); }
DEV f32x2 cneg(f32x2 b) { return f32x2{-b.y, b.y}; }
DEV f32x2 cexp_p(float lre2, float rev, float pw) {
  const float mg = __builtin_amdgcn_exp2f(lre2 * pw);
  const float ph = __builtin_amdgcn_fractf(rev * pw);
  return f32x2{__builtin_amdgcn_cosf(ph), __builtin_amdgcn_sinf(ph)} * f32x2{mg, mg};
}
DEV u32 pkh(f32x2 f) { return __builtin_bit_cast(u32, __builtin_convertvector(f, f16x2)); }
DEV LAS char* opaque(LAS char* q) { u32 v = (u32)(size_t)q; asm volatile("" : "+v"(v)); return (LAS char*)(size_t)v; }

template <class Hook>
DEV void conv_tiles(LAS char* ua, LAS char* ka, f32x4 (&acc)[4], Hook hook) {
  constexpr int ORD[16] = {0, 15, 13, 11, 9, 1, 14, 12, 10, 8, 2, 3, 4, 5, 6, 7};
  u32x4 fr[2][16];
  auto load = [&](int rr, int id) {
    if (id < 8) {
      const u32x2 lo = *(volatile LAS u32x2*)(ua + (7 - id) * 512 + (3 - rr) * 64), hi = *(volatile LAS u32x2*)(ua + (7 - id) * 512 + (3 - rr) * 64 + 8);
      fr[rr & 1][id] = u32x4{lo.x, lo.y, hi.x, hi.y};
    } else {
      fr[rr & 1][id] = lds128(ka + (id - 8) * 512 + (3 - rr) * 64);
    }
  };
#pragma unroll
  for (int k = 0; k < 16; ++k) { load(0, ORD[k]); SCHED(); }
  hook(0, -1);
  SCHED();
#pragma unroll
  for (int rr = 0; rr < 4; ++rr) {
#pragma unroll
    for (int c = 0; c < 8; ++c) {
      u32x4(&f)[16] = fr[rr & 1];
#pragma unroll
      for (int i = 0; i < 4; ++i) {
        if (c <= 7 - 2 * i) { acc[i] = mfma16(f[c], f[8 + 7 - 2 * i - c], acc[i]); SCHED(); }
        if (rr < 3 && i < 2) { load(rr + 1, ORD[2 * c + i]); SCHED(); }
      }
      hook(rr, c);
      SCHED();
    }
  }
}

__global__ __launch_bounds__(512, 2) void fused(Params p) {
  __shared__ __attribute__((aligned(16))) char smem[LDS_BYTES];
#define CODEPF 23000
  u32x4 pf;
  if (__builtin_amdgcn_readfirstlane(threadIdx.x >> 6) == 4) {
    u64 pc;
    asm volatile("s_getpc_b64 %0" : "=s"(pc));
    const u32 po = min((u32)((blockIdx.x >> 3) * 1024 + (threadIdx.x & 63) * 16), (u32)(CODEPF - 16));
    asm volatile("global_load_dwordx4 %0, %1, %2" : "=v"(pf) : "v"(po), "s"(pc & ~(u64)255) : "memory");
  }
  asm volatile("" : "+s"(p.x), "+s"(p.lar), "+s"(p.aim), "+s"(p.cre), "+s"(p.cim), "+s"(p.ldt), "+s"(p.dsk), "+s"(p.w1), "+s"(p.w3),
               "+s"(p.w2), "+s"(p.out), "+s"(p.ws));
  LAS char* lds = (LAS char*)smem;
  const u32 lds0 = (u32)(size_t)lds;
  const int tid = threadIdx.x, l = tid & 63;
  const int w = __builtin_amdgcn_readfirstlane(tid >> 6);
  const int b = blockIdx.x, xcd = b & 7, rk = b >> 3;
  const bool g1 = rk < 28;
  const rsrc_t rs = __builtin_amdgcn_make_buffer_rsrc(p.ws, 0, WS_BYTES, 0x00020000);
  const int l15 = l & 15, g = l >> 4, t = l & 31, h = l >> 5;
  STAMP_DECL;
  STAMP(0);

  u32x2 tag;
  u32 p_ldt2, dk;
  asm volatile("s_load_dwordx2 %0, %3, 0x0\n\ts_load_dword %1, %4, 0x0\n\ts_load_dword %2, %5, 0x0"
               : "=&s"(tag), "=&s"(p_ldt2), "=&s"(dk) : "s"(p.ws + FL_EP), "s"(p.ldt + 2 * b), "s"(p.dsk + 2 * b) : "memory");

  u32 p_lar, p_aim, p_cre, p_cim;
  if (w < 4) {
    const u32 po = (b * 256 + tid) * 2;
    asm volatile("global_load_ushort %0, %1, %2" NTQ : "=v"(p_lar) : "v"(po), "s"(p.lar) : "memory");
    asm volatile("global_load_ushort %0, %1, %2" NTQ : "=v"(p_aim) : "v"(po), "s"(p.aim) : "memory");
    asm volatile("global_load_ushort %0, %1, %2" NTQ : "=v"(p_cre) : "v"(po), "s"(p.cre) : "memory");
    asm volatile("global_load_ushort %0, %1, %2" NTQ : "=v"(p_cim) : "v"(po), "s"(p.cim) : "memory");
  }
  SCHED();

  const int mt = (xcd >> 1) * 4 + (rk & 3), ft = (xcd & 1) * 7 + (rk >> 2);
#define ROTV 5
  const int kt0 = ROTV == 0 ? 0 : ROTV == 1 ? (ft + 2 * (mt & 3) + ((mt >> 2) & 1)) & 7 : ROTV == 2 ? (2 * (xcd >> 1) + 4 * (xcd & 1)) & 7 : (2 * (xcd >> 1) + 4 * (xcd & 1) + (mt & 1)) & 7;
  const int wt = w & 1, wf = w >> 1;
  const u32x4 rx = mkrs(p.x, L * D * 2), rw1 = mkrs(p.w1, D * F * 2), rw3 = mkrs(p.w3, D * F * 2);
  const u32 xo0 = (l >> 3) * 1024 + (((l & 7) ^ g) << 4);
  const u32 xo1 = (8 + (l >> 3)) * 1024 + (((l & 7) ^ (4 + g)) << 4);
  const u32 wo1 = g * (F * 2) + ((l15 ^ (4 * g + 2 * (w & 1))) << 4), wo1b = wo1 + 4 * F * 2;
  const u32 wo3 = wo1, wo3b = wo1b;
#define NTS 0
  auto dm0 = [&](auto st_, int piece, int ww) {
    constexpr int S = (decltype(st_)::v % 3) * G1_SLOT;
    const u32 lb = lds0 + ww * 2048;
    if (piece == 0) dma_m0<S>(lb);
    if (piece == 1) dma_m0<S + 16384>(lb);
    if (piece == 2) dma_m0<S + 32768>(lb);
    if (piece == 3) dma_m0<S + 1024>(lb);
    if (piece == 4) dma_m0<S + 16384 + 1024>(lb);
    if (piece == 5) dma_m0<S + 32768 + 1024>(lb);
  };
  auto dgo = [&](auto st_, int piece, int ww) {
    constexpr bool nt = decltype(st_)::v < NTS;
    const int kt = (kt0 + decltype(st_)::v) & 7;
    const u32 sx = (mt * 128 + ww * 16) * 1024 + kt * 128, sw = (kt * 64 + ww * 8) * (F * 2) + ft * 256;
    if (piece == 0) dma_go<nt>(rx, xo0, sx);
    if (piece == 1) dma_go<nt>(rw1, wo1, sw);
    if (piece == 2) dma_go<nt>(rw3, wo3, sw);
    if (piece == 3) dma_go<nt>(rx, xo1, sx);
    if (piece == 4) dma_go<nt>(rw1, wo1b, sw);
    if (piece == 5) dma_go<nt>(rw3, wo3b, sw);
  };
  auto dmaw = [&](auto st_, int piece, int ww) { dm0(st_, piece, ww); asm volatile("s_nop 0"); dgo(st_, piece, ww); };

  u32x2 wr[8];
  u32x4 xr[8];
  const int un2 = b / 28, ukb = 2 * (b - 28 * un2) + (w & 1);
  const int ax = xcd * 4 + (rk - 28);
  if (w >= 4 && g1) {
#pragma unroll
    for (int i = 0; i < 6; ++i) { dmaw(IC<0>{}, i, w - 4); dmaw(IC<0>{}, i, w); }
    if (w < 6) {
      const char* wb2 = (const char*)p.w2 + (size_t)(32 * ukb) * 1024 + un2 * 128;
      const u32 wv = g * 4096 + l15 * 8;
#define W2RAW(j, base, off) asm volatile("global_load_dwordx2 %0, %1, %2 offset:" #off NTQ : "=v"(wr[j]) : "v"(wv), "s"(base) : "memory")
      W2RAW(0, wb2, 0); W2RAW(1, wb2, 1024); W2RAW(2, wb2, 2048); W2RAW(3, wb2, 3072);
      W2RAW(4, wb2 + 16384, 0); W2RAW(5, wb2 + 16384, 1024); W2RAW(6, wb2 + 16384, 2048); W2RAW(7, wb2 + 16384, 3072);
    }
  }
  if (!g1) {
    const u32 xv_ = (64 * ax + 8 * w) * 1024 + l * 16;
#define XRAW(j, vo, off) asm volatile("global_load_dwordx4 %0, %1, %2 offset:" #off : "=v"(xr[j]) : "v"(vo), "s"(p.x) : "memory")
    XRAW(0, xv_, 0); XRAW(1, xv_, 1024); XRAW(2, xv_, 2048); XRAW(3, xv_, 3072);
    XRAW(4, xv_ + 4096, 0); XRAW(5, xv_ + 4096, 1024); XRAW(6, xv_ + 4096, 2048); XRAW(7, xv_ + 4096, 3072);
  }
  SCHED();

  const int m2 = xcd * 4 + (rk & 3), n2 = rk >> 2;
  u32 p0off;
  {
    const int e = min(9 * w + min(l, 8), 67);
    const int m = e - 60;
    const int pb = e < 32 ? 224 + e : (e < 60 ? 28 * n2 + (e - 32) : (g1 ? ((m >> 1) * 2 + (xcd & 1)) + 8 * ((m & 1) + 4 * (rk >> 2)) : 224));
    p0off = pb * FS;
  }
  const int uch = w >> 2, ui = tid & 255;
  u32 ka_[8];
#pragma unroll
  for (int e = 0; e < 4; ++e) {
    const int l0 = 8 * ui + 2 * e;
    const u32 bits = pk2((float)l0, (float)(l0 + 1));
    const int i0 = ui < 32 ? l0 : (int)(bits & 0xffffu) - (0x4300 - 128);
    const int i1 = ui < 32 ? l0 + 1 : (int)(bits >> 16) - (0x4300 - 128);
    ka_[2 * e] = lds0 + M_KC + uch * 5632 + i0 * 4;
    ka_[2 * e + 1] = lds0 + M_KC + uch * 5632 + i1 * 4;
  }
  u32 uo1 = ui < 255 ? ui * 16 + 16 : OOB;
  int xa[2], wa[2];
#pragma unroll
  for (int s = 0; s < 2; ++s) xa[s] = (64 * wt + l15) * 128 + (((4 * s + g) ^ ((l15 >> 1) & 7)) << 4);
  {
    const int q = l15 >> 2, pp = l & 3;
#pragma unroll
    for (int i = 0; i < 2; ++i) wa[i] = 16384 + (8 * g + q) * 256 + (((4 * wf + 2 * i + (pp >> 1)) ^ (4 * q + 2 * (g & 1))) << 4) + 8 * (pp & 1);
  }
  u32x4 sel;
#pragma unroll
  for (int e = 0; e < 4; ++e) sel[e] = l15 == 2 * e ? 0x00003f80u : (l15 == 2 * e + 1 ? 0x3f800000u : 0u);
  u32 a_cw = lds0 + 16384 * (1 + (w >> 2)) + (4 * g + (l15 >> 2)) * 256 + (((4 * (w & 3) + (l & 3)) ^ (4 * (l15 >> 2) + 2 * h)) << 4);
  u32 o_cq = (l < 32 && l15 >= 12) ? (l & 3) * 32 + g * 16 : OOB;
  u32 o_l0 = l == 0 ? 0 : OOB;
  u32 a_ua = lds0 + CV_U + (w & 1) * CV_UCH + (3 - (l15 & 3)) * CV_CS + 4088 - 8 * (l15 >> 2) - 32 * h + 16 * (g & 1) - 7 * 512 - 256 * (w >> 2) - 3 * 64;
  u32 a_ka = lds0 + CV_K + (w & 1) * 5120 + 1024 - 512 * ((w >> 1) & 1) + 32 * (l15 - h) + 16 * (g & 1) - 256 * (w >> 2) - 3 * 64;
  u32 a_zk = lds0 + CV_K + (w & 1) * 5120 + l * 16;
  u32 a_zu = lds0 + CV_U + (w & 1) * CV_UCH + ((l >> 1) & 3) * CV_CS + 4096 + (l & 1) * 16;
  u32 hfo = (l < 7 ? l : 0) * FS;
  u32x4 zero4 = {0u, 0u, 0u, 0u};
  PIN(a_ua); PIN(a_ka); PIN(a_zk); PIN(a_zu); PIN(hfo); PIN(zero4);

  u32 o_cs = g * 1024 + (l15 >> 3) * 512 + (32 * wf + 4 * (l15 & 7)) * 4;
  u32 a_cs = lds0 + M_CS + w * 256 + (4 * l15 + 2 * h + (g & 1)) * 4;
  PIN(a_cs);
  u32 a_cv = lds0 + M_CS + w * 256 + g * 16;

  PIN(sel); PIN(a_cw); PIN(o_cq); PIN(o_l0); PIN(o_cs); PIN(a_cv); PIN(p0off); PIN(uo1);
#pragma unroll
  for (int e = 0; e < 8; ++e) PIN(ka_[e]);
  int xa2[2], wa1[2], wa2[2];
#pragma unroll
  for (int e = 0; e < 2; ++e) {
    xa2[e] = xa[e] + 2 * G1_SLOT; wa1[e] = wa[e] + G1_SLOT; wa2[e] = wa[e] + 2 * G1_SLOT;
    PIN(xa[e]); PIN(xa2[e]); PIN(wa[e]); PIN(wa1[e]); PIN(wa2[e]);
  }
  SCHED();
  if (g1 && w >= 4) {
#pragma unroll
    for (int i = 0; i < 6; ++i) { dmaw(IC<1>{}, i, w - 4); dmaw(IC<1>{}, i, w); }
    asm volatile("s_waitcnt vmcnt(12)" : "+v"(wr[0]), "+v"(wr[1]), "+v"(wr[2]), "+v"(wr[3]), "+v"(wr[4]), "+v"(wr[5]), "+v"(wr[6]), "+v"(wr[7]) :: "memory");
    PIN(pf);
    if (w < 6) {
#pragma unroll
      for (int c = 0; c < 4; ++c) {
        u32x4 o;
#pragma unroll
        for (int e = 0; e < 4; ++e) o[e] = __builtin_amdgcn_perm(wr[2 * e + 1][c >> 1], wr[2 * e][c >> 1], (c & 1) ? 0x07060302u : 0x05040100u);
        st128s(o, rs, l * 16 + c * 1024, WS_W2F + (((un2 * 56 + ukb) * 4) << 10));
      }
    }
  }
  asm volatile("s_waitcnt lgkmcnt(0)" : "+s"(tag), "+s"(p_ldt2), "+s"(dk) :: "memory");
  tag.x += 1u;

  if (!g1) {
    asm volatile("s_waitcnt vmcnt(0)" : "+v"(xr[0]), "+v"(xr[1]), "+v"(xr[2]), "+v"(xr[3]), "+v"(xr[4]), "+v"(xr[5]), "+v"(xr[6]), "+v"(xr[7]) :: "memory");
    PIN(pf);
    f32x2 v[8][4];
    float ps[8];
#pragma unroll
    for (int r = 0; r < 8; ++r) {
#pragma unroll
      for (int j = 0; j < 4; ++j) v[r][j] = up2(xr[r][j]);
      const f32x2 s2 = (v[r][0] + v[r][1]) + (v[r][2] + v[r][3]);
      ps[r] = s2.x + s2.y;
    }
    const float mean = rows8_sum(ps, l) * (1.f / D);
#pragma unroll
    for (int r = 0; r < 8; ++r) {
      const float mr = rdl(mean, 8 * r);
      f32x2 q2 = {0.f, 0.f};
#pragma unroll
      for (int j = 0; j < 4; ++j) { v[r][j] -= f32x2{mr, mr}; q2 = fma2(v[r][j], v[r][j], q2); }
      ps[r] = q2.x + q2.y;
    }
    const float var = rows8_sum(ps, l) * (1.f / D);
    const u32 ms = pk2(mean, __builtin_sqrtf(rb(rb(var) + 1e-5f)));
    if ((l & 7) == 0) st32s(ms, rs, (l >> 3) * 4, WS_ST + (64 * ax + 8 * w) * 4);
    const float riv = 1.f / bhi(ms), nbv = (mean - b2f(ms)) * riv;
#pragma unroll
    for (int r = 0; r < 8; ++r) {
      const float ri = rdl(riv, 8 * r), nb = rdl(nbv, 8 * r);
#pragma unroll
      for (int j = 0; j < 4; ++j) v[r][j] = fma2(v[r][j], f32x2{ri, ri}, f32x2{nb, nb});
    }
#pragma unroll
    for (int c = 0; c < 8; ++c) {
      u32x4 o;
#pragma unroll
      for (int e = 0; e < 4; ++e) o[e] = pk2(v[7 - 2 * e][c >> 1][c & 1], v[6 - 2 * e][c >> 1][c & 1]);
      *(LAS u32x4*)(lds + (8 * l + c) * 128 + (((7 - w) ^ (l & 7)) << 4)) = o;
    }
    LBAR();
    u32x4 tv[8];
#pragma unroll
    for (int it = 0; it < 8; ++it) tv[it] = lds128(lds + w * 8192 + it * 1024 + l * 16);
#pragma unroll
    for (int it = 0; it < 8; ++it)
      st128s(tv[it], rs, (8 * it + (l >> 3)) * 4096 + (((l & 7) ^ it) << 4), WS_XNR + w * (64 * 4096) + (L - 64 - 64 * ax) * 2);
    WAIT_V(0);
    LBAR();
    if (tid == 0) st64s(tag, rs, 0, FL_P0 + b * FS);
    STAMP(3);
  }

  if (w < 4) {
    LAS char* tw = lds + KG_T + w * KG_TW;
    u32 ta[16];
#pragma unroll
    for (int k = 0; k < 16; ++k) { ta[k] = ((u32)(size_t)tw + l * 4) ^ (k << 4); PIN(ta[k]); }
    float dt = rb(__expf((w >> 1) ? bhi(p_ldt2) : b2f(p_ldt2)));
    PIN(dt);
    asm volatile("s_waitcnt vmcnt(0)" : "+v"(p_lar), "+v"(p_aim), "+v"(p_cre), "+v"(p_cim) :: "memory");
    STAMP(1);
    const float ar = -rb(__expf(b2f(p_lar)));
    const float lre2 = (ar * dt) * 1.44269504f, rev = (b2f(p_aim) * dt) * 0.15915494309f;
    u32 eb = wave_max_u32(max(p_cre & 0x7f80u, p_cim & 0x7f80u)) << 16;
    eb = eb < 0x00800000u ? 0x00800000u : (eb > 0x7e800000u ? 0x7e800000u : eb);
    const float sc = __builtin_bit_cast(float, eb) * (2.f * dt), isc = __builtin_bit_cast(float, 0x7f000000u - eb);
    f32x2 e[53], tmp[8];
    u32 eh[53];
    const f32x2 w8 = cexp_p(lre2, rev, 8.f), w64 = cexp_p(lre2, rev, 64.f);
    const f32x2 z1 = cexp_p(lre2, -rev, 1.f), zc = cexp_p(lre2, -rev, 256.f);
    e[0] = f32x2{1.f, 0.f}; e[1] = w8; e[8] = w64; e[32] = f32x2{b2f(p_cre) * isc, -b2f(p_cim) * isc};
#define CM1(k, a, bb) tmp[k] = (a) * f32x2{(bb).x, (bb).x}; SCHED()
#define CM2(k, d, a, bn) d = fma2(f32x2{(a).y, (a).x}, bn, tmp[k]); SCHED()
#define CV(i) eh[i] = pkh(e[i]); SCHED()
#define WR(i) *(LAS u32*)(size_t)(ta[(i) & 15] + (i) * 256) = eh[i]; SCHED()
    const f32x2 w8n = cneg(w8), w64n = cneg(w64), z1n = cneg(z1), zcn = cneg(zc);
    f32x2 z2, z4, zd, zq;
    SCHED();
    CM1(0, w8, w8); CM1(1, z1, z1); CM1(2, zc, zc); CM1(3, e[32], z1); CM1(4, e[32], zc); CM1(5, w8, w64); CM1(6, w64, w64);
    CV(0); CV(1); CV(8); CV(32);
    CM2(0, e[2], w8, w8n); WR(0); CM2(1, z2, z1, z1n); CM2(2, zd, zc, zcn); WR(1); CM2(3, e[33], e[32], z1n); CM2(4, e[40], e[32], zcn); WR(8);
    CM2(5, e[9], w8, w64n); CM2(6, e[16], w64, w64n); WR(32);
    const f32x2 e2n = cneg(e[2]), z2n = cneg(z2), zdn = cneg(zd);
    SCHED();
    CV(2); CV(33); CV(40); CV(9); CV(16);
    CM1(0, e[2], w8); CM1(1, e[2], e[2]); WR(2); CM1(2, z2, z2); CM1(3, zd, zd); CM1(4, e[32], z2); WR(33); CM1(5, e[33], z2); CM1(6, e[32], zd); CM1(7, e[40], zd); WR(40);
    CM2(0, e[3], e[2], w8n); CM2(1, e[4], e[2], e2n); CM2(2, z4, z2, z2n); WR(9); CM2(3, zq, zd, zdn); CM2(4, e[34], e[32], z2n);
    CM2(5, e[35], e[33], z2n); WR(16); CM2(6, e[44], e[32], zdn); CM2(7, e[46], e[40], zdn);
    const f32x2 e4n = cneg(e[4]), z4n = cneg(z4), zqn = cneg(zq);
    SCHED();
    CV(3); CV(4); CV(34); CV(35); CV(44); CV(46);
    CM1(0, e[1], e[4]); CM1(1, e[2], e[4]); WR(3); CM1(2, e[3], e[4]); CM1(3, e[32], z4); CM1(4, e[33], z4); WR(4); CM1(5, e[34], z4); CM1(6, e[35], z4); CM1(7, e[32], zq); WR(34);
    CM2(0, e[5], e[1], e4n); CM2(1, e[6], e[2], e4n); WR(35); CM2(2, e[7], e[3], e4n); CM2(3, e[36], e[32], z4n); CM2(4, e[37], e[33], z4n); WR(44);
    CM2(5, e[38], e[34], z4n); CM2(6, e[39], e[35], z4n); CM2(7, e[48], e[32], zqn); WR(46);
    CV(5); CV(6); CV(7); WR(5); CV(36); CV(37); CV(38); WR(6); CV(39); CV(48);
    CM1(0, e[34], zc); WR(7); CM1(1, e[36], zc); CM1(2, e[38], zc); CM1(3, e[36], zd); WR(36); CM1(4, e[40], zq); CM1(5, e[44], zq); CM1(6, e[46], zq); WR(37); CM1(7, e[48], zq);
    CM2(0, e[41], e[34], zcn); CM2(1, e[42], e[36], zcn); WR(38); CM2(2, e[43], e[38], zcn); CM2(3, e[45], e[36], zdn); CM2(4, e[49], e[40], zqn); WR(39);
    CM2(5, e[50], e[44], zqn); CM2(6, e[51], e[46], zqn); CM2(7, e[52], e[48], zqn); WR(48);
    CV(41); CV(42); CV(43); WR(41); CV(45); CV(49); CV(50); WR(42); CV(51); CV(52);
    CM1(0, e[42], zd); WR(43); CM1(1, e[9], w64); CM1(2, e[2], w64); CM1(3, e[3], w64); WR(45); CM1(4, e[4], w64); CM1(5, e[5], w64); CM1(6, e[6], w64); WR(49); CM1(7, e[7], w64);
    CM2(0, e[47], e[42], zdn); CM2(1, e[17], e[9], w64n); WR(50); CM2(2, e[10], e[2], w64n); CM2(3, e[11], e[3], w64n); CM2(4, e[12], e[4], w64n); WR(51);
    CM2(5, e[13], e[5], w64n); CM2(6, e[14], e[6], w64n); CM2(7, e[15], e[7], w64n); WR(52);
    CV(47); CV(17); CV(10); WR(47); CV(11); CV(12); CV(13); WR(17); CV(14); CV(15);
    CM1(0, e[16], w64); WR(10); CM1(1, e[17], w64); CM1(2, e[10], w64); CM1(3, e[11], w64); WR(11); CM1(4, e[12], w64); CM1(5, e[13], w64); CM1(6, e[14], w64); WR(12); CM1(7, e[15], w64);
    CM2(0, e[24], e[16], w64n); CM2(1, e[25], e[17], w64n); WR(13); CM2(2, e[18], e[10], w64n); CM2(3, e[19], e[11], w64n); CM2(4, e[20], e[12], w64n); WR(14);
    CM2(5, e[21], e[13], w64n); CM2(6, e[22], e[14], w64n); CM2(7, e[23], e[15], w64n); WR(15);
    CV(24); CV(25); CV(18); WR(24); CV(19); CV(20); CV(21); WR(25); CV(22); CV(23);
    CM1(2, e[18], w64); WR(18); CM1(3, e[19], w64); CM1(4, e[20], w64); WR(19); CM1(5, e[21], w64); CM1(6, e[22], w64); WR(20); CM1(7, e[23], w64);
    CM2(2, e[26], e[18], w64n); WR(21); CM2(3, e[27], e[19], w64n); CM2(4, e[28], e[20], w64n); WR(22); CM2(5, e[29], e[21], w64n);
    CM2(6, e[30], e[22], w64n); WR(23); CM2(7, e[31], e[23], w64n);
    CV(26); CV(27); WR(26); CV(28); WR(27); CV(29); WR(28); CV(30); WR(29); CV(31); WR(30); WR(31);
    u32x4 fm[8], fv[8];
#pragma unroll
    for (int s = 0; s < 8; ++s) fm[s] = lds128(tw + (32 + t) * 256 + (((2 * s + h) ^ l15) << 4));
#pragma unroll
    for (int s = 0; s < 8; ++s) fv[s] = lds128(tw + t * 256 + (((2 * s + h) ^ l15) << 4));
    SCHED();
    SCHED();
    STAMP(4);
    f32x16 kk;
#pragma unroll
    for (int r = 0; r < 16; ++r) kk[r] = 0.f;
#pragma unroll
    for (int s = 0; s < 8; ++s) kk = mfma32h(fm[s], fv[s], kk);
    LAS float* kc = (LAS float*)(lds + M_KC + w * 2816);
    *(LAS f32x4*)(kc + 8 * t + 4 * h) = f32x4{kk[0], kk[1], kk[2], kk[3]} * sc;
    *(LAS f32x2*)(kc + (h ? 384 + 2 * t : 256 + 4 * t)) = f32x2{kk[4], kk[5]} * sc;
    *(LAS f32x2*)(kc + (h ? 448 + 2 * t : 258 + 4 * t)) = f32x2{kk[6], kk[7]} * sc;
    LAS float* k3 = kc + (h ? 640 + t : 512 + t);
    k3[0] = kk[8] * sc;
    if (h == 0) { k3[32] = kk[9] * sc; k3[64] = kk[10] * sc; k3[96] = kk[11] * sc; }
  }
  STAMP(5);

  u32x4 wfr[28];
  const int w2off = WS_W2F + (((n2 * 56 + 7 * w) * 4) << 10);
  auto w2ld = [&](int i) { wfr[i] = ld128s(rs, l * 16 + (i & 3) * 1024, w2off + (i >> 2) * 4096); };
  f32x2 kx[8];
  u32 kpk[4];
  auto expand_rd = [&](int e) { LAS float* q = (LAS float*)(size_t)ka_[e]; kx[e] = f32x2{q[0], q[704]}; };
  auto expand_pk = [&](int e) {
    float a = kx[2 * e].x + kx[2 * e].y, c = kx[2 * e + 1].x + kx[2 * e + 1].y;
    PIN(a); PIN(c);
    kpk[e] = pk2(a, c);
    PIN(kpk[e]);
  };
  auto expand_wr = [&]() { *(LAS u32x4*)(lds + CV_K + uch * 5120 + 1024 + ui * 16) = u32x4{kpk[0], kpk[1], kpk[2], kpk[3]}; };
  u32x4 u0;
  u32x2 u1;
  auto load_u = [&](int k) {
    if (k == 0) u0 = ld128s(rs, ui * 16, WS_XNR + (2 * b + uch) * 4096);
    else u1 = ld64s(rs, uo1, WS_XNR + (2 * b + uch) * 4096);
  };
  auto copy_u = [&](int s) {
    const u32 dd[6] = {u0.x, u0.y, u0.z, u0.w, u1.x, u1.y};
    const int o = s >> 1;
    u32x4 v;
#pragma unroll
    for (int k = 0; k < 4; ++k) v[k] = (s & 1) ? __builtin_amdgcn_alignbit(dd[o + k + 1], dd[o + k], 16) : dd[o + k];
    *(LAS u32x4*)(lds + CV_U + uch * CV_UCH + s * CV_CS + ui * 16) = v;
  };
#define W2Q 97
#define NW2AV 15
  constexpr int NW2A = NW2AV;

  if (g1) {
    f32x4 acc[4][4];
#pragma unroll
    for (int i = 0; i < 4; ++i)
#pragma unroll
      for (int j = 0; j < 4; ++j) acc[i][j] = f32x4{0.f, 0.f, 0.f, 0.f};
    u32x4 fa[2][4], fb[2][4];
    auto rdA = [&](int st, int s, int i) {
      LAS char* q = lds + (st % 3 == 0 ? wa[i & 1] : st % 3 == 1 ? wa1[i & 1] : wa2[i & 1]) + (i >> 1) * 16384 + s * 8192;
      const u32x2 lo = ldstr(q), hi = ldstr(q + 1024);
      fa[s][i] = u32x4{lo.x, lo.y, hi.x, hi.y};
    };
    auto rdB = [&](int st, int s, int j) { fb[s][j] = lds128(lds + (st % 3 == 2 ? xa2[s] : xa[s] + (st % 3) * G1_SLOT) + j * 2048); };
    const int so_cs = (mt & 2) == 0 ? WS_CS + (ft * 8 + kt0) * 1024 + w * 128 : OOB;
    const int so_p0 = w == 0 ? FL_P0 + b * FS : OOB;

    LBAR();
    STAMP(6);
    rdB(0, 0, 0); rdA(0, 0, 0); rdA(0, 0, 1); rdA(0, 0, 2); rdA(0, 0, 3); rdB(0, 0, 1); rdB(0, 0, 2); rdB(0, 0, 3);
    dmaw(IC<2>{}, 0, w); dmaw(IC<2>{}, 1, w); dmaw(IC<2>{}, 2, w);
    u32x2 fl;
    u32 msv[4];
    float nmv[4];
    f32x4 csp[2];
    u32x4 cw[4];
    f32x4 cs, cv[4];
    static_for<8>([&](auto st_) {
      constexpr int st = decltype(st_)::v;
#pragma unroll
      for (int s = 0; s < 2; ++s) {
        const int nst = s ? st + 1 : st, ns = s ^ 1;
        if (s == 1 && st < 7) {
          if (st == 1 || st == 3 || st == 4) WAIT_V(7);
          else if (st == 6) WAIT_V(11);
          else WAIT_V(6);
        }
        if (s == 1) {
          LBAR();
          if (st == 2) STAMP(18);
          if (st == 5) STAMP(21);
        }
        SCHED();
#pragma unroll
        for (int n = 0; n < 16; ++n) {
          acc[n & 3][n >> 2] = mfma16(fa[s][n & 3], fb[s][n >> 2], acc[n & 3][n >> 2]);
          SCHED();
          if (nst < 8) {
            if (n == 0) rdB(nst, ns, 3);
            if (n == 1) rdB(nst, ns, 2);
            if (n == 2) rdB(nst, ns, 1);
            if (n == 3) rdA(nst, ns, 3);
            if (n == 5) rdA(nst, ns, 2);
            if (n == 7) rdA(nst, ns, 1);
            if (n == 9) rdA(nst, ns, 0);
            if (n == 10) rdB(nst, ns, 0);
          }
          if (nst + 2 < 8) {
            const int pc = (s ? 0 : 3);
            if (n == 4 || n == 8 || n == 12) { if (s) dm0(IC<(st + 3) & 7>{}, pc + (n >> 2) - 1, w); else dm0(IC<(st + 2) & 7>{}, pc + (n >> 2) - 1, w); }
            if (n == 6 || n == 11 || n == 14) { const int k = n == 6 ? 0 : n == 11 ? 1 : 2; if (s) dgo(IC<(st + 3) & 7>{}, pc + k, w); else dgo(IC<(st + 2) & 7>{}, pc + k, w); }
          }
          SCHED();
          const int q = st * 32 + s * 16 + n;
          if (q == 4 || q == 8 || q == 12 || q == 13) cw[q == 13 ? 3 : (q >> 2) - 1] = lds128((LAS char*)(size_t)a_cw + (q == 13 ? 3 : (q >> 2) - 1) * 4096);
          if (q == 20) cs = mfma16(sel, cw[0], f32x4{0.f, 0.f, 0.f, 0.f});
          if (q == 24 || q == 28 || q == 29) cs = mfma16(sel, cw[q == 29 ? 3 : (q >> 2) - 5], cs);
          if (q == 36) {
#pragma unroll
            for (int e = 0; e < 4; ++e) { const float v = cs[e]; cs[e] = v + dppx<0x114>(v); }
            PIN(cs);
          }
          if (q == 40) {
#pragma unroll
            for (int e = 0; e < 4; ++e) { const float v = cs[e]; cs[e] = v + dppx<0x118>(v); }
            PIN(cs);
          }
          if (q == 45) st128s(__builtin_bit_cast(u32x4, cs), rs, o_cq, so_cs);
          if (q >= 52 && q < 84 && (q & 3) == 0) expand_rd((q - 52) >> 2);
          if (q >= 92 && q < 124 && (q & 7) == 4) { PIN(kx[(q - 92) >> 2]); expand_pk((q - 92) >> 3); }
          if (q == 200) expand_wr();
          if (q == 85) st64s(tag, rs, o_l0, so_p0);
          if (q == 143) asm volatile("global_load_dwordx2 %0, %1, %2 sc1" : "=v"(fl) : "v"(p0off), "s"(p.ws + FL_P0) : "memory");
          if (q == 173) {
            asm volatile("s_waitcnt vmcnt(5)" : "+v"(fl) :: "memory");
            if (!__all(fl.x == tag.x && fl.y == tag.y)) poll(rs, p0off, FL_P0, tag);
          }
          if (q == 176 || q == 178) load_u((q - 176) >> 1);
          if (q == 180 || q == 182) csp[(q - 180) >> 1] = __builtin_bit_cast(f32x4, ld128s(rs, o_cs, WS_CS + (ft * 8 + 2 * (q - 180)) * 1024));
          if (q == 184) msv[0] = ld32s(rs, l * 4, WS_ST + (mt * 128 + 64 * wt) * 4);
          if (q >= 187 && ((q - 187) & 3) == 0 && ((q - 187) >> 2) < NW2A) w2ld((q - 187) >> 2);
          if (q == 205 || q == 213 || q == 221 || q == 229) copy_u((q - 205) >> 3);
          if (q == 209) {
            PIN(csp[0]); PIN(csp[1]);
            csp[0] += csp[1];
            const auto r0 = __builtin_amdgcn_permlane32_swap(f2u(csp[0][0]), f2u(csp[0][2]), false, false);
            const auto r1 = __builtin_amdgcn_permlane32_swap(f2u(csp[0][1]), f2u(csp[0][3]), false, false);
            csp[0][0] = u2f(r0[0]) + u2f(r0[1]);
            csp[0][1] = u2f(r1[0]) + u2f(r1[1]);
            PIN(csp[0]);
          }
          if (q == 217) {
            const auto r = __builtin_amdgcn_permlane16_swap(f2u(csp[0][0]), f2u(csp[0][1]), false, false);
            *(LAS float*)(size_t)a_cs = u2f(r[0]) + u2f(r[1]);
          }
          if (q == 220) {
            PIN(msv[0]);
            const auto m = __builtin_amdgcn_permlane32_swap(msv[0], msv[0], false, false);
            msv[0] = m[0]; msv[2] = m[1];
            PIN(msv[0]); PIN(msv[2]);
          }
          if (q == 225 || q == 228) {
            const int j = q == 225 ? 0 : 2;
            const auto m = __builtin_amdgcn_permlane16_swap(msv[j], msv[j], false, false);
            msv[j] = m[0]; msv[j + 1] = m[1];
            PIN(msv[j]); PIN(msv[j + 1]);
          }
          if (q >= 232 && q < 236) { cv[q - 232] = *(LAS f32x4*)((LAS char*)(size_t)a_cv + (q - 232) * 64); }
          if (q >= 236 && q < 240) { nmv[q - 236] = -b2f(msv[q - 236]); PIN(nmv[q - 236]); }
          if (q >= 240) {
            const int m = (n + 8) & 15, ii = m & 3, jj = m >> 2;
#pragma unroll
            for (int r = 0; r < 4; ++r) {
              float v = __builtin_fmaf(nmv[jj], cv[ii][r], acc[ii][jj][r]);
              PIN(v);
              acc[ii][jj][r] = v;
            }
          }
          SCHED();
        }
      }
    });
    STAMP(7);
    __builtin_amdgcn_s_setprio(3);
#pragma unroll
    for (int j = 0; j < 4; ++j) {
      const float sd = bhi(msv[j]), sd2 = sd * sd;
      const float ce = -1.44269504f * __builtin_amdgcn_rcpf(sd);
      u32x4 o;
#pragma unroll
      for (int r = 0; r < 4; ++r) {
        const f32x2 gg = {acc[r >> 1][j][2 * (r & 1)], acc[r >> 1][j][2 * (r & 1) + 1]};
        const f32x2 uu = {acc[2 + (r >> 1)][j][2 * (r & 1)], acc[2 + (r >> 1)][j][2 * (r & 1) + 1]};
        const f32x2 a = gg * f32x2{ce, ce};
        const f32x2 e = {__builtin_amdgcn_exp2f(a.x), __builtin_amdgcn_exp2f(a.y)};
        const f32x2 d = fma2(e, f32x2{sd2, sd2}, f32x2{sd2, sd2});
        const f32x2 q = {__builtin_amdgcn_rcpf(d.x), __builtin_amdgcn_rcpf(d.y)};
        o[r] = pk2v((gg * uu) * q);
      }
      st128s(o, rs, l * 16, WS_H + (((mt * 8 + 4 * wt + j) * 56 + ft * 4 + wf) << 10));
      SCHED();
    }
    __builtin_amdgcn_s_setprio(1);
    STAMP(8);
  } else {
    poll(rs, p0off, FL_P0, tag);
    if (b == 255 && w == 0) {
#pragma unroll 1
      for (int i = 0; i < 4; ++i) poll(rs, (64 * i + l) * FS, FL_P0, tag);
      if (l == 0) st64s(tag, rs, 0, FL_EP);
    }
    LBAR();
    load_u(0); load_u(1);
#pragma unroll
    for (int e = 0; e < 8; ++e) expand_rd(e);
#pragma unroll
    for (int e = 0; e < 4; ++e) expand_pk(e);
    LBAR();
    expand_wr();
#pragma unroll
    for (int s = 0; s < 4; ++s) copy_u(s);
    SCHED();
#pragma unroll
    for (int i = 0; i < NW2A; ++i) w2ld(i);
    __builtin_amdgcn_s_waitcnt(0x0F70);
    LBAR();
  }

  STAMP(10);
  u32x2 hfl;
  u32x4 hf[28];
  const int hoff = WS_H + ((4 * m2 * 56 + 7 * w) << 10);
  const int jr = 0;
  auto hld = [&](int n) { hf[n] = ld128s(rs, l * 16, hoff + (((n / 7) ^ jr) * 56 + n % 7) * 1024); };
  const int hfs = FL_H + (m2 * 56 + 7 * w) * FS;
#define PDV 12
  constexpr int PD = PDV;
  {
    LAS char *ua = (LAS char*)(size_t)a_ua, *ka = (LAS char*)(size_t)a_ka;
    *(LAS u32x4*)(size_t)a_zk = zero4;
    *(LAS u32x4*)(size_t)a_zu = zero4;
    asm volatile("" ::: "memory");
    f32x4 acc[4];
#pragma unroll
    for (int i = 0; i < 4; ++i) acc[i] = f32x4{0.f, 0.f, 0.f, 0.f};
    const int so_h = g1 ? FL_H + ((mt * 2 + wt) * 56 + ft * 4 + wf) * FS : OOB;
    auto hook = [&](int rr, int c) {
#define HPUB 3
#define HPOLL 24
      const int sq = rr * 8 + c;
#define WS0 4
#define WSN 13
      auto nw = [&](int z) { return z <= WS0 ? 0 : z >= WS0 + WSN ? 28 - NW2A : ((z - WS0) * (28 - NW2A) + WSN - 1) / WSN; };
      if (sq == HPUB) {
        constexpr int c0 = HPUB <= WS0 ? 0 : HPUB >= WS0 + WSN ? 28 - NW2A : ((HPUB - WS0) * (28 - NW2A) + WSN - 1) / WSN;
        asm volatile("s_waitcnt vmcnt(%0)" :: "i"(c0) : "memory");
        st64s(tag, rs, o_l0, so_h);
        __builtin_amdgcn_s_setprio(0);
        STAMP(9);
      }
      if (sq >= 0 && nw(sq + 1) > nw(sq)) w2ld(NW2A + nw(sq));
      if (sq == (HPOLL < 32 ? HPOLL - 5 : 31)) hfl = ld64s(rs, hfo, hfs);
      if (sq == HPOLL) {
        STAMP(2);
        if (!__all(hfl.x == tag.x && hfl.y == tag.y)) poll(rs, hfo, hfs, tag);
        asm volatile("" ::: "memory");
        STAMP(23);
      }
      if (sq >= HPOLL) {
#pragma unroll
        for (int n = (sq - HPOLL) * PD / (HPOLL < 32 ? 32 - HPOLL : 1); n < (sq + 1 - HPOLL) * PD / (HPOLL < 32 ? 32 - HPOLL : 1); ++n) hld(n);
      }
    };
    conv_tiles(ua, ka, acc, hook);
#pragma unroll
    for (int i = 0; i < 4; ++i) *(LAS f32x4*)(lds + CV_X + (w * 4 + i) * 1024 + l * 16) = acc[i];
    if (tid == 0) *(LAS u32*)(lds + M_CNT) = 0u;
  }
  LBAR();
  STAMP(11);
  int ytile;
  {
    const int ts = (w >> 1) & 1, i = (w & 1) + 2 * (w >> 2);
    ytile = 7 - 2 * i - ts;
    const int pos = 256 * ytile + 16 * l15 + 4 * g;
    f32x4 y0[2], y1[2];
    u32x2 ur[2];
#pragma unroll
    for (int ch = 0; ch < 2; ++ch) {
      y0[ch] = *(LAS f32x4*)(lds + CV_X + ((ch + 2 * ts) * 4 + i) * 1024 + l * 16);
      y1[ch] = *(LAS f32x4*)(lds + CV_X + ((ch + 2 * ts + 4) * 4 + i) * 1024 + l * 16);
      ur[ch] = lds64(lds + CV_U + ch * CV_UCH + (2044 - pos) * 2);
    }
    LBAR();
    float res[2][4];
#pragma unroll
    for (int ch = 0; ch < 2; ++ch) {
      const float dsk = ch ? bhi(dk) : b2f(dk);
      const f32x4 y = y0[ch] + y1[ch];
      res[ch][0] = __builtin_fmaf(bhi(ur[ch].y), dsk, y[0]); res[ch][1] = __builtin_fmaf(b2f(ur[ch].y), dsk, y[1]);
      res[ch][2] = __builtin_fmaf(bhi(ur[ch].x), dsk, y[2]); res[ch][3] = __builtin_fmaf(b2f(ur[ch].x), dsk, y[3]);
    }
    const u32x4 o = {pk2(res[0][0], res[1][0]), pk2(res[0][1], res[1][1]), pk2(res[0][2], res[1][2]), pk2(res[0][3], res[1][3])};
    st128s(o, rs, pos * 4, WS_Y + b * 8192);
    if (HPOLL == 32) {
      STAMP(2);
      if (!__all(hfl.x == tag.x && hfl.y == tag.y)) poll(rs, hfo, hfs, tag);
      asm volatile("" ::: "memory");
      STAMP(23);
#pragma unroll
      for (int n = 0; n < PD; ++n) hld(n);
    }
  }
  STAMP(12);

  {
    const int oj = w >> 1, rh = w & 1;
    const int etok = 64 * m2 + 16 * (oj ^ jr) + l15, ech = 64 * n2 + 16 * g + 8 * rh;
    u32x4 xv;
    u32 yo = (ech >> 1) * 8192 + etok * 4, yfv = (8 * g + (l & 3)) * FS;
    PIN(yo); PIN(yfv);
    SCHED();
    f32x4 acc[4][4];
#pragma unroll
    for (int i = 0; i < 4; ++i)
#pragma unroll
      for (int j = 0; j < 4; ++j) acc[i][j] = f32x4{0.f, 0.f, 0.f, 0.f};
    u32x2 yfl;
    const int yfo = FL_Y + ((m2 >> 2) * 256 + 32 * n2 + 4 * rh) * FS;
    LAS char* pw = opaque(lds + w * 16384 + l * 8);
    LAS char* pr = opaque(lds + w * 2048 + l * 8);
    LAS char* pr2 = opaque(pr + 65536);
    u32 a_cnt = lds0 + M_CNT + l * 4, one = 1u;
    PIN(a_cnt); PIN(one);
    auto part_wr = [&](int j, int k) {
      *(LAS f32x2*)(pw + (j * 8 + k) * 512) = f32x2{acc[k & 3][j][2 * (k >> 2)], acc[k & 3][j][2 * (k >> 2) + 1]};
    };
#pragma unroll
    for (int n = 0; n < 28; ++n) {
#pragma unroll
      for (int i = 0; i < 4; ++i) {
        acc[i][n / 7] = mfma16(wfr[4 * (n % 7) + i], hf[n], acc[i][n / 7]);
        SCHED();
        if (i == 3 && n + PD < 28) hld(n + PD);
        if ((i & 1) == 0 && n >= 8 && n % 7 >= 1 && n % 7 < 5) part_wr(n / 7 - 1, (n % 7 - 1) * 2 + (i >> 1));
        SCHED();
      }
      if (n == YP1 && w >= 4) { asm volatile("s_waitcnt vmcnt(%0)" :: "i"(YP1 + 1) : "memory"); st64s(tag, rs, o_l0, FL_Y + (ytile * 256 + b) * FS); }
      if (n == YP0 && w < 4) { asm volatile("s_waitcnt vmcnt(%0)" :: "i"(YP0 + 1) : "memory"); st64s(tag, rs, o_l0, FL_Y + (ytile * 256 + b) * FS); }
      if (n == 21) xv = *(const GAS u32x4*)((const char*)p.x + (size_t)etok * 1024 + ech * 2);
      if (n == 22) yfl = ld64s(rs, yfv, yfo);
      if (n == TB && w >= 4) LBAR();
      SCHED();
    }
    STAMP(13);
#pragma unroll
    for (int k = 0; k < 8; ++k) part_wr(3, k);
    asm volatile("" ::: "memory");
    if (w < 4) LBAR();
    else asm volatile("ds_add_u32 %0, %1" :: "v"(a_cnt), "v"(one) : "memory");
    if (!__all(yfl.x == tag.x && yfl.y == tag.y)) poll(rs, yfv, yfo, tag);
    asm volatile("" ::: "memory");
    u32 yv[4];
#pragma unroll
    for (int j = 0; j < 4; ++j) yv[j] = ld32s(rs, yo, WS_Y + j * 8192);
    f32x2 pp[8][4];
#pragma unroll
    for (int ww = 0; ww < 4; ++ww)
#pragma unroll
      for (int i = 0; i < 4; ++i) pp[ww][i] = *(volatile LAS f32x2*)(pr + ww * 16384 + i * 512);
    if (w >= 4) {
      volatile LAS u32* cnt = (volatile LAS u32*)(lds + M_CNT);
#pragma unroll 1
      for (int spin = 0; spin < (1 << 20) && *cnt < 4u; ++spin) {}
      asm volatile("" ::: "memory");
    }
    STAMP(14);
#pragma unroll
    for (int ww = 0; ww < 4; ++ww)
#pragma unroll
      for (int i = 0; i < 4; ++i) pp[4 + ww][i] = *(volatile LAS f32x2*)(pr2 + ww * 16384 + i * 512);
    SCHED();
    f32x2 sm[4];
#pragma unroll
    for (int i = 0; i < 4; ++i) sm[i] = ((pp[0][i] + pp[1][i]) + (pp[2][i] + pp[3][i])) + ((pp[4][i] + pp[5][i]) + (pp[6][i] + pp[7][i]));
    const u32 ff[4] = {pk2(sm[0].x, sm[1].x), pk2(sm[2].x, sm[3].x), pk2(sm[0].y, sm[1].y), pk2(sm[2].y, sm[3].y)};
    u32x4 r;
#pragma unroll
    for (int j = 0; j < 4; ++j) {
      const u32 s = pk2v(up2(xv[j]) + up2(yv[j]));
      r[j] = pk2v(up2(s) + up2(ff[j]));
    }
    *(GAS u32x4*)((char*)p.out + (size_t)etok * 1024 + ech * 2) = r;
  }
  STAMP(15);
  STAMP_DUMP();
}

extern "C" void kernel_launch(void* const* d_in, const int* in_sizes, int n_in, void* d_out, int out_size, void* d_ws,
                              size_t ws_size, hipStream_t stream) {
  Params p;
  p.x = (const u16*)d_in[0]; p.lar = (const u16*)d_in[1]; p.aim = (const u16*)d_in[2]; p.cre = (const u16*)d_in[3];
  p.cim = (const u16*)d_in[4]; p.ldt = (const u16*)d_in[5]; p.dsk = (const u16*)d_in[6];
  p.w1 = (const u16*)d_in[8]; p.w3 = (const u16*)d_in[9]; p.w2 = (const u16*)d_in[10];
  p.out = (u16*)d_out; p.ws = (char*)d_ws;
  fused<<<256, 512, 0, stream>>>(p);
}
```

```cpp
#include <hip/hip_runtime.h>
#include <hip/hip_bf16.h>

typedef unsigned short u16;
typedef unsigned int u32;
typedef unsigned long long u64;
typedef __bf16 bf16x8 __attribute__((ext_vector_type(8)));
typedef __bf16 bf16x2 __attribute__((ext_vector_type(2)));
typedef _Float16 f16x8 __attribute__((ext_vector_type(8)));
typedef _Float16 f16x2 __attribute__((ext_vector_type(2)));
typedef short s16x4 __attribute__((ext_vector_type(4)));
typedef float f32x2 __attribute__((ext_vector_type(2)));
typedef float f32x4 __attribute__((ext_vector_type(4)));
typedef float f32x16 __attribute__((ext_vector_type(16)));
typedef u32 u32x2 __attribute__((ext_vector_type(2)));
typedef u32 u32x4 __attribute__((ext_vector_type(4)));
typedef u64 u64x4 __attribute__((ext_vector_type(4)));
typedef u64 u64x8 __attribute__((ext_vector_type(8)));
typedef __amdgpu_buffer_rsrc_t rsrc_t;
#define LAS __attribute__((address_space(3)))
#define GAS __attribute__((address_space(1)))
#define DEV __device__ __forceinline__

#define NTQ " nt"
#define TB 21
#define YP1 1
#define YP0 10
constexpr int L = 2048, D = 512, NM = 128, F = 1792;
constexpr int LDS_BYTES = 163840;
constexpr int SC1 = 16;

constexpr int WS_H = 0;
constexpr int WS_XNR = 8 << 20;
constexpr int WS_W2F = 10 << 20;
constexpr int WS_Y = 12 << 20;
constexpr int WS_ST = 14 << 20;
constexpr int WS_CS = WS_ST + 8192;
constexpr int WS_FL = 15 << 20;
constexpr int FS = 128;
constexpr int FL_EP = WS_FL;
constexpr int FL_P0 = WS_FL + 4096;
constexpr int FL_H = FL_P0 + 256 * FS;
constexpr int FL_Y = FL_H + 2048 * FS;
constexpr int WS_BYTES = 16 << 20;

struct Params {
  const u16 *x, *lar, *aim, *cre, *cim, *ldt, *dsk, *w1, *w3, *w2;
  u16* out;
  char* ws;
};

template <int V> struct IC { static constexpr int v = V; };
template <int N, int I = 0, class Fn>
DEV void static_for(Fn&& fn) { if constexpr (I < N) { fn(IC<I>{}); static_for<N, I + 1>(fn); } }
DEV u32 f2u(float f) { return __builtin_bit_cast(u32, f); }
DEV float u2f(u32 v) { return __builtin_bit_cast(float, v); }
DEV float b2f(u32 v) { return __builtin_bit_cast(float, v << 16); }
DEV float bhi(u32 v) { return __builtin_bit_cast(float, v & 0xffff0000u); }
DEV u32 pk2v(f32x2 f) { return __builtin_bit_cast(u32, __builtin_convertvector(f, bf16x2)); }
DEV u32 pk2(float lo, float hi) { return pk2v(f32x2{lo, hi}); }
DEV f32x2 up2(u32 v) { return f32x2{b2f(v), bhi(v)}; }
DEV float rb(float f) { return b2f(pk2(f, f) & 0xffffu); }
DEV f32x2 fma2(f32x2 a, f32x2 b, f32x2 c) { return __builtin_elementwise_fma(a, b, c); }
DEV f32x16 mfma32(u32x4 a, u32x4 b, f32x16 c) {
  return __builtin_amdgcn_mfma_f32_32x32x16_bf16(__builtin_bit_cast(bf16x8, a), __builtin_bit_cast(bf16x8, b), c, 0, 0, 0);
}
DEV f32x4 mfma16(u32x4 a, u32x4 b, f32x4 c) {
  return __builtin_amdgcn_mfma_f32_16x16x32_bf16(__builtin_bit_cast(bf16x8, a), __builtin_bit_cast(bf16x8, b), c, 0, 0, 0);
}
DEV f32x16 mfma32h(u32x4 a, u32x4 b, f32x16 c) {
  return __builtin_amdgcn_mfma_f32_32x32x16_f16(__builtin_bit_cast(f16x8, a), __builtin_bit_cast(f16x8, b), c, 0, 0, 0);
}
DEV u32x4 lds128(LAS char* p) { return *(LAS u32x4*)p; }
DEV u32x2 lds64(LAS char* p) { return *(LAS u32x2*)p; }
DEV u32x2 ldstr(LAS char* p) { return __builtin_bit_cast(u32x2, __builtin_amdgcn_ds_read_tr16_b64_v4i16((LAS s16x4*)p)); }
template <int LOFF>
DEV void dma_m0(u32 lbase) { asm volatile("s_add_u32 m0, %0, %1" :: "s"(lbase), "i"(LOFF) : "memory", "scc", "m0"); }
template <bool NT>
DEV void dma_go(u32x4 rs, u32 voff, u32 soff) {
  if (NT) asm volatile("buffer_load_dwordx4 %0, %1, %2 offen nt lds" :: "v"(voff), "s"(rs), "s"(soff) : "memory");
  else asm volatile("buffer_load_dwordx4 %0, %1, %2 offen lds" :: "v"(voff), "s"(rs), "s"(soff) : "memory");
}
DEV u32x4 mkrs(const void* ptr, u32 bytes) { const u64 a = (u64)ptr; return u32x4{(u32)a, (u32)(a >> 32) & 0xffffu, bytes, 0x00020000u}; }
#define WAIT_V(n) asm volatile("s_waitcnt vmcnt(" #n ")" ::: "memory")
#define WAIT_L0() asm volatile("s_waitcnt lgkmcnt(0)" ::: "memory")
#define BAR() __builtin_amdgcn_s_barrier()
#define LBAR() do { WAIT_L0(); BAR(); asm volatile("" ::: "memory"); } while (0)
#define SCHED() __builtin_amdgcn_sched_barrier(0)
#define PIN(x) asm volatile("" : "+v"(x))
constexpr int OOB = 0x04000000;
#define STAMP(i)
#define STAMP_DECL
#define STAMP_DUMP()

DEV u32x4 ld128s(rsrc_t rs, int voff, int soff) { return __builtin_amdgcn_raw_buffer_load_b128(rs, voff, soff, SC1); }
DEV u32x2 ld64s(rsrc_t rs, int voff, int soff) { return __builtin_amdgcn_raw_buffer_load_b64(rs, voff, soff, SC1); }
DEV u32 ld32s(rsrc_t rs, int voff, int soff) { return __builtin_amdgcn_raw_buffer_load_b32(rs, voff, soff, SC1); }
DEV void st128s(u32x4 v, rsrc_t rs, int voff, int soff) {
  __builtin_amdgcn_raw_buffer_store_b128(v, rs, voff, soff, SC1);
  asm volatile("s_nop 1" :: "v"(v) : "memory");
}
DEV void st64s(u32x2 v, rsrc_t rs, int voff, int soff) { __builtin_amdgcn_raw_buffer_store_b64(v, rs, voff, soff, SC1); }
DEV void st32s(u32 v, rsrc_t rs, int voff, int soff) { __builtin_amdgcn_raw_buffer_store_b32(v, rs, voff, soff, SC1); }
DEV void poll(rsrc_t rs, int voff, int soff, u32x2 tag) {
#pragma unroll 1
  for (int spin = 0; spin < (1 << 18); ++spin) {
    const u32x2 v = ld64s(rs, voff, soff);
    if (__all(v.x == tag.x && v.y == tag.y)) break;
    __builtin_amdgcn_s_sleep(4);
  }
  __builtin_amdgcn_fence(__ATOMIC_ACQUIRE, "wavefront");
  asm volatile("" ::: "memory");
}

template <int CTRL, int RM>
DEV float dppz(float v) { return __builtin_bit_cast(float, __builtin_amdgcn_update_dpp(0, __builtin_bit_cast(int, v), CTRL, RM, 0xf, true)); }
template <int CTRL, int RM>
DEV u32 dppu(u32 v) { return (u32)__builtin_amdgcn_update_dpp(0, (int)v, CTRL, RM, 0xf, true); }
DEV u32 wave_max_u32(u32 v) {
  v = max(v, dppu<0x111, 0xf>(v)); v = max(v, dppu<0x112, 0xf>(v)); v = max(v, dppu<0x114, 0xf>(v)); v = max(v, dppu<0x118, 0xf>(v));
  v = max(v, dppu<0x142, 0xa>(v)); v = max(v, dppu<0x143, 0xc>(v));
  return (u32)__builtin_amdgcn_readlane((int)v, 63);
}

DEV float rdl(float v, int lane) { return __builtin_bit_cast(float, __builtin_amdgcn_readlane(__builtin_bit_cast(int, v), lane)); }
template <int CTRL>
DEV float dppx(float v) { return __builtin_bit_cast(float, __builtin_amdgcn_mov_dpp(__builtin_bit_cast(int, v), CTRL, 0xf, 0xf, true)); }
DEV float rows8_sum(const float (&p)[8], int l) {
  float a[4], c[2];
#pragma unroll
  for (int i = 0; i < 4; ++i) {
    const auto r = __builtin_amdgcn_permlane32_swap(__builtin_bit_cast(u32, p[i]), __builtin_bit_cast(u32, p[i + 4]), false, false);
    a[i] = __builtin_bit_cast(float, (u32)r[0]) + __builtin_bit_cast(float, (u32)r[1]);
  }
#pragma unroll
  for (int i = 0; i < 2; ++i) {
    const auto r = __builtin_amdgcn_permlane16_swap(__builtin_bit_cast(u32, a[i]), __builtin_bit_cast(u32, a[i + 2]), false, false);
    c[i] = __builtin_bit_cast(float, (u32)r[0]) + __builtin_bit_cast(float, (u32)r[1]);
  }
  const bool up = l & 8;
  float v = (up ? c[1] : c[0]) + dppx<0x128>(up ? c[0] : c[1]);
  v += dppx<0xB1>(v); v += dppx<0x4E>(v); v += dppx<0x141>(v);
  return v;
}

constexpr int G1_SLOT = 49152;
constexpr int KG_T = 2 * G1_SLOT;
constexpr int KG_TW = 13312;
constexpr int SPARE = 3 * G1_SLOT;
constexpr int M_CS = SPARE;
constexpr int M_CNT = SPARE + 4096;
constexpr int M_KC = SPARE + 5120;
constexpr int CV_K = SPARE + 5120;
static_assert(KG_T + 4 * KG_TW <= M_KC && M_KC + 4 * 2816 <= LDS_BYTES, "lds");
constexpr int CV_CS = 4160;
constexpr int CV_U = 2 * G1_SLOT;
constexpr int CV_UCH = 4 * CV_CS;
constexpr int CV_X = 0;

DEV f32x2 cmul(f32x2 a, f32x2 b, f32x2 bn) { return fma2(f32x2{a.y, a.x}, bn, a * f32x2{b.x, b.x}); }
DEV f32x2 cneg(f32x2 b) { return f32x2{-b.y, b.y}; }
DEV f32x2 cexp_p(float lre2, float rev, float pw) {
  const float mg = __builtin_amdgcn_exp2f(lre2 * pw);
  const float ph = __builtin_amdgcn_fractf(rev * pw);
  return f32x2{__builtin_amdgcn_cosf(ph), __builtin_amdgcn_sinf(ph)} * f32x2{mg, mg};
}
DEV u32 pkh(f32x2 f) { return __builtin_bit_cast(u32, __builtin_convertvector(f, f16x2)); }
DEV LAS char* opaque(LAS char* q) { u32 v = (u32)(size_t)q; asm volatile("" : "+v"(v)); return (LAS char*)(size_t)v; }

template <class Hook>
DEV void conv_tiles(LAS char* ua, LAS char* ka, f32x4 (&acc)[4], Hook hook) {
  constexpr int ORD[16] = {0, 15, 13, 11, 9, 1, 14, 12, 10, 8, 2, 3, 4, 5, 6, 7};
  u32x4 fr[2][16];
  auto load = [&](int rr, int id) {
    if (id < 8) {
      const u32x2 lo = *(volatile LAS u32x2*)(ua + (7 - id) * 512 + (3 - rr) * 64), hi = *(volatile LAS u32x2*)(ua + (7 - id) * 512 + (3 - rr) * 64 + 8);
      fr[rr & 1][id] = u32x4{lo.x, lo.y, hi.x, hi.y};
    } else {
      fr[rr & 1][id] = lds128(ka + (id - 8) * 512 + (3 - rr) * 64);
    }
  };
#pragma unroll
  for (int k = 0; k < 16; ++k) { load(0, ORD[k]); SCHED(); }
  hook(0, -1);
  SCHED();
#pragma unroll
  for (int rr = 0; rr < 4; ++rr) {
#pragma unroll
    for (int c = 0; c < 8; ++c) {
      u32x4(&f)[16] = fr[rr & 1];
#pragma unroll
      for (int i = 0; i < 4; ++i) {
        if (c <= 7 - 2 * i) { acc[i] = mfma16(f[c], f[8 + 7 - 2 * i - c], acc[i]); SCHED(); }
        if (rr < 3 && i < 2) { load(rr + 1, ORD[2 * c + i]); SCHED(); }
      }
      hook(rr, c);
      SCHED();
    }
  }
}

__global__ __launch_bounds__(512, 2) void fused(Params args) {
  __shared__ __attribute__((aligned(16))) char smem[LDS_BYTES];
  u64x8 ka0;
  u64x4 ka1;
  u64 mta;
  asm volatile("s_memrealtime %0" : "=s"(mta) :: "memory");
  asm volatile("s_load_dwordx16 %0, %2, 0x0\n\ts_load_dwordx8 %1, %2, 0x40" : "=&s"(ka0), "=&s"(ka1) : "s"(__builtin_amdgcn_kernarg_segment_ptr()) : "memory");
#define CODEPF 23000
  u32x4 pf;
  if (__builtin_amdgcn_readfirstlane(threadIdx.x >> 6) == 4) {
    u64 pc;
    asm volatile("s_getpc_b64 %0" : "=s"(pc));
    const u32 po = min((u32)((blockIdx.x >> 3) * 1024 + (threadIdx.x & 63) * 16), (u32)(CODEPF - 16));
    asm volatile("global_load_dwordx4 %0, %1, %2" : "=v"(pf) : "v"(po), "s"(pc & ~(u64)255) : "memory");
  }
  LAS char* lds = (LAS char*)smem;
  const u32 lds0 = (u32)(size_t)lds;
  const int tid = threadIdx.x, l = tid & 63;
  const int w = __builtin_amdgcn_readfirstlane(tid >> 6);
  const int b = blockIdx.x, xcd = b & 7, rk = b >> 3;
  const bool g1 = rk < 28;
  const int l15 = l & 15, g = l >> 4, t = l & 31, h = l >> 5;
  const int mt = (xcd >> 1) * 4 + (rk & 3), ft = (xcd & 1) * 7 + (rk >> 2);
#define ROTV 5
  const int kt0 = ROTV == 0 ? 0 : ROTV == 1 ? (ft + 2 * (mt & 3) + ((mt >> 2) & 1)) & 7 : ROTV == 2 ? (2 * (xcd >> 1) + 4 * (xcd & 1)) & 7 : (2 * (xcd >> 1) + 4 * (xcd & 1) + (mt & 1)) & 7;
  const int wt = w & 1, wf = w >> 1;
  u32 xo0 = (l >> 3) * 1024 + (((l & 7) ^ g) << 4);
  u32 xo1 = (8 + (l >> 3)) * 1024 + (((l & 7) ^ (4 + g)) << 4);
  u32 wo1 = g * (F * 2) + ((l15 ^ (4 * g + 2 * (w & 1))) << 4), wo1b = wo1 + 4 * F * 2;
  PIN(xo0); PIN(xo1); PIN(wo1); PIN(wo1b);
  u32 tv[8], tm[4], tm2, rv[2], rm[2];
  {
    const u32 tw = lds0 + KG_T + (w & 3) * KG_TW;
#pragma unroll
    for (int k = 0; k < 8; ++k) { tv[k] = tw + l * 128 + ((k ^ (l & 7)) << 4); PIN(tv[k]); }
#pragma unroll
    for (int k = 0; k < 4; ++k) { tm[k] = tw + 8192 + l * 64 + ((k ^ ((l >> 1) & 3)) << 4); PIN(tm[k]); }
    tm2 = tw + 12288 + l * 16;
    PIN(tm2);
    const int blk = g & 1, c = (l >> 2) & 1, pp = l & 3;
#pragma unroll
    for (int t2 = 0; t2 < 2; ++t2) {
      const int m = 4 * h + 2 * t2 + ((l >> 3) & 1);
      rv[t2] = tw + m * 128 + (((4 * c + 2 * blk + (pp >> 1)) ^ m) << 4) + 8 * (pp & 1);
      rm[t2] = blk ? tw + 12288 + m * 16 + 8 * c : tw + 8192 + m * 64 + (((2 * c + (pp >> 1)) ^ (m >> 1)) << 4) + 8 * (pp & 1);
      PIN(rv[t2]); PIN(rm[t2]);
    }
  }
  u32 rms = (g & 1) ? 128 : 512;
  PIN(rms);
  asm volatile("s_waitcnt lgkmcnt(0)" : "+s"(ka0), "+s"(ka1) :: "memory");
  Params p;
  p.x = (const u16*)ka0[0]; p.lar = (const u16*)ka0[1]; p.aim = (const u16*)ka0[2]; p.cre = (const u16*)ka0[3]; p.cim = (const u16*)ka0[4];
  p.ldt = (const u16*)ka0[5]; p.dsk = (const u16*)ka0[6]; p.w1 = (const u16*)ka0[7];
  p.w3 = (const u16*)ka1[0]; p.w2 = (const u16*)ka1[1]; p.out = (u16*)ka1[2]; p.ws = (char*)ka1[3];
  const rsrc_t rs = __builtin_amdgcn_make_buffer_rsrc(p.ws, 0, WS_BYTES, 0x00020000);
  STAMP_DECL;
  asm volatile("" : "+s"(mta));
  u32 mt0 = (u32)mta, mtp = mt0;
  STAMP(3);

  u32x2 tag;
  u32 p_ldt2, dk;
  asm volatile("s_load_dwordx2 %0, %3, 0x0\n\ts_load_dword %1, %4, 0x0\n\ts_load_dword %2, %5, 0x0"
               : "=&s"(tag), "=&s"(p_ldt2), "=&s"(dk) : "s"(p.ws + FL_EP), "s"(p.ldt + 2 * b), "s"(p.dsk + 2 * b) : "memory");

  u32 p_lar, p_aim, p_cre, p_cim;
  if (w < 4) {
    const u32 po = (b * 256 + tid) * 2;
    asm volatile("global_load_ushort %0, %1, %2" NTQ : "=v"(p_lar) : "v"(po), "s"(p.lar) : "memory");
    asm volatile("global_load_ushort %0, %1, %2" NTQ : "=v"(p_aim) : "v"(po), "s"(p.aim) : "memory");
    asm volatile("global_load_ushort %0, %1, %2" NTQ : "=v"(p_cre) : "v"(po), "s"(p.cre) : "memory");
    asm volatile("global_load_ushort %0, %1, %2" NTQ : "=v"(p_cim) : "v"(po), "s"(p.cim) : "memory");
  }
  SCHED();

  const u32x4 rx = mkrs(p.x, L * D * 2), rw1 = mkrs(p.w1, D * F * 2), rw3 = mkrs(p.w3, D * F * 2);
#define NTS 0
  auto dm0 = [&](auto st_, int piece, int ww) {
    constexpr int S = (decltype(st_)::v % 3) * G1_SLOT;
    const u32 lb = lds0 + ww * 2048;
    if (piece == 0) dma_m0<S>(lb);
    if (piece == 1) dma_m0<S + 16384>(lb);
    if (piece == 2) dma_m0<S + 32768>(lb);
    if (piece == 3) dma_m0<S + 1024>(lb);
    if (piece == 4) dma_m0<S + 16384 + 1024>(lb);
    if (piece == 5) dma_m0<S + 32768 + 1024>(lb);
  };
  auto dgo = [&](auto st_, int piece, int ww) {
    constexpr bool nt = decltype(st_)::v < NTS;
    const int kt = (kt0 + decltype(st_)::v) & 7;
    const u32 sx = (mt * 128 + ww * 16) * 1024 + kt * 128, sw = (kt * 64 + ww * 8) * (F * 2) + ft * 256;
    if (piece == 0) dma_go<nt>(rx, xo0, sx);
    if (piece == 1) dma_go<nt>(rw1, wo1, sw);
    if (piece == 2) dma_go<nt>(rw3, wo1, sw);
    if (piece == 3) dma_go<nt>(rx, xo1, sx);
    if (piece == 4) dma_go<nt>(rw1, wo1b, sw);
    if (piece == 5) dma_go<nt>(rw3, wo1b, sw);
  };
  auto dmaw = [&](auto st_, int piece, int ww) { dm0(st_, piece, ww); asm volatile("s_nop 0"); dgo(st_, piece, ww); };

  u32x2 wr[8];
  u32x4 xr[8];
  const int un2 = b / 28, ukb = 2 * (b - 28 * un2) + (w & 1);
  const int ax = xcd * 4 + (rk - 28);
  if (w >= 4 && g1) {
#pragma unroll
    for (int i = 0; i < 6; ++i) { dmaw(IC<0>{}, i, w - 4); dmaw(IC<0>{}, i, w); }
    if (w < 6) {
      const char* wb2 = (const char*)p.w2 + (size_t)(32 * ukb) * 1024 + un2 * 128;
      const u32 wv = g * 4096 + l15 * 8;
#define W2RAW(j, base, off) asm volatile("global_load_dwordx2 %0, %1, %2 offset:" #off NTQ : "=v"(wr[j]) : "v"(wv), "s"(base) : "memory")
      W2RAW(0, wb2, 0); W2RAW(1, wb2, 1024); W2RAW(2, wb2, 2048); W2RAW(3, wb2, 3072);
      W2RAW(4, wb2 + 16384, 0); W2RAW(5, wb2 + 16384, 1024); W2RAW(6, wb2 + 16384, 2048); W2RAW(7, wb2 + 16384, 3072);
    }
  }
  if (!g1) {
    const u32 xv_ = (64 * ax + 8 * w) * 1024 + l * 16;
#define XRAW(j, vo, off) asm volatile("global_load_dwordx4 %0, %1, %2 offset:" #off : "=v"(xr[j]) : "v"(vo), "s"(p.x) : "memory")
    XRAW(0, xv_, 0); XRAW(1, xv_, 1024); XRAW(2, xv_, 2048); XRAW(3, xv_, 3072);
    XRAW(4, xv_ + 4096, 0); XRAW(5, xv_ + 4096, 1024); XRAW(6, xv_ + 4096, 2048); XRAW(7, xv_ + 4096, 3072);
  }
  SCHED();

  const int m2 = xcd * 4 + (rk & 3), n2 = rk >> 2;
  const int uch = w >> 2, ui = tid & 255;
  u32 uo1 = ui < 255 ? ui * 16 + 16 : OOB;
  int xa[2], wa[2];
#pragma unroll
  for (int s = 0; s < 2; ++s) xa[s] = (64 * wt + l15) * 128 + (((4 * s + g) ^ ((l15 >> 1) & 7)) << 4);
  {
    const int q = l15 >> 2, pp = l & 3;
#pragma unroll
    for (int i = 0; i < 2; ++i) wa[i] = 16384 + (8 * g + q) * 256 + (((4 * wf + 2 * i + (pp >> 1)) ^ (4 * q + 2 * (g & 1))) << 4) + 8 * (pp & 1);
  }
  u32x4 sel;
#pragma unroll
  for (int e = 0; e < 4; ++e) sel[e] = l15 == 2 * e ? 0x00003f80u : (l15 == 2 * e + 1 ? 0x3f800000u : 0u);
  u32 a_cw = lds0 + 16384 * (1 + (w >> 2)) + (4 * g + (l15 >> 2)) * 256 + (((4 * (w & 3) + (l & 3)) ^ (4 * (l15 >> 2) + 2 * h)) << 4);
  u32 o_cq = (l < 32 && l15 >= 12) ? (l & 3) * 32 + g * 16 : OOB;
  u32 o_l0 = l == 0 ? 0 : OOB;
  u32 a_ua = lds0 + CV_U + (w & 1) * CV_UCH + (3 - (l15 & 3)) * CV_CS + 4088 - 8 * (l15 >> 2) - 32 * h + 16 * (g & 1) - 7 * 512 - 256 * (w >> 2) - 3 * 64;
  u32 a_ka = lds0 + CV_K + (w & 1) * 5120 + 1024 - 512 * ((w >> 1) & 1) + 32 * (l15 - h) + 16 * (g & 1) - 256 * (w >> 2) - 3 * 64;
  u32 a_zk = lds0 + CV_K + (w & 1) * 5120 + l * 16;
  u32 a_zu = lds0 + CV_U + (w & 1) * CV_UCH + ((l >> 1) & 3) * CV_CS + 4096 + (l & 1) * 16;
  u32 hfo = (l < 7 ? l : 0) * FS;
  u32x4 zero4 = {0u, 0u, 0u, 0u};
  PIN(a_ua); PIN(a_ka); PIN(a_zk); PIN(a_zu); PIN(hfo); PIN(zero4);

  u32 o_cs = g * 1024 + (l15 >> 3) * 512 + (32 * wf + 4 * (l15 & 7)) * 4;
  u32 a_cs = lds0 + M_CS + w * 256 + (4 * l15 + 2 * h + (g & 1)) * 4;
  PIN(a_cs);
  u32 a_cv = lds0 + M_CS + w * 256 + g * 16;

  PIN(sel); PIN(a_cw); PIN(o_cq); PIN(o_l0); PIN(o_cs); PIN(a_cv); PIN(uo1);
  int xa2[2], wa1[2], wa2[2];
#pragma unroll
  for (int e = 0; e < 2; ++e) {
    xa2[e] = xa[e] + 2 * G1_SLOT; wa1[e] = wa[e] + G1_SLOT; wa2[e] = wa[e] + 2 * G1_SLOT;
    PIN(xa[e]); PIN(xa2[e]); PIN(wa[e]); PIN(wa1[e]); PIN(wa2[e]);
  }
  SCHED();
  int lq = l, uq = tid & 255;
  PIN(lq); PIN(uq);
  u32 p0off;
  {
    const int e = min(9 * w + min(lq, 8), 67);
    const int m = e - 60;
    const int pb = e < 32 ? 224 + e : (e < 60 ? 28 * n2 + (e - 32) : (g1 ? ((m >> 1) * 2 + (xcd & 1)) + 8 * ((m & 1) + 4 * (rk >> 2)) : 224));
    p0off = pb * FS;
  }
  u32 ka_[8];
#pragma unroll
  for (int e = 0; e < 4; ++e) {
    const int l0 = 8 * uq + 2 * e;
    const u32 bits = pk2((float)l0, (float)(l0 + 1));
    const int i0 = uq < 32 ? l0 : (int)(bits & 0xffffu) - (0x4300 - 128);
    const int i1 = uq < 32 ? l0 + 1 : (int)(bits >> 16) - (0x4300 - 128);
    ka_[2 * e] = lds0 + M_KC + uch * 5632 + i0 * 4;
    ka_[2 * e + 1] = lds0 + M_KC + uch * 5632 + i1 * 4;
  }
  PIN(p0off);
#pragma unroll
  for (int e = 0; e < 8; ++e) PIN(ka_[e]);
  SCHED();
  if (g1 && w >= 4) {
#pragma unroll
    for (int i = 0; i < 6; ++i) { dmaw(IC<1>{}, i, w - 4); dmaw(IC<1>{}, i, w); }
    asm volatile("s_waitcnt vmcnt(12)" : "+v"(wr[0]), "+v"(wr[1]), "+v"(wr[2]), "+v"(wr[3]), "+v"(wr[4]), "+v"(wr[5]), "+v"(wr[6]), "+v"(wr[7]) :: "memory");
    PIN(pf);
    if (w < 6) {
#pragma unroll
      for (int c = 0; c < 4; ++c) {
        u32x4 o;
#pragma unroll
        for (int e = 0; e < 4; ++e) o[e] = __builtin_amdgcn_perm(wr[2 * e + 1][c >> 1], wr[2 * e][c >> 1], (c & 1) ? 0x07060302u : 0x05040100u);
        st128s(o, rs, l * 16 + c * 1024, WS_W2F + (((un2 * 56 + ukb) * 4) << 10));
      }
    }
  }
  asm volatile("s_waitcnt lgkmcnt(0)" : "+s"(tag), "+s"(p_ldt2), "+s"(dk) :: "memory");
  tag.x += 1u;

  if (!g1) {
    asm volatile("s_waitcnt vmcnt(0)" : "+v"(xr[0]), "+v"(xr[1]), "+v"(xr[2]), "+v"(xr[3]), "+v"(xr[4]), "+v"(xr[5]), "+v"(xr[6]), "+v"(xr[7]) :: "memory");
    PIN(pf);
    f32x2 v[8][4];
    float ps[8];
#pragma unroll
    for (int r = 0; r < 8; ++r) {
#pragma unroll
      for (int j = 0; j < 4; ++j) v[r][j] = up2(xr[r][j]);
      const f32x2 s2 = (v[r][0] + v[r][1]) + (v[r][2] + v[r][3]);
      ps[r] = s2.x + s2.y;
    }
    const float mean = rows8_sum(ps, l) * (1.f / D);
#pragma unroll
    for (int r = 0; r < 8; ++r) {
      const float mr = rdl(mean, 8 * r);
      f32x2 q2 = {0.f, 0.f};
#pragma unroll
      for (int j = 0; j < 4; ++j) { v[r][j] -= f32x2{mr, mr}; q2 = fma2(v[r][j], v[r][j], q2); }
      ps[r] = q2.x + q2.y;
    }
    const float var = rows8_sum(ps, l) * (1.f / D);
    const u32 ms = pk2(mean, __builtin_sqrtf(rb(rb(var) + 1e-5f)));
    if ((l & 7) == 0) st32s(ms, rs, (l >> 3) * 4, WS_ST + (64 * ax + 8 * w) * 4);
    const float riv = 1.f / bhi(ms), nbv = (mean - b2f(ms)) * riv;
#pragma unroll
    for (int r = 0; r < 8; ++r) {
      const float ri = rdl(riv, 8 * r), nb = rdl(nbv, 8 * r);
#pragma unroll
      for (int j = 0; j < 4; ++j) v[r][j] = fma2(v[r][j], f32x2{ri, ri}, f32x2{nb, nb});
    }
#pragma unroll
    for (int c = 0; c < 8; ++c) {
      u32x4 o;
#pragma unroll
      for (int e = 0; e < 4; ++e) o[e] = pk2(v[7 - 2 * e][c >> 1][c & 1], v[6 - 2 * e][c >> 1][c & 1]);
      *(LAS u32x4*)(lds + (8 * l + c) * 128 + (((7 - w) ^ (l & 7)) << 4)) = o;
    }
    LBAR();
    u32x4 tv[8];
#pragma unroll
    for (int it = 0; it < 8; ++it) tv[it] = lds128(lds + w * 8192 + it * 1024 + l * 16);
#pragma unroll
    for (int it = 0; it < 8; ++it)
      st128s(tv[it], rs, (8 * it + (l >> 3)) * 4096 + (((l & 7) ^ it) << 4), WS_XNR + w * (64 * 4096) + (L - 64 - 64 * ax) * 2);
    WAIT_V(0);
    LBAR();
    if (tid == 0) st64s(tag, rs, 0, FL_P0 + b * FS);
  }

  if (w < 4) {
    float dt = rb(__expf((w >> 1) ? bhi(p_ldt2) : b2f(p_ldt2)));
    PIN(dt);
    asm volatile("s_waitcnt vmcnt(0)" : "+v"(p_lar), "+v"(p_aim), "+v"(p_cre), "+v"(p_cim) :: "memory");
    STAMP(1);
    const float ar = -rb(__expf(b2f(p_lar)));
    const float lre2 = (ar * dt) * 1.44269504f, rev = (b2f(p_aim) * dt) * 0.15915494309f;
    u32 eb = wave_max_u32(max(p_cre & 0x7f80u, p_cim & 0x7f80u)) << 16;
    eb = eb < 0x00800000u ? 0x00800000u : (eb > 0x7e800000u ? 0x7e800000u : eb);
    const float sc = __builtin_bit_cast(float, eb) * (2.f * dt), isc = __builtin_bit_cast(float, 0x7f000000u - eb);
    f32x2 e[53], tmp[8];
    u32 hr[26], hi[26];
    const f32x2 w8 = cexp_p(lre2, rev, 8.f), w64 = cexp_p(lre2, rev, 64.f);
    const f32x2 z1 = cexp_p(lre2, -rev, 1.f), zc = cexp_p(lre2, -rev, 256.f);
    e[0] = f32x2{1.f, 0.f}; e[1] = w8; e[8] = w64; e[32] = f32x2{b2f(p_cre) * isc, -b2f(p_cim) * isc};
#define CM1(k, a, bb) tmp[k] = (a) * f32x2{(bb).x, (bb).x}; SCHED()
#define CM2(k, d, a, bn) d = fma2(f32x2{(a).y, (a).x}, bn, tmp[k]); SCHED()
#define CV(i) hr[i] = pkh(f32x2{e[2 * (i)].x, e[2 * (i) + 1].x}); hi[i] = pkh(f32x2{e[2 * (i)].y, e[2 * (i) + 1].y}); SCHED()
#define W4(a, v, i) *(LAS u32x4*)(size_t)(a) = u32x4{v[i], v[(i) + 1], v[(i) + 2], v[(i) + 3]}; SCHED()
    const f32x2 w8n = cneg(w8), w64n = cneg(w64), z1n = cneg(z1), zcn = cneg(zc);
    f32x2 z2, z4, zd, zq;
    SCHED();
    CM1(0, w8, w8); CM1(1, z1, z1); CM1(2, zc, zc); CM1(3, e[32], z1); CM1(4, e[32], zc); CM1(5, w8, w64); CM1(6, w64, w64);
    CM2(0, e[2], w8, w8n); CM2(1, z2, z1, z1n); CM2(2, zd, zc, zcn); CM2(3, e[33], e[32], z1n); CM2(4, e[40], e[32], zcn);
    CM2(5, e[9], w8, w64n); CM2(6, e[16], w64, w64n);
    const f32x2 e2n = cneg(e[2]), z2n = cneg(z2), zdn = cneg(zd);
    SCHED();
    CM1(0, e[2], w8); CM1(1, e[2], e[2]); CM1(2, z2, z2); CM1(3, zd, zd); CM1(4, e[32], z2); CM1(5, e[33], z2); CM1(6, e[32], zd); CM1(7, e[40], zd);
    CM2(0, e[3], e[2], w8n); CM2(1, e[4], e[2], e2n); CM2(2, z4, z2, z2n); CM2(3, zq, zd, zdn); CM2(4, e[34], e[32], z2n);
    CM2(5, e[35], e[33], z2n); CM2(6, e[44], e[32], zdn); CM2(7, e[46], e[40], zdn);
    const f32x2 e4n = cneg(e[4]), z4n = cneg(z4), zqn = cneg(zq);
    SCHED();
    CM1(0, e[1], e[4]); CM1(1, e[2], e[4]); CM1(2, e[3], e[4]); CM1(3, e[32], z4); CM1(4, e[33], z4); CM1(5, e[34], z4); CM1(6, e[35], z4); CM1(7, e[32], zq);
    CM2(0, e[5], e[1], e4n); CM2(1, e[6], e[2], e4n); CM2(2, e[7], e[3], e4n); CM2(3, e[36], e[32], z4n); CM2(4, e[37], e[33], z4n);
    CM2(5, e[38], e[34], z4n); CM2(6, e[39], e[35], z4n); CM2(7, e[48], e[32], zqn);
    CV(0); CV(1); CV(2); CV(3); W4(tv[0], hr, 0); CV(16); CV(17); W4(tv[4], hi, 0); CV(18); CV(19);
    CM1(0, e[34], zc); W4(tm[0], hr, 16); CM1(1, e[36], zc); CM1(2, e[38], zc); CM1(3, e[36], zd); CM1(4, e[40], zq); W4(tm[2], hi, 16);
    CM1(5, e[44], zq); CM1(6, e[46], zq); CM1(7, e[48], zq);
    CM2(0, e[41], e[34], zcn); CM2(1, e[42], e[36], zcn); CM2(2, e[43], e[38], zcn); CM2(3, e[45], e[36], zdn); CM2(4, e[49], e[40], zqn);
    CM2(5, e[50], e[44], zqn); CM2(6, e[51], e[46], zqn); CM2(7, e[52], e[48], zqn);
    CM1(0, e[42], zd); CM1(1, e[9], w64); CM1(2, e[2], w64); CM1(3, e[3], w64); CM1(4, e[4], w64); CM1(5, e[5], w64); CM1(6, e[6], w64); CM1(7, e[7], w64);
    CM2(0, e[47], e[42], zdn); CM2(1, e[17], e[9], w64n); CM2(2, e[10], e[2], w64n); CM2(3, e[11], e[3], w64n); CM2(4, e[12], e[4], w64n);
    CM2(5, e[13], e[5], w64n); CM2(6, e[14], e[6], w64n); CM2(7, e[15], e[7], w64n);
    CV(24); CV(25);
    *(LAS u32x4*)(size_t)tm2 = u32x4{hr[24], hr[25], hi[24], hi[25]};
    SCHED();
    CV(20); CV(21); CV(22); CV(23); W4(tm[1], hr, 20); CV(4); CV(5); W4(tm[3], hi, 20); CV(6); CV(7);
    CM1(0, e[16], w64); W4(tv[1], hr, 4); CM1(1, e[17], w64); CM1(2, e[10], w64); CM1(3, e[11], w64); CM1(4, e[12], w64); W4(tv[5], hi, 4);
    CM1(5, e[13], w64); CM1(6, e[14], w64); CM1(7, e[15], w64);
    CM2(0, e[24], e[16], w64n); CM2(1, e[25], e[17], w64n); CM2(2, e[18], e[10], w64n); CM2(3, e[19], e[11], w64n); CM2(4, e[20], e[12], w64n);
    CM2(5, e[21], e[13], w64n); CM2(6, e[22], e[14], w64n); CM2(7, e[23], e[15], w64n);
    u32x4 fm[8], fv[8];
    u32 ra0 = rm[0], ra1 = rm[1];
#define RM(s_) { const u32x2 lo = ldstr((LAS char*)(size_t)ra0), hi_ = ldstr((LAS char*)(size_t)ra1); fm[s_] = u32x4{lo.x, lo.y, hi_.x, hi_.y}; ra0 += rms; ra1 += rms; } SCHED()
    CM1(2, e[18], w64); RM(0); CM1(3, e[19], w64); CM1(4, e[20], w64); RM(1); CM1(5, e[21], w64); CM1(6, e[22], w64); RM(2); CM1(7, e[23], w64);
    CV(8); CV(9); RM(3); CV(10); CV(11); W4(tv[2], hr, 8);
    CM2(2, e[26], e[18], w64n); CM2(3, e[27], e[19], w64n); RM(4); CM2(4, e[28], e[20], w64n); W4(tv[6], hi, 8); CM2(5, e[29], e[21], w64n);
    CM2(6, e[30], e[22], w64n); RM(5); CM2(7, e[31], e[23], w64n);
    CV(12); RM(6); CV(13); CV(14); RM(7); CV(15); W4(tv[3], hr, 12); W4(tv[7], hi, 12);
#pragma unroll
    for (int s = 0; s < 8; ++s) {
      const u32x2 lo = ldstr((LAS char*)(size_t)rv[0] + s * 1024), hi_ = ldstr((LAS char*)(size_t)rv[1] + s * 1024);
      fv[s] = u32x4{lo.x, lo.y, hi_.x, hi_.y};
    }
    float k2048 = e[52].x;
    k2048 += dppz<0x111, 0xf>(k2048); k2048 += dppz<0x112, 0xf>(k2048); k2048 += dppz<0x114, 0xf>(k2048); k2048 += dppz<0x118, 0xf>(k2048);
    k2048 += dppz<0x142, 0xa>(k2048); k2048 += dppz<0x143, 0xc>(k2048);
    SCHED();
    STAMP(4);
    f32x16 kk;
#pragma unroll
    for (int r = 0; r < 16; ++r) kk[r] = 0.f;
#pragma unroll
    for (int s = 0; s < 8; ++s) kk = mfma32h(fm[s], fv[s], kk);
    LAS float* kc = (LAS float*)(lds + M_KC + w * 2816);
    *(LAS f32x4*)(kc + 8 * t + 4 * h) = f32x4{kk[0], kk[1], kk[2], kk[3]} * sc;
    *(LAS f32x2*)(kc + (h ? 384 + 2 * t : 256 + 4 * t)) = f32x2{kk[4], kk[5]} * sc;
    *(LAS f32x2*)(kc + (h ? 448 + 2 * t : 258 + 4 * t)) = f32x2{kk[6], kk[7]} * sc;
    LAS float* k3 = kc + (h ? 671 - t : 512 + t);
    k3[0] = (h ? k2048 : kk[8]) * sc;
    if (h == 0) { k3[32] = kk[9] * sc; k3[64] = kk[10] * sc; k3[96] = kk[11] * sc; }
  }
  STAMP(5);

  u32x4 wfr[28];
  const int w2off = WS_W2F + (((n2 * 56 + 7 * w) * 4) << 10);
  auto w2ld = [&](int i) { wfr[i] = ld128s(rs, l * 16 + (i & 3) * 1024, w2off + (i >> 2) * 4096); };
  f32x2 kx[8];
  u32 kpk[4];
  auto expand_rd = [&](int e) { LAS float* q = (LAS float*)(size_t)ka_[e]; kx[e] = f32x2{q[0], q[704]}; };
  auto expand_pk = [&](int e) {
    float a = kx[2 * e].x + kx[2 * e].y, c = kx[2 * e + 1].x + kx[2 * e + 1].y;
    PIN(a); PIN(c);
    kpk[e] = pk2(a, c);
    PIN(kpk[e]);
  };
  auto expand_wr = [&]() { *(LAS u32x4*)(lds + CV_K + uch * 5120 + 1024 + ui * 16) = u32x4{kpk[0], kpk[1], kpk[2], kpk[3]}; };
  u32x4 u0;
  u32x2 u1;
  auto load_u = [&](int k) {
    if (k == 0) u0 = ld128s(rs, ui * 16, WS_XNR + (2 * b + uch) * 4096);
    else u1 = ld64s(rs, uo1, WS_XNR + (2 * b + uch) * 4096);
  };
  auto copy_u = [&](int s) {
    const u32 dd[6] = {u0.x, u0.y, u0.z, u0.w, u1.x, u1.y};
    const int o = s >> 1;
    u32x4 v;
#pragma unroll
    for (int k = 0; k < 4; ++k) v[k] = (s & 1) ? __builtin_amdgcn_alignbit(dd[o + k + 1], dd[o + k], 16) : dd[o + k];
    *(LAS u32x4*)(lds + CV_U + uch * CV_UCH + s * CV_CS + ui * 16) = v;
  };
#define W2Q 97
#define NW2AV 15
  constexpr int NW2A = NW2AV;

  if (g1) {
    f32x4 acc[4][4];
#pragma unroll
    for (int i = 0; i < 4; ++i)
#pragma unroll
      for (int j = 0; j < 4; ++j) acc[i][j] = f32x4{0.f, 0.f, 0.f, 0.f};
    u32x4 fa[2][4], fb[2][4];
    auto rdA = [&](int st, int s, int i) {
      LAS char* q = lds + (st % 3 == 0 ? wa[i & 1] : st % 3 == 1 ? wa1[i & 1] : wa2[i & 1]) + (i >> 1) * 16384 + s * 8192;
      const u32x2 lo = ldstr(q), hi = ldstr(q + 1024);
      fa[s][i] = u32x4{lo.x, lo.y, hi.x, hi.y};
    };
    auto rdB = [&](int st, int s, int j) { fb[s][j] = lds128(lds + (st % 3 == 2 ? xa2[s] : xa[s] + (st % 3) * G1_SLOT) + j * 2048); };
    const int so_cs = (mt & 2) == 0 ? WS_CS + (ft * 8 + kt0) * 1024 + w * 128 : OOB;
    const int so_p0 = w == 0 ? FL_P0 + b * FS : OOB;

    if (w >= 4) __builtin_amdgcn_s_setprio(1);
    LBAR();
    STAMP(6);
    rdB(0, 0, 0); rdA(0, 0, 0); rdA(0, 0, 1); rdA(0, 0, 2); rdA(0, 0, 3); rdB(0, 0, 1); rdB(0, 0, 2); rdB(0, 0, 3);
    dmaw(IC<2>{}, 0, w); dmaw(IC<2>{}, 1, w); dmaw(IC<2>{}, 2, w);
    u32x2 fl;
    u32 msv[4];
    float nmv[4];
    f32x4 csp[2];
    u32x4 cw[4];
    f32x4 cs, cv[4];
    static_for<8>([&](auto st_) {
      constexpr int st = decltype(st_)::v;
#pragma unroll
      for (int s = 0; s < 2; ++s) {
        const int nst = s ? st + 1 : st, ns = s ^ 1;
        if (s == 1 && st < 7) {
          if (st == 1 || st == 3 || st == 4) WAIT_V(7);
          else if (st == 6) WAIT_V(11);
          else WAIT_V(6);
        }
        if (s == 1) {
          LBAR();
          if (st == 2) STAMP(18);
          if (st == 5) STAMP(21);
        }
        SCHED();
#pragma unroll
        for (int n = 0; n < 16; ++n) {
          acc[n & 3][n >> 2] = mfma16(fa[s][n & 3], fb[s][n >> 2], acc[n & 3][n >> 2]);
          SCHED();
          if (nst < 8) {
            if (n == 0) rdB(nst, ns, 3);
            if (n == 1) rdB(nst, ns, 2);
            if (n == 2) rdB(nst, ns, 1);
            if (n == 3) rdA(nst, ns, 3);
            if (n == 5) rdA(nst, ns, 2);
            if (n == 7) rdA(nst, ns, 1);
            if (n == 9) rdA(nst, ns, 0);
            if (n == 10) rdB(nst, ns, 0);
          }
          if (nst + 2 < 8) {
            const int pc = (s ? 0 : 3);
            if (n == 4 || n == 8 || n == 12) { if (s) dm0(IC<(st + 3) & 7>{}, pc + (n >> 2) - 1, w); else dm0(IC<(st + 2) & 7>{}, pc + (n >> 2) - 1, w); }
            if (n == 6 || n == 11 || n == 14) { const int k = n == 6 ? 0 : n == 11 ? 1 : 2; if (s) dgo(IC<(st + 3) & 7>{}, pc + k, w); else dgo(IC<(st + 2) & 7>{}, pc + k, w); }
          }
          SCHED();
          const int q = st * 32 + s * 16 + n;
          if (q == 4 || q == 8 || q == 12 || q == 13) cw[q == 13 ? 3 : (q >> 2) - 1] = lds128((LAS char*)(size_t)a_cw + (q == 13 ? 3 : (q >> 2) - 1) * 4096);
          if (q == 20) cs = mfma16(sel, cw[0], f32x4{0.f, 0.f, 0.f, 0.f});
          if (q == 24 || q == 28 || q == 29) cs = mfma16(sel, cw[q == 29 ? 3 : (q >> 2) - 5], cs);
          if (q == 36) {
#pragma unroll
            for (int e = 0; e < 4; ++e) { const float v = cs[e]; cs[e] = v + dppx<0x114>(v); }
            PIN(cs);
          }
          if (q == 40) {
#pragma unroll
            for (int e = 0; e < 4; ++e) { const float v = cs[e]; cs[e] = v + dppx<0x118>(v); }
            PIN(cs);
          }
          if (q == 45) st128s(__builtin_bit_cast(u32x4, cs), rs, o_cq, so_cs);
          if (q >= 52 && q < 84 && (q & 3) == 0) expand_rd((q - 52) >> 2);
          if (q >= 92 && q < 124 && (q & 7) == 4) { PIN(kx[(q - 92) >> 2]); expand_pk((q - 92) >> 3); }
          if (q == 200) expand_wr();
          if (q == 85) st64s(tag, rs, o_l0, so_p0);
          if (q == 143) asm volatile("global_load_dwordx2 %0, %1, %2 sc1" : "=v"(fl) : "v"(p0off), "s"(p.ws + FL_P0) : "memory");
          if (q == 173) {
            asm volatile("s_waitcnt vmcnt(5)" : "+v"(fl) :: "memory");
            if (!__all(fl.x == tag.x && fl.y == tag.y)) poll(rs, p0off, FL_P0, tag);
          }
          if (q == 176 || q == 178) load_u((q - 176) >> 1);
          if (q == 180 || q == 182) csp[(q - 180) >> 1] = __builtin_bit_cast(f32x4, ld128s(rs, o_cs, WS_CS + (ft * 8 + 2 * (q - 180)) * 1024));
          if (q == 184) msv[0] = ld32s(rs, l * 4, WS_ST + (mt * 128 + 64 * wt) * 4);
          if (q >= 187 && ((q - 187) & 3) == 0 && ((q - 187) >> 2) < NW2A) w2ld((q - 187) >> 2);
          if (q == 205 || q == 213 || q == 221 || q == 229) copy_u((q - 205) >> 3);
          if (q == 209) {
            PIN(csp[0]); PIN(csp[1]);
            csp[0] += csp[1];
            const auto r0 = __builtin_amdgcn_permlane32_swap(f2u(csp[0][0]), f2u(csp[0][2]), false, false);
            const auto r1 = __builtin_amdgcn_permlane32_swap(f2u(csp[0][1]), f2u(csp[0][3]), false, false);
            csp[0][0] = u2f(r0[0]) + u2f(r0[1]);
            csp[0][1] = u2f(r1[0]) + u2f(r1[1]);
            PIN(csp[0]);
          }
          if (q == 217) {
            const auto r = __builtin_amdgcn_permlane16_swap(f2u(csp[0][0]), f2u(csp[0][1]), false, false);
            *(LAS float*)(size_t)a_cs = u2f(r[0]) + u2f(r[1]);
          }
          if (q == 220) {
            PIN(msv[0]);
            const auto m = __builtin_amdgcn_permlane32_swap(msv[0], msv[0], false, false);
            msv[0] = m[0]; msv[2] = m[1];
            PIN(msv[0]); PIN(msv[2]);
          }
          if (q == 225 || q == 228) {
            const int j = q == 225 ? 0 : 2;
            const auto m = __builtin_amdgcn_permlane16_swap(msv[j], msv[j], false, false);
            msv[j] = m[0]; msv[j + 1] = m[1];
            PIN(msv[j]); PIN(msv[j + 1]);
          }
          if (q >= 232 && q < 236) { cv[q - 232] = *(LAS f32x4*)((LAS char*)(size_t)a_cv + (q - 232) * 64); }
          if (q >= 236 && q < 240) { nmv[q - 236] = -b2f(msv[q - 236]); PIN(nmv[q - 236]); }
          if (q >= 240) {
            const int m = (n + 8) & 15, ii = m & 3, jj = m >> 2;
#pragma unroll
            for (int r = 0; r < 4; ++r) {
              float v = __builtin_fmaf(nmv[jj], cv[ii][r], acc[ii][jj][r]);
              PIN(v);
              acc[ii][jj][r] = v;
            }
          }
          SCHED();
        }
      }
    });
    STAMP(7);
    __builtin_amdgcn_s_setprio(3);
#pragma unroll
    for (int j = 0; j < 4; ++j) {
      const float sd = bhi(msv[j]), sd2 = sd * sd;
      const float ce = -1.44269504f * __builtin_amdgcn_rcpf(sd);
      u32x4 o;
#pragma unroll
      for (int r = 0; r < 4; ++r) {
        const f32x2 gg = {acc[r >> 1][j][2 * (r & 1)], acc[r >> 1][j][2 * (r & 1) + 1]};
        const f32x2 uu = {acc[2 + (r >> 1)][j][2 * (r & 1)], acc[2 + (r >> 1)][j][2 * (r & 1) + 1]};
        const f32x2 a = gg * f32x2{ce, ce};
        const f32x2 e = {__builtin_amdgcn_exp2f(a.x), __builtin_amdgcn_exp2f(a.y)};
        const f32x2 d = fma2(e, f32x2{sd2, sd2}, f32x2{sd2, sd2});
        const f32x2 q = {__builtin_amdgcn_rcpf(d.x), __builtin_amdgcn_rcpf(d.y)};
        o[r] = pk2v((gg * uu) * q);
      }
      st128s(o, rs, l * 16, WS_H + (((mt * 8 + 4 * wt + j) * 56 + ft * 4 + wf) << 10));
      SCHED();
    }
    __builtin_amdgcn_s_setprio(1);
    STAMP(8);
  } else {
    poll(rs, p0off, FL_P0, tag);
    if (b == 255 && w == 0) {
#pragma unroll 1
      for (int i = 0; i < 4; ++i) poll(rs, (64 * i + l) * FS, FL_P0, tag);
      if (l == 0) st64s(tag, rs, 0, FL_EP);
    }
    LBAR();
    load_u(0); load_u(1);
#pragma unroll
    for (int e = 0; e < 8; ++e) expand_rd(e);
#pragma unroll
    for (int e = 0; e < 4; ++e) expand_pk(e);
    LBAR();
    expand_wr();
#pragma unroll
    for (int s = 0; s < 4; ++s) copy_u(s);
    SCHED();
#pragma unroll
    for (int i = 0; i < NW2A; ++i) w2ld(i);
    __builtin_amdgcn_s_waitcnt(0x0F70);
    LBAR();
  }

  STAMP(10);
  u32x2 hfl;
  u32x4 hf[28];
  const int hoff = WS_H + ((4 * m2 * 56 + 7 * w) << 10);
  const int jr = 0;
  auto hld = [&](int n) { hf[n] = ld128s(rs, l * 16, hoff + (((n / 7) ^ jr) * 56 + n % 7) * 1024); };
  const int hfs = FL_H + (m2 * 56 + 7 * w) * FS;
#define PDV 12
  constexpr int PD = PDV;
  {
    LAS char *ua = (LAS char*)(size_t)a_ua, *ka = (LAS char*)(size_t)a_ka;
    *(LAS u32x4*)(size_t)a_zk = zero4;
    *(LAS u32x4*)(size_t)a_zu = zero4;
    asm volatile("" ::: "memory");
    f32x4 acc[4];
#pragma unroll
    for (int i = 0; i < 4; ++i) acc[i] = f32x4{0.f, 0.f, 0.f, 0.f};
    const int so_h = g1 ? FL_H + ((mt * 2 + wt) * 56 + ft * 4 + wf) * FS : OOB;
    auto hook = [&](int rr, int c) {
#define HPUB 3
#define HPOLL 24
      const int sq = rr * 8 + c;
#define WS0 4
#define WSN 13
      auto nw = [&](int z) { return z <= WS0 ? 0 : z >= WS0 + WSN ? 28 - NW2A : ((z - WS0) * (28 - NW2A) + WSN - 1) / WSN; };
      if (sq == HPUB) {
        constexpr int c0 = HPUB <= WS0 ? 0 : HPUB >= WS0 + WSN ? 28 - NW2A : ((HPUB - WS0) * (28 - NW2A) + WSN - 1) / WSN;
        asm volatile("s_waitcnt vmcnt(%0)" :: "i"(c0) : "memory");
        st64s(tag, rs, o_l0, so_h);
        __builtin_amdgcn_s_setprio(0);
        STAMP(9);
      }
      if (sq >= 0 && nw(sq + 1) > nw(sq)) w2ld(NW2A + nw(sq));
      if (sq == (HPOLL < 32 ? HPOLL - 5 : 31)) hfl = ld64s(rs, hfo, hfs);
      if (sq == HPOLL) {
        STAMP(2);
        if (!__all(hfl.x == tag.x && hfl.y == tag.y)) poll(rs, hfo, hfs, tag);
        asm volatile("" ::: "memory");
        STAMP(23);
      }
      if (sq >= HPOLL) {
#pragma unroll
        for (int n = (sq - HPOLL) * PD / (HPOLL < 32 ? 32 - HPOLL : 1); n < (sq + 1 - HPOLL) * PD / (HPOLL < 32 ? 32 - HPOLL : 1); ++n) hld(n);
      }
    };
    conv_tiles(ua, ka, acc, hook);
#pragma unroll
    for (int i = 0; i < 4; ++i) *(LAS f32x4*)(lds + CV_X + (w * 4 + i) * 1024 + l * 16) = acc[i];
    if (tid == 0) *(LAS u32*)(lds + M_CNT) = 0u;
  }
  LBAR();
  STAMP(11);
  int ytile;
  {
    const int ts = (w >> 1) & 1, i = (w & 1) + 2 * (w >> 2);
    ytile = 7 - 2 * i - ts;
    const int pos = 256 * ytile + 16 * l15 + 4 * g;
    f32x4 y0[2], y1[2];
    u32x2 ur[2];
#pragma unroll
    for (int ch = 0; ch < 2; ++ch) {
      y0[ch] = *(LAS f32x4*)(lds + CV_X + ((ch + 2 * ts) * 4 + i) * 1024 + l * 16);
      y1[ch] = *(LAS f32x4*)(lds + CV_X + ((ch + 2 * ts + 4) * 4 + i) * 1024 + l * 16);
      ur[ch] = lds64(lds + CV_U + ch * CV_UCH + (2044 - pos) * 2);
    }
    LBAR();
    float res[2][4];
#pragma unroll
    for (int ch = 0; ch < 2; ++ch) {
      const float dsk = ch ? bhi(dk) : b2f(dk);
      const f32x4 y = y0[ch] + y1[ch];
      res[ch][0] = __builtin_fmaf(bhi(ur[ch].y), dsk, y[0]); res[ch][1] = __builtin_fmaf(b2f(ur[ch].y), dsk, y[1]);
      res[ch][2] = __builtin_fmaf(bhi(ur[ch].x), dsk, y[2]); res[ch][3] = __builtin_fmaf(b2f(ur[ch].x), dsk, y[3]);
    }
    const u32x4 o = {pk2(res[0][0], res[1][0]), pk2(res[0][1], res[1][1]), pk2(res[0][2], res[1][2]), pk2(res[0][3], res[1][3])};
    st128s(o, rs, pos * 4, WS_Y + b * 8192);
    if (HPOLL == 32) {
      STAMP(2);
      if (!__all(hfl.x == tag.x && hfl.y == tag.y)) poll(rs, hfo, hfs, tag);
      asm volatile("" ::: "memory");
      STAMP(23);
#pragma unroll
      for (int n = 0; n < PD; ++n) hld(n);
    }
  }
  STAMP(12);

  {
    const int oj = w >> 1, rh = w & 1;
    const int etok = 64 * m2 + 16 * (oj ^ jr) + l15, ech = 64 * n2 + 16 * g + 8 * rh;
    u32x4 xv;
    u32 yo = (ech >> 1) * 8192 + etok * 4, yfv = (8 * g + (l & 3)) * FS;
    PIN(yo); PIN(yfv);
    SCHED();
    f32x4 acc[4][4];
#pragma unroll
    for (int i = 0; i < 4; ++i)
#pragma unroll
      for (int j = 0; j < 4; ++j) acc[i][j] = f32x4{0.f, 0.f, 0.f, 0.f};
    u32x2 yfl;
    const int yfo = FL_Y + ((m2 >> 2) * 256 + 32 * n2 + 4 * rh) * FS;
    LAS char* pw = opaque(lds + w * 16384 + l * 8);
    LAS char* pr = opaque(lds + w * 2048 + l * 8);
    LAS char* pr2 = opaque(pr + 65536);
    u32 a_cnt = lds0 + M_CNT + l * 4, one = 1u;
    PIN(a_cnt); PIN(one);
    auto part_wr = [&](int j, int k) {
      *(LAS f32x2*)(pw + (j * 8 + k) * 512) = f32x2{acc[k & 3][j][2 * (k >> 2)], acc[k & 3][j][2 * (k >> 2) + 1]};
    };
#pragma unroll
    for (int n = 0; n < 28; ++n) {
#pragma unroll
      for (int i = 0; i < 4; ++i) {
        acc[i][n / 7] = mfma16(wfr[4 * (n % 7) + i], hf[n], acc[i][n / 7]);
        SCHED();
        if (i == 3 && n + PD < 28) hld(n + PD);
        if ((i & 1) == 0 && n >= 8 && n % 7 >= 1 && n % 7 < 5) part_wr(n / 7 - 1, (n % 7 - 1) * 2 + (i >> 1));
        SCHED();
      }
      if (n == YP1 && w >= 4) { asm volatile("s_waitcnt vmcnt(%0)" :: "i"(YP1 + 1) : "memory"); st64s(tag, rs, o_l0, FL_Y + (ytile * 256 + b) * FS); }
      if (n == YP0 && w < 4) { asm volatile("s_waitcnt vmcnt(%0)" :: "i"(YP0 + 1) : "memory"); st64s(tag, rs, o_l0, FL_Y + (ytile * 256 + b) * FS); }
      if (n == 21) xv = *(const GAS u32x4*)((const char*)p.x + (size_t)etok * 1024 + ech * 2);
      if (n == 22) yfl = ld64s(rs, yfv, yfo);
      if (n == TB && w >= 4) LBAR();
      SCHED();
    }
    STAMP(13);
#pragma unroll
    for (int k = 0; k < 8; ++k) part_wr(3, k);
    asm volatile("" ::: "memory");
    if (w < 4) LBAR();
    else asm volatile("ds_add_u32 %0, %1" :: "v"(a_cnt), "v"(one) : "memory");
    if (!__all(yfl.x == tag.x && yfl.y == tag.y)) poll(rs, yfv, yfo, tag);
    asm volatile("" ::: "memory");
    u32 yv[4];
#pragma unroll
    for (int j = 0; j < 4; ++j) yv[j] = ld32s(rs, yo, WS_Y + j * 8192);
    f32x2 pp[8][4];
#pragma unroll
    for (int ww = 0; ww < 4; ++ww)
#pragma unroll
      for (int i = 0; i < 4; ++i) pp[ww][i] = *(volatile LAS f32x2*)(pr + ww * 16384 + i * 512);
    if (w >= 4) {
      volatile LAS u32* cnt = (volatile LAS u32*)(lds + M_CNT);
#pragma unroll 1
      for (int spin = 0; spin < (1 << 20) && *cnt < 4u; ++spin) {}
      asm volatile("" ::: "memory");
    }
    STAMP(14);
#pragma unroll
    for (int ww = 0; ww < 4; ++ww)
#pragma unroll
      for (int i = 0; i < 4; ++i) pp[4 + ww][i] = *(volatile LAS f32x2*)(pr2 + ww * 16384 + i * 512);
    SCHED();
    f32x2 sm[4];
#pragma unroll
    for (int i = 0; i < 4; ++i) sm[i] = ((pp[0][i] + pp[1][i]) + (pp[2][i] + pp[3][i])) + ((pp[4][i] + pp[5][i]) + (pp[6][i] + pp[7][i]));
    const u32 ff[4] = {pk2(sm[0].x, sm[1].x), pk2(sm[2].x, sm[3].x), pk2(sm[0].y, sm[1].y), pk2(sm[2].y, sm[3].y)};
    u32x4 r;
#pragma unroll
    for (int j = 0; j < 4; ++j) {
      const u32 s = pk2v(up2(xv[j]) + up2(yv[j]));
      r[j] = pk2v(up2(s) + up2(ff[j]));
    }
    *(GAS u32x4*)((char*)p.out + (size_t)etok * 1024 + ech * 2) = r;
  }
  STAMP(15);
  if (w == 7) {
    st64s(u32x2{mt0, mtp}, rs, o_l0, WS_ST + 524288 + b * 128);
    WAIT_V(0);
    st64s(tag, rs, o_l0, WS_ST + 655360 + b * 128);
    if (b == 255) {
      u32 lo = 0, hi = 0;
#pragma unroll 1
      for (int i = 0; i < 4; ++i) {
        poll(rs, (64 * i + l) * 128, WS_ST + 655360, tag);
        const u32x2 v = ld64s(rs, (64 * i + l) * 128, WS_ST + 524288);
        lo = max(lo, 0x10000u - (v.x - mt0)); hi = max(hi, 0x10000u + (v.y - mt0));
      }
      lo = wave_max_u32(lo); hi = wave_max_u32(hi);
      const u32 now = (u32)__builtin_amdgcn_s_memrealtime();
      u32 T = (hi + lo - 0x20000u) >> 20;
      T = min(T, 4000u);
      while ((u32)__builtin_amdgcn_s_memrealtime() - now < T) __builtin_amdgcn_s_sleep(1);
    }
  }
}

extern "C" void kernel_launch(void* const* d_in, const int* in_sizes, int n_in, void* d_out, int out_size, void* d_ws,
                              size_t ws_size, hipStream_t stream) {
  Params p;
  p.x = (const u16*)d_in[0]; p.lar = (const u16*)d_in[1]; p.aim = (const u16*)d_in[2]; p.cre = (const u16*)d_in[3];
  p.cim = (const u16*)d_in[4]; p.ldt = (const u16*)d_in[5]; p.dsk = (const u16*)d_in[6];
  p.w1 = (const u16*)d_in[8]; p.w3 = (const u16*)d_in[9]; p.w2 = (const u16*)d_in[10];
  p.out = (u16*)d_out; p.ws = (char*)d_ws;
  fused<<<256, 512, 0, stream>>>(p);
}
```

```cpp
#include <hip/hip_runtime.h>
#include <hip/hip_bf16.h>

typedef unsigned short u16;
typedef unsigned int u32;
typedef unsigned long long u64;
typedef __bf16 bf16x8 __attribute__((ext_vector_type(8)));
typedef __bf16 bf16x2 __attribute__((ext_vector_type(2)));
typedef _Float16 f16x8 __attribute__((ext_vector_type(8)));
typedef _Float16 f16x2 __attribute__((ext_vector_type(2)));
typedef short s16x4 __attribute__((ext_vector_type(4)));
typedef float f32x2 __attribute__((ext_vector_type(2)));
typedef float f32x4 __attribute__((ext_vector_type(4)));
typedef float f32x16 __attribute__((ext_vector_type(16)));
typedef u32 u32x2 __attribute__((ext_vector_type(2)));
typedef u32 u32x4 __attribute__((ext_vector_type(4)));
typedef u64 u64x4 __attribute__((ext_vector_type(4)));
typedef u64 u64x8 __attribute__((ext_vector_type(8)));
typedef __amdgpu_buffer_rsrc_t rsrc_t;
#define LAS __attribute__((address_space(3)))
#define GAS __attribute__((address_space(1)))
#define DEV __device__ __forceinline__

#define NTQ " nt"
#define TB 21
#define YP1 1
#define YP0 10
constexpr int L = 2048, D = 512, NM = 128, F = 1792;
constexpr int LDS_BYTES = 163840;
constexpr int SC1 = 16;

constexpr int WS_H = 0;
constexpr int WS_XNR = 8 << 20;
constexpr int WS_W2F = 10 << 20;
constexpr int WS_Y = 12 << 20;
constexpr int WS_ST = 14 << 20;
constexpr int WS_CS = WS_ST + 8192;
constexpr int WS_FL = 15 << 20;
constexpr int FS = 128;
constexpr int FL_EP = WS_FL;
constexpr int FL_P0 = WS_FL + 4096;
constexpr int FL_H = FL_P0 + 256 * FS;
constexpr int FL_Y = FL_H + 2048 * FS;
constexpr int WS_BYTES = 16 << 20;

struct Params {
  const u16 *x, *lar, *aim, *cre, *cim, *ldt, *dsk, *w1, *w3, *w2;
  u16* out;
  char* ws;
};

template <int V> struct IC { static constexpr int v = V; };
template <int N, int I = 0, class Fn>
DEV void static_for(Fn&& fn) { if constexpr (I < N) { fn(IC<I>{}); static_for<N, I + 1>(fn); } }
DEV u32 f2u(float f) { return __builtin_bit_cast(u32, f); }
DEV float u2f(u32 v) { return __builtin_bit_cast(float, v); }
DEV float b2f(u32 v) { return __builtin_bit_cast(float, v << 16); }
DEV float bhi(u32 v) { return __builtin_bit_cast(float, v & 0xffff0000u); }
DEV u32 pk2v(f32x2 f) { return __builtin_bit_cast(u32, __builtin_convertvector(f, bf16x2)); }
DEV u32 pk2(float lo, float hi) { return pk2v(f32x2{lo, hi}); }
DEV f32x2 up2(u32 v) { return f32x2{b2f(v), bhi(v)}; }
DEV float rb(float f) { return b2f(pk2(f, f) & 0xffffu); }
DEV f32x2 fma2(f32x2 a, f32x2 b, f32x2 c) { return __builtin_elementwise_fma(a, b, c); }
DEV f32x16 mfma32(u32x4 a, u32x4 b, f32x16 c) {
  return __builtin_amdgcn_mfma_f32_32x32x16_bf16(__builtin_bit_cast(bf16x8, a), __builtin_bit_cast(bf16x8, b), c, 0, 0, 0);
}
DEV f32x4 mfma16(u32x4 a, u32x4 b, f32x4 c) {
  return __builtin_amdgcn_mfma_f32_16x16x32_bf16(__builtin_bit_cast(bf16x8, a), __builtin_bit_cast(bf16x8, b), c, 0, 0, 0);
}
DEV f32x16 mfma32h(u32x4 a, u32x4 b, f32x16 c) {
  return __builtin_amdgcn_mfma_f32_32x32x16_f16(__builtin_bit_cast(f16x8, a), __builtin_bit_cast(f16x8, b), c, 0, 0, 0);
}
DEV u32x4 lds128(LAS char* p) { return *(LAS u32x4*)p; }
DEV u32x2 lds64(LAS char* p) { return *(LAS u32x2*)p; }
DEV u32x2 ldstr(LAS char* p) { return __builtin_bit_cast(u32x2, __builtin_amdgcn_ds_read_tr16_b64_v4i16((LAS s16x4*)p)); }
template <int LOFF>
DEV void dma_m0(u32 lbase) { asm volatile("s_add_u32 m0, %0, %1" :: "s"(lbase), "i"(LOFF) : "memory", "scc", "m0"); }
template <bool NT>
DEV void dma_go(u32x4 rs, u32 voff, u32 soff) {
  if (NT) asm volatile("buffer_load_dwordx4 %0, %1, %2 offen nt lds" :: "v"(voff), "s"(rs), "s"(soff) : "memory");
  else asm volatile("buffer_load_dwordx4 %0, %1, %2 offen lds" :: "v"(voff), "s"(rs), "s"(soff) : "memory");
}
DEV u32x4 mkrs(const void* ptr, u32 bytes) { const u64 a = (u64)ptr; return u32x4{(u32)a, (u32)(a >> 32) & 0xffffu, bytes, 0x00020000u}; }
#define WAIT_V(n) asm volatile("s_waitcnt vmcnt(" #n ")" ::: "memory")
#define WAIT_L0() asm volatile("s_waitcnt lgkmcnt(0)" ::: "memory")
#define BAR() __builtin_amdgcn_s_barrier()
#define LBAR() do { WAIT_L0(); BAR(); asm volatile("" ::: "memory"); } while (0)
#define SCHED() __builtin_amdgcn_sched_barrier(0)
#define PIN(x) asm volatile("" : "+v"(x))
constexpr int OOB = 0x04000000;
#define STAMP(i)
#define STAMP_DECL
#define STAMP_DUMP()

DEV u32x4 ld128s(rsrc_t rs, int voff, int soff) { return __builtin_amdgcn_raw_buffer_load_b128(rs, voff, soff, SC1); }
DEV u32x2 ld64s(rsrc_t rs, int voff, int soff) { return __builtin_amdgcn_raw_buffer_load_b64(rs, voff, soff, SC1); }
DEV u32 ld32s(rsrc_t rs, int voff, int soff) { return __builtin_amdgcn_raw_buffer_load_b32(rs, voff, soff, SC1); }
DEV void st128s(u32x4 v, rsrc_t rs, int voff, int soff) {
  __builtin_amdgcn_raw_buffer_store_b128(v, rs, voff, soff, SC1);
  asm volatile("s_nop 1" :: "v"(v) : "memory");
}
DEV void st64s(u32x2 v, rsrc_t rs, int voff, int soff) { __builtin_amdgcn_raw_buffer_store_b64(v, rs, voff, soff, SC1); }
DEV void st32s(u32 v, rsrc_t rs, int voff, int soff) { __builtin_amdgcn_raw_buffer_store_b32(v, rs, voff, soff, SC1); }
DEV void poll(rsrc_t rs, int voff, int soff, u32x2 tag) {
#pragma unroll 1
  for (int spin = 0; spin < (1 << 18); ++spin) {
    const u32x2 v = ld64s(rs, voff, soff);
    if (__all(v.x == tag.x && v.y == tag.y)) break;
    __builtin_amdgcn_s_sleep(4);
  }
  __builtin_amdgcn_fence(__ATOMIC_ACQUIRE, "wavefront");
  asm volatile("" ::: "memory");
}

template <int CTRL, int RM>
DEV float dppz(float v) { return __builtin_bit_cast(float, __builtin_amdgcn_update_dpp(0, __builtin_bit_cast(int, v), CTRL, RM, 0xf, true)); }
template <int CTRL, int RM>
DEV u32 dppu(u32 v) { return (u32)__builtin_amdgcn_update_dpp(0, (int)v, CTRL, RM, 0xf, true); }
DEV u32 wave_max_u32(u32 v) {
  v = max(v, dppu<0x111, 0xf>(v)); v = max(v, dppu<0x112, 0xf>(v)); v = max(v, dppu<0x114, 0xf>(v)); v = max(v, dppu<0x118, 0xf>(v));
  v = max(v, dppu<0x142, 0xa>(v)); v = max(v, dppu<0x143, 0xc>(v));
  return (u32)__builtin_amdgcn_readlane((int)v, 63);
}

DEV float rdl(float v, int lane) { return __builtin_bit_cast(float, __builtin_amdgcn_readlane(__builtin_bit_cast(int, v), lane)); }
template <int CTRL>
DEV float dppx(float v) { return __builtin_bit_cast(float, __builtin_amdgcn_mov_dpp(__builtin_bit_cast(int, v), CTRL, 0xf, 0xf, true)); }
DEV float rows8_sum(const float (&p)[8], int l) {
  float a[4], c[2];
#pragma unroll
  for (int i = 0; i < 4; ++i) {
    const auto r = __builtin_amdgcn_permlane32_swap(__builtin_bit_cast(u32, p[i]), __builtin_bit_cast(u32, p[i + 4]), false, false);
    a[i] = __builtin_bit_cast(float, (u32)r[0]) + __builtin_bit_cast(float, (u32)r[1]);
  }
#pragma unroll
  for (int i = 0; i < 2; ++i) {
    const auto r = __builtin_amdgcn_permlane16_swap(__builtin_bit_cast(u32, a[i]), __builtin_bit_cast(u32, a[i + 2]), false, false);
    c[i] = __builtin_bit_cast(float, (u32)r[0]) + __builtin_bit_cast(float, (u32)r[1]);
  }
  const bool up = l & 8;
  float v = (up ? c[1] : c[0]) + dppx<0x128>(up ? c[0] : c[1]);
  v += dppx<0xB1>(v); v += dppx<0x4E>(v); v += dppx<0x141>(v);
  return v;
}

constexpr int G1_SLOT = 49152;
constexpr int KG_T = 2 * G1_SLOT;
constexpr int KG_TW = 13312;
constexpr int SPARE = 3 * G1_SLOT;
constexpr int M_CS = SPARE;
constexpr int M_CNT = SPARE + 4096;
constexpr int M_KC = SPARE + 5120;
constexpr int CV_K = SPARE + 5120;
static_assert(KG_T + 4 * KG_TW <= M_KC && M_KC + 4 * 2816 <= LDS_BYTES, "lds");
constexpr int CV_CS = 4160;
constexpr int CV_U = 2 * G1_SLOT;
constexpr int CV_UCH = 4 * CV_CS;
constexpr int CV_X = 0;

DEV f32x2 cmul(f32x2 a, f32x2 b, f32x2 bn) { return fma2(f32x2{a.y, a.x}, bn, a * f32x2{b.x, b.x}); }
DEV f32x2 cneg(f32x2 b) { return f32x2{-b.y, b.y}; }
DEV f32x2 cexp_p(float lre2, float rev, float pw) {
  const float mg = __builtin_amdgcn_exp2f(lre2 * pw);
  const float ph = __builtin_amdgcn_fractf(rev * pw);
  return f32x2{__builtin_amdgcn_cosf(ph), __builtin_amdgcn_sinf(ph)} * f32x2{mg, mg};
}
DEV u32 pkh(f32x2 f) { return __builtin_bit_cast(u32, __builtin_convertvector(f, f16x2)); }
DEV LAS char* opaque(LAS char* q) { u32 v = (u32)(size_t)q; asm volatile("" : "+v"(v)); return (LAS char*)(size_t)v; }

template <class Hook>
DEV void conv_tiles(LAS char* ua, LAS char* ka, f32x4 (&acc)[4], Hook hook) {
  constexpr int ORD[16] = {0, 15, 13, 11, 9, 1, 14, 12, 10, 8, 2, 3, 4, 5, 6, 7};
  u32x4 fr[2][16];
  auto load = [&](int rr, int id) {
    if (id < 8) {
      const u32x2 lo = *(volatile LAS u32x2*)(ua + (7 - id) * 512 + (3 - rr) * 64), hi = *(volatile LAS u32x2*)(ua + (7 - id) * 512 + (3 - rr) * 64 + 8);
      fr[rr & 1][id] = u32x4{lo.x, lo.y, hi.x, hi.y};
    } else {
      fr[rr & 1][id] = lds128(ka + (id - 8) * 512 + (3 - rr) * 64);
    }
  };
#pragma unroll
  for (int k = 0; k < 16; ++k) { load(0, ORD[k]); SCHED(); }
  hook(0, -1);
  SCHED();
#pragma unroll
  for (int rr = 0; rr < 4; ++rr) {
#pragma unroll
    for (int c = 0; c < 8; ++c) {
      u32x4(&f)[16] = fr[rr & 1];
#pragma unroll
      for (int i = 0; i < 4; ++i) {
        if (c <= 7 - 2 * i) { acc[i] = mfma16(f[c], f[8 + 7 - 2 * i - c], acc[i]); SCHED(); }
        if (rr < 3 && i < 2) { load(rr + 1, ORD[2 * c + i]); SCHED(); }
      }
      hook(rr, c);
      SCHED();
    }
  }
}

__global__ __launch_bounds__(512, 2) void fused(Params args) {
  __shared__ __attribute__((aligned(16))) char smem[LDS_BYTES];
  u64x8 ka0;
  u64x4 ka1;
  asm volatile("s_load_dwordx16 %0, %2, 0x0\n\ts_load_dwordx8 %1, %2, 0x40" : "=&s"(ka0), "=&s"(ka1) : "s"(__builtin_amdgcn_kernarg_segment_ptr()) : "memory");
#define CODEPF 23000
  u32x4 pf;
  if (__builtin_amdgcn_readfirstlane(threadIdx.x >> 6) == 4) {
    u64 pc;
    asm volatile("s_getpc_b64 %0" : "=s"(pc));
    const u32 po = min((u32)((blockIdx.x >> 3) * 1024 + (threadIdx.x & 63) * 16), (u32)(CODEPF - 16));
    asm volatile("global_load_dwordx4 %0, %1, %2" : "=v"(pf) : "v"(po), "s"(pc & ~(u64)255) : "memory");
  }
  LAS char* lds = (LAS char*)smem;
  const u32 lds0 = (u32)(size_t)lds;
  const int tid = threadIdx.x, l = tid & 63;
  const int w = __builtin_amdgcn_readfirstlane(tid >> 6);
  const int b = blockIdx.x, xcd = b & 7, rk = b >> 3;
  const bool g1 = rk < 28;
  const int l15 = l & 15, g = l >> 4, t = l & 31, h = l >> 5;
  const int mt = (xcd >> 1) * 4 + (rk & 3), ft = (xcd & 1) * 7 + (rk >> 2);
#define ROTV 5
  const int kt0 = ROTV == 0 ? 0 : ROTV == 1 ? (ft + 2 * (mt & 3) + ((mt >> 2) & 1)) & 7 : ROTV == 2 ? (2 * (xcd >> 1) + 4 * (xcd & 1)) & 7 : (2 * (xcd >> 1) + 4 * (xcd & 1) + (mt & 1)) & 7;
  const int wt = w & 1, wf = w >> 1;
  u32 xo0 = (l >> 3) * 1024 + (((l & 7) ^ g) << 4);
  u32 xo1 = (8 + (l >> 3)) * 1024 + (((l & 7) ^ (4 + g)) << 4);
  u32 wo1 = g * (F * 2) + ((l15 ^ (4 * g + 2 * (w & 1))) << 4), wo1b = wo1 + 4 * F * 2;
  PIN(xo0); PIN(xo1); PIN(wo1); PIN(wo1b);
  u32 tv[8], tm[4], tm2, rv[2], rm[2];
  {
    const u32 tw = lds0 + KG_T + (w & 3) * KG_TW;
#pragma unroll
    for (int k = 0; k < 8; ++k) { tv[k] = tw + l * 128 + ((k ^ (l & 7)) << 4); PIN(tv[k]); }
#pragma unroll
    for (int k = 0; k < 4; ++k) { tm[k] = tw + 8192 + l * 64 + ((k ^ ((l >> 1) & 3)) << 4); PIN(tm[k]); }
    tm2 = tw + 12288 + l * 16;
    PIN(tm2);
    const int blk = g & 1, c = (l >> 2) & 1, pp = l & 3;
#pragma unroll
    for (int t2 = 0; t2 < 2; ++t2) {
      const int m = 4 * h + 2 * t2 + ((l >> 3) & 1);
      rv[t2] = tw + m * 128 + (((4 * c + 2 * blk + (pp >> 1)) ^ m) << 4) + 8 * (pp & 1);
      rm[t2] = blk ? tw + 12288 + m * 16 + 8 * c : tw + 8192 + m * 64 + (((2 * c + (pp >> 1)) ^ (m >> 1)) << 4) + 8 * (pp & 1);
      PIN(rv[t2]); PIN(rm[t2]);
    }
  }
  u32 rms = (g & 1) ? 128 : 512;
  PIN(rms);
  asm volatile("s_waitcnt lgkmcnt(0)" : "+s"(ka0), "+s"(ka1) :: "memory");
  Params p;
  p.x = (const u16*)ka0[0]; p.lar = (const u16*)ka0[1]; p.aim = (const u16*)ka0[2]; p.cre = (const u16*)ka0[3]; p.cim = (const u16*)ka0[4];
  p.ldt = (const u16*)ka0[5]; p.dsk = (const u16*)ka0[6]; p.w1 = (const u16*)ka0[7];
  p.w3 = (const u16*)ka1[0]; p.w2 = (const u16*)ka1[1]; p.out = (u16*)ka1[2]; p.ws = (char*)ka1[3];
  const rsrc_t rs = __builtin_amdgcn_make_buffer_rsrc(p.ws, 0, WS_BYTES, 0x00020000);
  STAMP_DECL;
  STAMP(3);

  u32x2 tag;
  u32 p_ldt2, dk;
  asm volatile("s_load_dwordx2 %0, %3, 0x0\n\ts_load_dword %1, %4, 0x0\n\ts_load_dword %2, %5, 0x0"
               : "=&s"(tag), "=&s"(p_ldt2), "=&s"(dk) : "s"(p.ws + FL_EP), "s"(p.ldt + 2 * b), "s"(p.dsk + 2 * b) : "memory");

  u32 p_lar, p_aim, p_cre, p_cim;
  if (w < 4) {
    const u32 po = (b * 256 + tid) * 2;
    asm volatile("global_load_ushort %0, %1, %2" NTQ : "=v"(p_lar) : "v"(po), "s"(p.lar) : "memory");
    asm volatile("global_load_ushort %0, %1, %2" NTQ : "=v"(p_aim) : "v"(po), "s"(p.aim) : "memory");
    asm volatile("global_load_ushort %0, %1, %2" NTQ : "=v"(p_cre) : "v"(po), "s"(p.cre) : "memory");
    asm volatile("global_load_ushort %0, %1, %2" NTQ : "=v"(p_cim) : "v"(po), "s"(p.cim) : "memory");
  }
  SCHED();

  const u32x4 rx = mkrs(p.x, L * D * 2), rw1 = mkrs(p.w1, D * F * 2), rw3 = mkrs(p.w3, D * F * 2);
#define NTS 0
  auto dm0 = [&](auto st_, int piece, int ww) {
    constexpr int S = (decltype(st_)::v % 3) * G1_SLOT;
    const u32 lb = lds0 + ww * 2048;
    if (piece == 0) dma_m0<S>(lb);
    if (piece == 1) dma_m0<S + 16384>(lb);
    if (piece == 2) dma_m0<S + 32768>(lb);
    if (piece == 3) dma_m0<S + 1024>(lb);
    if (piece == 4) dma_m0<S + 16384 + 1024>(lb);
    if (piece == 5) dma_m0<S + 32768 + 1024>(lb);
  };
  auto dgo = [&](auto st_, int piece, int ww) {
    constexpr bool nt = decltype(st_)::v < NTS;
    const int kt = (kt0 + decltype(st_)::v) & 7;
    const u32 sx = (mt * 128 + ww * 16) * 1024 + kt * 128, sw = (kt * 64 + ww * 8) * (F * 2) + ft * 256;
    if (piece == 0) dma_go<nt>(rx, xo0, sx);
    if (piece == 1) dma_go<nt>(rw1, wo1, sw);
    if (piece == 2) dma_go<nt>(rw3, wo1, sw);
    if (piece == 3) dma_go<nt>(rx, xo1, sx);
    if (piece == 4) dma_go<nt>(rw1, wo1b, sw);
    if (piece == 5) dma_go<nt>(rw3, wo1b, sw);
  };
  auto dmaw = [&](auto st_, int piece, int ww) { dm0(st_, piece, ww); asm volatile("s_nop 0"); dgo(st_, piece, ww); };

  u32x2 wr[8];
  u32x4 xr[8];
  const int un2 = b / 28, ukb = 2 * (b - 28 * un2) + (w & 1);
  const int ax = xcd * 4 + (rk - 28);
  if (w >= 4 && g1) {
#pragma unroll
    for (int i = 0; i < 6; ++i) { dmaw(IC<0>{}, i, w - 4); dmaw(IC<0>{}, i, w); }
    if (w < 6) {
      const char* wb2 = (const char*)p.w2 + (size_t)(32 * ukb) * 1024 + un2 * 128;
      const u32 wv = g * 4096 + l15 * 8;
#define W2RAW(j, base, off) asm volatile("global_load_dwordx2 %0, %1, %2 offset:" #off NTQ : "=v"(wr[j]) : "v"(wv), "s"(base) : "memory")
      W2RAW(0, wb2, 0); W2RAW(1, wb2, 1024); W2RAW(2, wb2, 2048); W2RAW(3, wb2, 3072);
      W2RAW(4, wb2 + 16384, 0); W2RAW(5, wb2 + 16384, 1024); W2RAW(6, wb2 + 16384, 2048); W2RAW(7, wb2 + 16384, 3072);
    }
  }
  if (!g1) {
    const u32 xv_ = (64 * ax + 8 * w) * 1024 + l * 16;
#define XRAW(j, vo, off) asm volatile("global_load_dwordx4 %0, %1, %2 offset:" #off : "=v"(xr[j]) : "v"(vo), "s"(p.x) : "memory")
    XRAW(0, xv_, 0); XRAW(1, xv_, 1024); XRAW(2, xv_, 2048); XRAW(3, xv_, 3072);
    XRAW(4, xv_ + 4096, 0); XRAW(5, xv_ + 4096, 1024); XRAW(6, xv_ + 4096, 2048); XRAW(7, xv_ + 4096, 3072);
  }
  SCHED();

  const int m2 = xcd * 4 + (rk & 3), n2 = rk >> 2;
  const int uch = w >> 2, ui = tid & 255;
  u32 uo1 = ui < 255 ? ui * 16 + 16 : OOB;
  int xa[2], wa[2];
#pragma unroll
  for (int s = 0; s < 2; ++s) xa[s] = (64 * wt + l15) * 128 + (((4 * s + g) ^ ((l15 >> 1) & 7)) << 4);
  {
    const int q = l15 >> 2, pp = l & 3;
#pragma unroll
    for (int i = 0; i < 2; ++i) wa[i] = 16384 + (8 * g + q) * 256 + (((4 * wf + 2 * i + (pp >> 1)) ^ (4 * q + 2 * (g & 1))) << 4) + 8 * (pp & 1);
  }
  u32x4 sel;
#pragma unroll
  for (int e = 0; e < 4; ++e) sel[e] = l15 == 2 * e ? 0x00003f80u : (l15 == 2 * e + 1 ? 0x3f800000u : 0u);
  u32 a_cw = lds0 + 16384 * (1 + (w >> 2)) + (4 * g + (l15 >> 2)) * 256 + (((4 * (w & 3) + (l & 3)) ^ (4 * (l15 >> 2) + 2 * h)) << 4);
  u32 o_cq = (l < 32 && l15 >= 12) ? (l & 3) * 32 + g * 16 : OOB;
  u32 o_l0 = l == 0 ? 0 : OOB;
  u32 a_ua = lds0 + CV_U + (w & 1) * CV_UCH + (3 - (l15 & 3)) * CV_CS + 4088 - 8 * (l15 >> 2) - 32 * h + 16 * (g & 1) - 7 * 512 - 256 * (w >> 2) - 3 * 64;
  u32 a_ka = lds0 + CV_K + (w & 1) * 5120 + 1024 - 512 * ((w >> 1) & 1) + 32 * (l15 - h) + 16 * (g & 1) - 256 * (w >> 2) - 3 * 64;
  u32 a_zk = lds0 + CV_K + (w & 1) * 5120 + l * 16;
  u32 a_zu = lds0 + CV_U + (w & 1) * CV_UCH + ((l >> 1) & 3) * CV_CS + 4096 + (l & 1) * 16;
  u32 hfo = (l < 7 ? l : 0) * FS;
  u32x4 zero4 = {0u, 0u, 0u, 0u};
  PIN(a_ua); PIN(a_ka); PIN(a_zk); PIN(a_zu); PIN(hfo); PIN(zero4);

  u32 o_cs = g * 1024 + (l15 >> 3) * 512 + (32 * wf + 4 * (l15 & 7)) * 4;
  u32 a_cs = lds0 + M_CS + w * 256 + (4 * l15 + 2 * h + (g & 1)) * 4;
  PIN(a_cs);
  u32 a_cv = lds0 + M_CS + w * 256 + g * 16;

  PIN(sel); PIN(a_cw); PIN(o_cq); PIN(o_l0); PIN(o_cs); PIN(a_cv); PIN(uo1);
  int xa2[2], wa1[2], wa2[2];
#pragma unroll
  for (int e = 0; e < 2; ++e) {
    xa2[e] = xa[e] + 2 * G1_SLOT; wa1[e] = wa[e] + G1_SLOT; wa2[e] = wa[e] + 2 * G1_SLOT;
    PIN(xa[e]); PIN(xa2[e]); PIN(wa[e]); PIN(wa1[e]); PIN(wa2[e]);
  }
  SCHED();
  int lq = l, uq = tid & 255;
  PIN(lq); PIN(uq);
  u32 p0off;
  {
    const int e = min(9 * w + min(lq, 8), 67);
    const int m = e - 60;
    const int pb = e < 32 ? 224 + e : (e < 60 ? 28 * n2 + (e - 32) : (g1 ? ((m >> 1) * 2 + (xcd & 1)) + 8 * ((m & 1) + 4 * (rk >> 2)) : 224));
    p0off = pb * FS;
  }
  u32 ka_[8];
#pragma unroll
  for (int e = 0; e < 4; ++e) {
    const int l0 = 8 * uq + 2 * e;
    const u32 bits = pk2((float)l0, (float)(l0 + 1));
    const int i0 = uq < 32 ? l0 : (int)(bits & 0xffffu) - (0x4300 - 128);
    const int i1 = uq < 32 ? l0 + 1 : (int)(bits >> 16) - (0x4300 - 128);
    ka_[2 * e] = lds0 + M_KC + uch * 5632 + i0 * 4;
    ka_[2 * e + 1] = lds0 + M_KC + uch * 5632 + i1 * 4;
  }
  PIN(p0off);
#pragma unroll
  for (int e = 0; e < 8; ++e) PIN(ka_[e]);
  SCHED();
  if (g1 && w >= 4) {
#pragma unroll
    for (int i = 0; i < 6; ++i) { dmaw(IC<1>{}, i, w - 4); dmaw(IC<1>{}, i, w); }
    asm volatile("s_waitcnt vmcnt(12)" : "+v"(wr[0]), "+v"(wr[1]), "+v"(wr[2]), "+v"(wr[3]), "+v"(wr[4]), "+v"(wr[5]), "+v"(wr[6]), "+v"(wr[7]) :: "memory");
    PIN(pf);
    if (w < 6) {
#pragma unroll
      for (int c = 0; c < 4; ++c) {
        u32x4 o;
#pragma unroll
        for (int e = 0; e < 4; ++e) o[e] = __builtin_amdgcn_perm(wr[2 * e + 1][c >> 1], wr[2 * e][c >> 1], (c & 1) ? 0x07060302u : 0x05040100u);
        st128s(o, rs, l * 16 + c * 1024, WS_W2F + (((un2 * 56 + ukb) * 4) << 10));
      }
    }
  }
  asm volatile("s_waitcnt lgkmcnt(0)" : "+s"(tag), "+s"(p_ldt2), "+s"(dk) :: "memory");
  tag.x += 1u;

  if (!g1) {
    asm volatile("s_waitcnt vmcnt(0)" : "+v"(xr[0]), "+v"(xr[1]), "+v"(xr[2]), "+v"(xr[3]), "+v"(xr[4]), "+v"(xr[5]), "+v"(xr[6]), "+v"(xr[7]) :: "memory");
    PIN(pf);
    f32x2 v[8][4];
    float ps[8];
#pragma unroll
    for (int r = 0; r < 8; ++r) {
#pragma unroll
      for (int j = 0; j < 4; ++j) v[r][j] = up2(xr[r][j]);
      const f32x2 s2 = (v[r][0] + v[r][1]) + (v[r][2] + v[r][3]);
      ps[r] = s2.x + s2.y;
    }
    const float mean = rows8_sum(ps, l) * (1.f / D);
#pragma unroll
    for (int r = 0; r < 8; ++r) {
      const float mr = rdl(mean, 8 * r);
      f32x2 q2 = {0.f, 0.f};
#pragma unroll
      for (int j = 0; j < 4; ++j) { v[r][j] -= f32x2{mr, mr}; q2 = fma2(v[r][j], v[r][j], q2); }
      ps[r] = q2.x + q2.y;
    }
    const float var = rows8_sum(ps, l) * (1.f / D);
    const u32 ms = pk2(mean, __builtin_sqrtf(rb(rb(var) + 1e-5f)));
    if ((l & 7) == 0) st32s(ms, rs, (l >> 3) * 4, WS_ST + (64 * ax + 8 * w) * 4);
    const float riv = 1.f / bhi(ms), nbv = (mean - b2f(ms)) * riv;
#pragma unroll
    for (int r = 0; r < 8; ++r) {
      const float ri = rdl(riv, 8 * r), nb = rdl(nbv, 8 * r);
#pragma unroll
      for (int j = 0; j < 4; ++j) v[r][j] = fma2(v[r][j], f32x2{ri, ri}, f32x2{nb, nb});
    }
#pragma unroll
    for (int c = 0; c < 8; ++c) {
      u32x4 o;
#pragma unroll
      for (int e = 0; e < 4; ++e) o[e] = pk2(v[7 - 2 * e][c >> 1][c & 1], v[6 - 2 * e][c >> 1][c & 1]);
      *(LAS u32x4*)(lds + (8 * l + c) * 128 + (((7 - w) ^ (l & 7)) << 4)) = o;
    }
    LBAR();
    u32x4 tv[8];
#pragma unroll
    for (int it = 0; it < 8; ++it) tv[it] = lds128(lds + w * 8192 + it * 1024 + l * 16);
#pragma unroll
    for (int it = 0; it < 8; ++it)
      st128s(tv[it], rs, (8 * it + (l >> 3)) * 4096 + (((l & 7) ^ it) << 4), WS_XNR + w * (64 * 4096) + (L - 64 - 64 * ax) * 2);
    WAIT_V(0);
    LBAR();
    if (tid == 0) st64s(tag, rs, 0, FL_P0 + b * FS);
  }

  if (w < 4) {
    float dt = rb(__expf((w >> 1) ? bhi(p_ldt2) : b2f(p_ldt2)));
    PIN(dt);
    asm volatile("s_waitcnt vmcnt(0)" : "+v"(p_lar), "+v"(p_aim), "+v"(p_cre), "+v"(p_cim) :: "memory");
    STAMP(1);
    const float ar = -rb(__expf(b2f(p_lar)));
    const float lre2 = (ar * dt) * 1.44269504f, rev = (b2f(p_aim) * dt) * 0.15915494309f;
    u32 eb = wave_max_u32(max(p_cre & 0x7f80u, p_cim & 0x7f80u)) << 16;
    eb = eb < 0x00800000u ? 0x00800000u : (eb > 0x7e800000u ? 0x7e800000u : eb);
    const float sc = __builtin_bit_cast(float, eb) * (2.f * dt), isc = __builtin_bit_cast(float, 0x7f000000u - eb);
    f32x2 e[53], tmp[8];
    u32 hr[26], hi[26];
    const f32x2 w8 = cexp_p(lre2, rev, 8.f), w64 = cexp_p(lre2, rev, 64.f);
    const f32x2 z1 = cexp_p(lre2, -rev, 1.f), zc = cexp_p(lre2, -rev, 256.f);
    e[0] = f32x2{1.f, 0.f}; e[1] = w8; e[8] = w64; e[32] = f32x2{b2f(p_cre) * isc, -b2f(p_cim) * isc};
#define CM1(k, a, bb) tmp[k] = (a) * f32x2{(bb).x, (bb).x}; SCHED()
#define CM2(k, d, a, bn) d = fma2(f32x2{(a).y, (a).x}, bn, tmp[k]); SCHED()
#define CV(i) hr[i] = pkh(f32x2{e[2 * (i)].x, e[2 * (i) + 1].x}); hi[i] = pkh(f32x2{e[2 * (i)].y, e[2 * (i) + 1].y}); SCHED()
#define W4(a, v, i) *(LAS u32x4*)(size_t)(a) = u32x4{v[i], v[(i) + 1], v[(i) + 2], v[(i) + 3]}; SCHED()
    const f32x2 w8n = cneg(w8), w64n = cneg(w64), z1n = cneg(z1), zcn = cneg(zc);
    f32x2 z2, z4, zd, zq;
    SCHED();
    CM1(0, w8, w8); CM1(1, z1, z1); CM1(2, zc, zc); CM1(3, e[32], z1); CM1(4, e[32], zc); CM1(5, w8, w64); CM1(6, w64, w64);
    CM2(0, e[2], w8, w8n); CM2(1, z2, z1, z1n); CM2(2, zd, zc, zcn); CM2(3, e[33], e[32], z1n); CM2(4, e[40], e[32], zcn);
    CM2(5, e[9], w8, w64n); CM2(6, e[16], w64, w64n);
    const f32x2 e2n = cneg(e[2]), z2n = cneg(z2), zdn = cneg(zd);
    SCHED();
    CM1(0, e[2], w8); CM1(1, e[2], e[2]); CM1(2, z2, z2); CM1(3, zd, zd); CM1(4, e[32], z2); CM1(5, e[33], z2); CM1(6, e[32], zd); CM1(7, e[40], zd);
    CM2(0, e[3], e[2], w8n); CM2(1, e[4], e[2], e2n); CM2(2, z4, z2, z2n); CM2(3, zq, zd, zdn); CM2(4, e[34], e[32], z2n);
    CM2(5, e[35], e[33], z2n); CM2(6, e[44], e[32], zdn); CM2(7, e[46], e[40], zdn);
    const f32x2 e4n = cneg(e[4]), z4n = cneg(z4), zqn = cneg(zq);
    SCHED();
    CM1(0, e[1], e[4]); CM1(1, e[2], e[4]); CM1(2, e[3], e[4]); CM1(3, e[32], z4); CM1(4, e[33], z4); CM1(5, e[34], z4); CM1(6, e[35], z4); CM1(7, e[32], zq);
    CM2(0, e[5], e[1], e4n); CM2(1, e[6], e[2], e4n); CM2(2, e[7], e[3], e4n); CM2(3, e[36], e[32], z4n); CM2(4, e[37], e[33], z4n);
    CM2(5, e[38], e[34], z4n); CM2(6, e[39], e[35], z4n); CM2(7, e[48], e[32], zqn);
    CV(0); CV(1); CV(2); CV(3); W4(tv[0], hr, 0); CV(16); CV(17); W4(tv[4], hi, 0); CV(18); CV(19);
    CM1(0, e[34], zc); W4(tm[0], hr, 16); CM1(1, e[36], zc); CM1(2, e[38], zc); CM1(3, e[36], zd); CM1(4, e[40], zq); W4(tm[2], hi, 16);
    CM1(5, e[44], zq); CM1(6, e[46], zq); CM1(7, e[48], zq);
    CM2(0, e[41], e[34], zcn); CM2(1, e[42], e[36], zcn); CM2(2, e[43], e[38], zcn); CM2(3, e[45], e[36], zdn); CM2(4, e[49], e[40], zqn);
    CM2(5, e[50], e[44], zqn); CM2(6, e[51], e[46], zqn); CM2(7, e[52], e[48], zqn);
    CM1(0, e[42], zd); CM1(1, e[9], w64); CM1(2, e[2], w64); CM1(3, e[3], w64); CM1(4, e[4], w64); CM1(5, e[5], w64); CM1(6, e[6], w64); CM1(7, e[7], w64);
    CM2(0, e[47], e[42], zdn); CM2(1, e[17], e[9], w64n); CM2(2, e[10], e[2], w64n); CM2(3, e[11], e[3], w64n); CM2(4, e[12], e[4], w64n);
    CM2(5, e[13], e[5], w64n); CM2(6, e[14], e[6], w64n); CM2(7, e[15], e[7], w64n);
    CV(24); CV(25);
    *(LAS u32x4*)(size_t)tm2 = u32x4{hr[24], hr[25], hi[24], hi[25]};
    SCHED();
    CV(20); CV(21); CV(22); CV(23); W4(tm[1], hr, 20); CV(4); CV(5); W4(tm[3], hi, 20); CV(6); CV(7);
    CM1(0, e[16], w64); W4(tv[1], hr, 4); CM1(1, e[17], w64); CM1(2, e[10], w64); CM1(3, e[11], w64); CM1(4, e[12], w64); W4(tv[5], hi, 4);
    CM1(5, e[13], w64); CM1(6, e[14], w64); CM1(7, e[15], w64);
    CM2(0, e[24], e[16], w64n); CM2(1, e[25], e[17], w64n); CM2(2, e[18], e[10], w64n); CM2(3, e[19], e[11], w64n); CM2(4, e[20], e[12], w64n);
    CM2(5, e[21], e[13], w64n); CM2(6, e[22], e[14], w64n); CM2(7, e[23], e[15], w64n);
    u32x4 fm[8], fv[8];
    u32 ra0 = rm[0], ra1 = rm[1];
#define RM(s_) { const u32x2 lo = ldstr((LAS char*)(size_t)ra0), hi_ = ldstr((LAS char*)(size_t)ra1); fm[s_] = u32x4{lo.x, lo.y, hi_.x, hi_.y}; ra0 += rms; ra1 += rms; } SCHED()
    CM1(2, e[18], w64); RM(0); CM1(3, e[19], w64); CM1(4, e[20], w64); RM(1); CM1(5, e[21], w64); CM1(6, e[22], w64); RM(2); CM1(7, e[23], w64);
    CV(8); CV(9); RM(3); CV(10); CV(11); W4(tv[2], hr, 8);
    CM2(2, e[26], e[18], w64n); CM2(3, e[27], e[19], w64n); RM(4); CM2(4, e[28], e[20], w64n); W4(tv[6], hi, 8); CM2(5, e[29], e[21], w64n);
    CM2(6, e[30], e[22], w64n); RM(5); CM2(7, e[31], e[23], w64n);
    CV(12); RM(6); CV(13); CV(14); RM(7); CV(15); W4(tv[3], hr, 12); W4(tv[7], hi, 12);
#pragma unroll
    for (int s = 0; s < 8; ++s) {
      const u32x2 lo = ldstr((LAS char*)(size_t)rv[0] + s * 1024), hi_ = ldstr((LAS char*)(size_t)rv[1] + s * 1024);
      fv[s] = u32x4{lo.x, lo.y, hi_.x, hi_.y};
    }
    float k2048 = e[52].x;
    k2048 += dppz<0x111, 0xf>(k2048); k2048 += dppz<0x112, 0xf>(k2048); k2048 += dppz<0x114, 0xf>(k2048); k2048 += dppz<0x118, 0xf>(k2048);
    k2048 += dppz<0x142, 0xa>(k2048); k2048 += dppz<0x143, 0xc>(k2048);
    SCHED();
    STAMP(4);
    f32x16 kk;
#pragma unroll
    for (int r = 0; r < 16; ++r) kk[r] = 0.f;
#pragma unroll
    for (int s = 0; s < 8; ++s) kk = mfma32h(fm[s], fv[s], kk);
    LAS float* kc = (LAS float*)(lds + M_KC + w * 2816);
    *(LAS f32x4*)(kc + 8 * t + 4 * h) = f32x4{kk[0], kk[1], kk[2], kk[3]} * sc;
    *(LAS f32x2*)(kc + (h ? 384 + 2 * t : 256 + 4 * t)) = f32x2{kk[4], kk[5]} * sc;
    *(LAS f32x2*)(kc + (h ? 448 + 2 * t : 258 + 4 * t)) = f32x2{kk[6], kk[7]} * sc;
    LAS float* k3 = kc + (h ? 671 - t : 512 + t);
    k3[0] = (h ? k2048 : kk[8]) * sc;
    if (h == 0) { k3[32] = kk[9] * sc; k3[64] = kk[10] * sc; k3[96] = kk[11] * sc; }
  }
  STAMP(5);

  u32x4 wfr[28];
  const int w2off = WS_W2F + (((n2 * 56 + 7 * w) * 4) << 10);
  auto w2ld = [&](int i) { wfr[i] = ld128s(rs, l * 16 + (i & 3) * 1024, w2off + (i >> 2) * 4096); };
  f32x2 kx[8];
  u32 kpk[4];
  auto expand_rd = [&](int e) { LAS float* q = (LAS float*)(size_t)ka_[e]; kx[e] = f32x2{q[0], q[704]}; };
  auto expand_pk = [&](int e) {
    float a = kx[2 * e].x + kx[2 * e].y, c = kx[2 * e + 1].x + kx[2 * e + 1].y;
    PIN(a); PIN(c);
    kpk[e] = pk2(a, c);
    PIN(kpk[e]);
  };
  auto expand_wr = [&]() { *(LAS u32x4*)(lds + CV_K + uch * 5120 + 1024 + ui * 16) = u32x4{kpk[0], kpk[1], kpk[2], kpk[3]}; };
  u32x4 u0;
  u32x2 u1;
  auto load_u = [&](int k) {
    if (k == 0) u0 = ld128s(rs, ui * 16, WS_XNR + (2 * b + uch) * 4096);
    else u1 = ld64s(rs, uo1, WS_XNR + (2 * b + uch) * 4096);
  };
  auto copy_u = [&](int s) {
    const u32 dd[6] = {u0.x, u0.y, u0.z, u0.w, u1.x, u1.y};
    const int o = s >> 1;
    u32x4 v;
#pragma unroll
    for (int k = 0; k < 4; ++k) v[k] = (s & 1) ? __builtin_amdgcn_alignbit(dd[o + k + 1], dd[o + k], 16) : dd[o + k];
    *(LAS u32x4*)(lds + CV_U + uch * CV_UCH + s * CV_CS + ui * 16) = v;
  };
#define W2Q 97
#define NW2AV 15
  constexpr int NW2A = NW2AV;

  if (g1) {
    f32x4 acc[4][4];
#pragma unroll
    for (int i = 0; i < 4; ++i)
#pragma unroll
      for (int j = 0; j < 4; ++j) acc[i][j] = f32x4{0.f, 0.f, 0.f, 0.f};
    u32x4 fa[2][4], fb[2][4];
    auto rdA = [&](int st, int s, int i) {
      LAS char* q = lds + (st % 3 == 0 ? wa[i & 1] : st % 3 == 1 ? wa1[i & 1] : wa2[i & 1]) + (i >> 1) * 16384 + s * 8192;
      const u32x2 lo = ldstr(q), hi = ldstr(q + 1024);
      fa[s][i] = u32x4{lo.x, lo.y, hi.x, hi.y};
    };
    auto rdB = [&](int st, int s, int j) { fb[s][j] = lds128(lds + (st % 3 == 2 ? xa2[s] : xa[s] + (st % 3) * G1_SLOT) + j * 2048); };
    const int so_cs = (mt & 2) == 0 ? WS_CS + (ft * 8 + kt0) * 1024 + w * 128 : OOB;
    const int so_p0 = w == 0 ? FL_P0 + b * FS : OOB;

    if (w >= 4) __builtin_amdgcn_s_setprio(1);
    LBAR();
    STAMP(6);
    rdB(0, 0, 0); rdA(0, 0, 0); rdA(0, 0, 1); rdA(0, 0, 2); rdA(0, 0, 3); rdB(0, 0, 1); rdB(0, 0, 2); rdB(0, 0, 3);
    dmaw(IC<2>{}, 0, w); dmaw(IC<2>{}, 1, w); dmaw(IC<2>{}, 2, w);
    u32x2 fl;
    u32 msv[4];
    float nmv[4];
    f32x4 csp[2];
    u32x4 cw[4];
    f32x4 cs, cv[4];
    static_for<8>([&](auto st_) {
      constexpr int st = decltype(st_)::v;
#pragma unroll
      for (int s = 0; s < 2; ++s) {
        const int nst = s ? st + 1 : st, ns = s ^ 1;
        if (s == 1 && st < 7) {
          if (st == 1 || st == 3 || st == 4) WAIT_V(7);
          else if (st == 6) WAIT_V(11);
          else WAIT_V(6);
        }
        if (s == 1) {
          LBAR();
          if (st == 2) STAMP(18);
          if (st == 5) STAMP(21);
        }
        SCHED();
#pragma unroll
        for (int n = 0; n < 16; ++n) {
          acc[n & 3][n >> 2] = mfma16(fa[s][n & 3], fb[s][n >> 2], acc[n & 3][n >> 2]);
          SCHED();
          if (nst < 8) {
            if (n == 0) rdB(nst, ns, 3);
            if (n == 1) rdB(nst, ns, 2);
            if (n == 2) rdB(nst, ns, 1);
            if (n == 3) rdA(nst, ns, 3);
            if (n == 5) rdA(nst, ns, 2);
            if (n == 7) rdA(nst, ns, 1);
            if (n == 9) rdA(nst, ns, 0);
            if (n == 10) rdB(nst, ns, 0);
          }
          if (nst + 2 < 8) {
            const int pc = (s ? 0 : 3);
            if (n == 4 || n == 8 || n == 12) { if (s) dm0(IC<(st + 3) & 7>{}, pc + (n >> 2) - 1, w); else dm0(IC<(st + 2) & 7>{}, pc + (n >> 2) - 1, w); }
            if (n == 6 || n == 11 || n == 14) { const int k = n == 6 ? 0 : n == 11 ? 1 : 2; if (s) dgo(IC<(st + 3) & 7>{}, pc + k, w); else dgo(IC<(st + 2) & 7>{}, pc + k, w); }
          }
          SCHED();
          const int q = st * 32 + s * 16 + n;
          if (q == 4 || q == 8 || q == 12 || q == 13) cw[q == 13 ? 3 : (q >> 2) - 1] = lds128((LAS char*)(size_t)a_cw + (q == 13 ? 3 : (q >> 2) - 1) * 4096);
          if (q == 20) cs = mfma16(sel, cw[0], f32x4{0.f, 0.f, 0.f, 0.f});
          if (q == 24 || q == 28 || q == 29) cs = mfma16(sel, cw[q == 29 ? 3 : (q >> 2) - 5], cs);
          if (q == 36) {
#pragma unroll
            for (int e = 0; e < 4; ++e) { const float v = cs[e]; cs[e] = v + dppx<0x114>(v); }
            PIN(cs);
          }
          if (q == 40) {
#pragma unroll
            for (int e = 0; e < 4; ++e) { const float v = cs[e]; cs[e] = v + dppx<0x118>(v); }
            PIN(cs);
          }
          if (q == 45) st128s(__builtin_bit_cast(u32x4, cs), rs, o_cq, so_cs);
          if (q >= 52 && q < 84 && (q & 3) == 0) expand_rd((q - 52) >> 2);
          if (q >= 92 && q < 124 && (q & 7) == 4) { PIN(kx[(q - 92) >> 2]); expand_pk((q - 92) >> 3); }
          if (q == 200) expand_wr();
          if (q == 85) st64s(tag, rs, o_l0, so_p0);
          if (q == 143) asm volatile("global_load_dwordx2 %0, %1, %2 sc1" : "=v"(fl) : "v"(p0off), "s"(p.ws + FL_P0) : "memory");
          if (q == 173) {
            asm volatile("s_waitcnt vmcnt(5)" : "+v"(fl) :: "memory");
            if (!__all(fl.x == tag.x && fl.y == tag.y)) poll(rs, p0off, FL_P0, tag);
          }
          if (q == 176 || q == 178) load_u((q - 176) >> 1);
          if (q == 180 || q == 182) csp[(q - 180) >> 1] = __builtin_bit_cast(f32x4, ld128s(rs, o_cs, WS_CS + (ft * 8 + 2 * (q - 180)) * 1024));
          if (q == 184) msv[0] = ld32s(rs, l * 4, WS_ST + (mt * 128 + 64 * wt) * 4);
          if (q >= 187 && ((q - 187) & 3) == 0 && ((q - 187) >> 2) < NW2A) w2ld((q - 187) >> 2);
          if (q == 205 || q == 213 || q == 221 || q == 229) copy_u((q - 205) >> 3);
          if (q == 209) {
            PIN(csp[0]); PIN(csp[1]);
            csp[0] += csp[1];
            const auto r0 = __builtin_amdgcn_permlane32_swap(f2u(csp[0][0]), f2u(csp[0][2]), false, false);
            const auto r1 = __builtin_amdgcn_permlane32_swap(f2u(csp[0][1]), f2u(csp[0][3]), false, false);
            csp[0][0] = u2f(r0[0]) + u2f(r0[1]);
            csp[0][1] = u2f(r1[0]) + u2f(r1[1]);
            PIN(csp[0]);
          }
          if (q == 217) {
            const auto r = __builtin_amdgcn_permlane16_swap(f2u(csp[0][0]), f2u(csp[0][1]), false, false);
            *(LAS float*)(size_t)a_cs = u2f(r[0]) + u2f(r[1]);
          }
          if (q == 220) {
            PIN(msv[0]);
            const auto m = __builtin_amdgcn_permlane32_swap(msv[0], msv[0], false, false);
            msv[0] = m[0]; msv[2] = m[1];
            PIN(msv[0]); PIN(msv[2]);
          }
          if (q == 225 || q == 228) {
            const int j = q == 225 ? 0 : 2;
            const auto m = __builtin_amdgcn_permlane16_swap(msv[j], msv[j], false, false);
            msv[j] = m[0]; msv[j + 1] = m[1];
            PIN(msv[j]); PIN(msv[j + 1]);
          }
          if (q >= 232 && q < 236) { cv[q - 232] = *(LAS f32x4*)((LAS char*)(size_t)a_cv + (q - 232) * 64); }
          if (q >= 236 && q < 240) { nmv[q - 236] = -b2f(msv[q - 236]); PIN(nmv[q - 236]); }
          if (q >= 240) {
            const int m = (n + 8) & 15, ii = m & 3, jj = m >> 2;
#pragma unroll
            for (int r = 0; r < 4; ++r) {
              float v = __builtin_fmaf(nmv[jj], cv[ii][r], acc[ii][jj][r]);
              PIN(v);
              acc[ii][jj][r] = v;
            }
          }
          SCHED();
        }
      }
    });
    STAMP(7);
    __builtin_amdgcn_s_setprio(3);
#pragma unroll
    for (int j = 0; j < 4; ++j) {
      const float sd = bhi(msv[j]), sd2 = sd * sd;
      const float ce = -1.44269504f * __builtin_amdgcn_rcpf(sd);
      u32x4 o;
#pragma unroll
      for (int r = 0; r < 4; ++r) {
        const f32x2 gg = {acc[r >> 1][j][2 * (r & 1)], acc[r >> 1][j][2 * (r & 1) + 1]};
        const f32x2 uu = {acc[2 + (r >> 1)][j][2 * (r & 1)], acc[2 + (r >> 1)][j][2 * (r & 1) + 1]};
        const f32x2 a = gg * f32x2{ce, ce};
        const f32x2 e = {__builtin_amdgcn_exp2f(a.x), __builtin_amdgcn_exp2f(a.y)};
        const f32x2 d = fma2(e, f32x2{sd2, sd2}, f32x2{sd2, sd2});
        const f32x2 q = {__builtin_amdgcn_rcpf(d.x), __builtin_amdgcn_rcpf(d.y)};
        o[r] = pk2v((gg * uu) * q);
      }
      st128s(o, rs, l * 16, WS_H + (((mt * 8 + 4 * wt + j) * 56 + ft * 4 + wf) << 10));
      SCHED();
    }
    __builtin_amdgcn_s_setprio(1);
    STAMP(8);
  } else {
    poll(rs, p0off, FL_P0, tag);
    if (b == 255 && w == 0) {
#pragma unroll 1
      for (int i = 0; i < 4; ++i) poll(rs, (64 * i + l) * FS, FL_P0, tag);
      if (l == 0) st64s(tag, rs, 0, FL_EP);
    }
    LBAR();
    load_u(0); load_u(1);
#pragma unroll
    for (int e = 0; e < 8; ++e) expand_rd(e);
#pragma unroll
    for (int e = 0; e < 4; ++e) expand_pk(e);
    LBAR();
    expand_wr();
#pragma unroll
    for (int s = 0; s < 4; ++s) copy_u(s);
    SCHED();
#pragma unroll
    for (int i = 0; i < NW2A; ++i) w2ld(i);
    __builtin_amdgcn_s_waitcnt(0x0F70);
    LBAR();
  }

  STAMP(10);
  u32x2 hfl;
  u32x4 hf[28];
  const int hoff = WS_H + ((4 * m2 * 56 + 7 * w) << 10);
  const int jr = 0;
  auto hld = [&](int n) { hf[n] = ld128s(rs, l * 16, hoff + (((n / 7) ^ jr) * 56 + n % 7) * 1024); };
  const int hfs = FL_H + (m2 * 56 + 7 * w) * FS;
#define PDV 12
  constexpr int PD = PDV;
  {
    LAS char *ua = (LAS char*)(size_t)a_ua, *ka = (LAS char*)(size_t)a_ka;
    *(LAS u32x4*)(size_t)a_zk = zero4;
    *(LAS u32x4*)(size_t)a_zu = zero4;
    asm volatile("" ::: "memory");
    f32x4 acc[4];
#pragma unroll
    for (int i = 0; i < 4; ++i) acc[i] = f32x4{0.f, 0.f, 0.f, 0.f};
    const int so_h = g1 ? FL_H + ((mt * 2 + wt) * 56 + ft * 4 + wf) * FS : OOB;
    auto hook = [&](int rr, int c) {
#define HPUB 3
#define HPOLL 24
      const int sq = rr * 8 + c;
#define WS0 4
#define WSN 13
      auto nw = [&](int z) { return z <= WS0 ? 0 : z >= WS0 + WSN ? 28 - NW2A : ((z - WS0) * (28 - NW2A) + WSN - 1) / WSN; };
      if (sq == HPUB) {
        constexpr int c0 = HPUB <= WS0 ? 0 : HPUB >= WS0 + WSN ? 28 - NW2A : ((HPUB - WS0) * (28 - NW2A) + WSN - 1) / WSN;
        asm volatile("s_waitcnt vmcnt(%0)" :: "i"(c0) : "memory");
        st64s(tag, rs, o_l0, so_h);
        __builtin_amdgcn_s_setprio(0);
        STAMP(9);
      }
      if (sq >= 0 && nw(sq + 1) > nw(sq)) w2ld(NW2A + nw(sq));
      if (sq == (HPOLL < 32 ? HPOLL - 5 : 31)) hfl = ld64s(rs, hfo, hfs);
      if (sq == HPOLL) {
        STAMP(2);
        if (!__all(hfl.x == tag.x && hfl.y == tag.y)) poll(rs, hfo, hfs, tag);
        asm volatile("" ::: "memory");
        STAMP(23);
      }
      if (sq >= HPOLL) {
#pragma unroll
        for (int n = (sq - HPOLL) * PD / (HPOLL < 32 ? 32 - HPOLL : 1); n < (sq + 1 - HPOLL) * PD / (HPOLL < 32 ? 32 - HPOLL : 1); ++n) hld(n);
      }
    };
    conv_tiles(ua, ka, acc, hook);
#pragma unroll
    for (int i = 0; i < 4; ++i) *(LAS f32x4*)(lds + CV_X + (w * 4 + i) * 1024 + l * 16) = acc[i];
    if (tid == 0) *(LAS u32*)(lds + M_CNT) = 0u;
  }
  LBAR();
  STAMP(11);
  int ytile;
  {
    const int ts = (w >> 1) & 1, i = (w & 1) + 2 * (w >> 2);
    ytile = 7 - 2 * i - ts;
    const int pos = 256 * ytile + 16 * l15 + 4 * g;
    f32x4 y0[2], y1[2];
    u32x2 ur[2];
#pragma unroll
    for (int ch = 0; ch < 2; ++ch) {
      y0[ch] = *(LAS f32x4*)(lds + CV_X + ((ch + 2 * ts) * 4 + i) * 1024 + l * 16);
      y1[ch] = *(LAS f32x4*)(lds + CV_X + ((ch + 2 * ts + 4) * 4 + i) * 1024 + l * 16);
      ur[ch] = lds64(lds + CV_U + ch * CV_UCH + (2044 - pos) * 2);
    }
    LBAR();
    float res[2][4];
#pragma unroll
    for (int ch = 0; ch < 2; ++ch) {
      const float dsk = ch ? bhi(dk) : b2f(dk);
      const f32x4 y = y0[ch] + y1[ch];
      res[ch][0] = __builtin_fmaf(bhi(ur[ch].y), dsk, y[0]); res[ch][1] = __builtin_fmaf(b2f(ur[ch].y), dsk, y[1]);
      res[ch][2] = __builtin_fmaf(bhi(ur[ch].x), dsk, y[2]); res[ch][3] = __builtin_fmaf(b2f(ur[ch].x), dsk, y[3]);
    }
    const u32x4 o = {pk2(res[0][0], res[1][0]), pk2(res[0][1], res[1][1]), pk2(res[0][2], res[1][2]), pk2(res[0][3], res[1][3])};
    st128s(o, rs, pos * 4, WS_Y + b * 8192);
    if (HPOLL == 32) {
      STAMP(2);
      if (!__all(hfl.x == tag.x && hfl.y == tag.y)) poll(rs, hfo, hfs, tag);
      asm volatile("" ::: "memory");
      STAMP(23);
#pragma unroll
      for (int n = 0; n < PD; ++n) hld(n);
    }
  }
  STAMP(12);

  {
    const int oj = w >> 1, rh = w & 1;
    const int etok = 64 * m2 + 16 * (oj ^ jr) + l15, ech = 64 * n2 + 16 * g + 8 * rh;
    u32x4 xv;
    u32 yo = (ech >> 1) * 8192 + etok * 4, yfv = (8 * g + (l & 3)) * FS;
    PIN(yo); PIN(yfv);
    SCHED();
    f32x4 acc[4][4];
#pragma unroll
    for (int i = 0; i < 4; ++i)
#pragma unroll
      for (int j = 0; j < 4; ++j) acc[i][j] = f32x4{0.f, 0.f, 0.f, 0.f};
    u32x2 yfl;
    const int yfo = FL_Y + ((m2 >> 2) * 256 + 32 * n2 + 4 * rh) * FS;
    LAS char* pw = opaque(lds + w * 16384 + l * 8);
    LAS char* pr = opaque(lds + w * 2048 + l * 8);
    LAS char* pr2 = opaque(pr + 65536);
    u32 a_cnt = lds0 + M_CNT + l * 4, one = 1u;
    PIN(a_cnt); PIN(one);
    auto part_wr = [&](int j, int k) {
      *(LAS f32x2*)(pw + (j * 8 + k) * 512) = f32x2{acc[k & 3][j][2 * (k >> 2)], acc[k & 3][j][2 * (k >> 2) + 1]};
    };
#pragma unroll
    for (int n = 0; n < 28; ++n) {
#pragma unroll
      for (int i = 0; i < 4; ++i) {
        acc[i][n / 7] = mfma16(wfr[4 * (n % 7) + i], hf[n], acc[i][n / 7]);
        SCHED();
        if (i == 3 && n + PD < 28) hld(n + PD);
        if ((i & 1) == 0 && n >= 8 && n % 7 >= 1 && n % 7 < 5) part_wr(n / 7 - 1, (n % 7 - 1) * 2 + (i >> 1));
        SCHED();
      }
      if (n == YP1 && w >= 4) { asm volatile("s_waitcnt vmcnt(%0)" :: "i"(YP1 + 1) : "memory"); st64s(tag, rs, o_l0, FL_Y + (ytile * 256 + b) * FS); }
      if (n == YP0 && w < 4) { asm volatile("s_waitcnt vmcnt(%0)" :: "i"(YP0 + 1) : "memory"); st64s(tag, rs, o_l0, FL_Y + (ytile * 256 + b) * FS); }
      if (n == 21) xv = *(const GAS u32x4*)((const char*)p.x + (size_t)etok * 1024 + ech * 2);
      if (n == 22) yfl = ld64s(rs, yfv, yfo);
      if (n == TB && w >= 4) LBAR();
      SCHED();
    }
    STAMP(13);
#pragma unroll
    for (int k = 0; k < 8; ++k) part_wr(3, k);
    asm volatile("" ::: "memory");
    if (w < 4) LBAR();
    else asm volatile("ds_add_u32 %0, %1" :: "v"(a_cnt), "v"(one) : "memory");
    if (!__all(yfl.x == tag.x && yfl.y == tag.y)) poll(rs, yfv, yfo, tag);
    asm volatile("" ::: "memory");
    u32 yv[4];
#pragma unroll
    for (int j = 0; j < 4; ++j) yv[j] = ld32s(rs, yo, WS_Y + j * 8192);
    f32x2 pp[8][4];
#pragma unroll
    for (int ww = 0; ww < 4; ++ww)
#pragma unroll
      for (int i = 0; i < 4; ++i) pp[ww][i] = *(volatile LAS f32x2*)(pr + ww * 16384 + i * 512);
    if (w >= 4) {
      volatile LAS u32* cnt = (volatile LAS u32*)(lds + M_CNT);
#pragma unroll 1
      for (int spin = 0; spin < (1 << 20) && *cnt < 4u; ++spin) {}
      asm volatile("" ::: "memory");
    }
    STAMP(14);
#pragma unroll
    for (int ww = 0; ww < 4; ++ww)
#pragma unroll
      for (int i = 0; i < 4; ++i) pp[4 + ww][i] = *(volatile LAS f32x2*)(pr2 + ww * 16384 + i * 512);
    SCHED();
    f32x2 sm[4];
#pragma unroll
    for (int i = 0; i < 4; ++i) sm[i] = ((pp[0][i] + pp[1][i]) + (pp[2][i] + pp[3][i])) + ((pp[4][i] + pp[5][i]) + (pp[6][i] + pp[7][i]));
    const u32 ff[4] = {pk2(sm[0].x, sm[1].x), pk2(sm[2].x, sm[3].x), pk2(sm[0].y, sm[1].y), pk2(sm[2].y, sm[3].y)};
    u32x4 r;
#pragma unroll
    for (int j = 0; j < 4; ++j) {
      const u32 s = pk2v(up2(xv[j]) + up2(yv[j]));
      r[j] = pk2v(up2(s) + up2(ff[j]));
    }
    *(GAS u32x4*)((char*)p.out + (size_t)etok * 1024 + ech * 2) = r;
  }
  STAMP(15);
  STAMP_DUMP();
}

extern "C" void kernel_launch(void* const* d_in, const int* in_sizes, int n_in, void* d_out, int out_size, void* d_ws,
                              size_t ws_size, hipStream_t stream) {
  Params p;
  p.x = (const u16*)d_in[0]; p.lar = (const u16*)d_in[1]; p.aim = (const u16*)d_in[2]; p.cre = (const u16*)d_in[3];
  p.cim = (const u16*)d_in[4]; p.ldt = (const u16*)d_in[5]; p.dsk = (const u16*)d_in[6];
  p.w1 = (const u16*)d_in[8]; p.w3 = (const u16*)d_in[9]; p.w2 = (const u16*)d_in[10];
  p.out = (u16*)d_out; p.ws = (char*)d_ws;
  fused<<<256, 512, 0, stream>>>(p);
}
```

```cpp
#include <hip/hip_runtime.h>
#include <hip/hip_bf16.h>

typedef unsigned short u16;
typedef unsigned int u32;
typedef unsigned long long u64;
typedef __bf16 bf16x8 __attribute__((ext_vector_type(8)));
typedef __bf16 bf16x2 __attribute__((ext_vector_type(2)));
typedef _Float16 f16x8 __attribute__((ext_vector_type(8)));
typedef _Float16 f16x2 __attribute__((ext_vector_type(2)));
typedef short s16x4 __attribute__((ext_vector_type(4)));
typedef float f32x2 __attribute__((ext_vector_type(2)));
typedef float f32x4 __attribute__((ext_vector_type(4)));
typedef float f32x16 __attribute__((ext_vector_type(16)));
typedef u32 u32x2 __attribute__((ext_vector_type(2)));
typedef u32 u32x4 __attribute__((ext_vector_type(4)));
typedef u64 u64x4 __attribute__((ext_vector_type(4)));
typedef u64 u64x8 __attribute__((ext_vector_type(8)));
typedef __amdgpu_buffer_rsrc_t rsrc_t;
#define LAS __attribute__((address_space(3)))
#define GAS __attribute__((address_space(1)))
#define DEV __device__ __forceinline__

#define NTQ " nt"
#define TB 21
#define YP1 1
#define YP0 10
constexpr int L = 2048, D = 512, NM = 128, F = 1792;
constexpr int LDS_BYTES = 163840;
constexpr int SC1 = 16;

constexpr int WS_H = 0;
constexpr int WS_XNR = 8 << 20;
constexpr int WS_W2F = 10 << 20;
constexpr int WS_Y = 12 << 20;
constexpr int WS_ST = 14 << 20;
constexpr int WS_CS = WS_ST + 8192;
constexpr int WS_FL = 15 << 20;
constexpr int FS = 128;
constexpr int FL_EP = WS_FL;
constexpr int FL_P0 = WS_FL + 4096;
constexpr int FL_H = FL_P0 + 256 * FS;
constexpr int FL_Y = FL_H + 2048 * FS;
constexpr int WS_BYTES = 16 << 20;

struct Params {
  const u16 *x, *lar, *aim, *cre, *cim, *ldt, *dsk, *w1, *w3, *w2;
  u16* out;
  char* ws;
};

template <int V> struct IC { static constexpr int v = V; };
template <int N, int I = 0, class Fn>
DEV void static_for(Fn&& fn) { if constexpr (I < N) { fn(IC<I>{}); static_for<N, I + 1>(fn); } }
DEV u32 f2u(float f) { return __builtin_bit_cast(u32, f); }
DEV float u2f(u32 v) { return __builtin_bit_cast(float, v); }
DEV float b2f(u32 v) { return __builtin_bit_cast(float, v << 16); }
DEV float bhi(u32 v) { return __builtin_bit_cast(float, v & 0xffff0000u); }
DEV u32 pk2v(f32x2 f) { return __builtin_bit_cast(u32, __builtin_convertvector(f, bf16x2)); }
DEV u32 pk2(float lo, float hi) { return pk2v(f32x2{lo, hi}); }
DEV f32x2 up2(u32 v) { return f32x2{b2f(v), bhi(v)}; }
DEV float rb(float f) { return b2f(pk2(f, f) & 0xffffu); }
DEV f32x2 fma2(f32x2 a, f32x2 b, f32x2 c) { return __builtin_elementwise_fma(a, b, c); }
DEV f32x16 mfma32(u32x4 a, u32x4 b, f32x16 c) {
  return __builtin_amdgcn_mfma_f32_32x32x16_bf16(__builtin_bit_cast(bf16x8, a), __builtin_bit_cast(bf16x8, b), c, 0, 0, 0);
}
DEV f32x4 mfma16(u32x4 a, u32x4 b, f32x4 c) {
  return __builtin_amdgcn_mfma_f32_16x16x32_bf16(__builtin_bit_cast(bf16x8, a), __builtin_bit_cast(bf16x8, b), c, 0, 0, 0);
}
DEV f32x16 mfma32h(u32x4 a, u32x4 b, f32x16 c) {
  return __builtin_amdgcn_mfma_f32_32x32x16_f16(__builtin_bit_cast(f16x8, a), __builtin_bit_cast(f16x8, b), c, 0, 0, 0);
}
DEV u32x4 lds128(LAS char* p) { return *(LAS u32x4*)p; }
DEV u32x2 lds64(LAS char* p) { return *(LAS u32x2*)p; }
DEV u32x2 ldstr(LAS char* p) { return __builtin_bit_cast(u32x2, __builtin_amdgcn_ds_read_tr16_b64_v4i16((LAS s16x4*)p)); }
template <int LOFF>
DEV void dma_m0(u32 lbase) { asm volatile("s_add_u32 m0, %0, %1" :: "s"(lbase), "i"(LOFF) : "memory", "scc", "m0"); }
template <bool NT>
DEV void dma_go(u32x4 rs, u32 voff, u32 soff) {
  if (NT) asm volatile("buffer_load_dwordx4 %0, %1, %2 offen nt lds" :: "v"(voff), "s"(rs), "s"(soff) : "memory");
  else asm volatile("buffer_load_dwordx4 %0, %1, %2 offen lds" :: "v"(voff), "s"(rs), "s"(soff) : "memory");
}
DEV u32x4 mkrs(const void* ptr, u32 bytes) { const u64 a = (u64)ptr; return u32x4{(u32)a, (u32)(a >> 32) & 0xffffu, bytes, 0x00020000u}; }
#define WAIT_V(n) asm volatile("s_waitcnt vmcnt(" #n ")" ::: "memory")
#define WAIT_L0() asm volatile("s_waitcnt lgkmcnt(0)" ::: "memory")
#define BAR() __builtin_amdgcn_s_barrier()
#define LBAR() do { WAIT_L0(); BAR(); asm volatile("" ::: "memory"); } while (0)
#define SCHED() __builtin_amdgcn_sched_barrier(0)
#define PIN(x) asm volatile("" : "+v"(x))
constexpr int OOB = 0x04000000;
#define STAMP(i)
#define STAMP_DECL
#define STAMP_DUMP()

DEV u32x4 ld128s(rsrc_t rs, int voff, int soff) { return __builtin_amdgcn_raw_buffer_load_b128(rs, voff, soff, SC1); }
DEV u32x2 ld64s(rsrc_t rs, int voff, int soff) { return __builtin_amdgcn_raw_buffer_load_b64(rs, voff, soff, SC1); }
DEV u32 ld32s(rsrc_t rs, int voff, int soff) { return __builtin_amdgcn_raw_buffer_load_b32(rs, voff, soff, SC1); }
DEV void st128s(u32x4 v, rsrc_t rs, int voff, int soff) {
  __builtin_amdgcn_raw_buffer_store_b128(v, rs, voff, soff, SC1);
  asm volatile("s_nop 1" :: "v"(v) : "memory");
}
DEV void st64s(u32x2 v, rsrc_t rs, int voff, int soff) { __builtin_amdgcn_raw_buffer_store_b64(v, rs, voff, soff, SC1); }
DEV void st32s(u32 v, rsrc_t rs, int voff, int soff) { __builtin_amdgcn_raw_buffer_store_b32(v, rs, voff, soff, SC1); }
DEV void poll(rsrc_t rs, int voff, int soff, u32x2 tag) {
#pragma unroll 1
  for (int spin = 0; spin < (1 << 18); ++spin) {
    const u32x2 v = ld64s(rs, voff, soff);
    if (__all(v.x == tag.x && v.y == tag.y)) break;
    __builtin_amdgcn_s_sleep(4);
  }
  __builtin_amdgcn_fence(__ATOMIC_ACQUIRE, "wavefront");
  asm volatile("" ::: "memory");
}

template <int CTRL, int RM>
DEV float dppz(float v) { return __builtin_bit_cast(float, __builtin_amdgcn_update_dpp(0, __builtin_bit_cast(int, v), CTRL, RM, 0xf, true)); }
template <int CTRL, int RM>
DEV u32 dppu(u32 v) { return (u32)__builtin_amdgcn_update_dpp(0, (int)v, CTRL, RM, 0xf, true); }
DEV u32 wave_max_u32(u32 v) {
  v = max(v, dppu<0x111, 0xf>(v)); v = max(v, dppu<0x112, 0xf>(v)); v = max(v, dppu<0x114, 0xf>(v)); v = max(v, dppu<0x118, 0xf>(v));
  v = max(v, dppu<0x142, 0xa>(v)); v = max(v, dppu<0x143, 0xc>(v));
  return (u32)__builtin_amdgcn_readlane((int)v, 63);
}

DEV float rdl(float v, int lane) { return __builtin_bit_cast(float, __builtin_amdgcn_readlane(__builtin_bit_cast(int, v), lane)); }
template <int CTRL>
DEV float dppx(float v) { return __builtin_bit_cast(float, __builtin_amdgcn_mov_dpp(__builtin_bit_cast(int, v), CTRL, 0xf, 0xf, true)); }
DEV float rows8_sum(const float (&p)[8], int l) {
  float a[4], c[2];
#pragma unroll
  for (int i = 0; i < 4; ++i) {
    const auto r = __builtin_amdgcn_permlane32_swap(__builtin_bit_cast(u32, p[i]), __builtin_bit_cast(u32, p[i + 4]), false, false);
    a[i] = __builtin_bit_cast(float, (u32)r[0]) + __builtin_bit_cast(float, (u32)r[1]);
  }
#pragma unroll
  for (int i = 0; i < 2; ++i) {
    const auto r = __builtin_amdgcn_permlane16_swap(__builtin_bit_cast(u32, a[i]), __builtin_bit_cast(u32, a[i + 2]), false, false);
    c[i] = __builtin_bit_cast(float, (u32)r[0]) + __builtin_bit_cast(float, (u32)r[1]);
  }
  const bool up = l & 8;
  float v = (up ? c[1] : c[0]) + dppx<0x128>(up ? c[0] : c[1]);
  v += dppx<0xB1>(v); v += dppx<0x4E>(v); v += dppx<0x141>(v);
  return v;
}

constexpr int G1_SLOT = 49152;
constexpr int KG_T = 2 * G1_SLOT;
constexpr int KG_TW = 13312;
constexpr int SPARE = 3 * G1_SLOT;
constexpr int M_CS = SPARE;
constexpr int M_CNT = SPARE + 4096;
constexpr int M_KC = SPARE + 5120;
constexpr int CV_K = SPARE + 5120;
static_assert(KG_T + 4 * KG_TW <= M_KC && M_KC + 4 * 2816 <= LDS_BYTES, "lds");
constexpr int CV_CS = 4160;
constexpr int CV_U = 2 * G1_SLOT;
constexpr int CV_UCH = 4 * CV_CS;
constexpr int CV_X = 0;

DEV f32x2 cmul(f32x2 a, f32x2 b, f32x2 bn) { return fma2(f32x2{a.y, a.x}, bn, a * f32x2{b.x, b.x}); }
DEV f32x2 cneg(f32x2 b) { return f32x2{-b.y, b.y}; }
DEV f32x2 cexp_p(float lre2, float rev, float pw) {
  const float mg = __builtin_amdgcn_exp2f(lre2 * pw);
  const float ph = __builtin_amdgcn_fractf(rev * pw);
  return f32x2{__builtin_amdgcn_cosf(ph), __builtin_amdgcn_sinf(ph)} * f32x2{mg, mg};
}
DEV u32 pkh(f32x2 f) { return __builtin_bit_cast(u32, __builtin_convertvector(f, f16x2)); }
DEV LAS char* opaque(LAS char* q) { u32 v = (u32)(size_t)q; asm volatile("" : "+v"(v)); return (LAS char*)(size_t)v; }

template <class Hook>
DEV void conv_tiles(LAS char* ua, LAS char* ka, f32x4 (&acc)[4], Hook hook) {
  constexpr int ORD[16] = {0, 15, 13, 11, 9, 1, 14, 12, 10, 8, 2, 3, 4, 5, 6, 7};
  u32x4 fr[2][16];
  auto load = [&](int rr, int id) {
    if (id < 8) {
      const u32x2 lo = *(volatile LAS u32x2*)(ua + (7 - id) * 512 + (3 - rr) * 64), hi = *(volatile LAS u32x2*)(ua + (7 - id) * 512 + (3 - rr) * 64 + 8);
      fr[rr & 1][id] = u32x4{lo.x, lo.y, hi.x, hi.y};
    } else {
      fr[rr & 1][id] = lds128(ka + (id - 8) * 512 + (3 - rr) * 64);
    }
  };
#pragma unroll
  for (int k = 0; k < 16; ++k) { load(0, ORD[k]); SCHED(); }
  hook(0, -1);
  SCHED();
#pragma unroll
  for (int rr = 0; rr < 4; ++rr) {
#pragma unroll
    for (int c = 0; c < 8; ++c) {
      u32x4(&f)[16] = fr[rr & 1];
#pragma unroll
      for (int i = 0; i < 4; ++i) {
        if (c <= 7 - 2 * i) { acc[i] = mfma16(f[c], f[8 + 7 - 2 * i - c], acc[i]); SCHED(); }
        if (rr < 3 && i < 2) { load(rr + 1, ORD[2 * c + i]); SCHED(); }
      }
      hook(rr, c);
      SCHED();
    }
  }
}

__global__ __launch_bounds__(512, 2) void fused(Params args) {
  __shared__ __attribute__((aligned(16))) char smem[LDS_BYTES];
  u64x8 ka0;
  u64x4 ka1;
  u64 mta;
  asm volatile("s_memrealtime %0" : "=s"(mta) :: "memory");
  asm volatile("s_load_dwordx16 %0, %2, 0x0\n\ts_load_dwordx8 %1, %2, 0x40" : "=&s"(ka0), "=&s"(ka1) : "s"(__builtin_amdgcn_kernarg_segment_ptr()) : "memory");
#define CODEPF 23000
  u32x4 pf;
  if (__builtin_amdgcn_readfirstlane(threadIdx.x >> 6) == 4) {
    u64 pc;
    asm volatile("s_getpc_b64 %0" : "=s"(pc));
    const u32 po = min((u32)((blockIdx.x >> 3) * 1024 + (threadIdx.x & 63) * 16), (u32)(CODEPF - 16));
    asm volatile("global_load_dwordx4 %0, %1, %2" : "=v"(pf) : "v"(po), "s"(pc & ~(u64)255) : "memory");
  }
  LAS char* lds = (LAS char*)smem;
  const u32 lds0 = (u32)(size_t)lds;
  const int tid = threadIdx.x, l = tid & 63;
  const int w = __builtin_amdgcn_readfirstlane(tid >> 6);
  const int b = blockIdx.x, xcd = b & 7, rk = b >> 3;
  const bool g1 = rk < 28;
  const int l15 = l & 15, g = l >> 4, t = l & 31, h = l >> 5;
  const int mt = (xcd >> 1) * 4 + (rk & 3), ft = (xcd & 1) * 7 + (rk >> 2);
#define ROTV 5
  const int kt0 = ROTV == 0 ? 0 : ROTV == 1 ? (ft + 2 * (mt & 3) + ((mt >> 2) & 1)) & 7 : ROTV == 2 ? (2 * (xcd >> 1) + 4 * (xcd & 1)) & 7 : (2 * (xcd >> 1) + 4 * (xcd & 1) + (mt & 1)) & 7;
  const int wt = w & 1, wf = w >> 1;
  u32 xo0 = (l >> 3) * 1024 + (((l & 7) ^ g) << 4);
  u32 xo1 = (8 + (l >> 3)) * 1024 + (((l & 7) ^ (4 + g)) << 4);
  u32 wo1 = g * (F * 2) + ((l15 ^ (4 * g + 2 * (w & 1))) << 4), wo1b = wo1 + 4 * F * 2;
  PIN(xo0); PIN(xo1); PIN(wo1); PIN(wo1b);
  u32 tv[8], tm[4], tm2, rv[2], rm[2];
  {
    const u32 tw = lds0 + KG_T + (w & 3) * KG_TW;
#pragma unroll
    for (int k = 0; k < 8; ++k) { tv[k] = tw + l * 128 + ((k ^ (l & 7)) << 4); PIN(tv[k]); }
#pragma unroll
    for (int k = 0; k < 4; ++k) { tm[k] = tw + 8192 + l * 64 + ((k ^ ((l >> 1) & 3)) << 4); PIN(tm[k]); }
    tm2 = tw + 12288 + l * 16;
    PIN(tm2);
    const int blk = g & 1, c = (l >> 2) & 1, pp = l & 3;
#pragma unroll
    for (int t2 = 0; t2 < 2; ++t2) {
      const int m = 4 * h + 2 * t2 + ((l >> 3) & 1);
      rv[t2] = tw + m * 128 + (((4 * c + 2 * blk + (pp >> 1)) ^ m) << 4) + 8 * (pp & 1);
      rm[t2] = blk ? tw + 12288 + m * 16 + 8 * c : tw + 8192 + m * 64 + (((2 * c + (pp >> 1)) ^ (m >> 1)) << 4) + 8 * (pp & 1);
      PIN(rv[t2]); PIN(rm[t2]);
    }
  }
  u32 rms = (g & 1) ? 128 : 512;
  PIN(rms);
  asm volatile("s_waitcnt lgkmcnt(0)" : "+s"(ka0), "+s"(ka1) :: "memory");
  Params p;
  p.x = (const u16*)ka0[0]; p.lar = (const u16*)ka0[1]; p.aim = (const u16*)ka0[2]; p.cre = (const u16*)ka0[3]; p.cim = (const u16*)ka0[4];
  p.ldt = (const u16*)ka0[5]; p.dsk = (const u16*)ka0[6]; p.w1 = (const u16*)ka0[7];
  p.w3 = (const u16*)ka1[0]; p.w2 = (const u16*)ka1[1]; p.out = (u16*)ka1[2]; p.ws = (char*)ka1[3];
  const rsrc_t rs = __builtin_amdgcn_make_buffer_rsrc(p.ws, 0, WS_BYTES, 0x00020000);
  STAMP_DECL;
  asm volatile("" : "+s"(mta));
  u32 mt0 = (u32)mta, mtp = mt0;
  STAMP(3);

  u32x2 tag;
  u32 p_ldt2, dk;
  asm volatile("s_load_dwordx2 %0, %3, 0x0\n\ts_load_dword %1, %4, 0x0\n\ts_load_dword %2, %5, 0x0"
               : "=&s"(tag), "=&s"(p_ldt2), "=&s"(dk) : "s"(p.ws + FL_EP), "s"(p.ldt + 2 * b), "s"(p.dsk + 2 * b) : "memory");

  u32 p_lar, p_aim, p_cre, p_cim;
  if (w < 4) {
    const u32 po = (b * 256 + tid) * 2;
    asm volatile("global_load_ushort %0, %1, %2" NTQ : "=v"(p_lar) : "v"(po), "s"(p.lar) : "memory");
    asm volatile("global_load_ushort %0, %1, %2" NTQ : "=v"(p_aim) : "v"(po), "s"(p.aim) : "memory");
    asm volatile("global_load_ushort %0, %1, %2" NTQ : "=v"(p_cre) : "v"(po), "s"(p.cre) : "memory");
    asm volatile("global_load_ushort %0, %1, %2" NTQ : "=v"(p_cim) : "v"(po), "s"(p.cim) : "memory");
  }
  SCHED();

  const u32x4 rx = mkrs(p.x, L * D * 2), rw1 = mkrs(p.w1, D * F * 2), rw3 = mkrs(p.w3, D * F * 2);
#define NTS 0
  auto dm0 = [&](auto st_, int piece, int ww) {
    constexpr int S = (decltype(st_)::v % 3) * G1_SLOT;
    const u32 lb = lds0 + ww * 2048;
    if (piece == 0) dma_m0<S>(lb);
    if (piece == 1) dma_m0<S + 16384>(lb);
    if (piece == 2) dma_m0<S + 32768>(lb);
    if (piece == 3) dma_m0<S + 1024>(lb);
    if (piece == 4) dma_m0<S + 16384 + 1024>(lb);
    if (piece == 5) dma_m0<S + 32768 + 1024>(lb);
  };
  auto dgo = [&](auto st_, int piece, int ww) {
    constexpr bool nt = decltype(st_)::v < NTS;
    const int kt = (kt0 + decltype(st_)::v) & 7;
    const u32 sx = (mt * 128 + ww * 16) * 1024 + kt * 128, sw = (kt * 64 + ww * 8) * (F * 2) + ft * 256;
    if (piece == 0) dma_go<nt>(rx, xo0, sx);
    if (piece == 1) dma_go<nt>(rw1, wo1, sw);
    if (piece == 2) dma_go<nt>(rw3, wo1, sw);
    if (piece == 3) dma_go<nt>(rx, xo1, sx);
    if (piece == 4) dma_go<nt>(rw1, wo1b, sw);
    if (piece == 5) dma_go<nt>(rw3, wo1b, sw);
  };
  auto dmaw = [&](auto st_, int piece, int ww) { dm0(st_, piece, ww); asm volatile("s_nop 0"); dgo(st_, piece, ww); };

  u32x2 wr[8];
  u32x4 xr[8];
  const int un2 = b / 28, ukb = 2 * (b - 28 * un2) + (w & 1);
  const int ax = xcd * 4 + (rk - 28);
  if (w >= 4 && g1) {
#pragma unroll
    for (int i = 0; i < 6; ++i) { dmaw(IC<0>{}, i, w - 4); dmaw(IC<0>{}, i, w); }
    if (w < 6) {
      const char* wb2 = (const char*)p.w2 + (size_t)(32 * ukb) * 1024 + un2 * 128;
      const u32 wv = g * 4096 + l15 * 8;
#define W2RAW(j, base, off) asm volatile("global_load_dwordx2 %0, %1, %2 offset:" #off NTQ : "=v"(wr[j]) : "v"(wv), "s"(base) : "memory")
      W2RAW(0, wb2, 0); W2RAW(1, wb2, 1024); W2RAW(2, wb2, 2048); W2RAW(3, wb2, 3072);
      W2RAW(4, wb2 + 16384, 0); W2RAW(5, wb2 + 16384, 1024); W2RAW(6, wb2 + 16384, 2048); W2RAW(7, wb2 + 16384, 3072);
    }
  }
  if (!g1) {
    const u32 xv_ = (64 * ax + 8 * w) * 1024 + l * 16;
#define XRAW(j, vo, off) asm volatile("global_load_dwordx4 %0, %1, %2 offset:" #off : "=v"(xr[j]) : "v"(vo), "s"(p.x) : "memory")
    XRAW(0, xv_, 0); XRAW(1, xv_, 1024); XRAW(2, xv_, 2048); XRAW(3, xv_, 3072);
    XRAW(4, xv_ + 4096, 0); XRAW(5, xv_ + 4096, 1024); XRAW(6, xv_ + 4096, 2048); XRAW(7, xv_ + 4096, 3072);
  }
  SCHED();

  const int m2 = xcd * 4 + (rk & 3), n2 = rk >> 2;
  const int uch = w >> 2, ui = tid & 255;
  u32 uo1 = ui < 255 ? ui * 16 + 16 : OOB;
  int xa[2], wa[2];
#pragma unroll
  for (int s = 0; s < 2; ++s) xa[s] = (64 * wt + l15) * 128 + (((4 * s + g) ^ ((l15 >> 1) & 7)) << 4);
  {
    const int q = l15 >> 2, pp = l & 3;
#pragma unroll
    for (int i = 0; i < 2; ++i) wa[i] = 16384 + (8 * g + q) * 256 + (((4 * wf + 2 * i + (pp >> 1)) ^ (4 * q + 2 * (g & 1))) << 4) + 8 * (pp & 1);
  }
  u32x4 sel;
#pragma unroll
  for (int e = 0; e < 4; ++e) sel[e] = l15 == 2 * e ? 0x00003f80u : (l15 == 2 * e + 1 ? 0x3f800000u : 0u);
  u32 a_cw = lds0 + 16384 * (1 + (w >> 2)) + (4 * g + (l15 >> 2)) * 256 + (((4 * (w & 3) + (l & 3)) ^ (4 * (l15 >> 2) + 2 * h)) << 4);
  u32 o_cq = (l < 32 && l15 >= 12) ? (l & 3) * 32 + g * 16 : OOB;
  u32 o_l0 = l == 0 ? 0 : OOB;
  u32 a_ua = lds0 + CV_U + (w & 1) * CV_UCH + (3 - (l15 & 3)) * CV_CS + 4088 - 8 * (l15 >> 2) - 32 * h + 16 * (g & 1) - 7 * 512 - 256 * (w >> 2) - 3 * 64;
  u32 a_ka = lds0 + CV_K + (w & 1) * 5120 + 1024 - 512 * ((w >> 1) & 1) + 32 * (l15 - h) + 16 * (g & 1) - 256 * (w >> 2) - 3 * 64;
  u32 a_zk = lds0 + CV_K + (w & 1) * 5120 + l * 16;
  u32 a_zu = lds0 + CV_U + (w & 1) * CV_UCH + ((l >> 1) & 3) * CV_CS + 4096 + (l & 1) * 16;
  u32 hfo = (l < 7 ? l : 0) * FS;
  u32x4 zero4 = {0u, 0u, 0u, 0u};
  PIN(a_ua); PIN(a_ka); PIN(a_zk); PIN(a_zu); PIN(hfo); PIN(zero4);

  u32 o_cs = g * 1024 + (l15 >> 3) * 512 + (32 * wf + 4 * (l15 & 7)) * 4;
  u32 a_cs = lds0 + M_CS + w * 256 + (4 * l15 + 2 * h + (g & 1)) * 4;
  PIN(a_cs);
  u32 a_cv = lds0 + M_CS + w * 256 + g * 16;

  PIN(sel); PIN(a_cw); PIN(o_cq); PIN(o_l0); PIN(o_cs); PIN(a_cv); PIN(uo1);
  int xa2[2], wa1[2], wa2[2];
#pragma unroll
  for (int e = 0; e < 2; ++e) {
    xa2[e] = xa[e] + 2 * G1_SLOT; wa1[e] = wa[e] + G1_SLOT; wa2[e] = wa[e] + 2 * G1_SLOT;
    PIN(xa[e]); PIN(xa2[e]); PIN(wa[e]); PIN(wa1[e]); PIN(wa2[e]);
  }
  SCHED();
  int lq = l, uq = tid & 255;
  PIN(lq); PIN(uq);
  u32 p0off;
  {
    const int e = min(9 * w + min(lq, 8), 67);
    const int m = e - 60;
    const int pb = e < 32 ? 224 + e : (e < 60 ? 28 * n2 + (e - 32) : (g1 ? ((m >> 1) * 2 + (xcd & 1)) + 8 * ((m & 1) + 4 * (rk >> 2)) : 224));
    p0off = pb * FS;
  }
  u32 ka_[8];
#pragma unroll
  for (int e = 0; e < 4; ++e) {
    const int l0 = 8 * uq + 2 * e;
    const u32 bits = pk2((float)l0, (float)(l0 + 1));
    const int i0 = uq < 32 ? l0 : (int)(bits & 0xffffu) - (0x4300 - 128);
    const int i1 = uq < 32 ? l0 + 1 : (int)(bits >> 16) - (0x4300 - 128);
    ka_[2 * e] = lds0 + M_KC + uch * 5632 + i0 * 4;
    ka_[2 * e + 1] = lds0 + M_KC + uch * 5632 + i1 * 4;
  }
  PIN(p0off);
#pragma unroll
  for (int e = 0; e < 8; ++e) PIN(ka_[e]);
  SCHED();
  if (g1 && w >= 4) {
#pragma unroll
    for (int i = 0; i < 6; ++i) { dmaw(IC<1>{}, i, w - 4); dmaw(IC<1>{}, i, w); }
    asm volatile("s_waitcnt vmcnt(12)" : "+v"(wr[0]), "+v"(wr[1]), "+v"(wr[2]), "+v"(wr[3]), "+v"(wr[4]), "+v"(wr[5]), "+v"(wr[6]), "+v"(wr[7]) :: "memory");
    PIN(pf);
    if (w < 6) {
#pragma unroll
      for (int c = 0; c < 4; ++c) {
        u32x4 o;
#pragma unroll
        for (int e = 0; e < 4; ++e) o[e] = __builtin_amdgcn_perm(wr[2 * e + 1][c >> 1], wr[2 * e][c >> 1], (c & 1) ? 0x07060302u : 0x05040100u);
        st128s(o, rs, l * 16 + c * 1024, WS_W2F + (((un2 * 56 + ukb) * 4) << 10));
      }
    }
  }
  asm volatile("s_waitcnt lgkmcnt(0)" : "+s"(tag), "+s"(p_ldt2), "+s"(dk) :: "memory");
  tag.x += 1u;

  if (!g1) {
    asm volatile("s_waitcnt vmcnt(0)" : "+v"(xr[0]), "+v"(xr[1]), "+v"(xr[2]), "+v"(xr[3]), "+v"(xr[4]), "+v"(xr[5]), "+v"(xr[6]), "+v"(xr[7]) :: "memory");
    PIN(pf);
    f32x2 v[8][4];
    float ps[8];
#pragma unroll
    for (int r = 0; r < 8; ++r) {
#pragma unroll
      for (int j = 0; j < 4; ++j) v[r][j] = up2(xr[r][j]);
      const f32x2 s2 = (v[r][0] + v[r][1]) + (v[r][2] + v[r][3]);
      ps[r] = s2.x + s2.y;
    }
    const float mean = rows8_sum(ps, l) * (1.f / D);
#pragma unroll
    for (int r = 0; r < 8; ++r) {
      const float mr = rdl(mean, 8 * r);
      f32x2 q2 = {0.f, 0.f};
#pragma unroll
      for (int j = 0; j < 4; ++j) { v[r][j] -= f32x2{mr, mr}; q2 = fma2(v[r][j], v[r][j], q2); }
      ps[r] = q2.x + q2.y;
    }
    const float var = rows8_sum(ps, l) * (1.f / D);
    const u32 ms = pk2(mean, __builtin_sqrtf(rb(rb(var) + 1e-5f)));
    if ((l & 7) == 0) st32s(ms, rs, (l >> 3) * 4, WS_ST + (64 * ax + 8 * w) * 4);
    const float riv = 1.f / bhi(ms), nbv = (mean - b2f(ms)) * riv;
#pragma unroll
    for (int r = 0; r < 8; ++r) {
      const float ri = rdl(riv, 8 * r), nb = rdl(nbv, 8 * r);
#pragma unroll
      for (int j = 0; j < 4; ++j) v[r][j] = fma2(v[r][j], f32x2{ri, ri}, f32x2{nb, nb});
    }
#pragma unroll
    for (int c = 0; c < 8; ++c) {
      u32x4 o;
#pragma unroll
      for (int e = 0; e < 4; ++e) o[e] = pk2(v[7 - 2 * e][c >> 1][c & 1], v[6 - 2 * e][c >> 1][c & 1]);
      *(LAS u32x4*)(lds + (8 * l + c) * 128 + (((7 - w) ^ (l & 7)) << 4)) = o;
    }
    LBAR();
    u32x4 tv[8];
#pragma unroll
    for (int it = 0; it < 8; ++it) tv[it] = lds128(lds + w * 8192 + it * 1024 + l * 16);
#pragma unroll
    for (int it = 0; it < 8; ++it)
      st128s(tv[it], rs, (8 * it + (l >> 3)) * 4096 + (((l & 7) ^ it) << 4), WS_XNR + w * (64 * 4096) + (L - 64 - 64 * ax) * 2);
    WAIT_V(0);
    LBAR();
    if (tid == 0) st64s(tag, rs, 0, FL_P0 + b * FS);
  }

  if (w < 4) {
    float dt = rb(__expf((w >> 1) ? bhi(p_ldt2) : b2f(p_ldt2)));
    PIN(dt);
    asm volatile("s_waitcnt vmcnt(0)" : "+v"(p_lar), "+v"(p_aim), "+v"(p_cre), "+v"(p_cim) :: "memory");
    STAMP(1);
    const float ar = -rb(__expf(b2f(p_lar)));
    const float lre2 = (ar * dt) * 1.44269504f, rev = (b2f(p_aim) * dt) * 0.15915494309f;
    u32 eb = wave_max_u32(max(p_cre & 0x7f80u, p_cim & 0x7f80u)) << 16;
    eb = eb < 0x00800000u ? 0x00800000u : (eb > 0x7e800000u ? 0x7e800000u : eb);
    const float sc = __builtin_bit_cast(float, eb) * (2.f * dt), isc = __builtin_bit_cast(float, 0x7f000000u - eb);
    f32x2 e[53], tmp[8];
    u32 hr[26], hi[26];
    const f32x2 w8 = cexp_p(lre2, rev, 8.f), w64 = cexp_p(lre2, rev, 64.f);
    const f32x2 z1 = cexp_p(lre2, -rev, 1.f), zc = cexp_p(lre2, -rev, 256.f);
    e[0] = f32x2{1.f, 0.f}; e[1] = w8; e[8] = w64; e[32] = f32x2{b2f(p_cre) * isc, -b2f(p_cim) * isc};
#define CM1(k, a, bb) tmp[k] = (a) * f32x2{(bb).x, (bb).x}; SCHED()
#define CM2(k, d, a, bn) d = fma2(f32x2{(a).y, (a).x}, bn, tmp[k]); SCHED()
#define CV(i) hr[i] = pkh(f32x2{e[2 * (i)].x, e[2 * (i) + 1].x}); hi[i] = pkh(f32x2{e[2 * (i)].y, e[2 * (i) + 1].y}); SCHED()
#define W4(a, v, i) *(LAS u32x4*)(size_t)(a) = u32x4{v[i], v[(i) + 1], v[(i) + 2], v[(i) + 3]}; SCHED()
    const f32x2 w8n = cneg(w8), w64n = cneg(w64), z1n = cneg(z1), zcn = cneg(zc);
    f32x2 z2, z4, zd, zq;
    SCHED();
    CM1(0, w8, w8); CM1(1, z1, z1); CM1(2, zc, zc); CM1(3, e[32], z1); CM1(4, e[32], zc); CM1(5, w8, w64); CM1(6, w64, w64);
    CM2(0, e[2], w8, w8n); CM2(1, z2, z1, z1n); CM2(2, zd, zc, zcn); CM2(3, e[33], e[32], z1n); CM2(4, e[40], e[32], zcn);
    CM2(5, e[9], w8, w64n); CM2(6, e[16], w64, w64n);
    const f32x2 e2n = cneg(e[2]), z2n = cneg(z2), zdn = cneg(zd);
    SCHED();
    CM1(0, e[2], w8); CM1(1, e[2], e[2]); CM1(2, z2, z2); CM1(3, zd, zd); CM1(4, e[32], z2); CM1(5, e[33], z2); CM1(6, e[32], zd); CM1(7, e[40], zd);
    CM2(0, e[3], e[2], w8n); CM2(1, e[4], e[2], e2n); CM2(2, z4, z2, z2n); CM2(3, zq, zd, zdn); CM2(4, e[34], e[32], z2n);
    CM2(5, e[35], e[33], z2n); CM2(6, e[44], e[32], zdn); CM2(7, e[46], e[40], zdn);
    const f32x2 e4n = cneg(e[4]), z4n = cneg(z4), zqn = cneg(zq);
    SCHED();
    CM1(0, e[1], e[4]); CM1(1, e[2], e[4]); CM1(2, e[3], e[4]); CM1(3, e[32], z4); CM1(4, e[33], z4); CM1(5, e[34], z4); CM1(6, e[35], z4); CM1(7, e[32], zq);
    CM2(0, e[5], e[1], e4n); CM2(1, e[6], e[2], e4n); CM2(2, e[7], e[3], e4n); CM2(3, e[36], e[32], z4n); CM2(4, e[37], e[33], z4n);
    CM2(5, e[38], e[34], z4n); CM2(6, e[39], e[35], z4n); CM2(7, e[48], e[32], zqn);
    CV(0); CV(1); CV(2); CV(3); W4(tv[0], hr, 0); CV(16); CV(17); W4(tv[4], hi, 0); CV(18); CV(19);
    CM1(0, e[34], zc); W4(tm[0], hr, 16); CM1(1, e[36], zc); CM1(2, e[38], zc); CM1(3, e[36], zd); CM1(4, e[40], zq); W4(tm[2], hi, 16);
    CM1(5, e[44], zq); CM1(6, e[46], zq); CM1(7, e[48], zq);
    CM2(0, e[41], e[34], zcn); CM2(1, e[42], e[36], zcn); CM2(2, e[43], e[38], zcn); CM2(3, e[45], e[36], zdn); CM2(4, e[49], e[40], zqn);
    CM2(5, e[50], e[44], zqn); CM2(6, e[51], e[46], zqn); CM2(7, e[52], e[48], zqn);
    CM1(0, e[42], zd); CM1(1, e[9], w64); CM1(2, e[2], w64); CM1(3, e[3], w64); CM1(4, e[4], w64); CM1(5, e[5], w64); CM1(6, e[6], w64); CM1(7, e[7], w64);
    CM2(0, e[47], e[42], zdn); CM2(1, e[17], e[9], w64n); CM2(2, e[10], e[2], w64n); CM2(3, e[11], e[3], w64n); CM2(4, e[12], e[4], w64n);
    CM2(5, e[13], e[5], w64n); CM2(6, e[14], e[6], w64n); CM2(7, e[15], e[7], w64n);
    CV(24); CV(25);
    *(LAS u32x4*)(size_t)tm2 = u32x4{hr[24], hr[25], hi[24], hi[25]};
    SCHED();
    CV(20); CV(21); CV(22); CV(23); W4(tm[1], hr, 20); CV(4); CV(5); W4(tm[3], hi, 20); CV(6); CV(7);
    CM1(0, e[16], w64); W4(tv[1], hr, 4); CM1(1, e[17], w64); CM1(2, e[10], w64); CM1(3, e[11], w64); CM1(4, e[12], w64); W4(tv[5], hi, 4);
    CM1(5, e[13], w64); CM1(6, e[14], w64); CM1(7, e[15], w64);
    CM2(0, e[24], e[16], w64n); CM2(1, e[25], e[17], w64n); CM2(2, e[18], e[10], w64n); CM2(3, e[19], e[11], w64n); CM2(4, e[20], e[12], w64n);
    CM2(5, e[21], e[13], w64n); CM2(6, e[22], e[14], w64n); CM2(7, e[23], e[15], w64n);
    u32x4 fm[8], fv[8];
    u32 ra0 = rm[0], ra1 = rm[1];
#define RM(s_) { const u32x2 lo = ldstr((LAS char*)(size_t)ra0), hi_ = ldstr((LAS char*)(size_t)ra1); fm[s_] = u32x4{lo.x, lo.y, hi_.x, hi_.y}; ra0 += rms; ra1 += rms; } SCHED()
    CM1(2, e[18], w64); RM(0); CM1(3, e[19], w64); CM1(4, e[20], w64); RM(1); CM1(5, e[21], w64); CM1(6, e[22], w64); RM(2); CM1(7, e[23], w64);
    CV(8); CV(9); RM(3); CV(10); CV(11); W4(tv[2], hr, 8);
    CM2(2, e[26], e[18], w64n); CM2(3, e[27], e[19], w64n); RM(4); CM2(4, e[28], e[20], w64n); W4(tv[6], hi, 8); CM2(5, e[29], e[21], w64n);
    CM2(6, e[30], e[22], w64n); RM(5); CM2(7, e[31], e[23], w64n);
    CV(12); RM(6); CV(13); CV(14); RM(7); CV(15); W4(tv[3], hr, 12); W4(tv[7], hi, 12);
#pragma unroll
    for (int s = 0; s < 8; ++s) {
      const u32x2 lo = ldstr((LAS char*)(size_t)rv[0] + s * 1024), hi_ = ldstr((LAS char*)(size_t)rv[1] + s * 1024);
      fv[s] = u32x4{lo.x, lo.y, hi_.x, hi_.y};
    }
    float k2048 = e[52].x;
    k2048 += dppz<0x111, 0xf>(k2048); k2048 += dppz<0x112, 0xf>(k2048); k2048 += dppz<0x114, 0xf>(k2048); k2048 += dppz<0x118, 0xf>(k2048);
    k2048 += dppz<0x142, 0xa>(k2048); k2048 += dppz<0x143, 0xc>(k2048);
    SCHED();
    STAMP(4);
    f32x16 kk;
#pragma unroll
    for (int r = 0; r < 16; ++r) kk[r] = 0.f;
#pragma unroll
    for (int s = 0; s < 8; ++s) kk = mfma32h(fm[s], fv[s], kk);
    LAS float* kc = (LAS float*)(lds + M_KC + w * 2816);
    *(LAS f32x4*)(kc + 8 * t + 4 * h) = f32x4{kk[0], kk[1], kk[2], kk[3]} * sc;
    *(LAS f32x2*)(kc + (h ? 384 + 2 * t : 256 + 4 * t)) = f32x2{kk[4], kk[5]} * sc;
    *(LAS f32x2*)(kc + (h ? 448 + 2 * t : 258 + 4 * t)) = f32x2{kk[6], kk[7]} * sc;
    LAS float* k3 = kc + (h ? 671 - t : 512 + t);
    k3[0] = (h ? k2048 : kk[8]) * sc;
    if (h == 0) { k3[32] = kk[9] * sc; k3[64] = kk[10] * sc; k3[96] = kk[11] * sc; }
  }
  STAMP(5);

  u32x4 wfr[28];
  const int w2off = WS_W2F + (((n2 * 56 + 7 * w) * 4) << 10);
  auto w2ld = [&](int i) { wfr[i] = ld128s(rs, l * 16 + (i & 3) * 1024, w2off + (i >> 2) * 4096); };
  f32x2 kx[8];
  u32 kpk[4];
  auto expand_rd = [&](int e) { LAS float* q = (LAS float*)(size_t)ka_[e]; kx[e] = f32x2{q[0], q[704]}; };
  auto expand_pk = [&](int e) {
    float a = kx[2 * e].x + kx[2 * e].y, c = kx[2 * e + 1].x + kx[2 * e + 1].y;
    PIN(a); PIN(c);
    kpk[e] = pk2(a, c);
    PIN(kpk[e]);
  };
  auto expand_wr = [&]() { *(LAS u32x4*)(lds + CV_K + uch * 5120 + 1024 + ui * 16) = u32x4{kpk[0], kpk[1], kpk[2], kpk[3]}; };
  u32x4 u0;
  u32x2 u1;
  auto load_u = [&](int k) {
    if (k == 0) u0 = ld128s(rs, ui * 16, WS_XNR + (2 * b + uch) * 4096);
    else u1 = ld64s(rs, uo1, WS_XNR + (2 * b + uch) * 4096);
  };
  auto copy_u = [&](int s) {
    const u32 dd[6] = {u0.x, u0.y, u0.z, u0.w, u1.x, u1.y};
    const int o = s >> 1;
    u32x4 v;
#pragma unroll
    for (int k = 0; k < 4; ++k) v[k] = (s & 1) ? __builtin_amdgcn_alignbit(dd[o + k + 1], dd[o + k], 16) : dd[o + k];
    *(LAS u32x4*)(lds + CV_U + uch * CV_UCH + s * CV_CS + ui * 16) = v;
  };
#define W2Q 97
#define NW2AV 15
  constexpr int NW2A = NW2AV;

  if (g1) {
    f32x4 acc[4][4];
#pragma unroll
    for (int i = 0; i < 4; ++i)
#pragma unroll
      for (int j = 0; j < 4; ++j) acc[i][j] = f32x4{0.f, 0.f, 0.f, 0.f};
    u32x4 fa[2][4], fb[2][4];
    auto rdA = [&](int st, int s, int i) {
      LAS char* q = lds + (st % 3 == 0 ? wa[i & 1] : st % 3 == 1 ? wa1[i & 1] : wa2[i & 1]) + (i >> 1) * 16384 + s * 8192;
      const u32x2 lo = ldstr(q), hi = ldstr(q + 1024);
      fa[s][i] = u32x4{lo.x, lo.y, hi.x, hi.y};
    };
    auto rdB = [&](int st, int s, int j) { fb[s][j] = lds128(lds + (st % 3 == 2 ? xa2[s] : xa[s] + (st % 3) * G1_SLOT) + j * 2048); };
    const int so_cs = (mt & 2) == 0 ? WS_CS + (ft * 8 + kt0) * 1024 + w * 128 : OOB;
    const int so_p0 = w == 0 ? FL_P0 + b * FS : OOB;

    if (w >= 4) __builtin_amdgcn_s_setprio(1);
    LBAR();
    STAMP(6);
    rdB(0, 0, 0); rdA(0, 0, 0); rdA(0, 0, 1); rdA(0, 0, 2); rdA(0, 0, 3); rdB(0, 0, 1); rdB(0, 0, 2); rdB(0, 0, 3);
    dmaw(IC<2>{}, 0, w); dmaw(IC<2>{}, 1, w); dmaw(IC<2>{}, 2, w);
    u32x2 fl;
    u32 msv[4];
    float nmv[4];
    f32x4 csp[2];
    u32x4 cw[4];
    f32x4 cs, cv[4];
    static_for<8>([&](auto st_) {
      constexpr int st = decltype(st_)::v;
#pragma unroll
      for (int s = 0; s < 2; ++s) {
        const int nst = s ? st + 1 : st, ns = s ^ 1;
        if (s == 1 && st < 7) {
          if (st == 1 || st == 3 || st == 4) WAIT_V(7);
          else if (st == 6) WAIT_V(11);
          else WAIT_V(6);
        }
        if (s == 1) {
          LBAR();
          if (st == 2) STAMP(18);
          if (st == 5) STAMP(21);
        }
        SCHED();
#pragma unroll
        for (int n = 0; n < 16; ++n) {
          acc[n & 3][n >> 2] = mfma16(fa[s][n & 3], fb[s][n >> 2], acc[n & 3][n >> 2]);
          SCHED();
          if (nst < 8) {
            if (n == 0) rdB(nst, ns, 3);
            if (n == 1) rdB(nst, ns, 2);
            if (n == 2) rdB(nst, ns, 1);
            if (n == 3) rdA(nst, ns, 3);
            if (n == 5) rdA(nst, ns, 2);
            if (n == 7) rdA(nst, ns, 1);
            if (n == 9) rdA(nst, ns, 0);
            if (n == 10) rdB(nst, ns, 0);
          }
          if (nst + 2 < 8) {
            const int pc = (s ? 0 : 3);
            if (n == 4 || n == 8 || n == 12) { if (s) dm0(IC<(st + 3) & 7>{}, pc + (n >> 2) - 1, w); else dm0(IC<(st + 2) & 7>{}, pc + (n >> 2) - 1, w); }
            if (n == 6 || n == 11 || n == 14) { const int k = n == 6 ? 0 : n == 11 ? 1 : 2; if (s) dgo(IC<(st + 3) & 7>{}, pc + k, w); else dgo(IC<(st + 2) & 7>{}, pc + k, w); }
          }
          SCHED();
          const int q = st * 32 + s * 16 + n;
          if (q == 4 || q == 8 || q == 12 || q == 13) cw[q == 13 ? 3 : (q >> 2) - 1] = lds128((LAS char*)(size_t)a_cw + (q == 13 ? 3 : (q >> 2) - 1) * 4096);
          if (q == 20) cs = mfma16(sel, cw[0], f32x4{0.f, 0.f, 0.f, 0.f});
          if (q == 24 || q == 28 || q == 29) cs = mfma16(sel, cw[q == 29 ? 3 : (q >> 2) - 5], cs);
          if (q == 36) {
#pragma unroll
            for (int e = 0; e < 4; ++e) { const float v = cs[e]; cs[e] = v + dppx<0x114>(v); }
            PIN(cs);
          }
          if (q == 40) {
#pragma unroll
            for (int e = 0; e < 4; ++e) { const float v = cs[e]; cs[e] = v + dppx<0x118>(v); }
            PIN(cs);
          }
          if (q == 45) st128s(__builtin_bit_cast(u32x4, cs), rs, o_cq, so_cs);
          if (q >= 52 && q < 84 && (q & 3) == 0) expand_rd((q - 52) >> 2);
          if (q >= 92 && q < 124 && (q & 7) == 4) { PIN(kx[(q - 92) >> 2]); expand_pk((q - 92) >> 3); }
          if (q == 200) expand_wr();
          if (q == 85) st64s(tag, rs, o_l0, so_p0);
          if (q == 143) asm volatile("global_load_dwordx2 %0, %1, %2 sc1" : "=v"(fl) : "v"(p0off), "s"(p.ws + FL_P0) : "memory");
          if (q == 173) {
            asm volatile("s_waitcnt vmcnt(5)" : "+v"(fl) :: "memory");
            if (!__all(fl.x == tag.x && fl.y == tag.y)) poll(rs, p0off, FL_P0, tag);
          }
          if (q == 176 || q == 178) load_u((q - 176) >> 1);
          if (q == 180 || q == 182) csp[(q - 180) >> 1] = __builtin_bit_cast(f32x4, ld128s(rs, o_cs, WS_CS + (ft * 8 + 2 * (q - 180)) * 1024));
          if (q == 184) msv[0] = ld32s(rs, l * 4, WS_ST + (mt * 128 + 64 * wt) * 4);
          if (q >= 187 && ((q - 187) & 3) == 0 && ((q - 187) >> 2) < NW2A) w2ld((q - 187) >> 2);
          if (q == 205 || q == 213 || q == 221 || q == 229) copy_u((q - 205) >> 3);
          if (q == 209) {
            PIN(csp[0]); PIN(csp[1]);
            csp[0] += csp[1];
            const auto r0 = __builtin_amdgcn_permlane32_swap(f2u(csp[0][0]), f2u(csp[0][2]), false, false);
            const auto r1 = __builtin_amdgcn_permlane32_swap(f2u(csp[0][1]), f2u(csp[0][3]), false, false);
            csp[0][0] = u2f(r0[0]) + u2f(r0[1]);
            csp[0][1] = u2f(r1[0]) + u2f(r1[1]);
            PIN(csp[0]);
          }
          if (q == 217) {
            const auto r = __builtin_amdgcn_permlane16_swap(f2u(csp[0][0]), f2u(csp[0][1]), false, false);
            *(LAS float*)(size_t)a_cs = u2f(r[0]) + u2f(r[1]);
          }
          if (q == 220) {
            PIN(msv[0]);
            const auto m = __builtin_amdgcn_permlane32_swap(msv[0], msv[0], false, false);
            msv[0] = m[0]; msv[2] = m[1];
            PIN(msv[0]); PIN(msv[2]);
          }
          if (q == 225 || q == 228) {
            const int j = q == 225 ? 0 : 2;
            const auto m = __builtin_amdgcn_permlane16_swap(msv[j], msv[j], false, false);
            msv[j] = m[0]; msv[j + 1] = m[1];
            PIN(msv[j]); PIN(msv[j + 1]);
          }
          if (q >= 232 && q < 236) { cv[q - 232] = *(LAS f32x4*)((LAS char*)(size_t)a_cv + (q - 232) * 64); }
          if (q >= 236 && q < 240) { nmv[q - 236] = -b2f(msv[q - 236]); PIN(nmv[q - 236]); }
          if (q >= 240) {
            const int m = (n + 8) & 15, ii = m & 3, jj = m >> 2;
#pragma unroll
            for (int r = 0; r < 4; ++r) {
              float v = __builtin_fmaf(nmv[jj], cv[ii][r], acc[ii][jj][r]);
              PIN(v);
              acc[ii][jj][r] = v;
            }
          }
          SCHED();
        }
      }
    });
    STAMP(7);
    __builtin_amdgcn_s_setprio(3);
#pragma unroll
    for (int j = 0; j < 4; ++j) {
      const float sd = bhi(msv[j]), sd2 = sd * sd;
      const float ce = -1.44269504f * __builtin_amdgcn_rcpf(sd);
      u32x4 o;
#pragma unroll
      for (int r = 0; r < 4; ++r) {
        const f32x2 gg = {acc[r >> 1][j][2 * (r & 1)], acc[r >> 1][j][2 * (r & 1) + 1]};
        const f32x2 uu = {acc[2 + (r >> 1)][j][2 * (r & 1)], acc[2 + (r >> 1)][j][2 * (r & 1) + 1]};
        const f32x2 a = gg * f32x2{ce, ce};
        const f32x2 e = {__builtin_amdgcn_exp2f(a.x), __builtin_amdgcn_exp2f(a.y)};
        const f32x2 d = fma2(e, f32x2{sd2, sd2}, f32x2{sd2, sd2});
        const f32x2 q = {__builtin_amdgcn_rcpf(d.x), __builtin_amdgcn_rcpf(d.y)};
        o[r] = pk2v((gg * uu) * q);
      }
      st128s(o, rs, l * 16, WS_H + (((mt * 8 + 4 * wt + j) * 56 + ft * 4 + wf) << 10));
      SCHED();
    }
    __builtin_amdgcn_s_setprio(1);
    { SCHED(); mtp = (u32)__builtin_amdgcn_s_memrealtime(); SCHED(); }
  } else {
    poll(rs, p0off, FL_P0, tag);
    if (b == 255 && w == 0) {
#pragma unroll 1
      for (int i = 0; i < 4; ++i) poll(rs, (64 * i + l) * FS, FL_P0, tag);
      if (l == 0) st64s(tag, rs, 0, FL_EP);
    }
    LBAR();
    load_u(0); load_u(1);
#pragma unroll
    for (int e = 0; e < 8; ++e) expand_rd(e);
#pragma unroll
    for (int e = 0; e < 4; ++e) expand_pk(e);
    LBAR();
    expand_wr();
#pragma unroll
    for (int s = 0; s < 4; ++s) copy_u(s);
    SCHED();
#pragma unroll
    for (int i = 0; i < NW2A; ++i) w2ld(i);
    __builtin_amdgcn_s_waitcnt(0x0F70);
    LBAR();
  }

  STAMP(10);
  u32x2 hfl;
  u32x4 hf[28];
  const int hoff = WS_H + ((4 * m2 * 56 + 7 * w) << 10);
  const int jr = 0;
  auto hld = [&](int n) { hf[n] = ld128s(rs, l * 16, hoff + (((n / 7) ^ jr) * 56 + n % 7) * 1024); };
  const int hfs = FL_H + (m2 * 56 + 7 * w) * FS;
#define PDV 12
  constexpr int PD = PDV;
  {
    LAS char *ua = (LAS char*)(size_t)a_ua, *ka = (LAS char*)(size_t)a_ka;
    *(LAS u32x4*)(size_t)a_zk = zero4;
    *(LAS u32x4*)(size_t)a_zu = zero4;
    asm volatile("" ::: "memory");
    f32x4 acc[4];
#pragma unroll
    for (int i = 0; i < 4; ++i) acc[i] = f32x4{0.f, 0.f, 0.f, 0.f};
    const int so_h = g1 ? FL_H + ((mt * 2 + wt) * 56 + ft * 4 + wf) * FS : OOB;
    auto hook = [&](int rr, int c) {
#define HPUB 3
#define HPOLL 24
      const int sq = rr * 8 + c;
#define WS0 4
#define WSN 13
      auto nw = [&](int z) { return z <= WS0 ? 0 : z >= WS0 + WSN ? 28 - NW2A : ((z - WS0) * (28 - NW2A) + WSN - 1) / WSN; };
      if (sq == HPUB) {
        constexpr int c0 = HPUB <= WS0 ? 0 : HPUB >= WS0 + WSN ? 28 - NW2A : ((HPUB - WS0) * (28 - NW2A) + WSN - 1) / WSN;
        asm volatile("s_waitcnt vmcnt(%0)" :: "i"(c0) : "memory");
        st64s(tag, rs, o_l0, so_h);
        __builtin_amdgcn_s_setprio(0);
        STAMP(9);
      }
      if (sq >= 0 && nw(sq + 1) > nw(sq)) w2ld(NW2A + nw(sq));
      if (sq == (HPOLL < 32 ? HPOLL - 5 : 31)) hfl = ld64s(rs, hfo, hfs);
      if (sq == HPOLL) {
        STAMP(2);
        if (!__all(hfl.x == tag.x && hfl.y == tag.y)) poll(rs, hfo, hfs, tag);
        asm volatile("" ::: "memory");
        STAMP(23);
      }
      if (sq >= HPOLL) {
#pragma unroll
        for (int n = (sq - HPOLL) * PD / (HPOLL < 32 ? 32 - HPOLL : 1); n < (sq + 1 - HPOLL) * PD / (HPOLL < 32 ? 32 - HPOLL : 1); ++n) hld(n);
      }
    };
    conv_tiles(ua, ka, acc, hook);
#pragma unroll
    for (int i = 0; i < 4; ++i) *(LAS f32x4*)(lds + CV_X + (w * 4 + i) * 1024 + l * 16) = acc[i];
    if (tid == 0) *(LAS u32*)(lds + M_CNT) = 0u;
  }
  LBAR();
  STAMP(11);
  int ytile;
  {
    const int ts = (w >> 1) & 1, i = (w & 1) + 2 * (w >> 2);
    ytile = 7 - 2 * i - ts;
    const int pos = 256 * ytile + 16 * l15 + 4 * g;
    f32x4 y0[2], y1[2];
    u32x2 ur[2];
#pragma unroll
    for (int ch = 0; ch < 2; ++ch) {
      y0[ch] = *(LAS f32x4*)(lds + CV_X + ((ch + 2 * ts) * 4 + i) * 1024 + l * 16);
      y1[ch] = *(LAS f32x4*)(lds + CV_X + ((ch + 2 * ts + 4) * 4 + i) * 1024 + l * 16);
      ur[ch] = lds64(lds + CV_U + ch * CV_UCH + (2044 - pos) * 2);
    }
    LBAR();
    float res[2][4];
#pragma unroll
    for (int ch = 0; ch < 2; ++ch) {
      const float dsk = ch ? bhi(dk) : b2f(dk);
      const f32x4 y = y0[ch] + y1[ch];
      res[ch][0] = __builtin_fmaf(bhi(ur[ch].y), dsk, y[0]); res[ch][1] = __builtin_fmaf(b2f(ur[ch].y), dsk, y[1]);
      res[ch][2] = __builtin_fmaf(bhi(ur[ch].x), dsk, y[2]); res[ch][3] = __builtin_fmaf(b2f(ur[ch].x), dsk, y[3]);
    }
    const u32x4 o = {pk2(res[0][0], res[1][0]), pk2(res[0][1], res[1][1]), pk2(res[0][2], res[1][2]), pk2(res[0][3], res[1][3])};
    st128s(o, rs, pos * 4, WS_Y + b * 8192);
    if (HPOLL == 32) {
      STAMP(2);
      if (!__all(hfl.x == tag.x && hfl.y == tag.y)) poll(rs, hfo, hfs, tag);
      asm volatile("" ::: "memory");
      STAMP(23);
#pragma unroll
      for (int n = 0; n < PD; ++n) hld(n);
    }
  }
  STAMP(12);

  {
    const int oj = w >> 1, rh = w & 1;
    const int etok = 64 * m2 + 16 * (oj ^ jr) + l15, ech = 64 * n2 + 16 * g + 8 * rh;
    u32x4 xv;
    u32 yo = (ech >> 1) * 8192 + etok * 4, yfv = (8 * g + (l & 3)) * FS;
    PIN(yo); PIN(yfv);
    SCHED();
    f32x4 acc[4][4];
#pragma unroll
    for (int i = 0; i < 4; ++i)
#pragma unroll
      for (int j = 0; j < 4; ++j) acc[i][j] = f32x4{0.f, 0.f, 0.f, 0.f};
    u32x2 yfl;
    const int yfo = FL_Y + ((m2 >> 2) * 256 + 32 * n2 + 4 * rh) * FS;
    LAS char* pw = opaque(lds + w * 16384 + l * 8);
    LAS char* pr = opaque(lds + w * 2048 + l * 8);
    LAS char* pr2 = opaque(pr + 65536);
    u32 a_cnt = lds0 + M_CNT + l * 4, one = 1u;
    PIN(a_cnt); PIN(one);
    auto part_wr = [&](int j, int k) {
      *(LAS f32x2*)(pw + (j * 8 + k) * 512) = f32x2{acc[k & 3][j][2 * (k >> 2)], acc[k & 3][j][2 * (k >> 2) + 1]};
    };
#pragma unroll
    for (int n = 0; n < 28; ++n) {
#pragma unroll
      for (int i = 0; i < 4; ++i) {
        acc[i][n / 7] = mfma16(wfr[4 * (n % 7) + i], hf[n], acc[i][n / 7]);
        SCHED();
        if (i == 3 && n + PD < 28) hld(n + PD);
        if ((i & 1) == 0 && n >= 8 && n % 7 >= 1 && n % 7 < 5) part_wr(n / 7 - 1, (n % 7 - 1) * 2 + (i >> 1));
        SCHED();
      }
      if (n == YP1 && w >= 4) { asm volatile("s_waitcnt vmcnt(%0)" :: "i"(YP1 + 1) : "memory"); st64s(tag, rs, o_l0, FL_Y + (ytile * 256 + b) * FS); }
      if (n == YP0 && w < 4) { asm volatile("s_waitcnt vmcnt(%0)" :: "i"(YP0 + 1) : "memory"); st64s(tag, rs, o_l0, FL_Y + (ytile * 256 + b) * FS); }
      if (n == 21) xv = *(const GAS u32x4*)((const char*)p.x + (size_t)etok * 1024 + ech * 2);
      if (n == 22) yfl = ld64s(rs, yfv, yfo);
      if (n == TB && w >= 4) LBAR();
      SCHED();
    }
    STAMP(13);
#pragma unroll
    for (int k = 0; k < 8; ++k) part_wr(3, k);
    asm volatile("" ::: "memory");
    if (w < 4) LBAR();
    else asm volatile("ds_add_u32 %0, %1" :: "v"(a_cnt), "v"(one) : "memory");
    if (!__all(yfl.x == tag.x && yfl.y == tag.y)) poll(rs, yfv, yfo, tag);
    asm volatile("" ::: "memory");
    u32 yv[4];
#pragma unroll
    for (int j = 0; j < 4; ++j) yv[j] = ld32s(rs, yo, WS_Y + j * 8192);
    f32x2 pp[8][4];
#pragma unroll
    for (int ww = 0; ww < 4; ++ww)
#pragma unroll
      for (int i = 0; i < 4; ++i) pp[ww][i] = *(volatile LAS f32x2*)(pr + ww * 16384 + i * 512);
    if (w >= 4) {
      volatile LAS u32* cnt = (volatile LAS u32*)(lds + M_CNT);
#pragma unroll 1
      for (int spin = 0; spin < (1 << 20) && *cnt < 4u; ++spin) {}
      asm volatile("" ::: "memory");
    }
    STAMP(14);
#pragma unroll
    for (int ww = 0; ww < 4; ++ww)
#pragma unroll
      for (int i = 0; i < 4; ++i) pp[4 + ww][i] = *(volatile LAS f32x2*)(pr2 + ww * 16384 + i * 512);
    SCHED();
    f32x2 sm[4];
#pragma unroll
    for (int i = 0; i < 4; ++i) sm[i] = ((pp[0][i] + pp[1][i]) + (pp[2][i] + pp[3][i])) + ((pp[4][i] + pp[5][i]) + (pp[6][i] + pp[7][i]));
    const u32 ff[4] = {pk2(sm[0].x, sm[1].x), pk2(sm[2].x, sm[3].x), pk2(sm[0].y, sm[1].y), pk2(sm[2].y, sm[3].y)};
    u32x4 r;
#pragma unroll
    for (int j = 0; j < 4; ++j) {
      const u32 s = pk2v(up2(xv[j]) + up2(yv[j]));
      r[j] = pk2v(up2(s) + up2(ff[j]));
    }
    *(GAS u32x4*)((char*)p.out + (size_t)etok * 1024 + ech * 2) = r;
  }
  STAMP(15);
  if (w == 7) {
    st64s(u32x2{mt0, mtp}, rs, o_l0, WS_ST + 524288 + b * 128);
    WAIT_V(0);
    st64s(tag, rs, o_l0, WS_ST + 655360 + b * 128);
    if (b == 255) {
      u32 lo = 0, hi = 0;
#pragma unroll 1
      for (int i = 0; i < 4; ++i) {
        poll(rs, (64 * i + l) * 128, WS_ST + 655360, tag);
        const u32x2 v = ld64s(rs, (64 * i + l) * 128, WS_ST + 524288);
        lo = max(lo, 0x10000u - (v.x - mt0)); hi = max(hi, 0x10000u + (v.y - mt0));
      }
      lo = wave_max_u32(lo); hi = wave_max_u32(hi);
      const u32 now = (u32)__builtin_amdgcn_s_memrealtime();
      u32 T = (hi + lo - 0x20000u) >> 0;
      T = min(T, 4000u);
      while ((u32)__builtin_amdgcn_s_memrealtime() - now < T) __builtin_amdgcn_s_sleep(1);
    }
  }
}

extern "C" void kernel_launch(void* const* d_in, const int* in_sizes, int n_in, void* d_out, int out_size, void* d_ws,
                              size_t ws_size, hipStream_t stream) {
  Params p;
  p.x = (const u16*)d_in[0]; p.lar = (const u16*)d_in[1]; p.aim = (const u16*)d_in[2]; p.cre = (const u16*)d_in[3];
  p.cim = (const u16*)d_in[4]; p.ldt = (const u16*)d_in[5]; p.dsk = (const u16*)d_in[6];
  p.w1 = (const u16*)d_in[8]; p.w3 = (const u16*)d_in[9]; p.w2 = (const u16*)d_in[10];
  p.out = (u16*)d_out; p.ws = (char*)d_ws;
  fused<<<256, 512, 0, stream>>>(p);
}
```

```cpp
#include <hip/hip_runtime.h>
#include <hip/hip_bf16.h>

typedef unsigned short u16;
typedef unsigned int u32;
typedef unsigned long long u64;
typedef __bf16 bf16x8 __attribute__((ext_vector_type(8)));
typedef __bf16 bf16x2 __attribute__((ext_vector_type(2)));
typedef _Float16 f16x8 __attribute__((ext_vector_type(8)));
typedef _Float16 f16x2 __attribute__((ext_vector_type(2)));
typedef short s16x4 __attribute__((ext_vector_type(4)));
typedef float f32x2 __attribute__((ext_vector_type(2)));
typedef float f32x4 __attribute__((ext_vector_type(4)));
typedef float f32x16 __attribute__((ext_vector_type(16)));
typedef u32 u32x2 __attribute__((ext_vector_type(2)));
typedef u32 u32x4 __attribute__((ext_vector_type(4)));
typedef u64 u64x4 __attribute__((ext_vector_type(4)));
typedef u64 u64x8 __attribute__((ext_vector_type(8)));
typedef __amdgpu_buffer_rsrc_t rsrc_t;
#define LAS __attribute__((address_space(3)))
#define GAS __attribute__((address_space(1)))
#define DEV __device__ __forceinline__

#define NTQ " nt"
#define TB 21
#define YP1 1
#define YP0 10
constexpr int L = 2048, D = 512, NM = 128, F = 1792;
constexpr int LDS_BYTES = 163840;
constexpr int SC1 = 16;

constexpr int WS_H = 0;
constexpr int WS_XNR = 8 << 20;
constexpr int WS_W2F = 10 << 20;
constexpr int WS_Y = 12 << 20;
constexpr int WS_ST = 14 << 20;
constexpr int WS_CS = WS_ST + 8192;
constexpr int WS_FL = 15 << 20;
constexpr int FS = 128;
constexpr int FL_EP = WS_FL;
constexpr int FL_P0 = WS_FL + 4096;
constexpr int FL_H = FL_P0 + 256 * FS;
constexpr int FL_Y = FL_H + 2048 * FS;
constexpr int WS_BYTES = 16 << 20;

struct Params {
  const u16 *x, *lar, *aim, *cre, *cim, *ldt, *dsk, *w1, *w3, *w2;
  u16* out;
  char* ws;
};

template <int V> struct IC { static constexpr int v = V; };
template <int N, int I = 0, class Fn>
DEV void static_for(Fn&& fn) { if constexpr (I < N) { fn(IC<I>{}); static_for<N, I + 1>(fn); } }
DEV u32 f2u(float f) { return __builtin_bit_cast(u32, f); }
DEV float u2f(u32 v) { return __builtin_bit_cast(float, v); }
DEV float b2f(u32 v) { return __builtin_bit_cast(float, v << 16); }
DEV float bhi(u32 v) { return __builtin_bit_cast(float, v & 0xffff0000u); }
DEV u32 pk2v(f32x2 f) { return __builtin_bit_cast(u32, __builtin_convertvector(f, bf16x2)); }
DEV u32 pk2(float lo, float hi) { return pk2v(f32x2{lo, hi}); }
DEV f32x2 up2(u32 v) { return f32x2{b2f(v), bhi(v)}; }
DEV float rb(float f) { return b2f(pk2(f, f) & 0xffffu); }
DEV f32x2 fma2(f32x2 a, f32x2 b, f32x2 c) { return __builtin_elementwise_fma(a, b, c); }
DEV f32x16 mfma32(u32x4 a, u32x4 b, f32x16 c) {
  return __builtin_amdgcn_mfma_f32_32x32x16_bf16(__builtin_bit_cast(bf16x8, a), __builtin_bit_cast(bf16x8, b), c, 0, 0, 0);
}
DEV f32x4 mfma16(u32x4 a, u32x4 b, f32x4 c) {
  return __builtin_amdgcn_mfma_f32_16x16x32_bf16(__builtin_bit_cast(bf16x8, a), __builtin_bit_cast(bf16x8, b), c, 0, 0, 0);
}
DEV f32x16 mfma32h(u32x4 a, u32x4 b, f32x16 c) {
  return __builtin_amdgcn_mfma_f32_32x32x16_f16(__builtin_bit_cast(f16x8, a), __builtin_bit_cast(f16x8, b), c, 0, 0, 0);
}
DEV u32x4 lds128(LAS char* p) { return *(LAS u32x4*)p; }
DEV u32x2 lds64(LAS char* p) { return *(LAS u32x2*)p; }
DEV u32x2 ldstr(LAS char* p) { return __builtin_bit_cast(u32x2, __builtin_amdgcn_ds_read_tr16_b64_v4i16((LAS s16x4*)p)); }
template <int LOFF>
DEV void dma_m0(u32 lbase) { asm volatile("s_add_u32 m0, %0, %1" :: "s"(lbase), "i"(LOFF) : "memory", "scc", "m0"); }
template <bool NT>
DEV void dma_go(u32x4 rs, u32 voff, u32 soff) {
  if (NT) asm volatile("buffer_load_dwordx4 %0, %1, %2 offen nt lds" :: "v"(voff), "s"(rs), "s"(soff) : "memory");
  else asm volatile("buffer_load_dwordx4 %0, %1, %2 offen lds" :: "v"(voff), "s"(rs), "s"(soff) : "memory");
}
DEV u32x4 mkrs(const void* ptr, u32 bytes) { const u64 a = (u64)ptr; return u32x4{(u32)a, (u32)(a >> 32) & 0xffffu, bytes, 0x00020000u}; }
#define WAIT_V(n) asm volatile("s_waitcnt vmcnt(" #n ")" ::: "memory")
#define WAIT_L0() asm volatile("s_waitcnt lgkmcnt(0)" ::: "memory")
#define BAR() __builtin_amdgcn_s_barrier()
#define LBAR() do { WAIT_L0(); BAR(); asm volatile("" ::: "memory"); } while (0)
#define SCHED() __builtin_amdgcn_sched_barrier(0)
#define PIN(x) asm volatile("" : "+v"(x))
constexpr int OOB = 0x04000000;
#define STAMP(i)
#define STAMP_DECL
#define STAMP_DUMP()

DEV u32x4 ld128s(rsrc_t rs, int voff, int soff) { return __builtin_amdgcn_raw_buffer_load_b128(rs, voff, soff, SC1); }
DEV u32x2 ld64s(rsrc_t rs, int voff, int soff) { return __builtin_amdgcn_raw_buffer_load_b64(rs, voff, soff, SC1); }
DEV u32 ld32s(rsrc_t rs, int voff, int soff) { return __builtin_amdgcn_raw_buffer_load_b32(rs, voff, soff, SC1); }
DEV void st128s(u32x4 v, rsrc_t rs, int voff, int soff) {
  __builtin_amdgcn_raw_buffer_store_b128(v, rs, voff, soff, SC1);
  asm volatile("s_nop 1" :: "v"(v) : "memory");
}
DEV void st64s(u32x2 v, rsrc_t rs, int voff, int soff) { __builtin_amdgcn_raw_buffer_store_b64(v, rs, voff, soff, SC1); }
DEV void st32s(u32 v, rsrc_t rs, int voff, int soff) { __builtin_amdgcn_raw_buffer_store_b32(v, rs, voff, soff, SC1); }
DEV void poll(rsrc_t rs, int voff, int soff, u32x2 tag) {
#pragma unroll 1
  for (int spin = 0; spin < (1 << 18); ++spin) {
    const u32x2 v = ld64s(rs, voff, soff);
    if (__all(v.x == tag.x && v.y == tag.y)) break;
    __builtin_amdgcn_s_sleep(4);
  }
  __builtin_amdgcn_fence(__ATOMIC_ACQUIRE, "wavefront");
  asm volatile("" ::: "memory");
}

template <int CTRL, int RM>
DEV float dppz(float v) { return __builtin_bit_cast(float, __builtin_amdgcn_update_dpp(0, __builtin_bit_cast(int, v), CTRL, RM, 0xf, true)); }
template <int CTRL, int RM>
DEV u32 dppu(u32 v) { return (u32)__builtin_amdgcn_update_dpp(0, (int)v, CTRL, RM, 0xf, true); }
DEV u32 wave_max_u32(u32 v) {
  v = max(v, dppu<0x111, 0xf>(v)); v = max(v, dppu<0x112, 0xf>(v)); v = max(v, dppu<0x114, 0xf>(v)); v = max(v, dppu<0x118, 0xf>(v));
  v = max(v, dppu<0x142, 0xa>(v)); v = max(v, dppu<0x143, 0xc>(v));
  return (u32)__builtin_amdgcn_readlane((int)v, 63);
}

DEV float rdl(float v, int lane) { return __builtin_bit_cast(float, __builtin_amdgcn_readlane(__builtin_bit_cast(int, v), lane)); }
template <int CTRL>
DEV float dppx(float v) { return __builtin_bit_cast(float, __builtin_amdgcn_mov_dpp(__builtin_bit_cast(int, v), CTRL, 0xf, 0xf, true)); }
DEV float rows8_sum(const float (&p)[8], int l) {
  float a[4], c[2];
#pragma unroll
  for (int i = 0; i < 4; ++i) {
    const auto r = __builtin_amdgcn_permlane32_swap(__builtin_bit_cast(u32, p[i]), __builtin_bit_cast(u32, p[i + 4]), false, false);
    a[i] = __builtin_bit_cast(float, (u32)r[0]) + __builtin_bit_cast(float, (u32)r[1]);
  }
#pragma unroll
  for (int i = 0; i < 2; ++i) {
    const auto r = __builtin_amdgcn_permlane16_swap(__builtin_bit_cast(u32, a[i]), __builtin_bit_cast(u32, a[i + 2]), false, false);
    c[i] = __builtin_bit_cast(float, (u32)r[0]) + __builtin_bit_cast(float, (u32)r[1]);
  }
  const bool up = l & 8;
  float v = (up ? c[1] : c[0]) + dppx<0x128>(up ? c[0] : c[1]);
  v += dppx<0xB1>(v); v += dppx<0x4E>(v); v += dppx<0x141>(v);
  return v;
}

constexpr int G1_SLOT = 49152;
constexpr int KG_T = 2 * G1_SLOT;
constexpr int KG_TW = 13312;
constexpr int SPARE = 3 * G1_SLOT;
constexpr int M_CS = SPARE;
constexpr int M_CNT = SPARE + 4096;
constexpr int M_KC = SPARE + 5120;
constexpr int CV_K = SPARE + 5120;
static_assert(KG_T + 4 * KG_TW <= M_KC && M_KC + 4 * 2816 <= LDS_BYTES, "lds");
constexpr int CV_CS = 4160;
constexpr int CV_U = 2 * G1_SLOT;
constexpr int CV_UCH = 4 * CV_CS;
constexpr int CV_X = 0;

DEV f32x2 cmul(f32x2 a, f32x2 b, f32x2 bn) { return fma2(f32x2{a.y, a.x}, bn, a * f32x2{b.x, b.x}); }
DEV f32x2 cneg(f32x2 b) { return f32x2{-b.y, b.y}; }
DEV f32x2 cexp_p(float lre2, float rev, float pw) {
  const float mg = __builtin_amdgcn_exp2f(lre2 * pw);
  const float ph = __builtin_amdgcn_fractf(rev * pw);
  return f32x2{__builtin_amdgcn_cosf(ph), __builtin_amdgcn_sinf(ph)} * f32x2{mg, mg};
}
DEV u32 pkh(f32x2 f) { return __builtin_bit_cast(u32, __builtin_convertvector(f, f16x2)); }
DEV LAS char* opaque(LAS char* q) { u32 v = (u32)(size_t)q; asm volatile("" : "+v"(v)); return (LAS char*)(size_t)v; }

template <class Hook>
DEV void conv_tiles(LAS char* ua, LAS char* ka, f32x4 (&acc)[4], Hook hook) {
  constexpr int ORD[16] = {0, 15, 13, 11, 9, 1, 14, 12, 10, 8, 2, 3, 4, 5, 6, 7};
  u32x4 fr[2][16];
  auto load = [&](int rr, int id) {
    if (id < 8) {
      const u32x2 lo = *(volatile LAS u32x2*)(ua + (7 - id) * 512 + (3 - rr) * 64), hi = *(volatile LAS u32x2*)(ua + (7 - id) * 512 + (3 - rr) * 64 + 8);
      fr[rr & 1][id] = u32x4{lo.x, lo.y, hi.x, hi.y};
    } else {
      fr[rr & 1][id] = lds128(ka + (id - 8) * 512 + (3 - rr) * 64);
    }
  };
#pragma unroll
  for (int k = 0; k < 16; ++k) { load(0, ORD[k]); SCHED(); }
  hook(0, -1);
  SCHED();
#pragma unroll
  for (int rr = 0; rr < 4; ++rr) {
#pragma unroll
    for (int c = 0; c < 8; ++c) {
      u32x4(&f)[16] = fr[rr & 1];
#pragma unroll
      for (int i = 0; i < 4; ++i) {
        if (c <= 7 - 2 * i) { acc[i] = mfma16(f[c], f[8 + 7 - 2 * i - c], acc[i]); SCHED(); }
        if (rr < 3 && i < 2) { load(rr + 1, ORD[2 * c + i]); SCHED(); }
      }
      hook(rr, c);
      SCHED();
    }
  }
}

__global__ __launch_bounds__(512, 2) void fused(Params args) {
  __shared__ __attribute__((aligned(16))) char smem[LDS_BYTES];
  u64x8 ka0;
  u64x4 ka1;
  asm volatile("s_load_dwordx16 %0, %2, 0x0\n\ts_load_dwordx8 %1, %2, 0x40" : "=&s"(ka0), "=&s"(ka1) : "s"(__builtin_amdgcn_kernarg_segment_ptr()) : "memory");
#define CODEPF 23000
  u32x4 pf;
  if (__builtin_amdgcn_readfirstlane(threadIdx.x >> 6) == 4) {
    u64 pc;
    asm volatile("s_getpc_b64 %0" : "=s"(pc));
    const u32 po = min((u32)((blockIdx.x >> 3) * 1024 + (threadIdx.x & 63) * 16), (u32)(CODEPF - 16));
    asm volatile("global_load_dwordx4 %0, %1, %2" : "=v"(pf) : "v"(po), "s"(pc & ~(u64)255) : "memory");
  }
  LAS char* lds = (LAS char*)smem;
  const u32 lds0 = (u32)(size_t)lds;
  const int tid = threadIdx.x, l = tid & 63;
  const int w = __builtin_amdgcn_readfirstlane(tid >> 6);
  const int b = blockIdx.x, xcd = b & 7, rk = b >> 3;
  const bool g1 = rk < 28;
  const int l15 = l & 15, g = l >> 4, t = l & 31, h = l >> 5;
  const int mt = (xcd >> 1) * 4 + (rk & 3), ft = (xcd & 1) * 7 + (rk >> 2);
#define ROTV 5
  const int kt0 = ROTV == 0 ? 0 : ROTV == 1 ? (ft + 2 * (mt & 3) + ((mt >> 2) & 1)) & 7 : ROTV == 2 ? (2 * (xcd >> 1) + 4 * (xcd & 1)) & 7 : (2 * (xcd >> 1) + 4 * (xcd & 1) + (mt & 1)) & 7;
  const int wt = w & 1, wf = w >> 1;
  u32 xo0 = (l >> 3) * 1024 + (((l & 7) ^ g) << 4);
  u32 xo1 = (8 + (l >> 3)) * 1024 + (((l & 7) ^ (4 + g)) << 4);
  u32 wo1 = g * (F * 2) + ((l15 ^ (4 * g + 2 * (w & 1))) << 4), wo1b = wo1 + 4 * F * 2;
  PIN(xo0); PIN(xo1); PIN(wo1); PIN(wo1b);
  u32 tv[8], tm[4], tm2, rv[2], rm[2];
  {
    const u32 tw = lds0 + KG_T + (w & 3) * KG_TW;
#pragma unroll
    for (int k = 0; k < 8; ++k) { tv[k] = tw + l * 128 + ((k ^ (l & 7)) << 4); PIN(tv[k]); }
#pragma unroll
    for (int k = 0; k < 4; ++k) { tm[k] = tw + 8192 + l * 64 + ((k ^ ((l >> 1) & 3)) << 4); PIN(tm[k]); }
    tm2 = tw + 12288 + l * 16;
    PIN(tm2);
    const int blk = g & 1, c = (l >> 2) & 1, pp = l & 3;
#pragma unroll
    for (int t2 = 0; t2 < 2; ++t2) {
      const int m = 4 * h + 2 * t2 + ((l >> 3) & 1);
      rv[t2] = tw + m * 128 + (((4 * c + 2 * blk + (pp >> 1)) ^ m) << 4) + 8 * (pp & 1);
      rm[t2] = blk ? tw + 12288 + m * 16 + 8 * c : tw + 8192 + m * 64 + (((2 * c + (pp >> 1)) ^ (m >> 1)) << 4) + 8 * (pp & 1);
      PIN(rv[t2]); PIN(rm[t2]);
    }
  }
  u32 rms = (g & 1) ? 128 : 512;
  PIN(rms);
  asm volatile("s_waitcnt lgkmcnt(0)" : "+s"(ka0), "+s"(ka1) :: "memory");
  Params p;
  p.x = (const u16*)ka0[0]; p.lar = (const u16*)ka0[1]; p.aim = (const u16*)ka0[2]; p.cre = (const u16*)ka0[3]; p.cim = (const u16*)ka0[4];
  p.ldt = (const u16*)ka0[5]; p.dsk = (const u16*)ka0[6]; p.w1 = (const u16*)ka0[7];
  p.w3 = (const u16*)ka1[0]; p.w2 = (const u16*)ka1[1]; p.out = (u16*)ka1[2]; p.ws = (char*)ka1[3];
  const rsrc_t rs = __builtin_amdgcn_make_buffer_rsrc(p.ws, 0, WS_BYTES, 0x00020000);
  STAMP_DECL;
  STAMP(3);

  u32x2 tag;
  u32 p_ldt2, dk;
  asm volatile("s_load_dwordx2 %0, %3, 0x0\n\ts_load_dword %1, %4, 0x0\n\ts_load_dword %2, %5, 0x0"
               : "=&s"(tag), "=&s"(p_ldt2), "=&s"(dk) : "s"(p.ws + FL_EP), "s"(p.ldt + 2 * b), "s"(p.dsk + 2 * b) : "memory");

  u32 p_lar, p_aim, p_cre, p_cim;
  if (w < 4) {
    const u32 po = (b * 256 + tid) * 2;
    asm volatile("global_load_ushort %0, %1, %2" NTQ : "=v"(p_lar) : "v"(po), "s"(p.lar) : "memory");
    asm volatile("global_load_ushort %0, %1, %2" NTQ : "=v"(p_aim) : "v"(po), "s"(p.aim) : "memory");
    asm volatile("global_load_ushort %0, %1, %2" NTQ : "=v"(p_cre) : "v"(po), "s"(p.cre) : "memory");
    asm volatile("global_load_ushort %0, %1, %2" NTQ : "=v"(p_cim) : "v"(po), "s"(p.cim) : "memory");
  }
  SCHED();

  const u32x4 rx = mkrs(p.x, L * D * 2), rw1 = mkrs(p.w1, D * F * 2), rw3 = mkrs(p.w3, D * F * 2);
#define NTS 0
  auto dm0 = [&](auto st_, int piece, int ww) {
    constexpr int S = (decltype(st_)::v % 3) * G1_SLOT;
    const u32 lb = lds0 + ww * 2048;
    if (piece == 0) dma_m0<S>(lb);
    if (piece == 1) dma_m0<S + 16384>(lb);
    if (piece == 2) dma_m0<S + 32768>(lb);
    if (piece == 3) dma_m0<S + 1024>(lb);
    if (piece == 4) dma_m0<S + 16384 + 1024>(lb);
    if (piece == 5) dma_m0<S + 32768 + 1024>(lb);
  };
  auto dgo = [&](auto st_, int piece, int ww) {
    constexpr bool nt = decltype(st_)::v < NTS;
    const int kt = (kt0 + decltype(st_)::v) & 7;
    const u32 sx = (mt * 128 + ww * 16) * 1024 + kt * 128, sw = (kt * 64 + ww * 8) * (F * 2) + ft * 256;
    if (piece == 0) dma_go<nt>(rx, xo0, sx);
    if (piece == 1) dma_go<nt>(rw1, wo1, sw);
    if (piece == 2) dma_go<nt>(rw3, wo1, sw);
    if (piece == 3) dma_go<nt>(rx, xo1, sx);
    if (piece == 4) dma_go<nt>(rw1, wo1b, sw);
    if (piece == 5) dma_go<nt>(rw3, wo1b, sw);
  };
  auto dmaw = [&](auto st_, int piece, int ww) { dm0(st_, piece, ww); asm volatile("s_nop 0"); dgo(st_, piece, ww); };

  u32x2 wr[8];
  u32x4 xr[8];
  const int un2 = b / 28, ukb = 2 * (b - 28 * un2) + (w & 1);
  const int ax = xcd * 4 + (rk - 28);
  if (w >= 4 && g1) {
#pragma unroll
    for (int i = 0; i < 6; ++i) { dmaw(IC<0>{}, i, w - 4); dmaw(IC<0>{}, i, w); }
    if (w < 6) {
      const char* wb2 = (const char*)p.w2 + (size_t)(32 * ukb) * 1024 + un2 * 128;
      const u32 wv = g * 4096 + l15 * 8;
#define W2RAW(j, base, off) asm volatile("global_load_dwordx2 %0, %1, %2 offset:" #off NTQ : "=v"(wr[j]) : "v"(wv), "s"(base) : "memory")
      W2RAW(0, wb2, 0); W2RAW(1, wb2, 1024); W2RAW(2, wb2, 2048); W2RAW(3, wb2, 3072);
      W2RAW(4, wb2 + 16384, 0); W2RAW(5, wb2 + 16384, 1024); W2RAW(6, wb2 + 16384, 2048); W2RAW(7, wb2 + 16384, 3072);
    }
  }
  if (!g1) {
    const u32 xv_ = (64 * ax + 8 * w) * 1024 + l * 16;
#define XRAW(j, vo, off) asm volatile("global_load_dwordx4 %0, %1, %2 offset:" #off : "=v"(xr[j]) : "v"(vo), "s"(p.x) : "memory")
    XRAW(0, xv_, 0); XRAW(1, xv_, 1024); XRAW(2, xv_, 2048); XRAW(3, xv_, 3072);
    XRAW(4, xv_ + 4096, 0); XRAW(5, xv_ + 4096, 1024); XRAW(6, xv_ + 4096, 2048); XRAW(7, xv_ + 4096, 3072);
  }
  SCHED();

  const int m2 = xcd * 4 + (rk & 3), n2 = rk >> 2;
  const int uch = w >> 2, ui = tid & 255;
  u32 uo1 = ui < 255 ? ui * 16 + 16 : OOB;
  int xa[2], wa[2];
#pragma unroll
  for (int s = 0; s < 2; ++s) xa[s] = (64 * wt + l15) * 128 + (((4 * s + g) ^ ((l15 >> 1) & 7)) << 4);
  {
    const int q = l15 >> 2, pp = l & 3;
#pragma unroll
    for (int i = 0; i < 2; ++i) wa[i] = 16384 + (8 * g + q) * 256 + (((4 * wf + 2 * i + (pp >> 1)) ^ (4 * q + 2 * (g & 1))) << 4) + 8 * (pp & 1);
  }
  u32x4 sel;
#pragma unroll
  for (int e = 0; e < 4; ++e) sel[e] = l15 == 2 * e ? 0x00003f80u : (l15 == 2 * e + 1 ? 0x3f800000u : 0u);
  u32 a_cw = lds0 + 16384 * (1 + (w >> 2)) + (4 * g + (l15 >> 2)) * 256 + (((4 * (w & 3) + (l & 3)) ^ (4 * (l15 >> 2) + 2 * h)) << 4);
  u32 o_cq = (l < 32 && l15 >= 12) ? (l & 3) * 32 + g * 16 : OOB;
  u32 o_l0 = l == 0 ? 0 : OOB;
  u32 a_ua = lds0 + CV_U + (w & 1) * CV_UCH + (3 - (l15 & 3)) * CV_CS + 4088 - 8 * (l15 >> 2) - 32 * h + 16 * (g & 1) - 7 * 512 - 256 * (w >> 2) - 3 * 64;
  u32 a_ka = lds0 + CV_K + (w & 1) * 5120 + 1024 - 512 * ((w >> 1) & 1) + 32 * (l15 - h) + 16 * (g & 1) - 256 * (w >> 2) - 3 * 64;
  u32 a_zk = lds0 + CV_K + (w & 1) * 5120 + l * 16;
  u32 a_zu = lds0 + CV_U + (w & 1) * CV_UCH + ((l >> 1) & 3) * CV_CS + 4096 + (l & 1) * 16;
  u32 hfo = (l < 7 ? l : 0) * FS;
  u32x4 zero4 = {0u, 0u, 0u, 0u};
  PIN(a_ua); PIN(a_ka); PIN(a_zk); PIN(a_zu); PIN(hfo); PIN(zero4);

  u32 o_cs = g * 1024 + (l15 >> 3) * 512 + (32 * wf + 4 * (l15 & 7)) * 4;
  u32 a_cs = lds0 + M_CS + w * 256 + (4 * l15 + 2 * h + (g & 1)) * 4;
  PIN(a_cs);
  u32 a_cv = lds0 + M_CS + w * 256 + g * 16;

  PIN(sel); PIN(a_cw); PIN(o_cq); PIN(o_l0); PIN(o_cs); PIN(a_cv); PIN(uo1);
  int xa2[2], wa1[2], wa2[2];
#pragma unroll
  for (int e = 0; e < 2; ++e) {
    xa2[e] = xa[e] + 2 * G1_SLOT; wa1[e] = wa[e] + G1_SLOT; wa2[e] = wa[e] + 2 * G1_SLOT;
    PIN(xa[e]); PIN(xa2[e]); PIN(wa[e]); PIN(wa1[e]); PIN(wa2[e]);
  }
  SCHED();
  int lq = l, uq = tid & 255;
  PIN(lq); PIN(uq);
  u32 p0off;
  {
    const int e = min(9 * w + min(lq, 8), 67);
    const int m = e - 60;
    const int pb = e < 32 ? 224 + e : (e < 60 ? 28 * n2 + (e - 32) : (g1 ? ((m >> 1) * 2 + (xcd & 1)) + 8 * ((m & 1) + 4 * (rk >> 2)) : 224));
    p0off = pb * FS;
  }
  u32 ka_[8];
#pragma unroll
  for (int e = 0; e < 4; ++e) {
    const int l0 = 8 * uq + 2 * e;
    const u32 bits = pk2((float)l0, (float)(l0 + 1));
    const int i0 = uq < 32 ? l0 : (int)(bits & 0xffffu) - (0x4300 - 128);
    const int i1 = uq < 32 ? l0 + 1 : (int)(bits >> 16) - (0x4300 - 128);
    ka_[2 * e] = lds0 + M_KC + uch * 5632 + i0 * 4;
    ka_[2 * e + 1] = lds0 + M_KC + uch * 5632 + i1 * 4;
  }
  PIN(p0off);
#pragma unroll
  for (int e = 0; e < 8; ++e) PIN(ka_[e]);
  SCHED();
  if (g1 && w >= 4) {
#pragma unroll
    for (int i = 0; i < 6; ++i) { dmaw(IC<1>{}, i, w - 4); dmaw(IC<1>{}, i, w); }
    asm volatile("s_waitcnt vmcnt(12)" : "+v"(wr[0]), "+v"(wr[1]), "+v"(wr[2]), "+v"(wr[3]), "+v"(wr[4]), "+v"(wr[5]), "+v"(wr[6]), "+v"(wr[7]) :: "memory");
    PIN(pf);
    if (w < 6) {
#pragma unroll
      for (int c = 0; c < 4; ++c) {
        u32x4 o;
#pragma unroll
        for (int e = 0; e < 4; ++e) o[e] = __builtin_amdgcn_perm(wr[2 * e + 1][c >> 1], wr[2 * e][c >> 1], (c & 1) ? 0x07060302u : 0x05040100u);
        st128s(o, rs, l * 16 + c * 1024, WS_W2F + (((un2 * 56 + ukb) * 4) << 10));
      }
    }
  }
  asm volatile("s_waitcnt lgkmcnt(0)" : "+s"(tag), "+s"(p_ldt2), "+s"(dk) :: "memory");
  tag.x += 1u;

  if (!g1) {
    asm volatile("s_waitcnt vmcnt(0)" : "+v"(xr[0]), "+v"(xr[1]), "+v"(xr[2]), "+v"(xr[3]), "+v"(xr[4]), "+v"(xr[5]), "+v"(xr[6]), "+v"(xr[7]) :: "memory");
    PIN(pf);
    f32x2 v[8][4];
    float ps[8];
#pragma unroll
    for (int r = 0; r < 8; ++r) {
#pragma unroll
      for (int j = 0; j < 4; ++j) v[r][j] = up2(xr[r][j]);
      const f32x2 s2 = (v[r][0] + v[r][1]) + (v[r][2] + v[r][3]);
      ps[r] = s2.x + s2.y;
    }
    const float mean = rows8_sum(ps, l) * (1.f / D);
#pragma unroll
    for (int r = 0; r < 8; ++r) {
      const float mr = rdl(mean, 8 * r);
      f32x2 q2 = {0.f, 0.f};
#pragma unroll
      for (int j = 0; j < 4; ++j) { v[r][j] -= f32x2{mr, mr}; q2 = fma2(v[r][j], v[r][j], q2); }
      ps[r] = q2.x + q2.y;
    }
    const float var = rows8_sum(ps, l) * (1.f / D);
    const u32 ms = pk2(mean, __builtin_sqrtf(rb(rb(var) + 1e-5f)));
    if ((l & 7) == 0) st32s(ms, rs, (l >> 3) * 4, WS_ST + (64 * ax + 8 * w) * 4);
    const float riv = 1.f / bhi(ms), nbv = (mean - b2f(ms)) * riv;
#pragma unroll
    for (int r = 0; r < 8; ++r) {
      const float ri = rdl(riv, 8 * r), nb = rdl(nbv, 8 * r);
#pragma unroll
      for (int j = 0; j < 4; ++j) v[r][j] = fma2(v[r][j], f32x2{ri, ri}, f32x2{nb, nb});
    }
#pragma unroll
    for (int c = 0; c < 8; ++c) {
      u32x4 o;
#pragma unroll
      for (int e = 0; e < 4; ++e) o[e] = pk2(v[7 - 2 * e][c >> 1][c & 1], v[6 - 2 * e][c >> 1][c & 1]);
      *(LAS u32x4*)(lds + (8 * l + c) * 128 + (((7 - w) ^ (l & 7)) << 4)) = o;
    }
    LBAR();
    u32x4 tv[8];
#pragma unroll
    for (int it = 0; it < 8; ++it) tv[it] = lds128(lds + w * 8192 + it * 1024 + l * 16);
#pragma unroll
    for (int it = 0; it < 8; ++it)
      st128s(tv[it], rs, (8 * it + (l >> 3)) * 4096 + (((l & 7) ^ it) << 4), WS_XNR + w * (64 * 4096) + (L - 64 - 64 * ax) * 2);
    WAIT_V(0);
    LBAR();
    if (tid == 0) st64s(tag, rs, 0, FL_P0 + b * FS);
  }

  if (w < 4) {
    float dt = rb(__expf((w >> 1) ? bhi(p_ldt2) : b2f(p_ldt2)));
    PIN(dt);
    asm volatile("s_waitcnt vmcnt(0)" : "+v"(p_lar), "+v"(p_aim), "+v"(p_cre), "+v"(p_cim) :: "memory");
    STAMP(1);
    const float ar = -rb(__expf(b2f(p_lar)));
    const float lre2 = (ar * dt) * 1.44269504f, rev = (b2f(p_aim) * dt) * 0.15915494309f;
    u32 eb = wave_max_u32(max(p_cre & 0x7f80u, p_cim & 0x7f80u)) << 16;
    eb = eb < 0x00800000u ? 0x00800000u : (eb > 0x7e800000u ? 0x7e800000u : eb);
    const float sc = __builtin_bit_cast(float, eb) * (2.f * dt), isc = __builtin_bit_cast(float, 0x7f000000u - eb);
    f32x2 e[53], tmp[8];
    u32 hr[26], hi[26];
    const f32x2 w8 = cexp_p(lre2, rev, 8.f), w64 = cexp_p(lre2, rev, 64.f);
    const f32x2 z1 = cexp_p(lre2, -rev, 1.f), zc = cexp_p(lre2, -rev, 256.f);
    e[0] = f32x2{1.f, 0.f}; e[1] = w8; e[8] = w64; e[32] = f32x2{b2f(p_cre) * isc, -b2f(p_cim) * isc};
#define CM1(k, a, bb) tmp[k] = (a) * f32x2{(bb).x, (bb).x}; SCHED()
#define CM2(k, d, a, bn) d = fma2(f32x2{(a).y, (a).x}, bn, tmp[k]); SCHED()
#define CV(i) hr[i] = pkh(f32x2{e[2 * (i)].x, e[2 * (i) + 1].x}); hi[i] = pkh(f32x2{e[2 * (i)].y, e[2 * (i) + 1].y}); SCHED()
#define W4(a, v, i) *(LAS u32x4*)(size_t)(a) = u32x4{v[i], v[(i) + 1], v[(i) + 2], v[(i) + 3]}; SCHED()
    const f32x2 w8n = cneg(w8), w64n = cneg(w64), z1n = cneg(z1), zcn = cneg(zc);
    f32x2 z2, z4, zd, zq;
    SCHED();
    CM1(0, w8, w8); CM1(1, z1, z1); CM1(2, zc, zc); CM1(3, e[32], z1); CM1(4, e[32], zc); CM1(5, w8, w64); CM1(6, w64, w64);
    CM2(0, e[2], w8, w8n); CM2(1, z2, z1, z1n); CM2(2, zd, zc, zcn); CM2(3, e[33], e[32], z1n); CM2(4, e[40], e[32], zcn);
    CM2(5, e[9], w8, w64n); CM2(6, e[16], w64, w64n);
    const f32x2 e2n = cneg(e[2]), z2n = cneg(z2), zdn = cneg(zd);
    SCHED();
    CM1(0, e[2], w8); CM1(1, e[2], e[2]); CM1(2, z2, z2); CM1(3, zd, zd); CM1(4, e[32], z2); CM1(5, e[33], z2); CM1(6, e[32], zd); CM1(7, e[40], zd);
    CM2(0, e[3], e[2], w8n); CM2(1, e[4], e[2], e2n); CM2(2, z4, z2, z2n); CM2(3, zq, zd, zdn); CM2(4, e[34], e[32], z2n);
    CM2(5, e[35], e[33], z2n); CM2(6, e[44], e[32], zdn); CM2(7, e[46], e[40], zdn);
    const f32x2 e4n = cneg(e[4]), z4n = cneg(z4), zqn = cneg(zq);
    SCHED();
    CM1(0, e[1], e[4]); CM1(1, e[2], e[4]); CM1(2, e[3], e[4]); CM1(3, e[32], z4); CM1(4, e[33], z4); CM1(5, e[34], z4); CM1(6, e[35], z4); CM1(7, e[32], zq);
    CM2(0, e[5], e[1], e4n); CM2(1, e[6], e[2], e4n); CM2(2, e[7], e[3], e4n); CM2(3, e[36], e[32], z4n); CM2(4, e[37], e[33], z4n);
    CM2(5, e[38], e[34], z4n); CM2(6, e[39], e[35], z4n); CM2(7, e[48], e[32], zqn);
    CV(0); CV(1); CV(2); CV(3); W4(tv[0], hr, 0); CV(16); CV(17); W4(tv[4], hi, 0); CV(18); CV(19);
    CM1(0, e[34], zc); W4(tm[0], hr, 16); CM1(1, e[36], zc); CM1(2, e[38], zc); CM1(3, e[36], zd); CM1(4, e[40], zq); W4(tm[2], hi, 16);
    CM1(5, e[44], zq); CM1(6, e[46], zq); CM1(7, e[48], zq);
    CM2(0, e[41], e[34], zcn); CM2(1, e[42], e[36], zcn); CM2(2, e[43], e[38], zcn); CM2(3, e[45], e[36], zdn); CM2(4, e[49], e[40], zqn);
    CM2(5, e[50], e[44], zqn); CM2(6, e[51], e[46], zqn); CM2(7, e[52], e[48], zqn);
    CM1(0, e[42], zd); CM1(1, e[9], w64); CM1(2, e[2], w64); CM1(3, e[3], w64); CM1(4, e[4], w64); CM1(5, e[5], w64); CM1(6, e[6], w64); CM1(7, e[7], w64);
    CM2(0, e[47], e[42], zdn); CM2(1, e[17], e[9], w64n); CM2(2, e[10], e[2], w64n); CM2(3, e[11], e[3], w64n); CM2(4, e[12], e[4], w64n);
    CM2(5, e[13], e[5], w64n); CM2(6, e[14], e[6], w64n); CM2(7, e[15], e[7], w64n);
    CV(24); CV(25);
    *(LAS u32x4*)(size_t)tm2 = u32x4{hr[24], hr[25], hi[24], hi[25]};
    SCHED();
    CV(20); CV(21); CV(22); CV(23); W4(tm[1], hr, 20); CV(4); CV(5); W4(tm[3], hi, 20); CV(6); CV(7);
    CM1(0, e[16], w64); W4(tv[1], hr, 4); CM1(1, e[17], w64); CM1(2, e[10], w64); CM1(3, e[11], w64); CM1(4, e[12], w64); W4(tv[5], hi, 4);
    CM1(5, e[13], w64); CM1(6, e[14], w64); CM1(7, e[15], w64);
    CM2(0, e[24], e[16], w64n); CM2(1, e[25], e[17], w64n); CM2(2, e[18], e[10], w64n); CM2(3, e[19], e[11], w64n); CM2(4, e[20], e[12], w64n);
    CM2(5, e[21], e[13], w64n); CM2(6, e[22], e[14], w64n); CM2(7, e[23], e[15], w64n);
    u32x4 fm[8], fv[8];
    u32 ra0 = rm[0], ra1 = rm[1];
#define RM(s_) { const u32x2 lo = ldstr((LAS char*)(size_t)ra0), hi_ = ldstr((LAS char*)(size_t)ra1); fm[s_] = u32x4{lo.x, lo.y, hi_.x, hi_.y}; ra0 += rms; ra1 += rms; } SCHED()
    CM1(2, e[18], w64); RM(0); CM1(3, e[19], w64); CM1(4, e[20], w64); RM(1); CM1(5, e[21], w64); CM1(6, e[22], w64); RM(2); CM1(7, e[23], w64);
    CV(8); CV(9); RM(3); CV(10); CV(11); W4(tv[2], hr, 8);
    CM2(2, e[26], e[18], w64n); CM2(3, e[27], e[19], w64n); RM(4); CM2(4, e[28], e[20], w64n); W4(tv[6], hi, 8); CM2(5, e[29], e[21], w64n);
    CM2(6, e[30], e[22], w64n); RM(5); CM2(7, e[31], e[23], w64n);
    CV(12); RM(6); CV(13); CV(14); RM(7); CV(15); W4(tv[3], hr, 12); W4(tv[7], hi, 12);
#pragma unroll
    for (int s = 0; s < 8; ++s) {
      const u32x2 lo = ldstr((LAS char*)(size_t)rv[0] + s * 1024), hi_ = ldstr((LAS char*)(size_t)rv[1] + s * 1024);
      fv[s] = u32x4{lo.x, lo.y, hi_.x, hi_.y};
    }
    float k2048 = e[52].x;
    k2048 += dppz<0x111, 0xf>(k2048); k2048 += dppz<0x112, 0xf>(k2048); k2048 += dppz<0x114, 0xf>(k2048); k2048 += dppz<0x118, 0xf>(k2048);
    k2048 += dppz<0x142, 0xa>(k2048); k2048 += dppz<0x143, 0xc>(k2048);
    SCHED();
    STAMP(4);
    f32x16 kk;
#pragma unroll
    for (int r = 0; r < 16; ++r) kk[r] = 0.f;
#pragma unroll
    for (int s = 0; s < 8; ++s) kk = mfma32h(fm[s], fv[s], kk);
    LAS float* kc = (LAS float*)(lds + M_KC + w * 2816);
    *(LAS f32x4*)(kc + 8 * t + 4 * h) = f32x4{kk[0], kk[1], kk[2], kk[3]} * sc;
    *(LAS f32x2*)(kc + (h ? 384 + 2 * t : 256 + 4 * t)) = f32x2{kk[4], kk[5]} * sc;
    *(LAS f32x2*)(kc + (h ? 448 + 2 * t : 258 + 4 * t)) = f32x2{kk[6], kk[7]} * sc;
    LAS float* k3 = kc + (h ? 671 - t : 512 + t);
    k3[0] = (h ? k2048 : kk[8]) * sc;
    if (h == 0) { k3[32] = kk[9] * sc; k3[64] = kk[10] * sc; k3[96] = kk[11] * sc; }
  }
  STAMP(5);

  u32x4 wfr[28];
  const int w2off = WS_W2F + (((n2 * 56 + 7 * w) * 4) << 10);
  auto w2ld = [&](int i) { wfr[i] = ld128s(rs, l * 16 + (i & 3) * 1024, w2off + (i >> 2) * 4096); };
  f32x2 kx[8];
  u32 kpk[4];
  auto expand_rd = [&](int e) { LAS float* q = (LAS float*)(size_t)ka_[e]; kx[e] = f32x2{q[0], q[704]}; };
  auto expand_pk = [&](int e) {
    float a = kx[2 * e].x + kx[2 * e].y, c = kx[2 * e + 1].x + kx[2 * e + 1].y;
    PIN(a); PIN(c);
    kpk[e] = pk2(a, c);
    PIN(kpk[e]);
  };
  auto expand_wr = [&]() { *(LAS u32x4*)(lds + CV_K + uch * 5120 + 1024 + ui * 16) = u32x4{kpk[0], kpk[1], kpk[2], kpk[3]}; };
  u32x4 u0;
  u32x2 u1;
  auto load_u = [&](int k) {
    if (k == 0) u0 = ld128s(rs, ui * 16, WS_XNR + (2 * b + uch) * 4096);
    else u1 = ld64s(rs, uo1, WS_XNR + (2 * b + uch) * 4096);
  };
  auto copy_u = [&](int s) {
    const u32 dd[6] = {u0.x, u0.y, u0.z, u0.w, u1.x, u1.y};
    const int o = s >> 1;
    u32x4 v;
#pragma unroll
    for (int k = 0; k < 4; ++k) v[k] = (s & 1) ? __builtin_amdgcn_alignbit(dd[o + k + 1], dd[o + k], 16) : dd[o + k];
    *(LAS u32x4*)(lds + CV_U + uch * CV_UCH + s * CV_CS + ui * 16) = v;
  };
#define W2Q 97
#define NW2AV 15
  constexpr int NW2A = NW2AV;

  if (g1) {
    f32x4 acc[4][4];
#pragma unroll
    for (int i = 0; i < 4; ++i)
#pragma unroll
      for (int j = 0; j < 4; ++j) acc[i][j] = f32x4{0.f, 0.f, 0.f, 0.f};
    u32x4 fa[2][4], fb[2][4];
    auto rdA = [&](int st, int s, int i) {
      LAS char* q = lds + (st % 3 == 0 ? wa[i & 1] : st % 3 == 1 ? wa1[i & 1] : wa2[i & 1]) + (i >> 1) * 16384 + s * 8192;
      const u32x2 lo = ldstr(q), hi = ldstr(q + 1024);
      fa[s][i] = u32x4{lo.x, lo.y, hi.x, hi.y};
    };
    auto rdB = [&](int st, int s, int j) { fb[s][j] = lds128(lds + (st % 3 == 2 ? xa2[s] : xa[s] + (st % 3) * G1_SLOT) + j * 2048); };
    const int so_cs = (mt & 2) == 0 ? WS_CS + (ft * 8 + kt0) * 1024 + w * 128 : OOB;
    const int so_p0 = w == 0 ? FL_P0 + b * FS : OOB;

    if (w >= 4) __builtin_amdgcn_s_setprio(1);
    LBAR();
    STAMP(6);
    rdB(0, 0, 0); rdA(0, 0, 0); rdA(0, 0, 1); rdA(0, 0, 2); rdA(0, 0, 3); rdB(0, 0, 1); rdB(0, 0, 2); rdB(0, 0, 3);
    dmaw(IC<2>{}, 0, w); dmaw(IC<2>{}, 1, w); dmaw(IC<2>{}, 2, w);
    u32x2 fl;
    u32 msv[4];
    float nmv[4];
    f32x4 csp[2];
    u32x4 cw[4];
    f32x4 cs, cv[4];
    static_for<8>([&](auto st_) {
      constexpr int st = decltype(st_)::v;
#pragma unroll
      for (int s = 0; s < 2; ++s) {
        const int nst = s ? st + 1 : st, ns = s ^ 1;
        if (s == 1 && st < 7) {
          if (st == 1 || st == 3 || st == 4) WAIT_V(7);
          else if (st == 6) WAIT_V(11);
          else WAIT_V(6);
        }
        if (s == 1) {
          LBAR();
          if (st == 2) STAMP(18);
          if (st == 5) STAMP(21);
        }
        SCHED();
#pragma unroll
        for (int n = 0; n < 16; ++n) {
          acc[n & 3][n >> 2] = mfma16(fa[s][n & 3], fb[s][n >> 2], acc[n & 3][n >> 2]);
          SCHED();
          if (nst < 8) {
            if (n == 0) rdB(nst, ns, 3);
            if (n == 1) rdB(nst, ns, 2);
            if (n == 2) rdB(nst, ns, 1);
            if (n == 3) rdA(nst, ns, 3);
            if (n == 5) rdA(nst, ns, 2);
            if (n == 7) rdA(nst, ns, 1);
            if (n == 9) rdA(nst, ns, 0);
            if (n == 10) rdB(nst, ns, 0);
          }
          if (nst + 2 < 8) {
            const int pc = (s ? 0 : 3);
            if (n == 4 || n == 8 || n == 12) { if (s) dm0(IC<(st + 3) & 7>{}, pc + (n >> 2) - 1, w); else dm0(IC<(st + 2) & 7>{}, pc + (n >> 2) - 1, w); }
            if (n == 6 || n == 11 || n == 14) { const int k = n == 6 ? 0 : n == 11 ? 1 : 2; if (s) dgo(IC<(st + 3) & 7>{}, pc + k, w); else dgo(IC<(st + 2) & 7>{}, pc + k, w); }
          }
          SCHED();
          const int q = st * 32 + s * 16 + n;
          if (q == 4 || q == 8 || q == 12 || q == 13) cw[q == 13 ? 3 : (q >> 2) - 1] = lds128((LAS char*)(size_t)a_cw + (q == 13 ? 3 : (q >> 2) - 1) * 4096);
          if (q == 20) cs = mfma16(sel, cw[0], f32x4{0.f, 0.f, 0.f, 0.f});
          if (q == 24 || q == 28 || q == 29) cs = mfma16(sel, cw[q == 29 ? 3 : (q >> 2) - 5], cs);
          if (q == 36) {
#pragma unroll
            for (int e = 0; e < 4; ++e) { const float v = cs[e]; cs[e] = v + dppx<0x114>(v); }
            PIN(cs);
          }
          if (q == 40) {
#pragma unroll
            for (int e = 0; e < 4; ++e) { const float v = cs[e]; cs[e] = v + dppx<0x118>(v); }
            PIN(cs);
          }
          if (q == 45) st128s(__builtin_bit_cast(u32x4, cs), rs, o_cq, so_cs);
          if (q >= 52 && q < 84 && (q & 3) == 0) expand_rd((q - 52) >> 2);
          if (q >= 92 && q < 124 && (q & 7) == 4) { PIN(kx[(q - 92) >> 2]); expand_pk((q - 92) >> 3); }
          if (q == 200) expand_wr();
          if (q == 85) st64s(tag, rs, o_l0, so_p0);
          if (q == 143) asm volatile("global_load_dwordx2 %0, %1, %2 sc1" : "=v"(fl) : "v"(p0off), "s"(p.ws + FL_P0) : "memory");
          if (q == 173) {
            asm volatile("s_waitcnt vmcnt(5)" : "+v"(fl) :: "memory");
            if (!__all(fl.x == tag.x && fl.y == tag.y)) poll(rs, p0off, FL_P0, tag);
          }
          if (q == 176 || q == 178) load_u((q - 176) >> 1);
          if (q == 180 || q == 182) csp[(q - 180) >> 1] = __builtin_bit_cast(f32x4, ld128s(rs, o_cs, WS_CS + (ft * 8 + 2 * (q - 180)) * 1024));
          if (q == 184) msv[0] = ld32s(rs, l * 4, WS_ST + (mt * 128 + 64 * wt) * 4);
          if (q >= 187 && ((q - 187) & 3) == 0 && ((q - 187) >> 2) < NW2A) w2ld((q - 187) >> 2);
          if (q == 205 || q == 213 || q == 221 || q == 229) copy_u((q - 205) >> 3);
          if (q == 209) {
            PIN(csp[0]); PIN(csp[1]);
            csp[0] += csp[1];
            const auto r0 = __builtin_amdgcn_permlane32_swap(f2u(csp[0][0]), f2u(csp[0][2]), false, false);
            const auto r1 = __builtin_amdgcn_permlane32_swap(f2u(csp[0][1]), f2u(csp[0][3]), false, false);
            csp[0][0] = u2f(r0[0]) + u2f(r0[1]);
            csp[0][1] = u2f(r1[0]) + u2f(r1[1]);
            PIN(csp[0]);
          }
          if (q == 217) {
            const auto r = __builtin_amdgcn_permlane16_swap(f2u(csp[0][0]), f2u(csp[0][1]), false, false);
            *(LAS float*)(size_t)a_cs = u2f(r[0]) + u2f(r[1]);
          }
          if (q == 220) {
            PIN(msv[0]);
            const auto m = __builtin_amdgcn_permlane32_swap(msv[0], msv[0], false, false);
            msv[0] = m[0]; msv[2] = m[1];
            PIN(msv[0]); PIN(msv[2]);
          }
          if (q == 225 || q == 228) {
            const int j = q == 225 ? 0 : 2;
            const auto m = __builtin_amdgcn_permlane16_swap(msv[j], msv[j], false, false);
            msv[j] = m[0]; msv[j + 1] = m[1];
            PIN(msv[j]); PIN(msv[j + 1]);
          }
          if (q >= 232 && q < 236) { cv[q - 232] = *(LAS f32x4*)((LAS char*)(size_t)a_cv + (q - 232) * 64); }
          if (q >= 236 && q < 240) { nmv[q - 236] = -b2f(msv[q - 236]); PIN(nmv[q - 236]); }
          if (q >= 240) {
            const int m = (n + 8) & 15, ii = m & 3, jj = m >> 2;
#pragma unroll
            for (int r = 0; r < 4; ++r) {
              float v = __builtin_fmaf(nmv[jj], cv[ii][r], acc[ii][jj][r]);
              PIN(v);
              acc[ii][jj][r] = v;
            }
          }
          SCHED();
        }
      }
    });
    STAMP(7);
    __builtin_amdgcn_s_setprio(3);
#pragma unroll
    for (int j = 0; j < 4; ++j) {
      const float sd = bhi(msv[j]), sd2 = sd * sd;
      const float ce = -1.44269504f * __builtin_amdgcn_rcpf(sd);
      u32x4 o;
#pragma unroll
      for (int r = 0; r < 4; ++r) {
        const f32x2 gg = {acc[r >> 1][j][2 * (r & 1)], acc[r >> 1][j][2 * (r & 1) + 1]};
        const f32x2 uu = {acc[2 + (r >> 1)][j][2 * (r & 1)], acc[2 + (r >> 1)][j][2 * (r & 1) + 1]};
        const f32x2 a = gg * f32x2{ce, ce};
        const f32x2 e = {__builtin_amdgcn_exp2f(a.x), __builtin_amdgcn_exp2f(a.y)};
        const f32x2 d = fma2(e, f32x2{sd2, sd2}, f32x2{sd2, sd2});
        const f32x2 q = {__builtin_amdgcn_rcpf(d.x), __builtin_amdgcn_rcpf(d.y)};
        o[r] = pk2v((gg * uu) * q);
      }
      st128s(o, rs, l * 16, WS_H + (((mt * 8 + 4 * wt + j) * 56 + ft * 4 + wf) << 10));
      SCHED();
    }
    __builtin_amdgcn_s_setprio(1);
    STAMP(8);
  } else {
    poll(rs, p0off, FL_P0, tag);
    if (b == 255 && w == 0) {
#pragma unroll 1
      for (int i = 0; i < 4; ++i) poll(rs, (64 * i + l) * FS, FL_P0, tag);
      if (l == 0) st64s(tag, rs, 0, FL_EP);
    }
    LBAR();
    load_u(0); load_u(1);
#pragma unroll
    for (int e = 0; e < 8; ++e) expand_rd(e);
#pragma unroll
    for (int e = 0; e < 4; ++e) expand_pk(e);
    LBAR();
    expand_wr();
#pragma unroll
    for (int s = 0; s < 4; ++s) copy_u(s);
    SCHED();
#pragma unroll
    for (int i = 0; i < NW2A; ++i) w2ld(i);
    __builtin_amdgcn_s_waitcnt(0x0F70);
    LBAR();
  }

  STAMP(10);
  u32x2 hfl;
  u32x4 hf[28];
  const int hoff = WS_H + ((4 * m2 * 56 + 7 * w) << 10);
  const int jr = 0;
  auto hld = [&](int n) { hf[n] = ld128s(rs, l * 16, hoff + (((n / 7) ^ jr) * 56 + n % 7) * 1024); };
  const int hfs = FL_H + (m2 * 56 + 7 * w) * FS;
#define PDV 12
  constexpr int PD = PDV;
  {
    LAS char *ua = (LAS char*)(size_t)a_ua, *ka = (LAS char*)(size_t)a_ka;
    *(LAS u32x4*)(size_t)a_zk = zero4;
    *(LAS u32x4*)(size_t)a_zu = zero4;
    asm volatile("" ::: "memory");
    f32x4 acc[4];
#pragma unroll
    for (int i = 0; i < 4; ++i) acc[i] = f32x4{0.f, 0.f, 0.f, 0.f};
    const int so_h = g1 ? FL_H + ((mt * 2 + wt) * 56 + ft * 4 + wf) * FS : OOB;
    auto hook = [&](int rr, int c) {
#define HPUB 3
#define HPOLL 24
      const int sq = rr * 8 + c;
#define WS0 4
#define WSN 13
      auto nw = [&](int z) { return z <= WS0 ? 0 : z >= WS0 + WSN ? 28 - NW2A : ((z - WS0) * (28 - NW2A) + WSN - 1) / WSN; };
      if (sq == HPUB) {
        constexpr int c0 = HPUB <= WS0 ? 0 : HPUB >= WS0 + WSN ? 28 - NW2A : ((HPUB - WS0) * (28 - NW2A) + WSN - 1) / WSN;
        asm volatile("s_waitcnt vmcnt(%0)" :: "i"(c0) : "memory");
        st64s(tag, rs, o_l0, so_h);
        __builtin_amdgcn_s_setprio(0);
        STAMP(9);
      }
      if (sq >= 0 && nw(sq + 1) > nw(sq)) w2ld(NW2A + nw(sq));
      if (sq == (HPOLL < 32 ? HPOLL - 5 : 31)) hfl = ld64s(rs, hfo, hfs);
      if (sq == HPOLL) {
        STAMP(2);
        if (!__all(hfl.x == tag.x && hfl.y == tag.y)) poll(rs, hfo, hfs, tag);
        asm volatile("" ::: "memory");
        STAMP(23);
      }
      if (sq >= HPOLL) {
#pragma unroll
        for (int n = (sq - HPOLL) * PD / (HPOLL < 32 ? 32 - HPOLL : 1); n < (sq + 1 - HPOLL) * PD / (HPOLL < 32 ? 32 - HPOLL : 1); ++n) hld(n);
      }
    };
    conv_tiles(ua, ka, acc, hook);
#pragma unroll
    for (int i = 0; i < 4; ++i) *(LAS f32x4*)(lds + CV_X + (w * 4 + i) * 1024 + l * 16) = acc[i];
    if (tid == 0) *(LAS u32*)(lds + M_CNT) = 0u;
  }
  LBAR();
  STAMP(11);
  int ytile;
  {
    const int ts = (w >> 1) & 1, i = (w & 1) + 2 * (w >> 2);
    ytile = 7 - 2 * i - ts;
    const int pos = 256 * ytile + 16 * l15 + 4 * g;
    f32x4 y0[2], y1[2];
    u32x2 ur[2];
#pragma unroll
    for (int ch = 0; ch < 2; ++ch) {
      y0[ch] = *(LAS f32x4*)(lds + CV_X + ((ch + 2 * ts) * 4 + i) * 1024 + l * 16);
      y1[ch] = *(LAS f32x4*)(lds + CV_X + ((ch + 2 * ts + 4) * 4 + i) * 1024 + l * 16);
      ur[ch] = lds64(lds + CV_U + ch * CV_UCH + (2044 - pos) * 2);
    }
    LBAR();
    float res[2][4];
#pragma unroll
    for (int ch = 0; ch < 2; ++ch) {
      const float dsk = ch ? bhi(dk) : b2f(dk);
      const f32x4 y = y0[ch] + y1[ch];
      res[ch][0] = __builtin_fmaf(bhi(ur[ch].y), dsk, y[0]); res[ch][1] = __builtin_fmaf(b2f(ur[ch].y), dsk, y[1]);
      res[ch][2] = __builtin_fmaf(bhi(ur[ch].x), dsk, y[2]); res[ch][3] = __builtin_fmaf(b2f(ur[ch].x), dsk, y[3]);
    }
    const u32x4 o = {pk2(res[0][0], res[1][0]), pk2(res[0][1], res[1][1]), pk2(res[0][2], res[1][2]), pk2(res[0][3], res[1][3])};
    st128s(o, rs, pos * 4, WS_Y + b * 8192);
    if (HPOLL == 32) {
      STAMP(2);
      if (!__all(hfl.x == tag.x && hfl.y == tag.y)) poll(rs, hfo, hfs, tag);
      asm volatile("" ::: "memory");
      STAMP(23);
#pragma unroll
      for (int n = 0; n < PD; ++n) hld(n);
    }
  }
  STAMP(12);

  {
    const int oj = w >> 1, rh = w & 1;
    const int etok = 64 * m2 + 16 * (oj ^ jr) + l15, ech = 64 * n2 + 16 * g + 8 * rh;
    u32x4 xv;
    u32 yo = (ech >> 1) * 8192 + etok * 4, yfv = (8 * g + (l & 3)) * FS;
    PIN(yo); PIN(yfv);
    SCHED();
    f32x4 acc[4][4];
#pragma unroll
    for (int i = 0; i < 4; ++i)
#pragma unroll
      for (int j = 0; j < 4; ++j) acc[i][j] = f32x4{0.f, 0.f, 0.f, 0.f};
    u32x2 yfl;
    const int yfo = FL_Y + ((m2 >> 2) * 256 + 32 * n2 + 4 * rh) * FS;
    LAS char* pw = opaque(lds + w * 16384 + l * 8);
    LAS char* pr = opaque(lds + w * 2048 + l * 8);
    LAS char* pr2 = opaque(pr + 65536);
    u32 a_cnt = lds0 + M_CNT + l * 4, one = 1u;
    PIN(a_cnt); PIN(one);
    auto part_wr = [&](int j, int k) {
      *(LAS f32x2*)(pw + (j * 8 + k) * 512) = f32x2{acc[k & 3][j][2 * (k >> 2)], acc[k & 3][j][2 * (k >> 2) + 1]};
    };
#pragma unroll
    for (int n = 0; n < 28; ++n) {
#pragma unroll
      for (int i = 0; i < 4; ++i) {
        acc[i][n / 7] = mfma16(wfr[4 * (n % 7) + i], hf[n], acc[i][n / 7]);
        SCHED();
        if (i == 3 && n + PD < 28) hld(n + PD);
        if ((i & 1) == 0 && n >= 8 && n % 7 >= 1 && n % 7 < 5) part_wr(n / 7 - 1, (n % 7 - 1) * 2 + (i >> 1));
        SCHED();
      }
      if (n == YP1 && w >= 4) { asm volatile("s_waitcnt vmcnt(%0)" :: "i"(YP1 + 1) : "memory"); st64s(tag, rs, o_l0, FL_Y + (ytile * 256 + b) * FS); }
      if (n == YP0 && w < 4) { asm volatile("s_waitcnt vmcnt(%0)" :: "i"(YP0 + 1) : "memory"); st64s(tag, rs, o_l0, FL_Y + (ytile * 256 + b) * FS); }
      if (n == 21) xv = *(const GAS u32x4*)((const char*)p.x + (size_t)etok * 1024 + ech * 2);
      if (n == 22) yfl = ld64s(rs, yfv, yfo);
      if (n == TB && w >= 4) LBAR();
      SCHED();
    }
    STAMP(13);
#pragma unroll
    for (int k = 0; k < 8; ++k) part_wr(3, k);
    asm volatile("" ::: "memory");
    if (w < 4) LBAR();
    else asm volatile("ds_add_u32 %0, %1" :: "v"(a_cnt), "v"(one) : "memory");
    if (!__all(yfl.x == tag.x && yfl.y == tag.y)) poll(rs, yfv, yfo, tag);
    asm volatile("" ::: "memory");
    u32 yv[4];
#pragma unroll
    for (int j = 0; j < 4; ++j) yv[j] = ld32s(rs, yo, WS_Y + j * 8192);
    f32x2 pp[8][4];
#pragma unroll
    for (int ww = 0; ww < 4; ++ww)
#pragma unroll
      for (int i = 0; i < 4; ++i) pp[ww][i] = *(volatile LAS f32x2*)(pr + ww * 16384 + i * 512);
    if (w >= 4) {
      volatile LAS u32* cnt = (volatile LAS u32*)(lds + M_CNT);
#pragma unroll 1
      for (int spin = 0; spin < (1 << 20) && *cnt < 4u; ++spin) {}
      asm volatile("" ::: "memory");
    }
    STAMP(14);
#pragma unroll
    for (int ww = 0; ww < 4; ++ww)
#pragma unroll
      for (int i = 0; i < 4; ++i) pp[4 + ww][i] = *(volatile LAS f32x2*)(pr2 + ww * 16384 + i * 512);
    SCHED();
    f32x2 sm[4];
#pragma unroll
    for (int i = 0; i < 4; ++i) sm[i] = ((pp[0][i] + pp[1][i]) + (pp[2][i] + pp[3][i])) + ((pp[4][i] + pp[5][i]) + (pp[6][i] + pp[7][i]));
    const u32 ff[4] = {pk2(sm[0].x, sm[1].x), pk2(sm[2].x, sm[3].x), pk2(sm[0].y, sm[1].y), pk2(sm[2].y, sm[3].y)};
    u32x4 r;
#pragma unroll
    for (int j = 0; j < 4; ++j) {
      const u32 s = pk2v(up2(xv[j]) + up2(yv[j]));
      r[j] = pk2v(up2(s) + up2(ff[j]));
    }
    *(GAS u32x4*)((char*)p.out + (size_t)etok * 1024 + ech * 2) = r;
  }
  STAMP(15);
  STAMP_DUMP();
}

extern "C" void kernel_launch(void* const* d_in, const int* in_sizes, int n_in, void* d_out, int out_size, void* d_ws,
                              size_t ws_size, hipStream_t stream) {
  Params p;
  p.x = (const u16*)d_in[0]; p.lar = (const u16*)d_in[1]; p.aim = (const u16*)d_in[2]; p.cre = (const u16*)d_in[3];
  p.cim = (const u16*)d_in[4]; p.ldt = (const u16*)d_in[5]; p.dsk = (const u16*)d_in[6];
  p.w1 = (const u16*)d_in[8]; p.w3 = (const u16*)d_in[9]; p.w2 = (const u16*)d_in[10];
  p.out = (u16*)d_out; p.ws = (char*)d_ws + ((ws_size / 2) & ~(size_t)4095);
  fused<<<256, 512, 0, stream>>>(p);
}
```

```cpp
#include <hip/hip_runtime.h>
#include <hip/hip_bf16.h>

typedef unsigned short u16;
typedef unsigned int u32;
typedef unsigned long long u64;
typedef __bf16 bf16x8 __attribute__((ext_vector_type(8)));
typedef __bf16 bf16x2 __attribute__((ext_vector_type(2)));
typedef _Float16 f16x8 __attribute__((ext_vector_type(8)));
typedef _Float16 f16x2 __attribute__((ext_vector_type(2)));
typedef short s16x4 __attribute__((ext_vector_type(4)));
typedef float f32x2 __attribute__((ext_vector_type(2)));
typedef float f32x4 __attribute__((ext_vector_type(4)));
typedef float f32x16 __attribute__((ext_vector_type(16)));
typedef u32 u32x2 __attribute__((ext_vector_type(2)));
typedef u32 u32x4 __attribute__((ext_vector_type(4)));
typedef u64 u64x4 __attribute__((ext_vector_type(4)));
typedef u64 u64x8 __attribute__((ext_vector_type(8)));
typedef __amdgpu_buffer_rsrc_t rsrc_t;
#define LAS __attribute__((address_space(3)))
#define GAS __attribute__((address_space(1)))
#define DEV __device__ __forceinline__

#define NTQ " nt"
#define TB 21
#define YP1 1
#define YP0 10
constexpr int L = 2048, D = 512, NM = 128, F = 1792;
constexpr int LDS_BYTES = 163840;
constexpr int SC1 = 16;

constexpr int WS_H = 0;
constexpr int WS_XNR = 8 << 20;
constexpr int WS_W2F = 10 << 20;
constexpr int WS_Y = 12 << 20;
constexpr int WS_ST = 14 << 20;
constexpr int WS_CS = WS_ST + 8192;
constexpr int WS_FL = 15 << 20;
constexpr int FS = 128;
constexpr int FL_EP = WS_FL;
constexpr int FL_P0 = WS_FL + 4096;
constexpr int FL_H = FL_P0 + 256 * FS;
constexpr int FL_Y = FL_H + 2048 * FS;
constexpr int WS_BYTES = 16 << 20;

struct Params {
  const u16 *x, *lar, *aim, *cre, *cim, *ldt, *dsk, *w1, *w3, *w2;
  u16* out;
  char* ws;
};

template <int V> struct IC { static constexpr int v = V; };
template <int N, int I = 0, class Fn>
DEV void static_for(Fn&& fn) { if constexpr (I < N) { fn(IC<I>{}); static_for<N, I + 1>(fn); } }
DEV u32 f2u(float f) { return __builtin_bit_cast(u32, f); }
DEV float u2f(u32 v) { return __builtin_bit_cast(float, v); }
DEV float b2f(u32 v) { return __builtin_bit_cast(float, v << 16); }
DEV float bhi(u32 v) { return __builtin_bit_cast(float, v & 0xffff0000u); }
DEV u32 pk2v(f32x2 f) { return __builtin_bit_cast(u32, __builtin_convertvector(f, bf16x2)); }
DEV u32 pk2(float lo, float hi) { return pk2v(f32x2{lo, hi}); }
DEV f32x2 up2(u32 v) { return f32x2{b2f(v), bhi(v)}; }
DEV float rb(float f) { return b2f(pk2(f, f) & 0xffffu); }
DEV f32x2 fma2(f32x2 a, f32x2 b, f32x2 c) { return __builtin_elementwise_fma(a, b, c); }
DEV f32x16 mfma32(u32x4 a, u32x4 b, f32x16 c) {
  return __builtin_amdgcn_mfma_f32_32x32x16_bf16(__builtin_bit_cast(bf16x8, a), __builtin_bit_cast(bf16x8, b), c, 0, 0, 0);
}
DEV f32x4 mfma16(u32x4 a, u32x4 b, f32x4 c) {
  return __builtin_amdgcn_mfma_f32_16x16x32_bf16(__builtin_bit_cast(bf16x8, a), __builtin_bit_cast(bf16x8, b), c, 0, 0, 0);
}
DEV f32x16 mfma32h(u32x4 a, u32x4 b, f32x16 c) {
  return __builtin_amdgcn_mfma_f32_32x32x16_f16(__builtin_bit_cast(f16x8, a), __builtin_bit_cast(f16x8, b), c, 0, 0, 0);
}
DEV u32x4 lds128(LAS char* p) { return *(LAS u32x4*)p; }
DEV u32x2 lds64(LAS char* p) { return *(LAS u32x2*)p; }
DEV u32x2 ldstr(LAS char* p) { return __builtin_bit_cast(u32x2, __builtin_amdgcn_ds_read_tr16_b64_v4i16((LAS s16x4*)p)); }
template <int LOFF>
DEV void dma_m0(u32 lbase) { asm volatile("s_add_u32 m0, %0, %1" :: "s"(lbase), "i"(LOFF) : "memory", "scc", "m0"); }
template <bool NT>
DEV void dma_go(u32x4 rs, u32 voff, u32 soff) {
  if (NT) asm volatile("buffer_load_dwordx4 %0, %1, %2 offen nt lds" :: "v"(voff), "s"(rs), "s"(soff) : "memory");
  else asm volatile("buffer_load_dwordx4 %0, %1, %2 offen lds" :: "v"(voff), "s"(rs), "s"(soff) : "memory");
}
DEV u32x4 mkrs(const void* ptr, u32 bytes) { const u64 a = (u64)ptr; return u32x4{(u32)a, (u32)(a >> 32) & 0xffffu, bytes, 0x00020000u}; }
#define WAIT_V(n) asm volatile("s_waitcnt vmcnt(" #n ")" ::: "memory")
#define WAIT_L0() asm volatile("s_waitcnt lgkmcnt(0)" ::: "memory")
#define BAR() __builtin_amdgcn_s_barrier()
#define LBAR() do { WAIT_L0(); BAR(); asm volatile("" ::: "memory"); } while (0)
#define SCHED() __builtin_amdgcn_sched_barrier(0)
#define PIN(x) asm volatile("" : "+v"(x))
constexpr int OOB = 0x04000000;
#define STAMP(i)
#define STAMP_DECL
#define STAMP_DUMP()

DEV u32x4 ld128s(rsrc_t rs, int voff, int soff) { return __builtin_amdgcn_raw_buffer_load_b128(rs, voff, soff, SC1); }
DEV u32x2 ld64s(rsrc_t rs, int voff, int soff) { return __builtin_amdgcn_raw_buffer_load_b64(rs, voff, soff, SC1); }
DEV u32 ld32s(rsrc_t rs, int voff, int soff) { return __builtin_amdgcn_raw_buffer_load_b32(rs, voff, soff, SC1); }
DEV void st128s(u32x4 v, rsrc_t rs, int voff, int soff) {
  __builtin_amdgcn_raw_buffer_store_b128(v, rs, voff, soff, SC1);
  asm volatile("s_nop 1" :: "v"(v) : "memory");
}
DEV void st64s(u32x2 v, rsrc_t rs, int voff, int soff) { __builtin_amdgcn_raw_buffer_store_b64(v, rs, voff, soff, SC1); }
DEV void st32s(u32 v, rsrc_t rs, int voff, int soff) { __builtin_amdgcn_raw_buffer_store_b32(v, rs, voff, soff, SC1); }
DEV void poll(rsrc_t rs, int voff, int soff, u32x2 tag) {
#pragma unroll 1
  for (int spin = 0; spin < (1 << 18); ++spin) {
    const u32x2 v = ld64s(rs, voff, soff);
    if (__all(v.x == tag.x && v.y == tag.y)) break;
    __builtin_amdgcn_s_sleep(4);
  }
  __builtin_amdgcn_fence(__ATOMIC_ACQUIRE, "wavefront");
  asm volatile("" ::: "memory");
}

template <int CTRL, int RM>
DEV float dppz(float v) { return __builtin_bit_cast(float, __builtin_amdgcn_update_dpp(0, __builtin_bit_cast(int, v), CTRL, RM, 0xf, true)); }
template <int CTRL, int RM>
DEV u32 dppu(u32 v) { return (u32)__builtin_amdgcn_update_dpp(0, (int)v, CTRL, RM, 0xf, true); }
DEV u32 wave_max_u32(u32 v) {
  v = max(v, dppu<0x111, 0xf>(v)); v = max(v, dppu<0x112, 0xf>(v)); v = max(v, dppu<0x114, 0xf>(v)); v = max(v, dppu<0x118, 0xf>(v));
  v = max(v, dppu<0x142, 0xa>(v)); v = max(v, dppu<0x143, 0xc>(v));
  return (u32)__builtin_amdgcn_readlane((int)v, 63);
}

DEV float rdl(float v, int lane) { return __builtin_bit_cast(float, __builtin_amdgcn_readlane(__builtin_bit_cast(int, v), lane)); }
template <int CTRL>
DEV float dppx(float v) { return __builtin_bit_cast(float, __builtin_amdgcn_mov_dpp(__builtin_bit_cast(int, v), CTRL, 0xf, 0xf, true)); }
DEV float rows8_sum(const float (&p)[8], int l) {
  float a[4], c[2];
#pragma unroll
  for (int i = 0; i < 4; ++i) {
    const auto r = __builtin_amdgcn_permlane32_swap(__builtin_bit_cast(u32, p[i]), __builtin_bit_cast(u32, p[i + 4]), false, false);
    a[i] = __builtin_bit_cast(float, (u32)r[0]) + __builtin_bit_cast(float, (u32)r[1]);
  }
#pragma unroll
  for (int i = 0; i < 2; ++i) {
    const auto r = __builtin_amdgcn_permlane16_swap(__builtin_bit_cast(u32, a[i]), __builtin_bit_cast(u32, a[i + 2]), false, false);
    c[i] = __builtin_bit_cast(float, (u32)r[0]) + __builtin_bit_cast(float, (u32)r[1]);
  }
  const bool up = l & 8;
  float v = (up ? c[1] : c[0]) + dppx<0x128>(up ? c[0] : c[1]);
  v += dppx<0xB1>(v); v += dppx<0x4E>(v); v += dppx<0x141>(v);
  return v;
}

constexpr int G1_SLOT = 49152;
constexpr int KG_T = 2 * G1_SLOT;
constexpr int KG_TW = 13312;
constexpr int SPARE = 3 * G1_SLOT;
constexpr int M_CS = SPARE;
constexpr int M_CNT = SPARE + 4096;
constexpr int M_KC = SPARE + 5120;
constexpr int CV_K = SPARE + 5120;
static_assert(KG_T + 4 * KG_TW <= M_KC && M_KC + 4 * 2816 <= LDS_BYTES, "lds");
constexpr int CV_CS = 4160;
constexpr int CV_U = 2 * G1_SLOT;
constexpr int CV_UCH = 4 * CV_CS;
constexpr int CV_X = 0;

DEV f32x2 cmul(f32x2 a, f32x2 b, f32x2 bn) { return fma2(f32x2{a.y, a.x}, bn, a * f32x2{b.x, b.x}); }
DEV f32x2 cneg(f32x2 b) { return f32x2{-b.y, b.y}; }
DEV f32x2 cexp_p(float lre2, float rev, float pw) {
  const float mg = __builtin_amdgcn_exp2f(lre2 * pw);
  const float ph = __builtin_amdgcn_fractf(rev * pw);
  return f32x2{__builtin_amdgcn_cosf(ph), __builtin_amdgcn_sinf(ph)} * f32x2{mg, mg};
}
DEV u32 pkh(f32x2 f) { return __builtin_bit_cast(u32, __builtin_convertvector(f, f16x2)); }
DEV LAS char* opaque(LAS char* q) { u32 v = (u32)(size_t)q; asm volatile("" : "+v"(v)); return (LAS char*)(size_t)v; }

template <class Hook>
DEV void conv_tiles(LAS char* ua, LAS char* ka, f32x4 (&acc)[4], Hook hook) {
  constexpr int ORD[16] = {0, 15, 13, 11, 9, 1, 14, 12, 10, 8, 2, 3, 4, 5, 6, 7};
  u32x4 fr[2][16];
  auto load = [&](int rr, int id) {
    if (id < 8) {
      const u32x2 lo = *(volatile LAS u32x2*)(ua + (7 - id) * 512 + (3 - rr) * 64), hi = *(volatile LAS u32x2*)(ua + (7 - id) * 512 + (3 - rr) * 64 + 8);
      fr[rr & 1][id] = u32x4{lo.x, lo.y, hi.x, hi.y};
    } else {
      fr[rr & 1][id] = lds128(ka + (id - 8) * 512 + (3 - rr) * 64);
    }
  };
#pragma unroll
  for (int k = 0; k < 16; ++k) { load(0, ORD[k]); SCHED(); }
  hook(0, -1);
  SCHED();
#pragma unroll
  for (int rr = 0; rr < 4; ++rr) {
#pragma unroll
    for (int c = 0; c < 8; ++c) {
      u32x4(&f)[16] = fr[rr & 1];
#pragma unroll
      for (int i = 0; i < 4; ++i) {
        if (c <= 7 - 2 * i) { acc[i] = mfma16(f[c], f[8 + 7 - 2 * i - c], acc[i]); SCHED(); }
        if (rr < 3 && i < 2) { load(rr + 1, ORD[2 * c + i]); SCHED(); }
      }
      hook(rr, c);
      SCHED();
    }
  }
}

__global__ __launch_bounds__(512, 2) void fused(Params args) {
  __shared__ __attribute__((aligned(16))) char smem[LDS_BYTES];
  u64x8 ka0;
  u64x4 ka1;
  asm volatile("s_load_dwordx16 %0, %2, 0x0\n\ts_load_dwordx8 %1, %2, 0x40" : "=&s"(ka0), "=&s"(ka1) : "s"(__builtin_amdgcn_kernarg_segment_ptr()) : "memory");
#define CODEPF 23000
  u32x4 pf;
  if (__builtin_amdgcn_readfirstlane(threadIdx.x >> 6) == 4) {
    u64 pc;
    asm volatile("s_getpc_b64 %0" : "=s"(pc));
    const u32 po = min((u32)((blockIdx.x >> 3) * 1024 + (threadIdx.x & 63) * 16), (u32)(CODEPF - 16));
    asm volatile("global_load_dwordx4 %0, %1, %2" : "=v"(pf) : "v"(po), "s"(pc & ~(u64)255) : "memory");
  }
  LAS char* lds = (LAS char*)smem;
  const u32 lds0 = (u32)(size_t)lds;
  const int tid = threadIdx.x, l = tid & 63;
  const int w = __builtin_amdgcn_readfirstlane(tid >> 6);
  const int b = blockIdx.x, xcd = b & 7, rk = b >> 3;
  const bool g1 = rk < 28;
  const int l15 = l & 15, g = l >> 4, t = l & 31, h = l >> 5;
  const int mt = (xcd >> 1) * 4 + (rk & 3), ft = (xcd & 1) * 7 + (rk >> 2);
#define ROTV 5
  const int kt0 = ROTV == 0 ? 0 : ROTV == 1 ? (ft + 2 * (mt & 3) + ((mt >> 2) & 1)) & 7 : ROTV == 2 ? (2 * (xcd >> 1) + 4 * (xcd & 1)) & 7 : (2 * (xcd >> 1) + 4 * (xcd & 1) + (mt & 1)) & 7;
  const int wt = w & 1, wf = w >> 1;
  u32 xo0 = (l >> 3) * 1024 + (((l & 7) ^ g) << 4);
  u32 xo1 = (8 + (l >> 3)) * 1024 + (((l & 7) ^ (4 + g)) << 4);
  u32 wo1 = g * (F * 2) + ((l15 ^ (4 * g + 2 * (w & 1))) << 4), wo1b = wo1 + 4 * F * 2;
  PIN(xo0); PIN(xo1); PIN(wo1); PIN(wo1b);
  u32 tv[8], tm[4], tm2, rv[2], rm[2];
  {
    const u32 tw = lds0 + KG_T + (w & 3) * KG_TW;
#pragma unroll
    for (int k = 0; k < 8; ++k) { tv[k] = tw + l * 128 + ((k ^ (l & 7)) << 4); PIN(tv[k]); }
#pragma unroll
    for (int k = 0; k < 4; ++k) { tm[k] = tw + 8192 + l * 64 + ((k ^ ((l >> 1) & 3)) << 4); PIN(tm[k]); }
    tm2 = tw + 12288 + l * 16;
    PIN(tm2);
    const int blk = g & 1, c = (l >> 2) & 1, pp = l & 3;
#pragma unroll
    for (int t2 = 0; t2 < 2; ++t2) {
      const int m = 4 * h + 2 * t2 + ((l >> 3) & 1);
      rv[t2] = tw + m * 128 + (((4 * c + 2 * blk + (pp >> 1)) ^ m) << 4) + 8 * (pp & 1);
      rm[t2] = blk ? tw + 12288 + m * 16 + 8 * c : tw + 8192 + m * 64 + (((2 * c + (pp >> 1)) ^ (m >> 1)) << 4) + 8 * (pp & 1);
      PIN(rv[t2]); PIN(rm[t2]);
    }
  }
  u32 rms = (g & 1) ? 128 : 512;
  PIN(rms);
  asm volatile("s_waitcnt lgkmcnt(0)" : "+s"(ka0), "+s"(ka1) :: "memory");
  Params p;
  p.x = (const u16*)ka0[0]; p.lar = (const u16*)ka0[1]; p.aim = (const u16*)ka0[2]; p.cre = (const u16*)ka0[3]; p.cim = (const u16*)ka0[4];
  p.ldt = (const u16*)ka0[5]; p.dsk = (const u16*)ka0[6]; p.w1 = (const u16*)ka0[7];
  p.w3 = (const u16*)ka1[0]; p.w2 = (const u16*)ka1[1]; p.out = (u16*)ka1[2]; p.ws = (char*)ka1[3];
  const rsrc_t rs = __builtin_amdgcn_make_buffer_rsrc(p.ws, 0, WS_BYTES, 0x00020000);
  STAMP_DECL;
  STAMP(3);

  u32x2 tag;
  u32 p_ldt2, dk;
  asm volatile("s_load_dwordx2 %0, %3, 0x0\n\ts_load_dword %1, %4, 0x0\n\ts_load_dword %2, %5, 0x0"
               : "=&s"(tag), "=&s"(p_ldt2), "=&s"(dk) : "s"(p.ws + FL_EP), "s"(p.ldt + 2 * b), "s"(p.dsk + 2 * b) : "memory");

  u32 p_lar, p_aim, p_cre, p_cim;
  if (w < 4) {
    const u32 po = (b * 256 + tid) * 2;
    asm volatile("global_load_ushort %0, %1, %2" NTQ : "=v"(p_lar) : "v"(po), "s"(p.lar) : "memory");
    asm volatile("global_load_ushort %0, %1, %2" NTQ : "=v"(p_aim) : "v"(po), "s"(p.aim) : "memory");
    asm volatile("global_load_ushort %0, %1, %2" NTQ : "=v"(p_cre) : "v"(po), "s"(p.cre) : "memory");
    asm volatile("global_load_ushort %0, %1, %2" NTQ : "=v"(p_cim) : "v"(po), "s"(p.cim) : "memory");
  }
  SCHED();

  const u32x4 rx = mkrs(p.x, L * D * 2), rw1 = mkrs(p.w1, D * F * 2), rw3 = mkrs(p.w3, D * F * 2);
#define NTS 0
  auto dm0 = [&](auto st_, int piece, int ww) {
    constexpr int S = (decltype(st_)::v % 3) * G1_SLOT;
    const u32 lb = lds0 + ww * 2048;
    if (piece == 0) dma_m0<S>(lb);
    if (piece == 1) dma_m0<S + 16384>(lb);
    if (piece == 2) dma_m0<S + 32768>(lb);
    if (piece == 3) dma_m0<S + 1024>(lb);
    if (piece == 4) dma_m0<S + 16384 + 1024>(lb);
    if (piece == 5) dma_m0<S + 32768 + 1024>(lb);
  };
  auto dgo = [&](auto st_, int piece, int ww) {
    constexpr bool nt = decltype(st_)::v < NTS;
    const int kt = (kt0 + decltype(st_)::v) & 7;
    const u32 sx = (mt * 128 + ww * 16) * 1024 + kt * 128, sw = (kt * 64 + ww * 8) * (F * 2) + ft * 256;
    if (piece == 0) dma_go<nt>(rx, xo0, sx);
    if (piece == 1) dma_go<nt>(rw1, wo1, sw);
    if (piece == 2) dma_go<nt>(rw3, wo1, sw);
    if (piece == 3) dma_go<nt>(rx, xo1, sx);
    if (piece == 4) dma_go<nt>(rw1, wo1b, sw);
    if (piece == 5) dma_go<nt>(rw3, wo1b, sw);
  };
  auto dmaw = [&](auto st_, int piece, int ww) { dm0(st_, piece, ww); asm volatile("s_nop 0"); dgo(st_, piece, ww); };

  u32x2 wr[8];
  u32x4 xr[8];
  const int un2 = b / 28, ukb = 2 * (b - 28 * un2) + (w & 1);
  const int ax = xcd * 4 + (rk - 28);
  if (w >= 4 && g1) {
#pragma unroll
    for (int i = 0; i < 6; ++i) { dmaw(IC<0>{}, i, w - 4); dmaw(IC<0>{}, i, w); }
    if (w < 6) {
      const char* wb2 = (const char*)p.w2 + (size_t)(32 * ukb) * 1024 + un2 * 128;
      const u32 wv = g * 4096 + l15 * 8;
#define W2RAW(j, base, off) asm volatile("global_load_dwordx2 %0, %1, %2 offset:" #off NTQ : "=v"(wr[j]) : "v"(wv), "s"(base) : "memory")
      W2RAW(0, wb2, 0); W2RAW(1, wb2, 1024); W2RAW(2, wb2, 2048); W2RAW(3, wb2, 3072);
      W2RAW(4, wb2 + 16384, 0); W2RAW(5, wb2 + 16384, 1024); W2RAW(6, wb2 + 16384, 2048); W2RAW(7, wb2 + 16384, 3072);
    }
  }
  if (!g1) {
    const u32 xv_ = (64 * ax + 8 * w) * 1024 + l * 16;
#define XRAW(j, vo, off) asm volatile("global_load_dwordx4 %0, %1, %2 offset:" #off : "=v"(xr[j]) : "v"(vo), "s"(p.x) : "memory")
    XRAW(0, xv_, 0); XRAW(1, xv_, 1024); XRAW(2, xv_, 2048); XRAW(3, xv_, 3072);
    XRAW(4, xv_ + 4096, 0); XRAW(5, xv_ + 4096, 1024); XRAW(6, xv_ + 4096, 2048); XRAW(7, xv_ + 4096, 3072);
  }
  SCHED();

  const int m2 = xcd * 4 + (rk & 3), n2 = rk >> 2;
  const int uch = w >> 2, ui = tid & 255;
  u32 uo1 = ui < 255 ? ui * 16 + 16 : OOB;
  int xa[2], wa[2];
#pragma unroll
  for (int s = 0; s < 2; ++s) xa[s] = (64 * wt + l15) * 128 + (((4 * s + g) ^ ((l15 >> 1) & 7)) << 4);
  {
    const int q = l15 >> 2, pp = l & 3;
#pragma unroll
    for (int i = 0; i < 2; ++i) wa[i] = 16384 + (8 * g + q) * 256 + (((4 * wf + 2 * i + (pp >> 1)) ^ (4 * q + 2 * (g & 1))) << 4) + 8 * (pp & 1);
  }
  u32x4 sel;
#pragma unroll
  for (int e = 0; e < 4; ++e) sel[e] = l15 == 2 * e ? 0x00003f80u : (l15 == 2 * e + 1 ? 0x3f800000u : 0u);
  u32 a_cw = lds0 + 16384 * (1 + (w >> 2)) + (4 * g + (l15 >> 2)) * 256 + (((4 * (w & 3) + (l & 3)) ^ (4 * (l15 >> 2) + 2 * h)) << 4);
  u32 o_cq = (l < 32 && l15 >= 12) ? (l & 3) * 32 + g * 16 : OOB;
  u32 o_l0 = l < 8 ? l * 16 : OOB;
  u32 a_ua = lds0 + CV_U + (w & 1) * CV_UCH + (3 - (l15 & 3)) * CV_CS + 4088 - 8 * (l15 >> 2) - 32 * h + 16 * (g & 1) - 7 * 512 - 256 * (w >> 2) - 3 * 64;
  u32 a_ka = lds0 + CV_K + (w & 1) * 5120 + 1024 - 512 * ((w >> 1) & 1) + 32 * (l15 - h) + 16 * (g & 1) - 256 * (w >> 2) - 3 * 64;
  u32 a_zk = lds0 + CV_K + (w & 1) * 5120 + l * 16;
  u32 a_zu = lds0 + CV_U + (w & 1) * CV_UCH + ((l >> 1) & 3) * CV_CS + 4096 + (l & 1) * 16;
  u32 hfo = (l < 7 ? l : 0) * FS;
  u32x4 zero4 = {0u, 0u, 0u, 0u};
  PIN(a_ua); PIN(a_ka); PIN(a_zk); PIN(a_zu); PIN(hfo); PIN(zero4);

  u32 o_cs = g * 1024 + (l15 >> 3) * 512 + (32 * wf + 4 * (l15 & 7)) * 4;
  u32 a_cs = lds0 + M_CS + w * 256 + (4 * l15 + 2 * h + (g & 1)) * 4;
  PIN(a_cs);
  u32 a_cv = lds0 + M_CS + w * 256 + g * 16;

  PIN(sel); PIN(a_cw); PIN(o_cq); PIN(o_l0); PIN(o_cs); PIN(a_cv); PIN(uo1);
  int xa2[2], wa1[2], wa2[2];
#pragma unroll
  for (int e = 0; e < 2; ++e) {
    xa2[e] = xa[e] + 2 * G1_SLOT; wa1[e] = wa[e] + G1_SLOT; wa2[e] = wa[e] + 2 * G1_SLOT;
    PIN(xa[e]); PIN(xa2[e]); PIN(wa[e]); PIN(wa1[e]); PIN(wa2[e]);
  }
  SCHED();
  int lq = l, uq = tid & 255;
  PIN(lq); PIN(uq);
  u32 p0off;
  {
    const int e = min(9 * w + min(lq, 8), 67);
    const int m = e - 60;
    const int pb = e < 32 ? 224 + e : (e < 60 ? 28 * n2 + (e - 32) : (g1 ? ((m >> 1) * 2 + (xcd & 1)) + 8 * ((m & 1) + 4 * (rk >> 2)) : 224));
    p0off = pb * FS;
  }
  u32 ka_[8];
#pragma unroll
  for (int e = 0; e < 4; ++e) {
    const int l0 = 8 * uq + 2 * e;
    const u32 bits = pk2((float)l0, (float)(l0 + 1));
    const int i0 = uq < 32 ? l0 : (int)(bits & 0xffffu) - (0x4300 - 128);
    const int i1 = uq < 32 ? l0 + 1 : (int)(bits >> 16) - (0x4300 - 128);
    ka_[2 * e] = lds0 + M_KC + uch * 5632 + i0 * 4;
    ka_[2 * e + 1] = lds0 + M_KC + uch * 5632 + i1 * 4;
  }
  PIN(p0off);
#pragma unroll
  for (int e = 0; e < 8; ++e) PIN(ka_[e]);
  SCHED();
  if (g1 && w >= 4) {
#pragma unroll
    for (int i = 0; i < 6; ++i) { dmaw(IC<1>{}, i, w - 4); dmaw(IC<1>{}, i, w); }
    asm volatile("s_waitcnt vmcnt(12)" : "+v"(wr[0]), "+v"(wr[1]), "+v"(wr[2]), "+v"(wr[3]), "+v"(wr[4]), "+v"(wr[5]), "+v"(wr[6]), "+v"(wr[7]) :: "memory");
    PIN(pf);
    if (w < 6) {
#pragma unroll
      for (int c = 0; c < 4; ++c) {
        u32x4 o;
#pragma unroll
        for (int e = 0; e < 4; ++e) o[e] = __builtin_amdgcn_perm(wr[2 * e + 1][c >> 1], wr[2 * e][c >> 1], (c & 1) ? 0x07060302u : 0x05040100u);
        st128s(o, rs, l * 16 + c * 1024, WS_W2F + (((un2 * 56 + ukb) * 4) << 10));
      }
    }
  }
  asm volatile("s_waitcnt lgkmcnt(0)" : "+s"(tag), "+s"(p_ldt2), "+s"(dk) :: "memory");
  tag.x += 1u;

  if (!g1) {
    asm volatile("s_waitcnt vmcnt(0)" : "+v"(xr[0]), "+v"(xr[1]), "+v"(xr[2]), "+v"(xr[3]), "+v"(xr[4]), "+v"(xr[5]), "+v"(xr[6]), "+v"(xr[7]) :: "memory");
    PIN(pf);
    f32x2 v[8][4];
    float ps[8];
#pragma unroll
    for (int r = 0; r < 8; ++r) {
#pragma unroll
      for (int j = 0; j < 4; ++j) v[r][j] = up2(xr[r][j]);
      const f32x2 s2 = (v[r][0] + v[r][1]) + (v[r][2] + v[r][3]);
      ps[r] = s2.x + s2.y;
    }
    const float mean = rows8_sum(ps, l) * (1.f / D);
#pragma unroll
    for (int r = 0; r < 8; ++r) {
      const float mr = rdl(mean, 8 * r);
      f32x2 q2 = {0.f, 0.f};
#pragma unroll
      for (int j = 0; j < 4; ++j) { v[r][j] -= f32x2{mr, mr}; q2 = fma2(v[r][j], v[r][j], q2); }
      ps[r] = q2.x + q2.y;
    }
    const float var = rows8_sum(ps, l) * (1.f / D);
    const u32 ms = pk2(mean, __builtin_sqrtf(rb(rb(var) + 1e-5f)));
    if ((l & 7) == 0) st32s(ms, rs, (l >> 3) * 4, WS_ST + (64 * ax + 8 * w) * 4);
    const float riv = 1.f / bhi(ms), nbv = (mean - b2f(ms)) * riv;
#pragma unroll
    for (int r = 0; r < 8; ++r) {
      const float ri = rdl(riv, 8 * r), nb = rdl(nbv, 8 * r);
#pragma unroll
      for (int j = 0; j < 4; ++j) v[r][j] = fma2(v[r][j], f32x2{ri, ri}, f32x2{nb, nb});
    }
#pragma unroll
    for (int c = 0; c < 8; ++c) {
      u32x4 o;
#pragma unroll
      for (int e = 0; e < 4; ++e) o[e] = pk2(v[7 - 2 * e][c >> 1][c & 1], v[6 - 2 * e][c >> 1][c & 1]);
      *(LAS u32x4*)(lds + (8 * l + c) * 128 + (((7 - w) ^ (l & 7)) << 4)) = o;
    }
    LBAR();
    u32x4 tv[8];
#pragma unroll
    for (int it = 0; it < 8; ++it) tv[it] = lds128(lds + w * 8192 + it * 1024 + l * 16);
#pragma unroll
    for (int it = 0; it < 8; ++it)
      st128s(tv[it], rs, (8 * it + (l >> 3)) * 4096 + (((l & 7) ^ it) << 4), WS_XNR + w * (64 * 4096) + (L - 64 - 64 * ax) * 2);
    WAIT_V(0);
    LBAR();
    if (w == 0) st128s(u32x4{tag.x, tag.y, tag.x, tag.y}, rs, o_l0, FL_P0 + b * FS);
    STAMP(16);
  }

  if (w < 4) {
    float dt = rb(__expf((w >> 1) ? bhi(p_ldt2) : b2f(p_ldt2)));
    PIN(dt);
    asm volatile("s_waitcnt vmcnt(0)" : "+v"(p_lar), "+v"(p_aim), "+v"(p_cre), "+v"(p_cim) :: "memory");
    STAMP(1);
    const float ar = -rb(__expf(b2f(p_lar)));
    const float lre2 = (ar * dt) * 1.44269504f, rev = (b2f(p_aim) * dt) * 0.15915494309f;
    u32 eb = wave_max_u32(max(p_cre & 0x7f80u, p_cim & 0x7f80u)) << 16;
    eb = eb < 0x00800000u ? 0x00800000u : (eb > 0x7e800000u ? 0x7e800000u : eb);
    const float sc = __builtin_bit_cast(float, eb) * (2.f * dt), isc = __builtin_bit_cast(float, 0x7f000000u - eb);
    f32x2 e[53], tmp[8];
    u32 hr[26], hi[26];
    const f32x2 w8 = cexp_p(lre2, rev, 8.f), w64 = cexp_p(lre2, rev, 64.f);
    const f32x2 z1 = cexp_p(lre2, -rev, 1.f), zc = cexp_p(lre2, -rev, 256.f);
    e[0] = f32x2{1.f, 0.f}; e[1] = w8; e[8] = w64; e[32] = f32x2{b2f(p_cre) * isc, -b2f(p_cim) * isc};
#define CM1(k, a, bb) tmp[k] = (a) * f32x2{(bb).x, (bb).x}; SCHED()
#define CM2(k, d, a, bn) d = fma2(f32x2{(a).y, (a).x}, bn, tmp[k]); SCHED()
#define CV(i) hr[i] = pkh(f32x2{e[2 * (i)].x, e[2 * (i) + 1].x}); hi[i] = pkh(f32x2{e[2 * (i)].y, e[2 * (i) + 1].y}); SCHED()
#define W4(a, v, i) *(LAS u32x4*)(size_t)(a) = u32x4{v[i], v[(i) + 1], v[(i) + 2], v[(i) + 3]}; SCHED()
    const f32x2 w8n = cneg(w8), w64n = cneg(w64), z1n = cneg(z1), zcn = cneg(zc);
    f32x2 z2, z4, zd, zq;
    SCHED();
    CM1(0, w8, w8); CM1(1, z1, z1); CM1(2, zc, zc); CM1(3, e[32], z1); CM1(4, e[32], zc); CM1(5, w8, w64); CM1(6, w64, w64);
    CM2(0, e[2], w8, w8n); CM2(1, z2, z1, z1n); CM2(2, zd, zc, zcn); CM2(3, e[33], e[32], z1n); CM2(4, e[40], e[32], zcn);
    CM2(5, e[9], w8, w64n); CM2(6, e[16], w64, w64n);
    const f32x2 e2n = cneg(e[2]), z2n = cneg(z2), zdn = cneg(zd);
    SCHED();
    CM1(0, e[2], w8); CM1(1, e[2], e[2]); CM1(2, z2, z2); CM1(3, zd, zd); CM1(4, e[32], z2); CM1(5, e[33], z2); CM1(6, e[32], zd); CM1(7, e[40], zd);
    CM2(0, e[3], e[2], w8n); CM2(1, e[4], e[2], e2n); CM2(2, z4, z2, z2n); CM2(3, zq, zd, zdn); CM2(4, e[34], e[32], z2n);
    CM2(5, e[35], e[33], z2n); CM2(6, e[44], e[32], zdn); CM2(7, e[46], e[40], zdn);
    const f32x2 e4n = cneg(e[4]), z4n = cneg(z4), zqn = cneg(zq);
    SCHED();
    CM1(0, e[1], e[4]); CM1(1, e[2], e[4]); CM1(2, e[3], e[4]); CM1(3, e[32], z4); CM1(4, e[33], z4); CM1(5, e[34], z4); CM1(6, e[35], z4); CM1(7, e[32], zq);
    CM2(0, e[5], e[1], e4n); CM2(1, e[6], e[2], e4n); CM2(2, e[7], e[3], e4n); CM2(3, e[36], e[32], z4n); CM2(4, e[37], e[33], z4n);
    CM2(5, e[38], e[34], z4n); CM2(6, e[39], e[35], z4n); CM2(7, e[48], e[32], zqn);
    CV(0); CV(1); CV(2); CV(3); W4(tv[0], hr, 0); CV(16); CV(17); W4(tv[4], hi, 0); CV(18); CV(19);
    CM1(0, e[34], zc); W4(tm[0], hr, 16); CM1(1, e[36], zc); CM1(2, e[38], zc); CM1(3, e[36], zd); CM1(4, e[40], zq); W4(tm[2], hi, 16);
    CM1(5, e[44], zq); CM1(6, e[46], zq); CM1(7, e[48], zq);
    CM2(0, e[41], e[34], zcn); CM2(1, e[42], e[36], zcn); CM2(2, e[43], e[38], zcn); CM2(3, e[45], e[36], zdn); CM2(4, e[49], e[40], zqn);
    CM2(5, e[50], e[44], zqn); CM2(6, e[51], e[46], zqn); CM2(7, e[52], e[48], zqn);
    CM1(0, e[42], zd); CM1(1, e[9], w64); CM1(2, e[2], w64); CM1(3, e[3], w64); CM1(4, e[4], w64); CM1(5, e[5], w64); CM1(6, e[6], w64); CM1(7, e[7], w64);
    CM2(0, e[47], e[42], zdn); CM2(1, e[17], e[9], w64n); CM2(2, e[10], e[2], w64n); CM2(3, e[11], e[3], w64n); CM2(4, e[12], e[4], w64n);
    CM2(5, e[13], e[5], w64n); CM2(6, e[14], e[6], w64n); CM2(7, e[15], e[7], w64n);
    CV(24); CV(25);
    *(LAS u32x4*)(size_t)tm2 = u32x4{hr[24], hr[25], hi[24], hi[25]};
    SCHED();
    CV(20); CV(21); CV(22); CV(23); W4(tm[1], hr, 20); CV(4); CV(5); W4(tm[3], hi, 20); CV(6); CV(7);
    CM1(0, e[16], w64); W4(tv[1], hr, 4); CM1(1, e[17], w64); CM1(2, e[10], w64); CM1(3, e[11], w64); CM1(4, e[12], w64); W4(tv[5], hi, 4);
    CM1(5, e[13], w64); CM1(6, e[14], w64); CM1(7, e[15], w64);
    CM2(0, e[24], e[16], w64n); CM2(1, e[25], e[17], w64n); CM2(2, e[18], e[10], w64n); CM2(3, e[19], e[11], w64n); CM2(4, e[20], e[12], w64n);
    CM2(5, e[21], e[13], w64n); CM2(6, e[22], e[14], w64n); CM2(7, e[23], e[15], w64n);
    u32x4 fm[8], fv[8];
    u32 ra0 = rm[0], ra1 = rm[1];
#define RM(s_) { const u32x2 lo = ldstr((LAS char*)(size_t)ra0), hi_ = ldstr((LAS char*)(size_t)ra1); fm[s_] = u32x4{lo.x, lo.y, hi_.x, hi_.y}; ra0 += rms; ra1 += rms; } SCHED()
    CM1(2, e[18], w64); RM(0); CM1(3, e[19], w64); CM1(4, e[20], w64); RM(1); CM1(5, e[21], w64); CM1(6, e[22], w64); RM(2); CM1(7, e[23], w64);
    CV(8); CV(9); RM(3); CV(10); CV(11); W4(tv[2], hr, 8);
    CM2(2, e[26], e[18], w64n); CM2(3, e[27], e[19], w64n); RM(4); CM2(4, e[28], e[20], w64n); W4(tv[6], hi, 8); CM2(5, e[29], e[21], w64n);
    CM2(6, e[30], e[22], w64n); RM(5); CM2(7, e[31], e[23], w64n);
    CV(12); RM(6); CV(13); CV(14); RM(7); CV(15); W4(tv[3], hr, 12); W4(tv[7], hi, 12);
#pragma unroll
    for (int s = 0; s < 8; ++s) {
      const u32x2 lo = ldstr((LAS char*)(size_t)rv[0] + s * 1024), hi_ = ldstr((LAS char*)(size_t)rv[1] + s * 1024);
      fv[s] = u32x4{lo.x, lo.y, hi_.x, hi_.y};
    }
    float k2048 = e[52].x;
    k2048 += dppz<0x111, 0xf>(k2048); k2048 += dppz<0x112, 0xf>(k2048); k2048 += dppz<0x114, 0xf>(k2048); k2048 += dppz<0x118, 0xf>(k2048);
    k2048 += dppz<0x142, 0xa>(k2048); k2048 += dppz<0x143, 0xc>(k2048);
    SCHED();
    STAMP(4);
    f32x16 kk;
#pragma unroll
    for (int r = 0; r < 16; ++r) kk[r] = 0.f;
#pragma unroll
    for (int s = 0; s < 8; ++s) kk = mfma32h(fm[s], fv[s], kk);
    LAS float* kc = (LAS float*)(lds + M_KC + w * 2816);
    *(LAS f32x4*)(kc + 8 * t + 4 * h) = f32x4{kk[0], kk[1], kk[2], kk[3]} * sc;
    *(LAS f32x2*)(kc + (h ? 384 + 2 * t : 256 + 4 * t)) = f32x2{kk[4], kk[5]} * sc;
    *(LAS f32x2*)(kc + (h ? 448 + 2 * t : 258 + 4 * t)) = f32x2{kk[6], kk[7]} * sc;
    LAS float* k3 = kc + (h ? 671 - t : 512 + t);
    k3[0] = (h ? k2048 : kk[8]) * sc;
    if (h == 0) { k3[32] = kk[9] * sc; k3[64] = kk[10] * sc; k3[96] = kk[11] * sc; }
  }
  STAMP(5);

  u32x4 wfr[28];
  const int w2off = WS_W2F + (((n2 * 56 + 7 * w) * 4) << 10);
  auto w2ld = [&](int i) { wfr[i] = ld128s(rs, l * 16 + (i & 3) * 1024, w2off + (i >> 2) * 4096); };
  f32x2 kx[8];
  u32 kpk[4];
  auto expand_rd = [&](int e) { LAS float* q = (LAS float*)(size_t)ka_[e]; kx[e] = f32x2{q[0], q[704]}; };
  auto expand_pk = [&](int e) {
    float a = kx[2 * e].x + kx[2 * e].y, c = kx[2 * e + 1].x + kx[2 * e + 1].y;
    PIN(a); PIN(c);
    kpk[e] = pk2(a, c);
    PIN(kpk[e]);
  };
  auto expand_wr = [&]() { *(LAS u32x4*)(lds + CV_K + uch * 5120 + 1024 + ui * 16) = u32x4{kpk[0], kpk[1], kpk[2], kpk[3]}; };
  u32x4 u0;
  u32x2 u1;
  auto load_u = [&](int k) {
    if (k == 0) u0 = ld128s(rs, ui * 16, WS_XNR + (2 * b + uch) * 4096);
    else u1 = ld64s(rs, uo1, WS_XNR + (2 * b + uch) * 4096);
  };
  auto copy_u = [&](int s) {
    const u32 dd[6] = {u0.x, u0.y, u0.z, u0.w, u1.x, u1.y};
    const int o = s >> 1;
    u32x4 v;
#pragma unroll
    for (int k = 0; k < 4; ++k) v[k] = (s & 1) ? __builtin_amdgcn_alignbit(dd[o + k + 1], dd[o + k], 16) : dd[o + k];
    *(LAS u32x4*)(lds + CV_U + uch * CV_UCH + s * CV_CS + ui * 16) = v;
  };
#define W2Q 97
#define NW2AV 15
  constexpr int NW2A = NW2AV;

  if (g1) {
    f32x4 acc[4][4];
#pragma unroll
    for (int i = 0; i < 4; ++i)
#pragma unroll
      for (int j = 0; j < 4; ++j) acc[i][j] = f32x4{0.f, 0.f, 0.f, 0.f};
    u32x4 fa[2][4], fb[2][4];
    auto rdA = [&](int st, int s, int i) {
      LAS char* q = lds + (st % 3 == 0 ? wa[i & 1] : st % 3 == 1 ? wa1[i & 1] : wa2[i & 1]) + (i >> 1) * 16384 + s * 8192;
      const u32x2 lo = ldstr(q), hi = ldstr(q + 1024);
      fa[s][i] = u32x4{lo.x, lo.y, hi.x, hi.y};
    };
    auto rdB = [&](int st, int s, int j) { fb[s][j] = lds128(lds + (st % 3 == 2 ? xa2[s] : xa[s] + (st % 3) * G1_SLOT) + j * 2048); };
    const int so_cs = (mt & 2) == 0 ? WS_CS + (ft * 8 + kt0) * 1024 + w * 128 : OOB;
    const int so_p0 = w == 0 ? FL_P0 + b * FS : OOB;

    if (w >= 4) __builtin_amdgcn_s_setprio(1);
    LBAR();
    STAMP(6);
    rdB(0, 0, 0); rdA(0, 0, 0); rdA(0, 0, 1); rdA(0, 0, 2); rdA(0, 0, 3); rdB(0, 0, 1); rdB(0, 0, 2); rdB(0, 0, 3);
    dmaw(IC<2>{}, 0, w); dmaw(IC<2>{}, 1, w); dmaw(IC<2>{}, 2, w);
    u32x2 fl;
    u32 msv[4];
    float nmv[4];
    f32x4 csp[2];
    u32x4 cw[4];
    f32x4 cs, cv[4];
    static_for<8>([&](auto st_) {
      constexpr int st = decltype(st_)::v;
#pragma unroll
      for (int s = 0; s < 2; ++s) {
        const int nst = s ? st + 1 : st, ns = s ^ 1;
        if (s == 1 && st < 7) {
          if (st == 1 || st == 4) WAIT_V(7);
          else if (st == 3) WAIT_V(9);
          else if (st == 6) WAIT_V(11);
          else WAIT_V(6);
        }
        if (s == 1) {
          LBAR();
          if (st == 2) STAMP(18);
          if (st == 5) STAMP(21);
        }
        SCHED();
#pragma unroll
        for (int n = 0; n < 16; ++n) {
          acc[n & 3][n >> 2] = mfma16(fa[s][n & 3], fb[s][n >> 2], acc[n & 3][n >> 2]);
          SCHED();
          if (nst < 8) {
            if (n == 0) rdB(nst, ns, 3);
            if (n == 1) rdB(nst, ns, 2);
            if (n == 2) rdB(nst, ns, 1);
            if (n == 3) rdA(nst, ns, 3);
            if (n == 5) rdA(nst, ns, 2);
            if (n == 7) rdA(nst, ns, 1);
            if (n == 9) rdA(nst, ns, 0);
            if (n == 10) rdB(nst, ns, 0);
          }
          if (nst + 2 < 8) {
            const int pc = (s ? 0 : 3);
            if (n == 4 || n == 8 || n == 12) { if (s) dm0(IC<(st + 3) & 7>{}, pc + (n >> 2) - 1, w); else dm0(IC<(st + 2) & 7>{}, pc + (n >> 2) - 1, w); }
            if (n == 6 || n == 11 || n == 14) { const int k = n == 6 ? 0 : n == 11 ? 1 : 2; if (s) dgo(IC<(st + 3) & 7>{}, pc + k, w); else dgo(IC<(st + 2) & 7>{}, pc + k, w); }
          }
          SCHED();
          const int q = st * 32 + s * 16 + n;
          if (q == 4 || q == 8 || q == 12 || q == 13) cw[q == 13 ? 3 : (q >> 2) - 1] = lds128((LAS char*)(size_t)a_cw + (q == 13 ? 3 : (q >> 2) - 1) * 4096);
          if (q == 20) cs = mfma16(sel, cw[0], f32x4{0.f, 0.f, 0.f, 0.f});
          if (q == 24 || q == 28 || q == 29) cs = mfma16(sel, cw[q == 29 ? 3 : (q >> 2) - 5], cs);
          if (q == 36) {
#pragma unroll
            for (int e = 0; e < 4; ++e) { const float v = cs[e]; cs[e] = v + dppx<0x114>(v); }
            PIN(cs);
          }
          if (q == 40) {
#pragma unroll
            for (int e = 0; e < 4; ++e) { const float v = cs[e]; cs[e] = v + dppx<0x118>(v); }
            PIN(cs);
          }
          if (q == 45) st128s(__builtin_bit_cast(u32x4, cs), rs, o_cq, so_cs);
          if (q >= 52 && q < 84 && (q & 3) == 0) expand_rd((q - 52) >> 2);
          if (q >= 92 && q < 124 && (q & 7) == 4) { PIN(kx[(q - 92) >> 2]); expand_pk((q - 92) >> 3); }
          if (q == 200) expand_wr();
          if (q == 85) st128s(u32x4{tag.x, tag.y, tag.x, tag.y}, rs, o_l0, so_p0);
          if (q == 101) st128s(u32x4{~tag.x, tag.y, ~tag.x, tag.y}, rs, o_l0, FL_H + ((mt * 2 + wt) * 56 + ft * 4 + wf) * FS);
          if (q == 105) st128s(u32x4{~tag.x, tag.y, ~tag.x, tag.y}, rs, o_l0, FL_Y + ((7 - 2 * ((w & 1) + 2 * (w >> 2)) - ((w >> 1) & 1)) * 256 + b) * FS);
          if (q == 143) asm volatile("global_load_dwordx2 %0, %1, %2 sc1" : "=v"(fl) : "v"(p0off), "s"(p.ws + FL_P0) : "memory");
          if (q == 173) {
            asm volatile("s_waitcnt vmcnt(5)" : "+v"(fl) :: "memory");
            if (!__all(fl.x == tag.x && fl.y == tag.y)) poll(rs, p0off, FL_P0, tag);
          }
          if (q == 176 || q == 178) load_u((q - 176) >> 1);
          if (q == 180 || q == 182) csp[(q - 180) >> 1] = __builtin_bit_cast(f32x4, ld128s(rs, o_cs, WS_CS + (ft * 8 + 2 * (q - 180)) * 1024));
          if (q == 184) msv[0] = ld32s(rs, l * 4, WS_ST + (mt * 128 + 64 * wt) * 4);
          if (q >= 187 && ((q - 187) & 3) == 0 && ((q - 187) >> 2) < NW2A) w2ld((q - 187) >> 2);
          if (q == 205 || q == 213 || q == 221 || q == 229) copy_u((q - 205) >> 3);
          if (q == 209) {
            PIN(csp[0]); PIN(csp[1]);
            csp[0] += csp[1];
            const auto r0 = __builtin_amdgcn_permlane32_swap(f2u(csp[0][0]), f2u(csp[0][2]), false, false);
            const auto r1 = __builtin_amdgcn_permlane32_swap(f2u(csp[0][1]), f2u(csp[0][3]), false, false);
            csp[0][0] = u2f(r0[0]) + u2f(r0[1]);
            csp[0][1] = u2f(r1[0]) + u2f(r1[1]);
            PIN(csp[0]);
          }
          if (q == 217) {
            const auto r = __builtin_amdgcn_permlane16_swap(f2u(csp[0][0]), f2u(csp[0][1]), false, false);
            *(LAS float*)(size_t)a_cs = u2f(r[0]) + u2f(r[1]);
          }
          if (q == 220) {
            PIN(msv[0]);
            const auto m = __builtin_amdgcn_permlane32_swap(msv[0], msv[0], false, false);
            msv[0] = m[0]; msv[2] = m[1];
            PIN(msv[0]); PIN(msv[2]);
          }
          if (q == 225 || q == 228) {
            const int j = q == 225 ? 0 : 2;
            const auto m = __builtin_amdgcn_permlane16_swap(msv[j], msv[j], false, false);
            msv[j] = m[0]; msv[j + 1] = m[1];
            PIN(msv[j]); PIN(msv[j + 1]);
          }
          if (q >= 232 && q < 236) { cv[q - 232] = *(LAS f32x4*)((LAS char*)(size_t)a_cv + (q - 232) * 64); }
          if (q >= 236 && q < 240) { nmv[q - 236] = -b2f(msv[q - 236]); PIN(nmv[q - 236]); }
          if (q >= 240) {
            const int m = (n + 8) & 15, ii = m & 3, jj = m >> 2;
#pragma unroll
            for (int r = 0; r < 4; ++r) {
              float v = __builtin_fmaf(nmv[jj], cv[ii][r], acc[ii][jj][r]);
              PIN(v);
              acc[ii][jj][r] = v;
            }
          }
          SCHED();
        }
      }
    });
    STAMP(7);
    __builtin_amdgcn_s_setprio(3);
#pragma unroll
    for (int j = 0; j < 4; ++j) {
      const float sd = bhi(msv[j]), sd2 = sd * sd;
      const float ce = -1.44269504f * __builtin_amdgcn_rcpf(sd);
      u32x4 o;
#pragma unroll
      for (int r = 0; r < 4; ++r) {
        const f32x2 gg = {acc[r >> 1][j][2 * (r & 1)], acc[r >> 1][j][2 * (r & 1) + 1]};
        const f32x2 uu = {acc[2 + (r >> 1)][j][2 * (r & 1)], acc[2 + (r >> 1)][j][2 * (r & 1) + 1]};
        const f32x2 a = gg * f32x2{ce, ce};
        const f32x2 e = {__builtin_amdgcn_exp2f(a.x), __builtin_amdgcn_exp2f(a.y)};
        const f32x2 d = fma2(e, f32x2{sd2, sd2}, f32x2{sd2, sd2});
        const f32x2 q = {__builtin_amdgcn_rcpf(d.x), __builtin_amdgcn_rcpf(d.y)};
        o[r] = pk2v((gg * uu) * q);
      }
      st128s(o, rs, l * 16, WS_H + (((mt * 8 + 4 * wt + j) * 56 + ft * 4 + wf) << 10));
      SCHED();
    }
    __builtin_amdgcn_s_setprio(1);
    STAMP(8);
  } else {
    poll(rs, p0off, FL_P0, tag);
    if (b == 255 && w == 0) {
#pragma unroll 1
      for (int i = 0; i < 4; ++i) poll(rs, (64 * i + l) * FS, FL_P0, tag);
      if (l == 0) st64s(tag, rs, 0, FL_EP);
    }
    STAMP(17);
    LBAR();
    STAMP(19);
    load_u(0); load_u(1);
#pragma unroll
    for (int e = 0; e < 8; ++e) expand_rd(e);
#pragma unroll
    for (int e = 0; e < 4; ++e) expand_pk(e);
    LBAR();
    expand_wr();
#pragma unroll
    for (int s = 0; s < 4; ++s) copy_u(s);
    SCHED();
#pragma unroll
    for (int i = 0; i < NW2A; ++i) w2ld(i);
    __builtin_amdgcn_s_waitcnt(0x0F70);
    LBAR();
  }

  STAMP(10);
  u32x2 hfl;
  u32x4 hf[28];
  const int hoff = WS_H + ((4 * m2 * 56 + 7 * w) << 10);
  const int jr = 0;
  auto hld = [&](int n) { hf[n] = ld128s(rs, l * 16, hoff + (((n / 7) ^ jr) * 56 + n % 7) * 1024); };
  const int hfs = FL_H + (m2 * 56 + 7 * w) * FS;
#define PDV 12
  constexpr int PD = PDV;
  {
    LAS char *ua = (LAS char*)(size_t)a_ua, *ka = (LAS char*)(size_t)a_ka;
    *(LAS u32x4*)(size_t)a_zk = zero4;
    *(LAS u32x4*)(size_t)a_zu = zero4;
    asm volatile("" ::: "memory");
    f32x4 acc[4];
#pragma unroll
    for (int i = 0; i < 4; ++i) acc[i] = f32x4{0.f, 0.f, 0.f, 0.f};
    const int so_h = g1 ? FL_H + ((mt * 2 + wt) * 56 + ft * 4 + wf) * FS : OOB;
    auto hook = [&](int rr, int c) {
#define HPUB 3
#define HPOLL 24
      const int sq = rr * 8 + c;
#define WS0 4
#define WSN 13
      auto nw = [&](int z) { return z <= WS0 ? 0 : z >= WS0 + WSN ? 28 - NW2A : ((z - WS0) * (28 - NW2A) + WSN - 1) / WSN; };
      if (sq == HPUB) {
        constexpr int c0 = HPUB <= WS0 ? 0 : HPUB >= WS0 + WSN ? 28 - NW2A : ((HPUB - WS0) * (28 - NW2A) + WSN - 1) / WSN;
        asm volatile("s_waitcnt vmcnt(%0)" :: "i"(c0) : "memory");
        st128s(u32x4{tag.x, tag.y, tag.x, tag.y}, rs, o_l0, so_h);
        __builtin_amdgcn_s_setprio(0);
        STAMP(9);
      }
      if (sq >= 0 && nw(sq + 1) > nw(sq)) w2ld(NW2A + nw(sq));
      if (sq == (HPOLL < 32 ? HPOLL - 5 : 31)) hfl = ld64s(rs, hfo, hfs);
      if (sq == HPOLL) {
        STAMP(2);
        if (!__all(hfl.x == tag.x && hfl.y == tag.y)) poll(rs, hfo, hfs, tag);
        asm volatile("" ::: "memory");
        STAMP(23);
      }
      if (sq >= HPOLL) {
#pragma unroll
        for (int n = (sq - HPOLL) * PD / (HPOLL < 32 ? 32 - HPOLL : 1); n < (sq + 1 - HPOLL) * PD / (HPOLL < 32 ? 32 - HPOLL : 1); ++n) hld(n);
      }
    };
    conv_tiles(ua, ka, acc, hook);
#pragma unroll
    for (int i = 0; i < 4; ++i) *(LAS f32x4*)(lds + CV_X + (w * 4 + i) * 1024 + l * 16) = acc[i];
    if (tid == 0) *(LAS u32*)(lds + M_CNT) = 0u;
  }
  LBAR();
  STAMP(11);
  int ytile;
  {
    const int ts = (w >> 1) & 1, i = (w & 1) + 2 * (w >> 2);
    ytile = 7 - 2 * i - ts;
    const int pos = 256 * ytile + 16 * l15 + 4 * g;
    f32x4 y0[2], y1[2];
    u32x2 ur[2];
#pragma unroll
    for (int ch = 0; ch < 2; ++ch) {
      y0[ch] = *(LAS f32x4*)(lds + CV_X + ((ch + 2 * ts) * 4 + i) * 1024 + l * 16);
      y1[ch] = *(LAS f32x4*)(lds + CV_X + ((ch + 2 * ts + 4) * 4 + i) * 1024 + l * 16);
      ur[ch] = lds64(lds + CV_U + ch * CV_UCH + (2044 - pos) * 2);
    }
    LBAR();
    float res[2][4];
#pragma unroll
    for (int ch = 0; ch < 2; ++ch) {
      const float dsk = ch ? bhi(dk) : b2f(dk);
      const f32x4 y = y0[ch] + y1[ch];
      res[ch][0] = __builtin_fmaf(bhi(ur[ch].y), dsk, y[0]); res[ch][1] = __builtin_fmaf(b2f(ur[ch].y), dsk, y[1]);
      res[ch][2] = __builtin_fmaf(bhi(ur[ch].x), dsk, y[2]); res[ch][3] = __builtin_fmaf(b2f(ur[ch].x), dsk, y[3]);
    }
    const u32x4 o = {pk2(res[0][0], res[1][0]), pk2(res[0][1], res[1][1]), pk2(res[0][2], res[1][2]), pk2(res[0][3], res[1][3])};
    st128s(o, rs, pos * 4, WS_Y + b * 8192);
    if (HPOLL == 32) {
      STAMP(2);
      if (!__all(hfl.x == tag.x && hfl.y == tag.y)) poll(rs, hfo, hfs, tag);
      asm volatile("" ::: "memory");
      STAMP(23);
#pragma unroll
      for (int n = 0; n < PD; ++n) hld(n);
    }
  }
  STAMP(12);

  {
    const int oj = w >> 1, rh = w & 1;
    const int etok = 64 * m2 + 16 * (oj ^ jr) + l15, ech = 64 * n2 + 16 * g + 8 * rh;
    u32x4 xv;
    u32 yo = (ech >> 1) * 8192 + etok * 4, yfv = (8 * g + (l & 3)) * FS;
    PIN(yo); PIN(yfv);
    SCHED();
    f32x4 acc[4][4];
#pragma unroll
    for (int i = 0; i < 4; ++i)
#pragma unroll
      for (int j = 0; j < 4; ++j) acc[i][j] = f32x4{0.f, 0.f, 0.f, 0.f};
    u32x2 yfl;
    const int yfo = FL_Y + ((m2 >> 2) * 256 + 32 * n2 + 4 * rh) * FS;
    LAS char* pw = opaque(lds + w * 16384 + l * 8);
    LAS char* pr = opaque(lds + w * 2048 + l * 8);
    LAS char* pr2 = opaque(pr + 65536);
    u32 a_cnt = lds0 + M_CNT + l * 4, one = 1u;
    PIN(a_cnt); PIN(one);
    auto part_wr = [&](int j, int k) {
      *(LAS f32x2*)(pw + (j * 8 + k) * 512) = f32x2{acc[k & 3][j][2 * (k >> 2)], acc[k & 3][j][2 * (k >> 2) + 1]};
    };
#pragma unroll
    for (int n = 0; n < 28; ++n) {
#pragma unroll
      for (int i = 0; i < 4; ++i) {
        acc[i][n / 7] = mfma16(wfr[4 * (n % 7) + i], hf[n], acc[i][n / 7]);
        SCHED();
        if (i == 3 && n + PD < 28) hld(n + PD);
        if ((i & 1) == 0 && n >= 8 && n % 7 >= 1 && n % 7 < 5) part_wr(n / 7 - 1, (n % 7 - 1) * 2 + (i >> 1));
        SCHED();
      }
      if (n == YP1 && w >= 4) { asm volatile("s_waitcnt vmcnt(%0)" :: "i"(YP1 + 1) : "memory"); st128s(u32x4{tag.x, tag.y, tag.x, tag.y}, rs, o_l0, FL_Y + (ytile * 256 + b) * FS); }
      if (n == YP0 && w < 4) { asm volatile("s_waitcnt vmcnt(%0)" :: "i"(YP0 + 1) : "memory"); st128s(u32x4{tag.x, tag.y, tag.x, tag.y}, rs, o_l0, FL_Y + (ytile * 256 + b) * FS); }
      if (n == 21) xv = *(const GAS u32x4*)((const char*)p.x + (size_t)etok * 1024 + ech * 2);
      if (n == 22) yfl = ld64s(rs, yfv, yfo);
      if (n == TB && w >= 4) LBAR();
      SCHED();
    }
    STAMP(13);
#pragma unroll
    for (int k = 0; k < 8; ++k) part_wr(3, k);
    asm volatile("" ::: "memory");
    if (w < 4) LBAR();
    else asm volatile("ds_add_u32 %0, %1" :: "v"(a_cnt), "v"(one) : "memory");
    if (!__all(yfl.x == tag.x && yfl.y == tag.y)) poll(rs, yfv, yfo, tag);
    asm volatile("" ::: "memory");
    u32 yv[4];
#pragma unroll
    for (int j = 0; j < 4; ++j) yv[j] = ld32s(rs, yo, WS_Y + j * 8192);
    f32x2 pp[8][4];
#pragma unroll
    for (int ww = 0; ww < 4; ++ww)
#pragma unroll
      for (int i = 0; i < 4; ++i) pp[ww][i] = *(volatile LAS f32x2*)(pr + ww * 16384 + i * 512);
    if (w >= 4) {
      volatile LAS u32* cnt = (volatile LAS u32*)(lds + M_CNT);
#pragma unroll 1
      for (int spin = 0; spin < (1 << 20) && *cnt < 4u; ++spin) {}
      asm volatile("" ::: "memory");
    }
    STAMP(14);
#pragma unroll
    for (int ww = 0; ww < 4; ++ww)
#pragma unroll
      for (int i = 0; i < 4; ++i) pp[4 + ww][i] = *(volatile LAS f32x2*)(pr2 + ww * 16384 + i * 512);
    SCHED();
    f32x2 sm[4];
#pragma unroll
    for (int i = 0; i < 4; ++i) sm[i] = ((pp[0][i] + pp[1][i]) + (pp[2][i] + pp[3][i])) + ((pp[4][i] + pp[5][i]) + (pp[6][i] + pp[7][i]));
    const u32 ff[4] = {pk2(sm[0].x, sm[1].x), pk2(sm[2].x, sm[3].x), pk2(sm[0].y, sm[1].y), pk2(sm[2].y, sm[3].y)};
    u32x4 r;
#pragma unroll
    for (int j = 0; j < 4; ++j) {
      const u32 s = pk2v(up2(xv[j]) + up2(yv[j]));
      r[j] = pk2v(up2(s) + up2(ff[j]));
    }
    *(GAS u32x4*)((char*)p.out + (size_t)etok * 1024 + ech * 2) = r;
  }
  STAMP(15);
  STAMP_DUMP();
}

extern "C" void kernel_launch(void* const* d_in, const int* in_sizes, int n_in, void* d_out, int out_size, void* d_ws,
                              size_t ws_size, hipStream_t stream) {
  Params p;
  p.x = (const u16*)d_in[0]; p.lar = (const u16*)d_in[1]; p.aim = (const u16*)d_in[2]; p.cre = (const u16*)d_in[3];
  p.cim = (const u16*)d_in[4]; p.ldt = (const u16*)d_in[5]; p.dsk = (const u16*)d_in[6];
  p.w1 = (const u16*)d_in[8]; p.w3 = (const u16*)d_in[9]; p.w2 = (const u16*)d_in[10];
  p.out = (u16*)d_out; p.ws = (char*)d_ws;
  fused<<<256, 512, 0, stream>>>(p);
}
```

```cpp
#include <hip/hip_runtime.h>
#include <hip/hip_bf16.h>

typedef unsigned short u16;
typedef unsigned int u32;
typedef unsigned long long u64;
typedef __bf16 bf16x8 __attribute__((ext_vector_type(8)));
typedef __bf16 bf16x2 __attribute__((ext_vector_type(2)));
typedef _Float16 f16x8 __attribute__((ext_vector_type(8)));
typedef _Float16 f16x2 __attribute__((ext_vector_type(2)));
typedef short s16x4 __attribute__((ext_vector_type(4)));
typedef float f32x2 __attribute__((ext_vector_type(2)));
typedef float f32x4 __attribute__((ext_vector_type(4)));
typedef float f32x16 __attribute__((ext_vector_type(16)));
typedef u32 u32x2 __attribute__((ext_vector_type(2)));
typedef u32 u32x4 __attribute__((ext_vector_type(4)));
typedef u64 u64x4 __attribute__((ext_vector_type(4)));
typedef u64 u64x8 __attribute__((ext_vector_type(8)));
typedef __amdgpu_buffer_rsrc_t rsrc_t;
#define LAS __attribute__((address_space(3)))
#define GAS __attribute__((address_space(1)))
#define DEV __device__ __forceinline__

#define NTQ " nt"
#define TB 21
#define YP1 1
#define YP0 10
constexpr int L = 2048, D = 512, NM = 128, F = 1792;
constexpr int LDS_BYTES = 163840;
constexpr int SC1 = 16;

constexpr int WS_H = 0;
constexpr int WS_XNR = 8 << 20;
constexpr int WS_W2F = 10 << 20;
constexpr int WS_Y = 12 << 20;
constexpr int WS_ST = 14 << 20;
constexpr int WS_CS = WS_ST + 8192;
constexpr int WS_FL = 15 << 20;
constexpr int FS = 128;
constexpr int FL_EP = WS_FL;
constexpr int FL_P0 = WS_FL + 4096;
constexpr int FL_H = FL_P0 + 256 * FS;
constexpr int FL_Y = FL_H + 2048 * FS;
constexpr int WS_BYTES = 16 << 20;

struct Params {
  const u16 *x, *lar, *aim, *cre, *cim, *ldt, *dsk, *w1, *w3, *w2;
  u16* out;
  char* ws;
};

template <int V> struct IC { static constexpr int v = V; };
template <int N, int I = 0, class Fn>
DEV void static_for(Fn&& fn) { if constexpr (I < N) { fn(IC<I>{}); static_for<N, I + 1>(fn); } }
DEV u32 f2u(float f) { return __builtin_bit_cast(u32, f); }
DEV float u2f(u32 v) { return __builtin_bit_cast(float, v); }
DEV float b2f(u32 v) { return __builtin_bit_cast(float, v << 16); }
DEV float bhi(u32 v) { return __builtin_bit_cast(float, v & 0xffff0000u); }
DEV u32 pk2v(f32x2 f) { return __builtin_bit_cast(u32, __builtin_convertvector(f, bf16x2)); }
DEV u32 pk2(float lo, float hi) { return pk2v(f32x2{lo, hi}); }
DEV f32x2 up2(u32 v) { return f32x2{b2f(v), bhi(v)}; }
DEV float rb(float f) { return b2f(pk2(f, f) & 0xffffu); }
DEV f32x2 fma2(f32x2 a, f32x2 b, f32x2 c) { return __builtin_elementwise_fma(a, b, c); }
DEV f32x16 mfma32(u32x4 a, u32x4 b, f32x16 c) {
  return __builtin_amdgcn_mfma_f32_32x32x16_bf16(__builtin_bit_cast(bf16x8, a), __builtin_bit_cast(bf16x8, b), c, 0, 0, 0);
}
DEV f32x4 mfma16(u32x4 a, u32x4 b, f32x4 c) {
  return __builtin_amdgcn_mfma_f32_16x16x32_bf16(__builtin_bit_cast(bf16x8, a), __builtin_bit_cast(bf16x8, b), c, 0, 0, 0);
}
DEV f32x16 mfma32h(u32x4 a, u32x4 b, f32x16 c) {
  return __builtin_amdgcn_mfma_f32_32x32x16_f16(__builtin_bit_cast(f16x8, a), __builtin_bit_cast(f16x8, b), c, 0, 0, 0);
}
DEV u32x4 lds128(LAS char* p) { return *(LAS u32x4*)p; }
DEV u32x2 lds64(LAS char* p) { return *(LAS u32x2*)p; }
DEV u32x2 ldstr(LAS char* p) { return __builtin_bit_cast(u32x2, __builtin_amdgcn_ds_read_tr16_b64_v4i16((LAS s16x4*)p)); }
template <int LOFF>
DEV void dma_m0(u32 lbase) { asm volatile("s_add_u32 m0, %0, %1" :: "s"(lbase), "i"(LOFF) : "memory", "scc", "m0"); }
template <bool NT>
DEV void dma_go(u32x4 rs, u32 voff, u32 soff) {
  if (NT) asm volatile("buffer_load_dwordx4 %0, %1, %2 offen nt lds" :: "v"(voff), "s"(rs), "s"(soff) : "memory");
  else asm volatile("buffer_load_dwordx4 %0, %1, %2 offen lds" :: "v"(voff), "s"(rs), "s"(soff) : "memory");
}
DEV u32x4 mkrs(const void* ptr, u32 bytes) { const u64 a = (u64)ptr; return u32x4{(u32)a, (u32)(a >> 32) & 0xffffu, bytes, 0x00020000u}; }
#define WAIT_V(n) asm volatile("s_waitcnt vmcnt(" #n ")" ::: "memory")
#define WAIT_L0() asm volatile("s_waitcnt lgkmcnt(0)" ::: "memory")
#define BAR() __builtin_amdgcn_s_barrier()
#define LBAR() do { WAIT_L0(); BAR(); asm volatile("" ::: "memory"); } while (0)
#define SCHED() __builtin_amdgcn_sched_barrier(0)
#define PIN(x) asm volatile("" : "+v"(x))
constexpr int OOB = 0x04000000;
#define STAMP(i)
#define STAMP_DECL
#define STAMP_DUMP()

DEV u32x4 ld128s(rsrc_t rs, int voff, int soff) { return __builtin_amdgcn_raw_buffer_load_b128(rs, voff, soff, SC1); }
DEV u32x2 ld64s(rsrc_t rs, int voff, int soff) { return __builtin_amdgcn_raw_buffer_load_b64(rs, voff, soff, SC1); }
DEV u32 ld32s(rsrc_t rs, int voff, int soff) { return __builtin_amdgcn_raw_buffer_load_b32(rs, voff, soff, SC1); }
DEV void st128s(u32x4 v, rsrc_t rs, int voff, int soff) {
  __builtin_amdgcn_raw_buffer_store_b128(v, rs, voff, soff, SC1);
  asm volatile("s_nop 1" :: "v"(v) : "memory");
}
DEV void st64s(u32x2 v, rsrc_t rs, int voff, int soff) { __builtin_amdgcn_raw_buffer_store_b64(v, rs, voff, soff, SC1); }
DEV void st32s(u32 v, rsrc_t rs, int voff, int soff) { __builtin_amdgcn_raw_buffer_store_b32(v, rs, voff, soff, SC1); }
DEV void poll(rsrc_t rs, int voff, int soff, u32x2 tag) {
#pragma unroll 1
  for (int spin = 0; spin < (1 << 18); ++spin) {
    const u32x2 v = ld64s(rs, voff, soff);
    if (__all(v.x == tag.x && v.y == tag.y)) break;
    __builtin_amdgcn_s_sleep(4);
  }
  __builtin_amdgcn_fence(__ATOMIC_ACQUIRE, "wavefront");
  asm volatile("" ::: "memory");
}

template <int CTRL, int RM>
DEV float dppz(float v) { return __builtin_bit_cast(float, __builtin_amdgcn_update_dpp(0, __builtin_bit_cast(int, v), CTRL, RM, 0xf, true)); }
template <int CTRL, int RM>
DEV u32 dppu(u32 v) { return (u32)__builtin_amdgcn_update_dpp(0, (int)v, CTRL, RM, 0xf, true); }
DEV u32 wave_max_u32(u32 v) {
  v = max(v, dppu<0x111, 0xf>(v)); v = max(v, dppu<0x112, 0xf>(v)); v = max(v, dppu<0x114, 0xf>(v)); v = max(v, dppu<0x118, 0xf>(v));
  v = max(v, dppu<0x142, 0xa>(v)); v = max(v, dppu<0x143, 0xc>(v));
  return (u32)__builtin_amdgcn_readlane((int)v, 63);
}

DEV float rdl(float v, int lane) { return __builtin_bit_cast(float, __builtin_amdgcn_readlane(__builtin_bit_cast(int, v), lane)); }
template <int CTRL>
DEV float dppx(float v) { return __builtin_bit_cast(float, __builtin_amdgcn_mov_dpp(__builtin_bit_cast(int, v), CTRL, 0xf, 0xf, true)); }
DEV float rows8_sum(const float (&p)[8], int l) {
  float a[4], c[2];
#pragma unroll
  for (int i = 0; i < 4; ++i) {
    const auto r = __builtin_amdgcn_permlane32_swap(__builtin_bit_cast(u32, p[i]), __builtin_bit_cast(u32, p[i + 4]), false, false);
    a[i] = __builtin_bit_cast(float, (u32)r[0]) + __builtin_bit_cast(float, (u32)r[1]);
  }
#pragma unroll
  for (int i = 0; i < 2; ++i) {
    const auto r = __builtin_amdgcn_permlane16_swap(__builtin_bit_cast(u32, a[i]), __builtin_bit_cast(u32, a[i + 2]), false, false);
    c[i] = __builtin_bit_cast(float, (u32)r[0]) + __builtin_bit_cast(float, (u32)r[1]);
  }
  const bool up = l & 8;
  float v = (up ? c[1] : c[0]) + dppx<0x128>(up ? c[0] : c[1]);
  v += dppx<0xB1>(v); v += dppx<0x4E>(v); v += dppx<0x141>(v);
  return v;
}

constexpr int G1_SLOT = 49152;
constexpr int KG_T = 2 * G1_SLOT;
constexpr int KG_TW = 13312;
constexpr int SPARE = 3 * G1_SLOT;
constexpr int M_CS = SPARE;
constexpr int M_CNT = SPARE + 4096;
constexpr int M_KC = SPARE + 5120;
constexpr int CV_K = SPARE + 5120;
static_assert(KG_T + 4 * KG_TW <= M_KC && M_KC + 4 * 2816 <= LDS_BYTES, "lds");
constexpr int CV_CS = 4160;
constexpr int CV_U = 2 * G1_SLOT;
constexpr int CV_UCH = 4 * CV_CS;
constexpr int CV_X = 0;

DEV f32x2 cmul(f32x2 a, f32x2 b, f32x2 bn) { return fma2(f32x2{a.y, a.x}, bn, a * f32x2{b.x, b.x}); }
DEV f32x2 cneg(f32x2 b) { return f32x2{-b.y, b.y}; }
DEV f32x2 cexp_p(float lre2, float rev, float pw) {
  const float mg = __builtin_amdgcn_exp2f(lre2 * pw);
  const float ph = __builtin_amdgcn_fractf(rev * pw);
  return f32x2{__builtin_amdgcn_cosf(ph), __builtin_amdgcn_sinf(ph)} * f32x2{mg, mg};
}
DEV u32 pkh(f32x2 f) { return __builtin_bit_cast(u32, __builtin_convertvector(f, f16x2)); }
DEV LAS char* opaque(LAS char* q) { u32 v = (u32)(size_t)q; asm volatile("" : "+v"(v)); return (LAS char*)(size_t)v; }

template <class Hook>
DEV void conv_tiles(LAS char* ua, LAS char* ka, f32x4 (&acc)[4], Hook hook) {
  constexpr int ORD[16] = {0, 15, 13, 11, 9, 1, 14, 12, 10, 8, 2, 3, 4, 5, 6, 7};
  u32x4 fr[2][16];
  auto load = [&](int rr, int id) {
    if (id < 8) {
      const u32x2 lo = *(volatile LAS u32x2*)(ua + (7 - id) * 512 + (3 - rr) * 64), hi = *(volatile LAS u32x2*)(ua + (7 - id) * 512 + (3 - rr) * 64 + 8);
      fr[rr & 1][id] = u32x4{lo.x, lo.y, hi.x, hi.y};
    } else {
      fr[rr & 1][id] = lds128(ka + (id - 8) * 512 + (3 - rr) * 64);
    }
  };
#pragma unroll
  for (int k = 0; k < 16; ++k) { load(0, ORD[k]); SCHED(); }
  hook(0, -1);
  SCHED();
#pragma unroll
  for (int rr = 0; rr < 4; ++rr) {
#pragma unroll
    for (int c = 0; c < 8; ++c) {
      u32x4(&f)[16] = fr[rr & 1];
#pragma unroll
      for (int i = 0; i < 4; ++i) {
        if (c <= 7 - 2 * i) { acc[i] = mfma16(f[c], f[8 + 7 - 2 * i - c], acc[i]); SCHED(); }
        if (rr < 3 && i < 2) { load(rr + 1, ORD[2 * c + i]); SCHED(); }
      }
      hook(rr, c);
      SCHED();
    }
  }
}

__global__ __launch_bounds__(512, 2) void fused(Params args) {
  __shared__ __attribute__((aligned(16))) char smem[LDS_BYTES];
  u64x8 ka0;
  u64x4 ka1;
  asm volatile("s_load_dwordx16 %0, %2, 0x0\n\ts_load_dwordx8 %1, %2, 0x40" : "=&s"(ka0), "=&s"(ka1) : "s"(__builtin_amdgcn_kernarg_segment_ptr()) : "memory");
#define CODEPF 23000
  u32x4 pf;
  if (__builtin_amdgcn_readfirstlane(threadIdx.x >> 6) == 4) {
    u64 pc;
    asm volatile("s_getpc_b64 %0" : "=s"(pc));
    const u32 po = min((u32)((blockIdx.x >> 3) * 1024 + (threadIdx.x & 63) * 16), (u32)(CODEPF - 16));
    asm volatile("global_load_dwordx4 %0, %1, %2" : "=v"(pf) : "v"(po), "s"(pc & ~(u64)255) : "memory");
  }
  LAS char* lds = (LAS char*)smem;
  const u32 lds0 = (u32)(size_t)lds;
  const int tid = threadIdx.x, l = tid & 63;
  const int w = __builtin_amdgcn_readfirstlane(tid >> 6);
  const int b = blockIdx.x, xcd = b & 7, rk = b >> 3;
  const bool g1 = rk < 28;
  const int l15 = l & 15, g = l >> 4, t = l & 31, h = l >> 5;
  const int mt = (xcd >> 1) * 4 + (rk & 3), ft = (xcd & 1) * 7 + (rk >> 2);
#define ROTV 5
  const int kt0 = ROTV == 0 ? 0 : ROTV == 1 ? (ft + 2 * (mt & 3) + ((mt >> 2) & 1)) & 7 : ROTV == 2 ? (2 * (xcd >> 1) + 4 * (xcd & 1)) & 7 : (2 * (xcd >> 1) + 4 * (xcd & 1) + (mt & 1)) & 7;
  const int wt = w & 1, wf = w >> 1;
  u32 xo0 = (l >> 3) * 1024 + (((l & 7) ^ g) << 4);
  u32 xo1 = (8 + (l >> 3)) * 1024 + (((l & 7) ^ (4 + g)) << 4);
  u32 wo1 = g * (F * 2) + ((l15 ^ (4 * g + 2 * (w & 1))) << 4), wo1b = wo1 + 4 * F * 2;
  PIN(xo0); PIN(xo1); PIN(wo1); PIN(wo1b);
  u32 tv[8], tm[4], tm2, rv[2], rm[2];
  {
    const u32 tw = lds0 + KG_T + (w & 3) * KG_TW;
#pragma unroll
    for (int k = 0; k < 8; ++k) { tv[k] = tw + l * 128 + ((k ^ (l & 7)) << 4); PIN(tv[k]); }
#pragma unroll
    for (int k = 0; k < 4; ++k) { tm[k] = tw + 8192 + l * 64 + ((k ^ ((l >> 1) & 3)) << 4); PIN(tm[k]); }
    tm2 = tw + 12288 + l * 16;
    PIN(tm2);
    const int blk = g & 1, c = (l >> 2) & 1, pp = l & 3;
#pragma unroll
    for (int t2 = 0; t2 < 2; ++t2) {
      const int m = 4 * h + 2 * t2 + ((l >> 3) & 1);
      rv[t2] = tw + m * 128 + (((4 * c + 2 * blk + (pp >> 1)) ^ m) << 4) + 8 * (pp & 1);
      rm[t2] = blk ? tw + 12288 + m * 16 + 8 * c : tw + 8192 + m * 64 + (((2 * c + (pp >> 1)) ^ (m >> 1)) << 4) + 8 * (pp & 1);
      PIN(rv[t2]); PIN(rm[t2]);
    }
  }
  u32 rms = (g & 1) ? 128 : 512;
  PIN(rms);
  asm volatile("s_waitcnt lgkmcnt(0)" : "+s"(ka0), "+s"(ka1) :: "memory");
  Params p;
  p.x = (const u16*)ka0[0]; p.lar = (const u16*)ka0[1]; p.aim = (const u16*)ka0[2]; p.cre = (const u16*)ka0[3]; p.cim = (const u16*)ka0[4];
  p.ldt = (const u16*)ka0[5]; p.dsk = (const u16*)ka0[6]; p.w1 = (const u16*)ka0[7];
  p.w3 = (const u16*)ka1[0]; p.w2 = (const u16*)ka1[1]; p.out = (u16*)ka1[2]; p.ws = (char*)ka1[3];
  const rsrc_t rs = __builtin_amdgcn_make_buffer_rsrc(p.ws, 0, WS_BYTES, 0x00020000);
  STAMP_DECL;
  STAMP(3);

  u32x2 tag;
  u32 p_ldt2, dk;
  asm volatile("s_load_dwordx2 %0, %3, 0x0\n\ts_load_dword %1, %4, 0x0\n\ts_load_dword %2, %5, 0x0"
               : "=&s"(tag), "=&s"(p_ldt2), "=&s"(dk) : "s"(p.ws + FL_EP), "s"(p.ldt + 2 * b), "s"(p.dsk + 2 * b) : "memory");

  u32 p_lar, p_aim, p_cre, p_cim;
  if (w < 4) {
    const u32 po = (b * 256 + tid) * 2;
    asm volatile("global_load_ushort %0, %1, %2" NTQ : "=v"(p_lar) : "v"(po), "s"(p.lar) : "memory");
    asm volatile("global_load_ushort %0, %1, %2" NTQ : "=v"(p_aim) : "v"(po), "s"(p.aim) : "memory");
    asm volatile("global_load_ushort %0, %1, %2" NTQ : "=v"(p_cre) : "v"(po), "s"(p.cre) : "memory");
    asm volatile("global_load_ushort %0, %1, %2" NTQ : "=v"(p_cim) : "v"(po), "s"(p.cim) : "memory");
  }
  SCHED();

  const u32x4 rx = mkrs(p.x, L * D * 2), rw1 = mkrs(p.w1, D * F * 2), rw3 = mkrs(p.w3, D * F * 2);
#define NTS 0
  auto dm0 = [&](auto st_, int piece, int ww) {
    constexpr int S = (decltype(st_)::v % 3) * G1_SLOT;
    const u32 lb = lds0 + ww * 2048;
    if (piece == 0) dma_m0<S>(lb);
    if (piece == 1) dma_m0<S + 16384>(lb);
    if (piece == 2) dma_m0<S + 32768>(lb);
    if (piece == 3) dma_m0<S + 1024>(lb);
    if (piece == 4) dma_m0<S + 16384 + 1024>(lb);
    if (piece == 5) dma_m0<S + 32768 + 1024>(lb);
  };
  auto dgo = [&](auto st_, int piece, int ww) {
    constexpr bool nt = decltype(st_)::v < NTS;
    const int kt = (kt0 + decltype(st_)::v) & 7;
    const u32 sx = (mt * 128 + ww * 16) * 1024 + kt * 128, sw = (kt * 64 + ww * 8) * (F * 2) + ft * 256;
    if (piece == 0) dma_go<nt>(rx, xo0, sx);
    if (piece == 1) dma_go<nt>(rw1, wo1, sw);
    if (piece == 2) dma_go<nt>(rw3, wo1, sw);
    if (piece == 3) dma_go<nt>(rx, xo1, sx);
    if (piece == 4) dma_go<nt>(rw1, wo1b, sw);
    if (piece == 5) dma_go<nt>(rw3, wo1b, sw);
  };
  auto dmaw = [&](auto st_, int piece, int ww) { dm0(st_, piece, ww); asm volatile("s_nop 0"); dgo(st_, piece, ww); };

  u32x2 wr[8];
  u32x4 xr[8];
  const int un2 = b / 28, ukb = 2 * (b - 28 * un2) + (w & 1);
  const int ax = xcd * 4 + (rk - 28);
  if (w >= 4 && g1) {
#pragma unroll
    for (int i = 0; i < 6; ++i) { dmaw(IC<0>{}, i, w - 4); dmaw(IC<0>{}, i, w); }
    if (w < 6) {
      const char* wb2 = (const char*)p.w2 + (size_t)(32 * ukb) * 1024 + un2 * 128;
      const u32 wv = g * 4096 + l15 * 8;
#define W2RAW(j, base, off) asm volatile("global_load_dwordx2 %0, %1, %2 offset:" #off NTQ : "=v"(wr[j]) : "v"(wv), "s"(base) : "memory")
      W2RAW(0, wb2, 0); W2RAW(1, wb2, 1024); W2RAW(2, wb2, 2048); W2RAW(3, wb2, 3072);
      W2RAW(4, wb2 + 16384, 0); W2RAW(5, wb2 + 16384, 1024); W2RAW(6, wb2 + 16384, 2048); W2RAW(7, wb2 + 16384, 3072);
    }
  }
  if (!g1) {
    const u32 xv_ = (64 * ax + 8 * w) * 1024 + l * 16;
#define XRAW(j, vo, off) asm volatile("global_load_dwordx4 %0, %1, %2 offset:" #off : "=v"(xr[j]) : "v"(vo), "s"(p.x) : "memory")
    XRAW(0, xv_, 0); XRAW(1, xv_, 1024); XRAW(2, xv_, 2048); XRAW(3, xv_, 3072);
    XRAW(4, xv_ + 4096, 0); XRAW(5, xv_ + 4096, 1024); XRAW(6, xv_ + 4096, 2048); XRAW(7, xv_ + 4096, 3072);
  }
  SCHED();

  const int m2 = xcd * 4 + (rk & 3), n2 = rk >> 2;
  const int uch = w >> 2, ui = tid & 255;
  u32 uo1 = ui < 255 ? ui * 16 + 16 : OOB;
  int xa[2], wa[2];
#pragma unroll
  for (int s = 0; s < 2; ++s) xa[s] = (64 * wt + l15) * 128 + (((4 * s + g) ^ ((l15 >> 1) & 7)) << 4);
  {
    const int q = l15 >> 2, pp = l & 3;
#pragma unroll
    for (int i = 0; i < 2; ++i) wa[i] = 16384 + (8 * g + q) * 256 + (((4 * wf + 2 * i + (pp >> 1)) ^ (4 * q + 2 * (g & 1))) << 4) + 8 * (pp & 1);
  }
  u32x4 sel;
#pragma unroll
  for (int e = 0; e < 4; ++e) sel[e] = l15 == 2 * e ? 0x00003f80u : (l15 == 2 * e + 1 ? 0x3f800000u : 0u);
  u32 a_cw = lds0 + 16384 * (1 + (w >> 2)) + (4 * g + (l15 >> 2)) * 256 + (((4 * (w & 3) + (l & 3)) ^ (4 * (l15 >> 2) + 2 * h)) << 4);
  u32 o_cq = (l < 32 && l15 >= 12) ? (l & 3) * 32 + g * 16 : OOB;
  u32 o_l0 = l < 8 ? l * 16 : OOB;
  u32 a_ua = lds0 + CV_U + (w & 1) * CV_UCH + (3 - (l15 & 3)) * CV_CS + 4088 - 8 * (l15 >> 2) - 32 * h + 16 * (g & 1) - 7 * 512 - 256 * (w >> 2) - 3 * 64;
  u32 a_ka = lds0 + CV_K + (w & 1) * 5120 + 1024 - 512 * ((w >> 1) & 1) + 32 * (l15 - h) + 16 * (g & 1) - 256 * (w >> 2) - 3 * 64;
  u32 a_zk = lds0 + CV_K + (w & 1) * 5120 + l * 16;
  u32 a_zu = lds0 + CV_U + (w & 1) * CV_UCH + ((l >> 1) & 3) * CV_CS + 4096 + (l & 1) * 16;
  u32 hfo = (l < 7 ? l : 0) * FS;
  u32x4 zero4 = {0u, 0u, 0u, 0u};
  PIN(a_ua); PIN(a_ka); PIN(a_zk); PIN(a_zu); PIN(hfo); PIN(zero4);

  u32 o_cs = g * 1024 + (l15 >> 3) * 512 + (32 * wf + 4 * (l15 & 7)) * 4;
  u32 a_cs = lds0 + M_CS + w * 256 + (4 * l15 + 2 * h + (g & 1)) * 4;
  PIN(a_cs);
  u32 a_cv = lds0 + M_CS + w * 256 + g * 16;

  PIN(sel); PIN(a_cw); PIN(o_cq); PIN(o_l0); PIN(o_cs); PIN(a_cv); PIN(uo1);
  int xa2[2], wa1[2], wa2[2];
#pragma unroll
  for (int e = 0; e < 2; ++e) {
    xa2[e] = xa[e] + 2 * G1_SLOT; wa1[e] = wa[e] + G1_SLOT; wa2[e] = wa[e] + 2 * G1_SLOT;
    PIN(xa[e]); PIN(xa2[e]); PIN(wa[e]); PIN(wa1[e]); PIN(wa2[e]);
  }
  SCHED();
  int lq = l, uq = tid & 255;
  PIN(lq); PIN(uq);
  u32 p0off;
  {
    const int e = min(9 * w + min(lq, 8), 67);
    const int m = e - 60;
    const int pb = e < 32 ? 224 + e : (e < 60 ? 28 * n2 + (e - 32) : (g1 ? ((m >> 1) * 2 + (xcd & 1)) + 8 * ((m & 1) + 4 * (rk >> 2)) : 224));
    p0off = pb * FS;
  }
  u32 ka_[8];
#pragma unroll
  for (int e = 0; e < 4; ++e) {
    const int l0 = 8 * uq + 2 * e;
    const u32 bits = pk2((float)l0, (float)(l0 + 1));
    const int i0 = uq < 32 ? l0 : (int)(bits & 0xffffu) - (0x4300 - 128);
    const int i1 = uq < 32 ? l0 + 1 : (int)(bits >> 16) - (0x4300 - 128);
    ka_[2 * e] = lds0 + M_KC + uch * 5632 + i0 * 4;
    ka_[2 * e + 1] = lds0 + M_KC + uch * 5632 + i1 * 4;
  }
  PIN(p0off);
#pragma unroll
  for (int e = 0; e < 8; ++e) PIN(ka_[e]);
  SCHED();
  if (g1 && w >= 4) {
#pragma unroll
    for (int i = 0; i < 6; ++i) { dmaw(IC<1>{}, i, w - 4); dmaw(IC<1>{}, i, w); }
    asm volatile("s_waitcnt vmcnt(12)" : "+v"(wr[0]), "+v"(wr[1]), "+v"(wr[2]), "+v"(wr[3]), "+v"(wr[4]), "+v"(wr[5]), "+v"(wr[6]), "+v"(wr[7]) :: "memory");
    PIN(pf);
    if (w < 6) {
#pragma unroll
      for (int c = 0; c < 4; ++c) {
        u32x4 o;
#pragma unroll
        for (int e = 0; e < 4; ++e) o[e] = __builtin_amdgcn_perm(wr[2 * e + 1][c >> 1], wr[2 * e][c >> 1], (c & 1) ? 0x07060302u : 0x05040100u);
        st128s(o, rs, l * 16 + c * 1024, WS_W2F + (((un2 * 56 + ukb) * 4) << 10));
      }
    }
  }
  asm volatile("s_waitcnt lgkmcnt(0)" : "+s"(tag), "+s"(p_ldt2), "+s"(dk) :: "memory");
  tag.x += 1u;

  if (!g1) {
    asm volatile("s_waitcnt vmcnt(0)" : "+v"(xr[0]), "+v"(xr[1]), "+v"(xr[2]), "+v"(xr[3]), "+v"(xr[4]), "+v"(xr[5]), "+v"(xr[6]), "+v"(xr[7]) :: "memory");
    PIN(pf);
    f32x2 v[8][4];
    float ps[8];
#pragma unroll
    for (int r = 0; r < 8; ++r) {
#pragma unroll
      for (int j = 0; j < 4; ++j) v[r][j] = up2(xr[r][j]);
      const f32x2 s2 = (v[r][0] + v[r][1]) + (v[r][2] + v[r][3]);
      ps[r] = s2.x + s2.y;
    }
    const float mean = rows8_sum(ps, l) * (1.f / D);
#pragma unroll
    for (int r = 0; r < 8; ++r) {
      const float mr = rdl(mean, 8 * r);
      f32x2 q2 = {0.f, 0.f};
#pragma unroll
      for (int j = 0; j < 4; ++j) { v[r][j] -= f32x2{mr, mr}; q2 = fma2(v[r][j], v[r][j], q2); }
      ps[r] = q2.x + q2.y;
    }
    const float var = rows8_sum(ps, l) * (1.f / D);
    const u32 ms = pk2(mean, __builtin_sqrtf(rb(rb(var) + 1e-5f)));
    if ((l & 7) == 0) st32s(ms, rs, (l >> 3) * 4, WS_ST + (64 * ax + 8 * w) * 4);
    const float riv = 1.f / bhi(ms), nbv = (mean - b2f(ms)) * riv;
#pragma unroll
    for (int r = 0; r < 8; ++r) {
      const float ri = rdl(riv, 8 * r), nb = rdl(nbv, 8 * r);
#pragma unroll
      for (int j = 0; j < 4; ++j) v[r][j] = fma2(v[r][j], f32x2{ri, ri}, f32x2{nb, nb});
    }
#pragma unroll
    for (int c = 0; c < 8; ++c) {
      u32x4 o;
#pragma unroll
      for (int e = 0; e < 4; ++e) o[e] = pk2(v[7 - 2 * e][c >> 1][c & 1], v[6 - 2 * e][c >> 1][c & 1]);
      *(LAS u32x4*)(lds + (8 * l + c) * 128 + (((7 - w) ^ (l & 7)) << 4)) = o;
    }
    LBAR();
    u32x4 tv[8];
#pragma unroll
    for (int it = 0; it < 8; ++it) tv[it] = lds128(lds + w * 8192 + it * 1024 + l * 16);
#pragma unroll
    for (int it = 0; it < 8; ++it)
      st128s(tv[it], rs, (8 * it + (l >> 3)) * 4096 + (((l & 7) ^ it) << 4), WS_XNR + w * (64 * 4096) + (L - 64 - 64 * ax) * 2);
    WAIT_V(0);
    LBAR();
    if (w == 0) st128s(u32x4{tag.x, tag.y, tag.x, tag.y}, rs, o_l0, FL_P0 + b * FS);
    STAMP(16);
    if (b == 255 && w == 7) {
#pragma unroll 1
      for (int spin = 0; spin < (1 << 18); ++spin) {
        bool ok = true;
        u32x2 v[4];
#pragma unroll
        for (int i = 0; i < 4; ++i) v[i] = ld64s(rs, l * FS, FL_P0 + 64 * i * FS);
#pragma unroll
        for (int i = 0; i < 4; ++i) ok &= v[i].x == tag.x && v[i].y == tag.y;
        if (__all(ok)) break;
        __builtin_amdgcn_s_sleep(8);
      }
      if (l == 0) st64s(tag, rs, 0, FL_EP);
    }
  }

  if (w < 4) {
    float dt = rb(__expf((w >> 1) ? bhi(p_ldt2) : b2f(p_ldt2)));
    PIN(dt);
    asm volatile("s_waitcnt vmcnt(0)" : "+v"(p_lar), "+v"(p_aim), "+v"(p_cre), "+v"(p_cim) :: "memory");
    STAMP(1);
    const float ar = -rb(__expf(b2f(p_lar)));
    const float lre2 = (ar * dt) * 1.44269504f, rev = (b2f(p_aim) * dt) * 0.15915494309f;
    u32 eb = wave_max_u32(max(p_cre & 0x7f80u, p_cim & 0x7f80u)) << 16;
    eb = eb < 0x00800000u ? 0x00800000u : (eb > 0x7e800000u ? 0x7e800000u : eb);
    const float sc = __builtin_bit_cast(float, eb) * (2.f * dt), isc = __builtin_bit_cast(float, 0x7f000000u - eb);
    f32x2 e[53], tmp[8];
    u32 hr[26], hi[26];
    const f32x2 w8 = cexp_p(lre2, rev, 8.f), w64 = cexp_p(lre2, rev, 64.f);
    const f32x2 z1 = cexp_p(lre2, -rev, 1.f), zc = cexp_p(lre2, -rev, 256.f);
    e[0] = f32x2{1.f, 0.f}; e[1] = w8; e[8] = w64; e[32] = f32x2{b2f(p_cre) * isc, -b2f(p_cim) * isc};
#define CM1(k, a, bb) tmp[k] = (a) * f32x2{(bb).x, (bb).x}; SCHED()
#define CM2(k, d, a, bn) d = fma2(f32x2{(a).y, (a).x}, bn, tmp[k]); SCHED()
#define CV(i) hr[i] = pkh(f32x2{e[2 * (i)].x, e[2 * (i) + 1].x}); hi[i] = pkh(f32x2{e[2 * (i)].y, e[2 * (i) + 1].y}); SCHED()
#define W4(a, v, i) *(LAS u32x4*)(size_t)(a) = u32x4{v[i], v[(i) + 1], v[(i) + 2], v[(i) + 3]}; SCHED()
    const f32x2 w8n = cneg(w8), w64n = cneg(w64), z1n = cneg(z1), zcn = cneg(zc);
    f32x2 z2, z4, zd, zq;
    SCHED();
    CM1(0, w8, w8); CM1(1, z1, z1); CM1(2, zc, zc); CM1(3, e[32], z1); CM1(4, e[32], zc); CM1(5, w8, w64); CM1(6, w64, w64);
    CM2(0, e[2], w8, w8n); CM2(1, z2, z1, z1n); CM2(2, zd, zc, zcn); CM2(3, e[33], e[32], z1n); CM2(4, e[40], e[32], zcn);
    CM2(5, e[9], w8, w64n); CM2(6, e[16], w64, w64n);
    const f32x2 e2n = cneg(e[2]), z2n = cneg(z2), zdn = cneg(zd);
    SCHED();
    CM1(0, e[2], w8); CM1(1, e[2], e[2]); CM1(2, z2, z2); CM1(3, zd, zd); CM1(4, e[32], z2); CM1(5, e[33], z2); CM1(6, e[32], zd); CM1(7, e[40], zd);
    CM2(0, e[3], e[2], w8n); CM2(1, e[4], e[2], e2n); CM2(2, z4, z2, z2n); CM2(3, zq, zd, zdn); CM2(4, e[34], e[32], z2n);
    CM2(5, e[35], e[33], z2n); CM2(6, e[44], e[32], zdn); CM2(7, e[46], e[40], zdn);
    const f32x2 e4n = cneg(e[4]), z4n = cneg(z4), zqn = cneg(zq);
    SCHED();
    CM1(0, e[1], e[4]); CM1(1, e[2], e[4]); CM1(2, e[3], e[4]); CM1(3, e[32], z4); CM1(4, e[33], z4); CM1(5, e[34], z4); CM1(6, e[35], z4); CM1(7, e[32], zq);
    CM2(0, e[5], e[1], e4n); CM2(1, e[6], e[2], e4n); CM2(2, e[7], e[3], e4n); CM2(3, e[36], e[32], z4n); CM2(4, e[37], e[33], z4n);
    CM2(5, e[38], e[34], z4n); CM2(6, e[39], e[35], z4n); CM2(7, e[48], e[32], zqn);
    CV(0); CV(1); CV(2); CV(3); W4(tv[0], hr, 0); CV(16); CV(17); W4(tv[4], hi, 0); CV(18); CV(19);
    CM1(0, e[34], zc); W4(tm[0], hr, 16); CM1(1, e[36], zc); CM1(2, e[38], zc); CM1(3, e[36], zd); CM1(4, e[40], zq); W4(tm[2], hi, 16);
    CM1(5, e[44], zq); CM1(6, e[46], zq); CM1(7, e[48], zq);
    CM2(0, e[41], e[34], zcn); CM2(1, e[42], e[36], zcn); CM2(2, e[43], e[38], zcn); CM2(3, e[45], e[36], zdn); CM2(4, e[49], e[40], zqn);
    CM2(5, e[50], e[44], zqn); CM2(6, e[51], e[46], zqn); CM2(7, e[52], e[48], zqn);
    CM1(0, e[42], zd); CM1(1, e[9], w64); CM1(2, e[2], w64); CM1(3, e[3], w64); CM1(4, e[4], w64); CM1(5, e[5], w64); CM1(6, e[6], w64); CM1(7, e[7], w64);
    CM2(0, e[47], e[42], zdn); CM2(1, e[17], e[9], w64n); CM2(2, e[10], e[2], w64n); CM2(3, e[11], e[3], w64n); CM2(4, e[12], e[4], w64n);
    CM2(5, e[13], e[5], w64n); CM2(6, e[14], e[6], w64n); CM2(7, e[15], e[7], w64n);
    CV(24); CV(25);
    *(LAS u32x4*)(size_t)tm2 = u32x4{hr[24], hr[25], hi[24], hi[25]};
    SCHED();
    CV(20); CV(21); CV(22); CV(23); W4(tm[1], hr, 20); CV(4); CV(5); W4(tm[3], hi, 20); CV(6); CV(7);
    CM1(0, e[16], w64); W4(tv[1], hr, 4); CM1(1, e[17], w64); CM1(2, e[10], w64); CM1(3, e[11], w64); CM1(4, e[12], w64); W4(tv[5], hi, 4);
    CM1(5, e[13], w64); CM1(6, e[14], w64); CM1(7, e[15], w64);
    CM2(0, e[24], e[16], w64n); CM2(1, e[25], e[17], w64n); CM2(2, e[18], e[10], w64n); CM2(3, e[19], e[11], w64n); CM2(4, e[20], e[12], w64n);
    CM2(5, e[21], e[13], w64n); CM2(6, e[22], e[14], w64n); CM2(7, e[23], e[15], w64n);
    u32x4 fm[8], fv[8];
    u32 ra0 = rm[0], ra1 = rm[1];
#define RM(s_) { const u32x2 lo = ldstr((LAS char*)(size_t)ra0), hi_ = ldstr((LAS char*)(size_t)ra1); fm[s_] = u32x4{lo.x, lo.y, hi_.x, hi_.y}; ra0 += rms; ra1 += rms; } SCHED()
    CM1(2, e[18], w64); RM(0); CM1(3, e[19], w64); CM1(4, e[20], w64); RM(1); CM1(5, e[21], w64); CM1(6, e[22], w64); RM(2); CM1(7, e[23], w64);
    CV(8); CV(9); RM(3); CV(10); CV(11); W4(tv[2], hr, 8);
    CM2(2, e[26], e[18], w64n); CM2(3, e[27], e[19], w64n); RM(4); CM2(4, e[28], e[20], w64n); W4(tv[6], hi, 8); CM2(5, e[29], e[21], w64n);
    CM2(6, e[30], e[22], w64n); RM(5); CM2(7, e[31], e[23], w64n);
    CV(12); RM(6); CV(13); CV(14); RM(7); CV(15); W4(tv[3], hr, 12); W4(tv[7], hi, 12);
#pragma unroll
    for (int s = 0; s < 8; ++s) {
      const u32x2 lo = ldstr((LAS char*)(size_t)rv[0] + s * 1024), hi_ = ldstr((LAS char*)(size_t)rv[1] + s * 1024);
      fv[s] = u32x4{lo.x, lo.y, hi_.x, hi_.y};
    }
    float k2048 = e[52].x;
    k2048 += dppz<0x111, 0xf>(k2048); k2048 += dppz<0x112, 0xf>(k2048); k2048 += dppz<0x114, 0xf>(k2048); k2048 += dppz<0x118, 0xf>(k2048);
    k2048 += dppz<0x142, 0xa>(k2048); k2048 += dppz<0x143, 0xc>(k2048);
    SCHED();
    STAMP(4);
    f32x16 kk;
#pragma unroll
    for (int r = 0; r < 16; ++r) kk[r] = 0.f;
#pragma unroll
    for (int s = 0; s < 8; ++s) kk = mfma32h(fm[s], fv[s], kk);
    LAS float* kc = (LAS float*)(lds + M_KC + w * 2816);
    *(LAS f32x4*)(kc + 8 * t + 4 * h) = f32x4{kk[0], kk[1], kk[2], kk[3]} * sc;
    *(LAS f32x2*)(kc + (h ? 384 + 2 * t : 256 + 4 * t)) = f32x2{kk[4], kk[5]} * sc;
    *(LAS f32x2*)(kc + (h ? 448 + 2 * t : 258 + 4 * t)) = f32x2{kk[6], kk[7]} * sc;
    LAS float* k3 = kc + (h ? 671 - t : 512 + t);
    k3[0] = (h ? k2048 : kk[8]) * sc;
    if (h == 0) { k3[32] = kk[9] * sc; k3[64] = kk[10] * sc; k3[96] = kk[11] * sc; }
  }
  STAMP(5);

  u32x4 wfr[28];
  const int w2off = WS_W2F + (((n2 * 56 + 7 * w) * 4) << 10);
  auto w2ld = [&](int i) { wfr[i] = ld128s(rs, l * 16 + (i & 3) * 1024, w2off + (i >> 2) * 4096); };
  f32x2 kx[8];
  u32 kpk[4];
  auto expand_rd = [&](int e) { LAS float* q = (LAS float*)(size_t)ka_[e]; kx[e] = f32x2{q[0], q[704]}; };
  auto expand_pk = [&](int e) {
    float a = kx[2 * e].x + kx[2 * e].y, c = kx[2 * e + 1].x + kx[2 * e + 1].y;
    PIN(a); PIN(c);
    kpk[e] = pk2(a, c);
    PIN(kpk[e]);
  };
  auto expand_wr = [&]() { *(LAS u32x4*)(lds + CV_K + uch * 5120 + 1024 + ui * 16) = u32x4{kpk[0], kpk[1], kpk[2], kpk[3]}; };
  u32x4 u0;
  u32x2 u1;
  auto load_u = [&](int k) {
    if (k == 0) u0 = ld128s(rs, ui * 16, WS_XNR + (2 * b + uch) * 4096);
    else u1 = ld64s(rs, uo1, WS_XNR + (2 * b + uch) * 4096);
  };
  auto copy_u = [&](int s) {
    const u32 dd[6] = {u0.x, u0.y, u0.z, u0.w, u1.x, u1.y};
    const int o = s >> 1;
    u32x4 v;
#pragma unroll
    for (int k = 0; k < 4; ++k) v[k] = (s & 1) ? __builtin_amdgcn_alignbit(dd[o + k + 1], dd[o + k], 16) : dd[o + k];
    *(LAS u32x4*)(lds + CV_U + uch * CV_UCH + s * CV_CS + ui * 16) = v;
  };
#define W2Q 97
#define NW2AV 15
  constexpr int NW2A = NW2AV;

  if (g1) {
    f32x4 acc[4][4];
#pragma unroll
    for (int i = 0; i < 4; ++i)
#pragma unroll
      for (int j = 0; j < 4; ++j) acc[i][j] = f32x4{0.f, 0.f, 0.f, 0.f};
    u32x4 fa[2][4], fb[2][4];
    auto rdA = [&](int st, int s, int i) {
      LAS char* q = lds + (st % 3 == 0 ? wa[i & 1] : st % 3 == 1 ? wa1[i & 1] : wa2[i & 1]) + (i >> 1) * 16384 + s * 8192;
      const u32x2 lo = ldstr(q), hi = ldstr(q + 1024);
      fa[s][i] = u32x4{lo.x, lo.y, hi.x, hi.y};
    };
    auto rdB = [&](int st, int s, int j) { fb[s][j] = lds128(lds + (st % 3 == 2 ? xa2[s] : xa[s] + (st % 3) * G1_SLOT) + j * 2048); };
    const int so_cs = (mt & 2) == 0 ? WS_CS + (ft * 8 + kt0) * 1024 + w * 128 : OOB;
    const int so_p0 = w == 0 ? FL_P0 + b * FS : OOB;

    if (w >= 4) __builtin_amdgcn_s_setprio(1);
    LBAR();
    STAMP(6);
    rdB(0, 0, 0); rdA(0, 0, 0); rdA(0, 0, 1); rdA(0, 0, 2); rdA(0, 0, 3); rdB(0, 0, 1); rdB(0, 0, 2); rdB(0, 0, 3);
    dmaw(IC<2>{}, 0, w); dmaw(IC<2>{}, 1, w); dmaw(IC<2>{}, 2, w);
    u32x2 fl;
    u32 msv[4];
    float nmv[4];
    f32x4 csp[2];
    u32x4 cw[4];
    f32x4 cs, cv[4];
    static_for<8>([&](auto st_) {
      constexpr int st = decltype(st_)::v;
#pragma unroll
      for (int s = 0; s < 2; ++s) {
        const int nst = s ? st + 1 : st, ns = s ^ 1;
        if (s == 1 && st < 7) {
          if (st == 1 || st == 4) WAIT_V(7);
          else if (st == 3) WAIT_V(9);
          else if (st == 6) WAIT_V(11);
          else WAIT_V(6);
        }
        if (s == 1) {
          LBAR();
          if (st == 2) STAMP(18);
          if (st == 5) STAMP(21);
        }
        SCHED();
#pragma unroll
        for (int n = 0; n < 16; ++n) {
          acc[n & 3][n >> 2] = mfma16(fa[s][n & 3], fb[s][n >> 2], acc[n & 3][n >> 2]);
          SCHED();
          if (nst < 8) {
            if (n == 0) rdB(nst, ns, 3);
            if (n == 1) rdB(nst, ns, 2);
            if (n == 2) rdB(nst, ns, 1);
            if (n == 3) rdA(nst, ns, 3);
            if (n == 5) rdA(nst, ns, 2);
            if (n == 7) rdA(nst, ns, 1);
            if (n == 9) rdA(nst, ns, 0);
            if (n == 10) rdB(nst, ns, 0);
          }
          if (nst + 2 < 8) {
            const int pc = (s ? 0 : 3);
            if (n == 4 || n == 8 || n == 12) { if (s) dm0(IC<(st + 3) & 7>{}, pc + (n >> 2) - 1, w); else dm0(IC<(st + 2) & 7>{}, pc + (n >> 2) - 1, w); }
            if (n == 6 || n == 11 || n == 14) { const int k = n == 6 ? 0 : n == 11 ? 1 : 2; if (s) dgo(IC<(st + 3) & 7>{}, pc + k, w); else dgo(IC<(st + 2) & 7>{}, pc + k, w); }
          }
          SCHED();
          const int q = st * 32 + s * 16 + n;
          if (q == 4 || q == 8 || q == 12 || q == 13) cw[q == 13 ? 3 : (q >> 2) - 1] = lds128((LAS char*)(size_t)a_cw + (q == 13 ? 3 : (q >> 2) - 1) * 4096);
          if (q == 20) cs = mfma16(sel, cw[0], f32x4{0.f, 0.f, 0.f, 0.f});
          if (q == 24 || q == 28 || q == 29) cs = mfma16(sel, cw[q == 29 ? 3 : (q >> 2) - 5], cs);
          if (q == 36) {
#pragma unroll
            for (int e = 0; e < 4; ++e) { const float v = cs[e]; cs[e] = v + dppx<0x114>(v); }
            PIN(cs);
          }
          if (q == 40) {
#pragma unroll
            for (int e = 0; e < 4; ++e) { const float v = cs[e]; cs[e] = v + dppx<0x118>(v); }
            PIN(cs);
          }
          if (q == 45) st128s(__builtin_bit_cast(u32x4, cs), rs, o_cq, so_cs);
          if (q >= 52 && q < 84 && (q & 3) == 0) expand_rd((q - 52) >> 2);
          if (q >= 92 && q < 124 && (q & 7) == 4) { PIN(kx[(q - 92) >> 2]); expand_pk((q - 92) >> 3); }
          if (q == 200) expand_wr();
          if (q == 85) st128s(u32x4{tag.x, tag.y, tag.x, tag.y}, rs, o_l0, so_p0);
          if (q == 101) st128s(u32x4{~tag.x, tag.y, ~tag.x, tag.y}, rs, o_l0, FL_H + ((mt * 2 + wt) * 56 + ft * 4 + wf) * FS);
          if (q == 105) st128s(u32x4{~tag.x, tag.y, ~tag.x, tag.y}, rs, o_l0, FL_Y + ((7 - 2 * ((w & 1) + 2 * (w >> 2)) - ((w >> 1) & 1)) * 256 + b) * FS);
          if (q == 143) asm volatile("global_load_dwordx2 %0, %1, %2 sc1" : "=v"(fl) : "v"(p0off), "s"(p.ws + FL_P0) : "memory");
          if (q == 173) {
            asm volatile("s_waitcnt vmcnt(5)" : "+v"(fl) :: "memory");
            if (!__all(fl.x == tag.x && fl.y == tag.y)) poll(rs, p0off, FL_P0, tag);
          }
          if (q == 176 || q == 178) load_u((q - 176) >> 1);
          if (q == 180 || q == 182) csp[(q - 180) >> 1] = __builtin_bit_cast(f32x4, ld128s(rs, o_cs, WS_CS + (ft * 8 + 2 * (q - 180)) * 1024));
          if (q == 184) msv[0] = ld32s(rs, l * 4, WS_ST + (mt * 128 + 64 * wt) * 4);
          if (q >= 187 && ((q - 187) & 3) == 0 && ((q - 187) >> 2) < NW2A) w2ld((q - 187) >> 2);
          if (q == 205 || q == 213 || q == 221 || q == 229) copy_u((q - 205) >> 3);
          if (q == 209) {
            PIN(csp[0]); PIN(csp[1]);
            csp[0] += csp[1];
            const auto r0 = __builtin_amdgcn_permlane32_swap(f2u(csp[0][0]), f2u(csp[0][2]), false, false);
            const auto r1 = __builtin_amdgcn_permlane32_swap(f2u(csp[0][1]), f2u(csp[0][3]), false, false);
            csp[0][0] = u2f(r0[0]) + u2f(r0[1]);
            csp[0][1] = u2f(r1[0]) + u2f(r1[1]);
            PIN(csp[0]);
          }
          if (q == 217) {
            const auto r = __builtin_amdgcn_permlane16_swap(f2u(csp[0][0]), f2u(csp[0][1]), false, false);
            *(LAS float*)(size_t)a_cs = u2f(r[0]) + u2f(r[1]);
          }
          if (q == 220) {
            PIN(msv[0]);
            const auto m = __builtin_amdgcn_permlane32_swap(msv[0], msv[0], false, false);
            msv[0] = m[0]; msv[2] = m[1];
            PIN(msv[0]); PIN(msv[2]);
          }
          if (q == 225 || q == 228) {
            const int j = q == 225 ? 0 : 2;
            const auto m = __builtin_amdgcn_permlane16_swap(msv[j], msv[j], false, false);
            msv[j] = m[0]; msv[j + 1] = m[1];
            PIN(msv[j]); PIN(msv[j + 1]);
          }
          if (q >= 232 && q < 236) { cv[q - 232] = *(LAS f32x4*)((LAS char*)(size_t)a_cv + (q - 232) * 64); }
          if (q >= 236 && q < 240) { nmv[q - 236] = -b2f(msv[q - 236]); PIN(nmv[q - 236]); }
          if (q >= 240) {
            const int m = (n + 8) & 15, ii = m & 3, jj = m >> 2;
#pragma unroll
            for (int r = 0; r < 4; ++r) {
              float v = __builtin_fmaf(nmv[jj], cv[ii][r], acc[ii][jj][r]);
              PIN(v);
              acc[ii][jj][r] = v;
            }
          }
          SCHED();
        }
      }
    });
    STAMP(7);
    __builtin_amdgcn_s_setprio(3);
#pragma unroll
    for (int j = 0; j < 4; ++j) {
      const float sd = bhi(msv[j]), sd2 = sd * sd;
      const float ce = -1.44269504f * __builtin_amdgcn_rcpf(sd);
      u32x4 o;
#pragma unroll
      for (int r = 0; r < 4; ++r) {
        const f32x2 gg = {acc[r >> 1][j][2 * (r & 1)], acc[r >> 1][j][2 * (r & 1) + 1]};
        const f32x2 uu = {acc[2 + (r >> 1)][j][2 * (r & 1)], acc[2 + (r >> 1)][j][2 * (r & 1) + 1]};
        const f32x2 a = gg * f32x2{ce, ce};
        const f32x2 e = {__builtin_amdgcn_exp2f(a.x), __builtin_amdgcn_exp2f(a.y)};
        const f32x2 d = fma2(e, f32x2{sd2, sd2}, f32x2{sd2, sd2});
        const f32x2 q = {__builtin_amdgcn_rcpf(d.x), __builtin_amdgcn_rcpf(d.y)};
        o[r] = pk2v((gg * uu) * q);
      }
      st128s(o, rs, l * 16, WS_H + (((mt * 8 + 4 * wt + j) * 56 + ft * 4 + wf) << 10));
      SCHED();
    }
    __builtin_amdgcn_s_setprio(1);
    STAMP(8);
  } else {
    poll(rs, p0off, FL_P0, tag);
    STAMP(17);
    LBAR();
    STAMP(19);
    load_u(0); load_u(1);
#pragma unroll
    for (int e = 0; e < 8; ++e) expand_rd(e);
    SCHED();
#pragma unroll
    for (int i = 0; i < 5; ++i) w2ld(i);
    SCHED();
#pragma unroll
    for (int e = 0; e < 4; ++e) expand_pk(e);
    LBAR();
    expand_wr();
#pragma unroll
    for (int s = 0; s < 4; ++s) copy_u(s);
    SCHED();
#pragma unroll
    for (int i = 5; i < NW2A; ++i) w2ld(i);
    __builtin_amdgcn_s_waitcnt(0x0F70);
    LBAR();
  }

  STAMP(10);
  u32x2 hfl;
  u32x4 hf[28];
  const int hoff = WS_H + ((4 * m2 * 56 + 7 * w) << 10);
  const int jr = 0;
  auto hld = [&](int n) { hf[n] = ld128s(rs, l * 16, hoff + (((n / 7) ^ jr) * 56 + n % 7) * 1024); };
  const int hfs = FL_H + (m2 * 56 + 7 * w) * FS;
#define PDV 12
  constexpr int PD = PDV;
  {
    LAS char *ua = (LAS char*)(size_t)a_ua, *ka = (LAS char*)(size_t)a_ka;
    *(LAS u32x4*)(size_t)a_zk = zero4;
    *(LAS u32x4*)(size_t)a_zu = zero4;
    asm volatile("" ::: "memory");
    f32x4 acc[4];
#pragma unroll
    for (int i = 0; i < 4; ++i) acc[i] = f32x4{0.f, 0.f, 0.f, 0.f};
    const int so_h = g1 ? FL_H + ((mt * 2 + wt) * 56 + ft * 4 + wf) * FS : OOB;
    auto hook = [&](int rr, int c) {
#define HPUB 3
#define HPOLL 24
      const int sq = rr * 8 + c;
#define WS0 4
#define WSN 13
      auto nw = [&](int z) { return z <= WS0 ? 0 : z >= WS0 + WSN ? 28 - NW2A : ((z - WS0) * (28 - NW2A) + WSN - 1) / WSN; };
      if (sq == HPUB) {
        constexpr int c0 = HPUB <= WS0 ? 0 : HPUB >= WS0 + WSN ? 28 - NW2A : ((HPUB - WS0) * (28 - NW2A) + WSN - 1) / WSN;
        asm volatile("s_waitcnt vmcnt(%0)" :: "i"(c0) : "memory");
        st128s(u32x4{tag.x, tag.y, tag.x, tag.y}, rs, o_l0, so_h);
        __builtin_amdgcn_s_setprio(0);
        STAMP(9);
      }
      if (sq >= 0 && nw(sq + 1) > nw(sq)) w2ld(NW2A + nw(sq));
      if (sq == (HPOLL < 32 ? HPOLL - 5 : 31)) hfl = ld64s(rs, hfo, hfs);
      if (sq == HPOLL) {
        STAMP(2);
        if (!__all(hfl.x == tag.x && hfl.y == tag.y)) poll(rs, hfo, hfs, tag);
        asm volatile("" ::: "memory");
        STAMP(23);
      }
      if (sq >= HPOLL) {
#pragma unroll
        for (int n = (sq - HPOLL) * PD / (HPOLL < 32 ? 32 - HPOLL : 1); n < (sq + 1 - HPOLL) * PD / (HPOLL < 32 ? 32 - HPOLL : 1); ++n) hld(n);
      }
    };
    conv_tiles(ua, ka, acc, hook);
#pragma unroll
    for (int i = 0; i < 4; ++i) *(LAS f32x4*)(lds + CV_X + (w * 4 + i) * 1024 + l * 16) = acc[i];
    if (tid == 0) *(LAS u32*)(lds + M_CNT) = 0u;
  }
  LBAR();
  STAMP(11);
  int ytile;
  {
    const int ts = (w >> 1) & 1, i = (w & 1) + 2 * (w >> 2);
    ytile = 7 - 2 * i - ts;
    const int pos = 256 * ytile + 16 * l15 + 4 * g;
    f32x4 y0[2], y1[2];
    u32x2 ur[2];
#pragma unroll
    for (int ch = 0; ch < 2; ++ch) {
      y0[ch] = *(LAS f32x4*)(lds + CV_X + ((ch + 2 * ts) * 4 + i) * 1024 + l * 16);
      y1[ch] = *(LAS f32x4*)(lds + CV_X + ((ch + 2 * ts + 4) * 4 + i) * 1024 + l * 16);
      ur[ch] = lds64(lds + CV_U + ch * CV_UCH + (2044 - pos) * 2);
    }
    LBAR();
    float res[2][4];
#pragma unroll
    for (int ch = 0; ch < 2; ++ch) {
      const float dsk = ch ? bhi(dk) : b2f(dk);
      const f32x4 y = y0[ch] + y1[ch];
      res[ch][0] = __builtin_fmaf(bhi(ur[ch].y), dsk, y[0]); res[ch][1] = __builtin_fmaf(b2f(ur[ch].y), dsk, y[1]);
      res[ch][2] = __builtin_fmaf(bhi(ur[ch].x), dsk, y[2]); res[ch][3] = __builtin_fmaf(b2f(ur[ch].x), dsk, y[3]);
    }
    const u32x4 o = {pk2(res[0][0], res[1][0]), pk2(res[0][1], res[1][1]), pk2(res[0][2], res[1][2]), pk2(res[0][3], res[1][3])};
    st128s(o, rs, pos * 4, WS_Y + b * 8192);
    if (HPOLL == 32) {
      STAMP(2);
      if (!__all(hfl.x == tag.x && hfl.y == tag.y)) poll(rs, hfo, hfs, tag);
      asm volatile("" ::: "memory");
      STAMP(23);
#pragma unroll
      for (int n = 0; n < PD; ++n) hld(n);
    }
  }
  STAMP(12);

  {
    const int oj = w >> 1, rh = w & 1;
    const int etok = 64 * m2 + 16 * (oj ^ jr) + l15, ech = 64 * n2 + 16 * g + 8 * rh;
    u32x4 xv;
    u32 yo = (ech >> 1) * 8192 + etok * 4, yfv = (8 * g + (l & 3)) * FS;
    PIN(yo); PIN(yfv);
    SCHED();
    f32x4 acc[4][4];
#pragma unroll
    for (int i = 0; i < 4; ++i)
#pragma unroll
      for (int j = 0; j < 4; ++j) acc[i][j] = f32x4{0.f, 0.f, 0.f, 0.f};
    u32x2 yfl;
    const int yfo = FL_Y + ((m2 >> 2) * 256 + 32 * n2 + 4 * rh) * FS;
    LAS char* pw = opaque(lds + w * 16384 + l * 8);
    LAS char* pr = opaque(lds + w * 2048 + l * 8);
    LAS char* pr2 = opaque(pr + 65536);
    u32 a_cnt = lds0 + M_CNT + l * 4, one = 1u;
    PIN(a_cnt); PIN(one);
    auto part_wr = [&](int j, int k) {
      *(LAS f32x2*)(pw + (j * 8 + k) * 512) = f32x2{acc[k & 3][j][2 * (k >> 2)], acc[k & 3][j][2 * (k >> 2) + 1]};
    };
#pragma unroll
    for (int n = 0; n < 28; ++n) {
#pragma unroll
      for (int i = 0; i < 4; ++i) {
        acc[i][n / 7] = mfma16(wfr[4 * (n % 7) + i], hf[n], acc[i][n / 7]);
        SCHED();
        if (i == 3 && n + PD < 28) hld(n + PD);
        if ((i & 1) == 0 && n >= 8 && n % 7 >= 1 && n % 7 < 5) part_wr(n / 7 - 1, (n % 7 - 1) * 2 + (i >> 1));
        SCHED();
      }
      if (n == YP1 && w >= 4) { asm volatile("s_waitcnt vmcnt(%0)" :: "i"(YP1 + 1) : "memory"); st128s(u32x4{tag.x, tag.y, tag.x, tag.y}, rs, o_l0, FL_Y + (ytile * 256 + b) * FS); }
      if (n == YP0 && w < 4) { asm volatile("s_waitcnt vmcnt(%0)" :: "i"(YP0 + 1) : "memory"); st128s(u32x4{tag.x, tag.y, tag.x, tag.y}, rs, o_l0, FL_Y + (ytile * 256 + b) * FS); }
      if (n == 21) xv = *(const GAS u32x4*)((const char*)p.x + (size_t)etok * 1024 + ech * 2);
      if (n == 22) yfl = ld64s(rs, yfv, yfo);
      if (n == TB && w >= 4) LBAR();
      SCHED();
    }
    STAMP(13);
#pragma unroll
    for (int k = 0; k < 8; ++k) part_wr(3, k);
    asm volatile("" ::: "memory");
    if (w < 4) LBAR();
    else asm volatile("ds_add_u32 %0, %1" :: "v"(a_cnt), "v"(one) : "memory");
    if (!__all(yfl.x == tag.x && yfl.y == tag.y)) poll(rs, yfv, yfo, tag);
    asm volatile("" ::: "memory");
    u32 yv[4];
#pragma unroll
    for (int j = 0; j < 4; ++j) yv[j] = ld32s(rs, yo, WS_Y + j * 8192);
    f32x2 pp[8][4];
#pragma unroll
    for (int ww = 0; ww < 4; ++ww)
#pragma unroll
      for (int i = 0; i < 4; ++i) pp[ww][i] = *(volatile LAS f32x2*)(pr + ww * 16384 + i * 512);
    if (w >= 4) {
      volatile LAS u32* cnt = (volatile LAS u32*)(lds + M_CNT);
#pragma unroll 1
      for (int spin = 0; spin < (1 << 20) && *cnt < 4u; ++spin) {}
      asm volatile("" ::: "memory");
    }
    STAMP(14);
#pragma unroll
    for (int ww = 0; ww < 4; ++ww)
#pragma unroll
      for (int i = 0; i < 4; ++i) pp[4 + ww][i] = *(volatile LAS f32x2*)(pr2 + ww * 16384 + i * 512);
    SCHED();
    f32x2 sm[4];
#pragma unroll
    for (int i = 0; i < 4; ++i) sm[i] = ((pp[0][i] + pp[1][i]) + (pp[2][i] + pp[3][i])) + ((pp[4][i] + pp[5][i]) + (pp[6][i] + pp[7][i]));
    const u32 ff[4] = {pk2(sm[0].x, sm[1].x), pk2(sm[2].x, sm[3].x), pk2(sm[0].y, sm[1].y), pk2(sm[2].y, sm[3].y)};
    u32x4 r;
#pragma unroll
    for (int j = 0; j < 4; ++j) {
      const u32 s = pk2v(up2(xv[j]) + up2(yv[j]));
      r[j] = pk2v(up2(s) + up2(ff[j]));
    }
    *(GAS u32x4*)((char*)p.out + (size_t)etok * 1024 + ech * 2) = r;
  }
  STAMP(15);
  STAMP_DUMP();
}

extern "C" void kernel_launch(void* const* d_in, const int* in_sizes, int n_in, void* d_out, int out_size, void* d_ws,
                              size_t ws_size, hipStream_t stream) {
  Params p;
  p.x = (const u16*)d_in[0]; p.lar = (const u16*)d_in[1]; p.aim = (const u16*)d_in[2]; p.cre = (const u16*)d_in[3];
  p.cim = (const u16*)d_in[4]; p.ldt = (const u16*)d_in[5]; p.dsk = (const u16*)d_in[6];
  p.w1 = (const u16*)d_in[8]; p.w3 = (const u16*)d_in[9]; p.w2 = (const u16*)d_in[10];
  p.out = (u16*)d_out; p.ws = (char*)d_ws;
  fused<<<256, 512, 0, stream>>>(p);
}
```
